```python
import torch

def _softmax(x, axis=-1):
    return torch.softmax(x, dim=axis)
def _gelu(x):
    return torch.nn.functional.gelu(x, approximate="tanh")
def _silu(x):
    return torch.nn.functional.silu(x)
def _sigmoid(x):
    return torch.sigmoid(x)
def _layernorm(x, axis=-1, eps=1e-5):
    m = x.mean(dim=axis, keepdim=True)
    v = x.var(dim=axis, keepdim=True, unbiased=False)
    return (x - m) / torch.sqrt(v + eps)
def _scatter_add(tgt, idx, src, axis=0):
    return tgt.clone().index_add_(axis, idx, src)
def _segment_sum(src, seg, n):
    out = torch.zeros((n,) + src.shape[1:], dtype=src.dtype)
    return out.index_add_(0, seg, src)
def _pad(x, pw):
    flat = tuple(v for pair in reversed(pw) for v in pair)
    return torch.nn.functional.pad(x, flat)

kernel_name = 'sequential_preLN__mha_block__glu_gated_reglu__prompt'

def setup_inputs():

    _w = (lambda a: (lambda t: t.to(torch.bfloat16) if t.is_floating_point() else t)(torch.as_tensor(a)))
    def _A_setup():
        B, S, D, H = 1, 2048, 1024, 32
        return {
            "x": _w((lambda *s: torch.randn(*s, dtype=torch.bfloat16) * 0.05)(B, S, D)),
            "wqkv": _w((lambda *s: torch.randn(*s, dtype=torch.bfloat16) * 0.05)(D, 3 * D)),
            "wo": _w((lambda *s: torch.randn(*s, dtype=torch.bfloat16) * 0.05)(D, D)),
        }
    _A_in = _A_setup()

    def _B_setup():
        B, S, D, F = 1, 2048, 1024, 3584
        return {
            "x": _w((lambda *s: torch.randn(*s, dtype=torch.bfloat16) * 0.05)(B, S, D)),
            "W_gate": _w((lambda *s: torch.randn(*s, dtype=torch.bfloat16) * 0.8)(D, F) * (2.0 / D) ** 0.5),
            "W_up": _w((lambda *s: torch.randn(*s, dtype=torch.bfloat16) * 0.8)(D, F) * (2.0 / D) ** 0.5),
            "W_down": _w((lambda *s: torch.randn(*s, dtype=torch.bfloat16) * 0.8)(F, D) * (1.0 / F) ** 0.5),
        }
    _B_in = _B_setup()

    return {
        "A_x": _A_in["x"],
        "A_wqkv": _A_in["wqkv"],
        "A_wo": _A_in["wo"],
        "B_x": _B_in["x"],
        "B_W_gate": _B_in["W_gate"],
        "B_W_up": _B_in["W_up"],
        "B_W_down": _B_in["W_down"],
    }

def reference(A_x, A_wqkv, A_wo, B_x, B_W_gate, B_W_up, B_W_down):
    def _A_ref(x, wqkv, wo):
        B, S, D = x.shape
        H = 32
        Dh = D // H
        qkv = (lambda x, *d: x.permute(*d))((x @ wqkv).reshape(B, S, 3, H, Dh), 2, 0, 3, 1, 4)
        q, k, v = qkv[0], qkv[1], qkv[2]
        scores = torch.einsum("bhqd,bhkd->bhqk", q, k) * (Dh ** -0.5)
        p = _softmax(scores, axis=-1)
        o = torch.einsum("bhqk,bhkd->bhqd", p, v)
        return (lambda x, *d: x.permute(*d))(o, 0, 2, 1, 3).reshape(B, S, D) @ wo

    def _B_ref(x, W_gate, W_up, W_down):
        gate = torch.einsum("bsd,df->bsf", x, W_gate)
        up = torch.einsum("bsd,df->bsf", x, W_up)
        gate = gate * (lambda x, dt: x.to(dt))(gate > 0, gate.dtype)
        h = gate * up
        return torch.einsum("bsf,fd->bsd", h, W_down)

    x = A_x
    h = x + _A_ref(_layernorm(x, axis=-1), A_wqkv, A_wo)
    h2 = h + _B_ref(_layernorm(h, axis=-1), B_W_gate, B_W_up, B_W_down)
    return h2
```

```cpp
#include <hip/hip_runtime.h>
#include <hip/hip_bf16.h>
#include <stdint.h>
typedef unsigned short u16;
using bf16x8 = __attribute__((ext_vector_type(8))) short;
using f32x4 = __attribute__((ext_vector_type(4))) float;
constexpr int S=2048, D=1024, H=32, DH=32, F=3584;
__device__ __forceinline__ float bf2f(u16 v){ return __uint_as_float(((unsigned)v)<<16); }
__device__ __forceinline__ u16 f2bf(float f){ unsigned u=__float_as_uint(f); u += 0x7fffu + ((u>>16)&1u); return (u16)(u>>16); }

__global__ void ln_k(const u16* x, u16* y){
  int row=blockIdx.x; int t=threadIdx.x; __shared__ float red[8];
  float v[4]; float s=0; for(int i=0;i<4;i++){ v[i]=bf2f(x[row*D+t*4+i]); s+=v[i]; }
  for(int o=32;o;o>>=1) s+=__shfl_xor(s,o);
  if((t&63)==0) red[t>>6]=s; __syncthreads();
  float m=(red[0]+red[1]+red[2]+red[3])/D; __syncthreads();
  float q=0; for(int i=0;i<4;i++){ float d=v[i]-m; q+=d*d; }
  for(int o=32;o;o>>=1) q+=__shfl_xor(q,o);
  if((t&63)==0) red[t>>6]=q; __syncthreads();
  float var=(red[0]+red[1]+red[2]+red[3])/D; float r=rsqrtf(var+1e-5f);
  for(int i=0;i<4;i++) y[row*D+t*4+i]=f2bf((v[i]-m)*r);
}
template<int MODE>
__global__ void gemm_k(const u16* A, const u16* B, const u16* B2, const u16* R, u16* C, int M,int N,int K){
  __shared__ u16 As[64][40]; __shared__ u16 Bs[64][40]; __shared__ u16 Bs2[64][40];
  int t=threadIdx.x, w=t>>6, l=t&63; int m0=blockIdx.y*64, n0=blockIdx.x*64;
  int wr=w>>1, wc=w&1; f32x4 acc[2][2]={}, acc2[2][2]={};
  for(int k0=0;k0<K;k0+=32){
    { int r=t>>2, c=(t&3)*8; *(bf16x8*)&As[r][c] = *(const bf16x8*)&A[(long)(m0+r)*K+k0+c]; }
    { int kr=t>>3, c=(t&7)*8; bf16x8 v=*(const bf16x8*)&B[(long)(k0+kr)*N+n0+c]; for(int j=0;j<8;j++) Bs[c+j][kr]=v[j];
      if(MODE==2){ bf16x8 v2=*(const bf16x8*)&B2[(long)(k0+kr)*N+n0+c]; for(int j=0;j<8;j++) Bs2[c+j][kr]=v2[j]; } }
    __syncthreads();
    bf16x8 a[2],b[2],b2[2];
    for(int i=0;i<2;i++){ a[i]=*(bf16x8*)&As[wr*32+i*16+(l&15)][(l>>4)*8]; b[i]=*(bf16x8*)&Bs[wc*32+i*16+(l&15)][(l>>4)*8]; if(MODE==2) b2[i]=*(bf16x8*)&Bs2[wc*32+i*16+(l&15)][(l>>4)*8]; }
    for(int i=0;i<2;i++)for(int j=0;j<2;j++){ acc[i][j]=__builtin_amdgcn_mfma_f32_16x16x32_bf16(a[i],b[j],acc[i][j],0,0,0); if(MODE==2) acc2[i][j]=__builtin_amdgcn_mfma_f32_16x16x32_bf16(a[i],b2[j],acc2[i][j],0,0,0);}
    __syncthreads();
  }
  for(int i=0;i<2;i++)for(int j=0;j<2;j++)for(int r=0;r<4;r++){
    int row=m0+wr*32+i*16+(l>>4)*4+r, col=n0+wc*32+j*16+(l&15); float v=acc[i][j][r];
    if(MODE==1) v+=bf2f(R[(long)row*N+col]);
    if(MODE==2) v=fmaxf(v,0.f)*acc2[i][j][r];
    C[(long)row*N+col]=f2bf(v);
  }
}
__global__ void attn_k(const u16* qkv, u16* o){
  int h=blockIdx.y; int q=blockIdx.x*256+threadIdx.x; __shared__ float Ks[64][33], Vs[64][33];
  float qv[32], acc[32]; for(int d=0;d<32;d++){ qv[d]=bf2f(qkv[(long)q*3072+h*32+d])*0.17677669529663687f; acc[d]=0; }
  float m=-1e30f,lsum=0;
  for(int k0=0;k0<S;k0+=64){
    __syncthreads();
    for(int i=threadIdx.x;i<64*32;i+=256){ int r=i>>5,c=i&31; Ks[r][c]=bf2f(qkv[(long)(k0+r)*3072+1024+h*32+c]); Vs[r][c]=bf2f(qkv[(long)(k0+r)*3072+2048+h*32+c]); }
    __syncthreads();
    for(int j=0;j<64;j++){ float s=0;
      #pragma unroll
      for(int d=0;d<32;d++) s+=qv[d]*Ks[j][d];
      float mn=fmaxf(m,s); float a=__expf(m-mn), p=__expf(s-mn); lsum=lsum*a+p;
      #pragma unroll
      for(int d=0;d<32;d++) acc[d]=acc[d]*a+p*Vs[j][d]; m=mn; }
  }
  for(int d=0;d<32;d++) o[(long)q*D+h*32+d]=f2bf(acc[d]/lsum);
}
extern "C" void kernel_launch(void* const* d_in, const int* in_sizes, int n_in, void* d_out, int out_size, void* d_ws, size_t ws_size, hipStream_t stream){
  const u16* x=(const u16*)d_in[0]; const u16* wqkv=(const u16*)d_in[1]; const u16* wo=(const u16*)d_in[2];
  const u16* wg=(const u16*)d_in[4]; const u16* wu=(const u16*)d_in[5]; const u16* wd=(const u16*)d_in[6];
  u16* ws=(u16*)d_ws; u16* xn=ws; u16* qkv=xn+S*D; u16* o=qkv+S*3*D; u16* h=o+S*D; u16* hn=h+S*D; u16* g=hn+S*D;
  ln_k<<<S,256,0,stream>>>(x,xn);
  gemm_k<0><<<dim3(3*D/64,S/64),256,0,stream>>>(xn,wqkv,nullptr,nullptr,qkv,S,3*D,D);
  attn_k<<<dim3(S/256,H),256,0,stream>>>(qkv,o);
  gemm_k<1><<<dim3(D/64,S/64),256,0,stream>>>(o,wo,nullptr,x,h,S,D,D);
  ln_k<<<S,256,0,stream>>>(h,hn);
  gemm_k<2><<<dim3(F/64,S/64),256,0,stream>>>(hn,wg,wu,nullptr,g,S,F,D);
  gemm_k<1><<<dim3(D/64,S/64),256,0,stream>>>(g,wd,nullptr,h,(u16*)d_out,S,D,F);
}
```

```cpp
#include <hip/hip_runtime.h>
#include <stdint.h>
#define CLOB "v1","v2","v3","v4","v5","v6","v7","v8","v9","v10","v11","v12","v13","v14","v15","v16","v17","v18","v19","v20","v21","v22","v23","v24","v25","v26","v27","v28","v29","v30","v31","v32","v33","v34","v35","v36","v37","v38","v39","v40","v41","v42","v43","v44","v45","v46","v47","v48","v49","v50","v51","v52","v53","v54","v55","v56","v57","v58","v59","v60","v61","v62","v63","v64","v65","v66","v67","v68","v69","v70","v71","v72","v73","v74","v75","v76","v77","v78","v79","v80","v81","v82","v83","v84","v85","v86","v87","v88","v89","v90","v91","v92","v93","v94","v95","v96","v97","v98","v99","v100","v101","v102","v103","v104","v105","v106","v107","v108","v109","v110","v111","v112","v113","v114","v115","v116","v117","v118","v119","v120","v121","v122","v123","v124","v125","v126","v127","v128","v129","v130","v131","v132","v133","v134","v135","v136","v137","v138","v139","v140","v141","v142","v143","v144","v145","v146","v147","v148","v149","v150","v151","v152","v153","v154","v155","v156","v157","v158","v159","v160","v161","v162","v163","v164","v165","v166","v167","v168","v169","v170","v171","v172","v173","v174","v175","v176","v177","v178","v179","v180","v181","v182","v183","v184","v185","v186","v187","v188","v189","v190","v191","v192","v193","v194","v195","v196","v197","v198","v199","v200","v201","v202","v203","v204","v205","v206","v207","v208","v209","v210","v211","v212","v213","v214","v215","v216","v217","v218","v219","v220","v221","v222","v223","v224","v225","v226","v227","v228","v229","v230","v231","v232","v233","v234","v235","v236","v237","v238","v239","v240","v241","v242","v243","v244","v245","v246","v247","v248","v249","v250","v251","v252","v253","v254","v255","a0","a1","a2","a3","a4","a5","a6","a7","a8","a9","a10","a11","a12","a13","a14","a15","a16","a17","a18","a19","a20","a21","a22","a23","a24","a25","a26","a27","a28","a29","a30","a31","a32","a33","a34","a35","a36","a37","a38","a39","a40","a41","a42","a43","a44","a45","a46","a47","a48","a49","a50","a51","a52","a53","a54","a55","a56","a57","a58","a59","a60","a61","a62","a63","a64","a65","a66","a67","a68","a69","a70","a71","a72","a73","a74","a75","a76","a77","a78","a79","a80","a81","a82","a83","a84","a85","a86","a87","a88","a89","a90","a91","a92","a93","a94","a95","a96","a97","a98","a99","a100","a101","a102","a103","a104","a105","a106","a107","a108","a109","a110","a111","a112","a113","a114","a115","a116","a117","a118","a119","a120","a121","a122","a123","a124","a125","a126","a127","a128","a129","a130","a131","a132","a133","a134","a135","a136","a137","a138","a139","a140","a141","a142","a143","a144","a145","a146","a147","a148","a149","a150","a151","a152","a153","a154","a155","a156","a157","a158","a159","a160","a161","a162","a163","a164","a165","a166","a167","a168","a169","a170","a171","a172","a173","a174","a175","a176","a177","a178","a179","a180","a181","a182","a183","a184","a185","a186","a187","a188","a189","a190","a191","a192","a193","a194","a195","a196","a197","a198","a199","a200","a201","a202","a203","a204","a205","a206","a207","a208","a209","a210","a211","a212","a213","a214","a215","a216","a217","a218","a219","a220","a221","a222","a223","a224","a225","a226","a227","a228","a229","a230","a231","a232","a233","a234","a235","a236","a237","a238","a239","a240","a241","a242","a243","a244","a245","a246","a247","a248","a249","a250","a251","a252","a253","a254","a255","s4","s5","s6","s7","s8","s9","s10","s11","s12","s13","s14","s15","s16","s17","s18","s19","s20","s21","s22","s23","s24","s25","s26","s27","s28","s29","s30","s31","s33","s34","s35","s36","s37","s38","s39","s40","s41","s42","s43","s44","s45","s46","s47","s48","s49","s50","s51","s52","s53","s54","s55","s56","s57","s58","s59","s60","s61","s62","s63","s64","s65","s66","s67","s68","s69","s70","s71","s72","s73","s74","s75","s76","s77","s78","s79","s80","s81","s82","s83","s84","s85","s86","s87","s88","s89","s90","s91","s92","s93","s94","s95","s96","s97","s98","s99","vcc","scc","memory"
__global__ __launch_bounds__(256, 1) void k_block(const void* x, const void* wqkv, const void* wo, const void* wg, const void* wu, const void* wd, void* out, void* ws) {
  __shared__ __attribute__((aligned(16))) char lds[163840];
  uint64_t ka = (uint64_t)__builtin_amdgcn_kernarg_segment_ptr();
  unsigned bid = blockIdx.x, tid = threadIdx.x, lb = (unsigned)(size_t)lds;
  asm volatile(
  "; ---- phase 1: QKV projection with LayerNorm 1 folded in\n"
  "s_load_dwordx16 s[16:31], s[0:1], 0\n"
  "v_lshrrev_b32 v1, 6, v0\n"
  "v_and_b32 v255, 63, v0\n"
  "v_readfirstlane_b32 s3, v1\n"
  "s_and_b32 s4, s2, 7\n"
  "s_lshr_b32 s5, s2, 3\n"
  "s_and_b32 s6, s5, 15\n"
  "s_lshl_b32 s8, s4, 1\n"
  "s_lshr_b32 s9, s5, 4\n"
  "s_add_u32 s7, s8, s9\n"
  "v_lshrrev_b32 v1, 3, v255\n"
  "v_and_b32 v2, 7, v255\n"
  "v_lshrrev_b32 v3, 1, v1\n"
  "v_xor_b32 v3, v3, v2\n"
  "v_lshlrev_b32 v3, 4, v3\n"
  "v_lshl_add_u32 v220, v1, 11, v3\n"
  "s_xor_b32 s8, s6, s7\n"
  "s_lshl_b32 s44, s8, 7\n"
  "s_lshl_b32 s8, s3, 12\n"
  "s_add_u32 s47, s8, 98304\n"
  "s_mov_b32 m0, s47\n"
  "s_lshl_b32 s8, s6, 2\n"
  "s_add_u32 s8, s8, s3\n"
  "s_lshl_b32 s14, s8, 16\n"
  "s_mov_b32 s34, 65536\n"
  "s_mov_b32 s35, 0x20000\n"
  "v_xor_b32 v3, 64, v220\n"
  "v_add_u32 v221, 15360, v3\n"
  "v_add_u32 v222, 30720, v220\n"
  "v_add_u32 v223, 46080, v3\n"
  "s_waitcnt lgkmcnt(0)\n"
  "s_add_u32 s32, s16, s14\n"
  "s_addc_u32 s33, s17, 0\n"
  "s_and_b32 s33, s33, 0xffff\n"
  "buffer_load_dwordx4 v220, s[32:35], s44 offen offset:0 lds\n"
  "buffer_load_dwordx4 v221, s[32:35], s44 offen offset:1024 lds\n"
  "buffer_load_dwordx4 v222, s[32:35], s44 offen offset:2048 lds\n"
  "buffer_load_dwordx4 v223, s[32:35], s44 offen offset:3072 lds\n"
  "s_getpc_b64 s[8:9]\n"
  "L_pc0_%=:\n"
  "s_mul_i32 s13, s5, 2560\n"
  "s_cmp_eq_u32 s3, 3\n"
  "s_cselect_b32 s15, L_end_%=-L_pc0_%=, 0\n"
  "s_sub_u32 s15, s15, s13\n"
  "s_cselect_b32 s15, 0, s15\n"
  "s_min_u32 s15, s15, 2560\n"
  "s_add_u32 s64, s8, s13\n"
  "s_addc_u32 s65, s9, 0\n"
  "s_and_b32 s65, s65, 0xffff\n"
  "s_mov_b32 s66, s15\n"
  "s_mov_b32 s67, 0x20000\n"
  "v_lshlrev_b32 v4, 7, v255\n"
  "buffer_load_dword v247, v4, s[64:67], 0 offen\n"
  "v_and_b32 v3, 2, v1\n"
  "v_xor_b32 v3, v3, v2\n"
  "v_mul_u32_u24 v1, 6144, v1\n"
  "v_lshl_add_u32 v1, v3, 4, v1\n"
  "v_xor_b32 v2, 64, v1\n"
  "v_add_u32 v216, 2048, v1\n"
  "v_add_u32 v217, 50176, v2\n"
  "v_add_u32 v218, 0, v1\n"
  "v_add_u32 v219, 48128, v2\n"
  "s_mul_i32 s45, s44, 3072\n"
  "s_mul_i32 s46, s3, 6144\n"
  "s_lshl_b32 s13, s7, 7\n"
  "s_mul_i32 s8, s3, 98304\n"
  "s_add_u32 s13, s13, s8\n"
  "s_sub_u32 s15, 6291456, s13\n"
  "s_add_u32 s36, s18, s13\n"
  "s_addc_u32 s37, s19, 0\n"
  "s_and_b32 s37, s37, 0xffff\n"
  "s_mov_b32 s38, s15\n"
  "s_mov_b32 s39, 0x20000\n"
  "s_add_u32 m0, s46, 0\n"
  "s_nop 0\n"
  "buffer_load_dwordx4 v216, s[36:39], s45 offen offset:0 nt lds\n"
  "buffer_load_dwordx4 v217, s[36:39], s45 offen offset:1024 nt lds\n"
  "buffer_load_dwordx4 v216, s[36:39], s45 offen offset:2048 nt lds\n"
  "buffer_load_dwordx4 v217, s[36:39], s45 offen offset:3072 nt lds\n"
  "s_add_u32 m0, s46, 4096\n"
  "s_nop 0\n"
  "buffer_load_dwordx4 v218, s[36:39], s45 offen offset:0 nt lds\n"
  "buffer_load_dwordx4 v219, s[36:39], s45 offen offset:1024 nt lds\n"
  "s_xor_b32 s44, s44, 128\n"
  "s_mul_i32 s45, s44, 3072\n"
  "v_lshlrev_b32 v254, 4, v255\n"
  "s_lshl_b32 s12, s6, 4\n"
  "s_add_u32 s12, s12, s7\n"
  "s_lshl_b32 s12, s12, 2\n"
  "s_add_u32 s12, s12, s3\n"
  "s_lshr_b32 s90, s3, 1\n"
  "s_and_b32 s91, s3, 1\n"
  "s_lshl_b32 s92, s7, 1\n"
  "s_add_u32 s92, s92, s90\n"
  "s_mov_b32 s99, 0x200000\n"
  "s_mov_b32 s56, 0x3f803f80\n"
  "s_add_u32 s52, s30, 41943040\n"
  "s_addc_u32 s53, s31, 0\n"
  "s_and_b32 s53, s53, 0xffff\n"
  "s_mov_b32 s54, 2506752\n"
  "s_mov_b32 s55, 0x20000\n"
  "s_mov_b32 s8, 917504\n"
  "buffer_load_dwordx2 v[248:249], off, s[52:55], s8 sc1\n"
  "s_add_u32 s40, s30, 8388608\n"
  "s_addc_u32 s41, s31, 0\n"
  "s_and_b32 s41, s41, 0xffff\n"
  "s_mov_b32 s42, 8388608\n"
  "s_mov_b32 s43, 0x20000\n"
  "v_lshrrev_b32 v1, 5, v255\n"
  "v_mul_u32_u24 v1, 6144, v1\n"
  "v_bfe_u32 v2, v255, 4, 1\n"
  "v_lshl_add_u32 v1, v2, 10, v1\n"
  "v_bfe_u32 v2, v255, 2, 2\n"
  "v_lshl_add_u32 v1, v2, 7, v1\n"
  "v_and_b32 v2, 3, v255\n"
  "v_lshl_add_u32 v1, v2, 3, v1\n"
  "v_bfe_u32 v2, v255, 3, 2\n"
  "s_lshl_b32 s8, s90, 1\n"
  "v_xor_b32 v2, s8, v2\n"
  "v_lshl_add_u32 v224, v2, 5, v1\n"
  "v_xor_b32 v225, 32, v224\n"
  "v_add_u32 v226, 0xc000, v224\n"
  "v_add_u32 v227, 0xc000, v225\n"
  "v_and_b32 v4, 15, v255\n"
  "v_lshrrev_b32 v5, 4, v255\n"
  "v_lshrrev_b32 v1, 1, v4\n"
  "v_xor_b32 v1, v1, v5\n"
  "v_lshlrev_b32 v1, 4, v1\n"
  "v_lshl_add_u32 v1, v4, 7, v1\n"
  "s_lshl_b32 s8, s91, 13\n"
  "s_add_u32 s9, s8, 98304\n"
  "v_add_u32 v228, s9, v1\n"
  "v_xor_b32 v229, 64, v228\n"
  "s_lshl_b32 s9, s90, 12\n"
  "v_add_u32 v238, s9, v228\n"
  "v_add_u32 v239, s9, v229\n"
  "v_lshlrev_b32 v1, 9, v5\n"
  "v_lshl_add_u32 v230, v4, 4, v1\n"
  "v_and_b32 v2, 3, v255\n"
  "v_lshl_add_u32 v1, v2, 4, v1\n"
  "v_bfe_u32 v2, v255, 2, 2\n"
  "v_lshl_add_u32 v231, v2, 7, v1\n"
  "s_lshl_b32 s9, s3, 13\n"
  "v_add_u32 v232, s9, v230\n"
  "v_add_u32 v233, s9, v231\n"
  "s_lshl_b32 s9, s3, 12\n"
  "s_add_u32 s9, s9, 32768\n"
  "v_add_u32 v234, s9, v230\n"
  "s_lshl_b32 s9, s92, 18\n"
  "s_add_u32 s8, s8, s9\n"
  "s_lshl_b32 s9, s6, 14\n"
  "s_add_u32 s48, s8, s9\n"
  "s_add_u32 s49, s48, 4096\n"
  "v_cmp_eq_u32 vcc, 0, v255\n"
  "v_mov_b32 v1, 0x40000000\n"
  "v_mov_b32 v2, s56\n"
  "v_cndmask_b32 v251, v1, 0, vcc\n"
  "v_cmp_gt_u32 vcc, 16, v255\n"
  "v_accvgpr_write_b32 a120, v2\n"
  "v_accvgpr_write_b32 a121, v2\n"
  "v_accvgpr_write_b32 a122, v2\n"
  "v_accvgpr_write_b32 a123, v2\n"
  "v_lshrrev_b32 v3, 2, v255\n"
  "v_lshl_add_u32 v251, v3, 24, v254\n"
  "v_cndmask_b32 v250, v1, v254, vcc\n"
  "s_cmp_eq_u32 s91, 1\n"
  "s_cselect_b64 s[78:79], -1, 0\n"
  "v_and_b32 v1, 3, v4\n"
  "v_lshrrev_b32 v2, 2, v4\n"
  "v_cmp_eq_u32 s[70:71], 1, v1\n"
  "v_cmp_eq_u32 s[72:73], 2, v1\n"
  "v_cmp_eq_u32 s[74:75], 3, v1\n"
  "v_cmp_eq_u32 s[76:77], v2, v5\n"
  "s_lshl_b32 s8, s6, 1\n"
  "s_add_u32 s8, s8, s91\n"
  "s_lshl_b32 s8, s8, 1\n"
  "s_add_u32 s8, s8, s90\n"
  "s_lshl_b32 s58, s8, 12\n"
  "s_add_u32 s58, s58, 131072\n"
  "s_lshl_b32 s9, s7, 8\n"
  "s_add_u32 s64, s58, s9\n"
  "s_lshl_b32 s8, s8, 7\n"
  "s_lshl_b32 s9, s7, 13\n"
  "s_add_u32 s57, s8, s9\n"
  "s_cmp_eq_u32 s90, 0\n"
  "s_cselect_b32 s57, s57, 2506752\n"
  "s_lshl_b32 s8, s7, 2\n"
  "s_add_u32 s8, s8, s3\n"
  "s_lshl_b32 s66, s8, 12\n"
  "s_add_u32 s66, s66, 393216\n"
  "s_lshl_b32 s9, s6, 8\n"
  "s_add_u32 s65, s66, s9\n"
  "s_mul_i32 s68, s12, 64\n"
  "s_add_u32 s68, s68, 1048576\n"
  "s_mov_b32 s69, 1048576\n"
  "v_lshl_add_u32 v2, v4, 6, s8\n"
  "s_lshl_b32 s8, s6, 6\n"
  "s_add_u32 s8, s8, s3\n"
  "v_lshl_add_u32 v1, v4, 2, s8\n"
  "v_cmp_lt_u32 vcc, 1, v5\n"
  "s_nop 1\n"
  "v_cndmask_b32 v1, v1, v2, vcc\n"
  "v_mul_u32_u24 v235, 64, v1\n"
  "s_lshl_b32 s13, s12, 11\n"
  "s_add_u32 s80, s20, s13\n"
  "s_addc_u32 s81, s21, 0\n"
  "s_and_b32 s81, s81, 0xffff\n"
  "s_mov_b32 s82, 2048\n"
  "s_mov_b32 s83, 0x20000\n"
  "s_lshl_b32 s8, s4, 1\n"
  "s_and_b32 s9, s5, 1\n"
  "s_add_u32 s8, s8, s9\n"
  "s_lshl_b32 s8, s8, 2\n"
  "s_add_u32 s8, s8, s3\n"
  "s_lshl_b32 s8, s8, 16\n"
  "s_lshr_b32 s9, s5, 1\n"
  "s_lshl_b32 s9, s9, 7\n"
  "s_add_u32 s13, s8, s9\n"
  "s_add_u32 s84, s16, s13\n"
  "s_addc_u32 s85, s17, 0\n"
  "s_and_b32 s85, s85, 0xffff\n"
  "s_mov_b32 s86, 63616\n"
  "s_mov_b32 s87, 0x20000\n"
  "s_waitcnt lgkmcnt(0)\n"
  "s_add_u32 m0, s47, 16384\n"
  "s_nop 0\n"
  "buffer_load_dwordx4 v220, s[32:35], s44 offen offset:0 lds\n"
  "buffer_load_dwordx4 v221, s[32:35], s44 offen offset:1024 lds\n"
  "buffer_load_dwordx4 v222, s[32:35], s44 offen offset:2048 lds\n"
  "buffer_load_dwordx4 v223, s[32:35], s44 offen offset:3072 lds\n"
  "s_add_u32 m0, s46, 24576\n"
  "s_nop 0\n"
  "buffer_load_dwordx4 v216, s[36:39], s45 offen offset:0 lds\n"
  "buffer_load_dwordx4 v217, s[36:39], s45 offen offset:1024 lds\n"
  "buffer_load_dwordx4 v216, s[36:39], s45 offen offset:2048 lds\n"
  "buffer_load_dwordx4 v217, s[36:39], s45 offen offset:3072 lds\n"
  "s_add_u32 m0, s46, 28672\n"
  "s_nop 0\n"
  "buffer_load_dwordx4 v218, s[36:39], s45 offen offset:0 lds\n"
  "buffer_load_dwordx4 v219, s[36:39], s45 offen offset:1024 lds\n"
  "s_xor_b32 s44, s44, 128\n"
  "s_add_u32 s44, s44, 256\n"
  "s_and_b32 s44, s44, 0x7ff\n"
  "s_mul_i32 s45, s44, 3072\n"
  "s_waitcnt vmcnt(11)\n"
  "s_barrier\n"
  "ds_read_b128 a[0:3], v228 offset:0\n"
  "ds_read_b128 a[4:7], v228 offset:2048\n"
  "ds_read_b128 a[8:11], v228 offset:4096\n"
  "ds_read_b128 a[12:15], v228 offset:6144\n"
  "ds_read_b128 a[16:19], v229 offset:0\n"
  "ds_read_b128 a[20:23], v229 offset:2048\n"
  "ds_read_b128 a[24:27], v229 offset:4096\n"
  "ds_read_b128 a[28:31], v229 offset:6144\n"
  "ds_read_b64_tr_b16 a[128:129], v224 offset:0\n"
  "ds_read_b64_tr_b16 a[130:131], v224 offset:512\n"
  "ds_read_b64_tr_b16 a[132:133], v224 offset:12288\n"
  "ds_read_b64_tr_b16 a[134:135], v224 offset:12800\n"
  "ds_read_b64_tr_b16 a[136:137], v225 offset:0\n"
  "ds_read_b64_tr_b16 a[138:139], v225 offset:512\n"
  "ds_read_b64_tr_b16 a[140:141], v225 offset:12288\n"
  "s_waitcnt lgkmcnt(11)\n"
  "ds_read_b64_tr_b16 a[142:143], v225 offset:12800\n"
  "s_waitcnt lgkmcnt(4)\n"
  "v_mfma_f32_16x16x32_bf16 v[0:3], a[128:131], a[0:3], 0\n"
  "ds_read_b64_tr_b16 a[144:145], v224 offset:2048\n"
  "s_add_u32 m0, s47, 32768\n"
  "v_mfma_f32_16x16x32_bf16 v[0:3], a[132:135], a[16:19], v[0:3]\n"
  "ds_read_b64_tr_b16 a[146:147], v224 offset:2560\n"
  "v_mfma_f32_16x16x32_bf16 v[4:7], a[132:135], a[20:23], 0\n"
  "ds_read_b128 v[120:123], v238 offset:0\n"
  "v_mfma_f32_16x16x32_bf16 v[4:7], a[128:131], a[4:7], v[4:7]\n"
  "buffer_load_dwordx4 v220, s[32:35], s44 offen offset:0 lds\n"
  "v_mfma_f32_16x16x32_bf16 v[96:99], a[120:123], a[128:131], 0\n"
  "buffer_load_dwordx4 v221, s[32:35], s44 offen offset:1024 lds\n"
  "v_mfma_f32_16x16x32_bf16 v[8:11], a[128:131], a[8:11], 0\n"
  "ds_read_b64_tr_b16 a[148:149], v224 offset:14336\n"
  "v_mfma_f32_16x16x32_bf16 v[8:11], a[132:135], a[24:27], v[8:11]\n"
  "ds_read_b64_tr_b16 a[150:151], v224 offset:14848\n"
  "v_mfma_f32_16x16x32_bf16 v[12:15], a[132:135], a[28:31], 0\n"
  "ds_read_b128 v[124:127], v239 offset:0\n"
  "s_waitcnt lgkmcnt(6)\n"
  "v_mfma_f32_16x16x32_bf16 v[12:15], a[128:131], a[12:15], v[12:15]\n"
  "buffer_load_dwordx4 v222, s[32:35], s44 offen offset:2048 lds\n"
  "v_mfma_f32_16x16x32_bf16 v[96:99], a[120:123], a[132:135], v[96:99]\n"
  "buffer_load_dwordx4 v223, s[32:35], s44 offen offset:3072 lds\n"
  "v_mfma_f32_16x16x32_bf16 v[28:31], a[136:139], a[12:15], 0\n"
  "ds_read_b64_tr_b16 a[152:153], v225 offset:2048\n"
  "s_add_u32 m0, s46, 49152\n"
  "v_mfma_f32_16x16x32_bf16 v[28:31], a[140:143], a[28:31], v[28:31]\n"
  "ds_read_b64_tr_b16 a[154:155], v225 offset:2560\n"
  "v_mfma_f32_16x16x32_bf16 v[24:27], a[140:143], a[24:27], 0\n"
  "ds_read_b128 v[128:131], v238 offset:2048\n"
  "v_mfma_f32_16x16x32_bf16 v[24:27], a[136:139], a[8:11], v[24:27]\n"
  "buffer_load_dwordx4 v216, s[36:39], s45 offen offset:0 lds\n"
  "v_mfma_f32_16x16x32_bf16 v[100:103], a[120:123], a[136:139], 0\n"
  "buffer_load_dwordx4 v217, s[36:39], s45 offen offset:1024 lds\n"
  "v_mfma_f32_16x16x32_bf16 v[20:23], a[136:139], a[4:7], 0\n"
  "ds_read_b64_tr_b16 a[156:157], v225 offset:14336\n"
  "v_mfma_f32_16x16x32_bf16 v[20:23], a[140:143], a[20:23], v[20:23]\n"
  "ds_read_b64_tr_b16 a[158:159], v225 offset:14848\n"
  "v_mfma_f32_16x16x32_bf16 v[16:19], a[140:143], a[16:19], 0\n"
  "ds_read_b128 v[132:135], v239 offset:2048\n"
  "s_waitcnt lgkmcnt(7)\n"
  "v_mfma_f32_16x16x32_bf16 v[16:19], a[136:139], a[0:3], v[16:19]\n"
  "buffer_load_dwordx4 v216, s[36:39], s45 offen offset:2048 lds\n"
  "v_mfma_f32_16x16x32_bf16 v[100:103], a[120:123], a[140:143], v[100:103]\n"
  "buffer_load_dwordx4 v217, s[36:39], s45 offen offset:3072 lds\n"
  "v_mfma_f32_16x16x32_bf16 v[32:35], a[0:3], a[144:147], 0\n"
  "ds_read_b64_tr_b16 a[128:129], v224 offset:4096\n"
  "s_add_u32 m0, s46, 53248\n"
  "v_mfma_f32_16x16x32_bf16 v[32:35], a[16:19], a[148:151], v[32:35]\n"
  "ds_read_b64_tr_b16 a[130:131], v224 offset:4608\n"
  "v_mfma_f32_16x16x32_bf16 v[36:39], a[20:23], a[148:151], 0\n"
  "buffer_load_dwordx4 v218, s[36:39], s45 offen offset:0 lds\n"
  "v_mfma_f32_16x16x32_bf16 v[36:39], a[4:7], a[144:147], v[36:39]\n"
  "buffer_load_dwordx4 v219, s[36:39], s45 offen offset:1024 lds\n"
  "v_mfma_f32_16x16x32_bf16 v[104:107], a[120:123], a[144:147], 0\n"
  "s_xor_b32 s44, s44, 128\n"
  "s_mul_i32 s45, s44, 3072\n"
  "v_mfma_f32_16x16x32_bf16 v[40:43], a[8:11], a[144:147], 0\n"
  "ds_read_b64_tr_b16 a[132:133], v224 offset:16384\n"
  "v_mfma_f32_16x16x32_bf16 v[40:43], a[24:27], a[148:151], v[40:43]\n"
  "ds_read_b64_tr_b16 a[134:135], v224 offset:16896\n"
  "s_waitcnt lgkmcnt(5)\n"
  "v_mfma_f32_16x16x32_bf16 v[44:47], a[28:31], a[148:151], 0\n"
  "v_mfma_f32_16x16x32_bf16 v[44:47], a[12:15], a[144:147], v[44:47]\n"
  "v_mfma_f32_16x16x32_bf16 v[104:107], a[120:123], a[148:151], v[104:107]\n"
  "s_waitcnt lgkmcnt(4)\n"
  "v_mfma_f32_16x16x32_bf16 v[136:139], a[120:123], v[120:123], 0\n"
  "v_mfma_f32_16x16x32_bf16 v[140:143], v[120:123], v[120:123], 0\n"
  "v_mfma_f32_16x16x32_bf16 v[60:63], a[12:15], a[152:155], 0\n"
  "s_waitcnt vmcnt(10)\n"
  "s_barrier\n"
  "ds_read_b64_tr_b16 a[136:137], v225 offset:4096\n"
  "v_mfma_f32_16x16x32_bf16 v[60:63], a[28:31], a[156:159], v[60:63]\n"
  "ds_read_b64_tr_b16 a[138:139], v225 offset:4608\n"
  "s_add_u32 m0, s47, 0\n"
  "v_mfma_f32_16x16x32_bf16 v[56:59], a[24:27], a[156:159], 0\n"
  "ds_read_b128 a[32:35], v228 offset:16384\n"
  "v_mfma_f32_16x16x32_bf16 v[56:59], a[8:11], a[152:155], v[56:59]\n"
  "ds_read_b128 a[48:51], v229 offset:16384\n"
  "v_readfirstlane_b32 s10, v248\n"
  "v_mfma_f32_16x16x32_bf16 v[108:111], a[120:123], a[152:155], 0\n"
  "buffer_load_dwordx4 v220, s[32:35], s44 offen offset:0 lds\n"
  "v_mfma_f32_16x16x32_bf16 v[144:147], a[120:123], v[128:131], 0\n"
  "buffer_load_dwordx4 v221, s[32:35], s44 offen offset:1024 lds\n"
  "v_mfma_f32_16x16x32_bf16 v[148:151], v[128:131], v[128:131], 0\n"
  "v_readfirstlane_b32 s11, v249\n"
  "v_mfma_f32_16x16x32_bf16 v[52:55], a[4:7], a[152:155], 0\n"
  "ds_read_b64_tr_b16 a[140:141], v225 offset:16384\n"
  "s_mul_i32 s10, s10, 1664525\n"
  "v_mfma_f32_16x16x32_bf16 v[52:55], a[20:23], a[156:159], v[52:55]\n"
  "ds_read_b64_tr_b16 a[142:143], v225 offset:16896\n"
  "s_mul_i32 s11, s11, 22695477\n"
  "v_mfma_f32_16x16x32_bf16 v[48:51], a[16:19], a[156:159], 0\n"
  "ds_read_b128 a[36:39], v228 offset:18432\n"
  "s_add_u32 s10, s10, 1013904223\n"
  "v_mfma_f32_16x16x32_bf16 v[48:51], a[0:3], a[152:155], v[48:51]\n"
  "ds_read_b128 a[52:55], v229 offset:18432\n"
  "s_waitcnt lgkmcnt(8)\n"
  "v_mfma_f32_16x16x32_bf16 v[108:111], a[120:123], a[156:159], v[108:111]\n"
  "buffer_load_dwordx4 v222, s[32:35], s44 offen offset:2048 lds\n"
  "v_mfma_f32_16x16x32_bf16 v[136:139], a[120:123], v[124:127], v[136:139]\n"
  "buffer_load_dwordx4 v223, s[32:35], s44 offen offset:3072 lds\n"
  "v_mfma_f32_16x16x32_bf16 v[140:143], v[124:127], v[124:127], v[140:143]\n"
  "s_add_u32 m0, s46, 73728\n"
  "s_add_u32 s11, s11, 0x9e3779b1\n"
  "v_mov_b32 v248, s10\n"
  "v_mfma_f32_16x16x32_bf16 v[64:67], a[128:131], a[0:3], 0\n"
  "ds_read_b64_tr_b16 a[144:145], v224 offset:24576\n"
  "v_mov_b32 v249, s11\n"
  "v_mfma_f32_16x16x32_bf16 v[64:67], a[132:135], a[16:19], v[64:67]\n"
  "ds_read_b64_tr_b16 a[146:147], v224 offset:25088\n"
  "v_mfma_f32_16x16x32_bf16 v[68:71], a[132:135], a[20:23], 0\n"
  "ds_read_b128 a[40:43], v228 offset:20480\n"
  "v_mfma_f32_16x16x32_bf16 v[68:71], a[128:131], a[4:7], v[68:71]\n"
  "ds_read_b128 a[56:59], v229 offset:20480\n"
  "v_mfma_f32_16x16x32_bf16 v[112:115], a[120:123], a[128:131], 0\n"
  "buffer_load_dwordx4 v216, s[36:39], s45 offen offset:0 lds\n"
  "v_mfma_f32_16x16x32_bf16 v[144:147], a[120:123], v[132:135], v[144:147]\n"
  "buffer_load_dwordx4 v217, s[36:39], s45 offen offset:1024 lds\n"
  "v_mfma_f32_16x16x32_bf16 v[148:151], v[132:135], v[132:135], v[148:151]\n"
  "v_mfma_f32_16x16x32_bf16 v[72:75], a[128:131], a[8:11], 0\n"
  "ds_read_b64_tr_b16 a[148:149], v224 offset:36864\n"
  "v_mfma_f32_16x16x32_bf16 v[72:75], a[132:135], a[24:27], v[72:75]\n"
  "ds_read_b64_tr_b16 a[150:151], v224 offset:37376\n"
  "v_mfma_f32_16x16x32_bf16 v[76:79], a[132:135], a[28:31], 0\n"
  "ds_read_b128 a[44:47], v228 offset:22528\n"
  "v_mfma_f32_16x16x32_bf16 v[76:79], a[128:131], a[12:15], v[76:79]\n"
  "s_waitcnt lgkmcnt(11)\n"
  "ds_read_b128 a[60:63], v229 offset:22528\n"
  "s_waitcnt lgkmcnt(10)\n"
  "v_mfma_f32_16x16x32_bf16 v[112:115], a[120:123], a[132:135], v[112:115]\n"
  "buffer_load_dwordx4 v216, s[36:39], s45 offen offset:2048 lds\n"
  "v_mfma_f32_16x16x32_bf16 v[92:95], a[136:139], a[12:15], 0\n"
  "ds_read_b64_tr_b16 a[152:153], v225 offset:24576\n"
  "v_mfma_f32_16x16x32_bf16 v[92:95], a[140:143], a[28:31], v[92:95]\n"
  "ds_read_b64_tr_b16 a[154:155], v225 offset:25088\n"
  "v_mfma_f32_16x16x32_bf16 v[88:91], a[140:143], a[24:27], 0\n"
  "buffer_load_dwordx4 v217, s[36:39], s45 offen offset:3072 lds\n"
  "v_mfma_f32_16x16x32_bf16 v[88:91], a[136:139], a[8:11], v[88:91]\n"
  "s_add_u32 m0, s46, 77824\n"
  "v_mfma_f32_16x16x32_bf16 v[116:119], a[120:123], a[136:139], 0\n"
  "buffer_load_dwordx4 v218, s[36:39], s45 offen offset:0 lds\n"
  "v_mfma_f32_16x16x32_bf16 v[84:87], a[136:139], a[4:7], 0\n"
  "ds_read_b64_tr_b16 a[156:157], v225 offset:36864\n"
  "v_mfma_f32_16x16x32_bf16 v[84:87], a[140:143], a[20:23], v[84:87]\n"
  "ds_read_b64_tr_b16 a[158:159], v225 offset:37376\n"
  "s_waitcnt lgkmcnt(4)\n"
  "v_mfma_f32_16x16x32_bf16 v[80:83], a[140:143], a[16:19], 0\n"
  "buffer_load_dwordx4 v219, s[36:39], s45 offen offset:1024 lds\n"
  "v_mfma_f32_16x16x32_bf16 v[80:83], a[136:139], a[0:3], v[80:83]\n"
  "s_xor_b32 s44, s44, 128\n"
  "s_add_u32 s44, s44, 256\n"
  "s_and_b32 s44, s44, 0x7ff\n"
  "v_mfma_f32_16x16x32_bf16 v[116:119], a[120:123], a[140:143], v[116:119]\n"
  "s_mul_i32 s45, s44, 3072\n"
  "v_mfma_f32_16x16x32_bf16 v[0:3], a[144:147], a[32:35], v[0:3]\n"
  "ds_read_b64_tr_b16 a[128:129], v224 offset:26624\n"
  "v_mov_b32 v242, v136\n"
  "v_mfma_f32_16x16x32_bf16 v[0:3], a[148:151], a[48:51], v[0:3]\n"
  "ds_read_b64_tr_b16 a[130:131], v224 offset:27136\n"
  "v_cndmask_b32 v140, v140, v141, s[70:71]\n"
  "v_mfma_f32_16x16x32_bf16 v[4:7], a[148:151], a[52:55], v[4:7]\n"
  "v_cndmask_b32 v140, v140, v142, s[72:73]\n"
  "v_cndmask_b32 v140, v140, v143, s[74:75]\n"
  "v_mfma_f32_16x16x32_bf16 v[4:7], a[144:147], a[36:39], v[4:7]\n"
  "v_cndmask_b32 v243, 0, v140, s[76:77]\n"
  "v_mov_b32 v244, v144\n"
  "v_mfma_f32_16x16x32_bf16 v[8:11], a[144:147], a[40:43], v[8:11]\n"
  "ds_read_b64_tr_b16 a[132:133], v224 offset:38912\n"
  "v_cndmask_b32 v148, v148, v149, s[70:71]\n"
  "v_mfma_f32_16x16x32_bf16 v[8:11], a[148:151], a[56:59], v[8:11]\n"
  "ds_read_b64_tr_b16 a[134:135], v224 offset:39424\n"
  "s_waitcnt lgkmcnt(4)\n"
  "v_mfma_f32_16x16x32_bf16 v[12:15], a[148:151], a[60:63], v[12:15]\n"
  "v_cndmask_b32 v148, v148, v150, s[72:73]\n"
  "v_cndmask_b32 v148, v148, v151, s[74:75]\n"
  "v_mfma_f32_16x16x32_bf16 v[12:15], a[144:147], a[44:47], v[12:15]\n"
  "v_cndmask_b32 v245, 0, v148, s[76:77]\n"
  "v_cndmask_b32 v96, v96, v108, s[78:79]\n"
  "v_mfma_f32_16x16x32_bf16 v[28:31], a[152:155], a[44:47], v[28:31]\n"
  "s_waitcnt vmcnt(10)\n"
  "s_barrier\n"
  "s_add_u32 m0, s47, 16384\n"
  "v_mfma_f32_16x16x32_bf16 v[28:31], a[156:159], a[60:63], v[28:31]\n"
  "ds_read_b64_tr_b16 a[136:137], v225 offset:26624\n"
  "v_cndmask_b32 v97, v100, v112, s[78:79]\n"
  "v_mfma_f32_16x16x32_bf16 v[24:27], a[156:159], a[56:59], v[24:27]\n"
  "ds_read_b64_tr_b16 a[138:139], v225 offset:27136\n"
  "v_cndmask_b32 v98, v104, v116, s[78:79]\n"
  "v_mfma_f32_16x16x32_bf16 v[24:27], a[152:155], a[40:43], v[24:27]\n"
  "ds_read_b128 a[0:3], v228 offset:32768\n"
  "v_mov_b32 v136, v243\n"
  "v_mfma_f32_16x16x32_bf16 v[20:23], a[152:155], a[36:39], v[20:23]\n"
  "ds_read_b64_tr_b16 a[140:141], v225 offset:38912\n"
  "v_mov_b32 v137, v245\n"
  "v_mfma_f32_16x16x32_bf16 v[20:23], a[156:159], a[52:55], v[20:23]\n"
  "ds_read_b64_tr_b16 a[142:143], v225 offset:39424\n"
  "v_permlane16_swap_b32 v136, v243\n"
  "v_mfma_f32_16x16x32_bf16 v[16:19], a[156:159], a[48:51], v[16:19]\n"
  "ds_read_b128 a[16:19], v229 offset:32768\n"
  "s_waitcnt lgkmcnt(6)\n"
  "v_mfma_f32_16x16x32_bf16 v[16:19], a[152:155], a[32:35], v[16:19]\n"
  "buffer_store_dwordx4 v[96:99], v250, s[52:55], s65 offen sc1\n"
  "v_mfma_f32_16x16x32_bf16 v[32:35], a[32:35], a[128:131], v[32:35]\n"
  "ds_read_b64_tr_b16 a[144:145], v224 offset:28672\n"
  "v_permlane16_swap_b32 v137, v245\n"
  "v_mfma_f32_16x16x32_bf16 v[32:35], a[48:51], a[132:135], v[32:35]\n"
  "ds_read_b64_tr_b16 a[146:147], v224 offset:29184\n"
  "v_add_f32 v243, v136, v243\n"
  "v_mfma_f32_16x16x32_bf16 v[36:39], a[52:55], a[132:135], v[36:39]\n"
  "ds_read_b128 a[4:7], v228 offset:34816\n"
  "v_add_f32 v245, v137, v245\n"
  "v_mfma_f32_16x16x32_bf16 v[36:39], a[36:39], a[128:131], v[36:39]\n"
  "buffer_load_dwordx4 v220, s[32:35], s44 offen offset:0 lds\n"
  "v_mfma_f32_16x16x32_bf16 v[40:43], a[40:43], a[128:131], v[40:43]\n"
  "ds_read_b64_tr_b16 a[148:149], v224 offset:40960\n"
  "v_mov_b32 v136, v243\n"
  "v_mfma_f32_16x16x32_bf16 v[40:43], a[56:59], a[132:135], v[40:43]\n"
  "ds_read_b64_tr_b16 a[150:151], v224 offset:41472\n"
  "v_mov_b32 v137, v245\n"
  "v_mfma_f32_16x16x32_bf16 v[44:47], a[60:63], a[132:135], v[44:47]\n"
  "ds_read_b128 a[20:23], v229 offset:34816\n"
  "s_waitcnt lgkmcnt(7)\n"
  "v_mfma_f32_16x16x32_bf16 v[44:47], a[44:47], a[128:131], v[44:47]\n"
  "buffer_load_dwordx4 v221, s[32:35], s44 offen offset:1024 lds\n"
  "v_mfma_f32_16x16x32_bf16 v[60:63], a[44:47], a[136:139], v[60:63]\n"
  "ds_read_b64_tr_b16 a[152:153], v225 offset:28672\n"
  "v_permlane32_swap_b32 v136, v243\n"
  "v_mfma_f32_16x16x32_bf16 v[60:63], a[60:63], a[140:143], v[60:63]\n"
  "ds_read_b64_tr_b16 a[154:155], v225 offset:29184\n"
  "v_permlane32_swap_b32 v137, v245\n"
  "v_mfma_f32_16x16x32_bf16 v[56:59], a[56:59], a[140:143], v[56:59]\n"
  "ds_read_b128 a[8:11], v228 offset:36864\n"
  "v_add_f32 v243, v136, v243\n"
  "v_mfma_f32_16x16x32_bf16 v[56:59], a[40:43], a[136:139], v[56:59]\n"
  "buffer_load_dwordx4 v222, s[32:35], s44 offen offset:2048 lds\n"
  "v_mfma_f32_16x16x32_bf16 v[52:55], a[36:39], a[136:139], v[52:55]\n"
  "ds_read_b64_tr_b16 a[156:157], v225 offset:40960\n"
  "v_add_f32 v245, v137, v245\n"
  "v_mfma_f32_16x16x32_bf16 v[52:55], a[52:55], a[140:143], v[52:55]\n"
  "ds_read_b64_tr_b16 a[158:159], v225 offset:41472\n"
  "buffer_store_dwordx4 v[242:245], v250, s[52:55], s64 offen sc1\n"
  "v_mfma_f32_16x16x32_bf16 v[48:51], a[48:51], a[140:143], v[48:51]\n"
  "ds_read_b128 a[24:27], v229 offset:36864\n"
  "s_waitcnt lgkmcnt(7)\n"
  "v_mfma_f32_16x16x32_bf16 v[48:51], a[32:35], a[136:139], v[48:51]\n"
  "buffer_load_dwordx4 v223, s[32:35], s44 offen offset:3072 lds\n"
  "v_mfma_f32_16x16x32_bf16 v[64:67], a[144:147], a[32:35], v[64:67]\n"
  "ds_read_b64_tr_b16 a[128:129], v226 offset:0\n"
  "s_add_u32 m0, s46, 0\n"
  "v_mfma_f32_16x16x32_bf16 v[64:67], a[148:151], a[48:51], v[64:67]\n"
  "ds_read_b64_tr_b16 a[130:131], v226 offset:512\n"
  "v_mfma_f32_16x16x32_bf16 v[68:71], a[148:151], a[52:55], v[68:71]\n"
  "ds_read_b128 a[12:15], v228 offset:38912\n"
  "v_mfma_f32_16x16x32_bf16 v[68:71], a[144:147], a[36:39], v[68:71]\n"
  "buffer_load_dwordx4 v216, s[36:39], s45 offen offset:0 lds\n"
  "v_mfma_f32_16x16x32_bf16 v[72:75], a[144:147], a[40:43], v[72:75]\n"
  "ds_read_b64_tr_b16 a[132:133], v226 offset:12288\n"
  "v_mfma_f32_16x16x32_bf16 v[72:75], a[148:151], a[56:59], v[72:75]\n"
  "ds_read_b64_tr_b16 a[134:135], v226 offset:12800\n"
  "v_mfma_f32_16x16x32_bf16 v[76:79], a[148:151], a[60:63], v[76:79]\n"
  "ds_read_b128 a[28:31], v229 offset:38912\n"
  "s_waitcnt lgkmcnt(7)\n"
  "v_mfma_f32_16x16x32_bf16 v[76:79], a[144:147], a[44:47], v[76:79]\n"
  "buffer_load_dwordx4 v217, s[36:39], s45 offen offset:1024 lds\n"
  "v_mfma_f32_16x16x32_bf16 v[92:95], a[152:155], a[44:47], v[92:95]\n"
  "ds_read_b64_tr_b16 a[136:137], v227 offset:0\n"
  "v_mfma_f32_16x16x32_bf16 v[92:95], a[156:159], a[60:63], v[92:95]\n"
  "ds_read_b64_tr_b16 a[138:139], v227 offset:512\n"
  "v_mfma_f32_16x16x32_bf16 v[88:91], a[156:159], a[56:59], v[88:91]\n"
  "buffer_load_dwordx4 v216, s[36:39], s45 offen offset:2048 lds\n"
  "v_mfma_f32_16x16x32_bf16 v[88:91], a[152:155], a[40:43], v[88:91]\n"
  "v_mfma_f32_16x16x32_bf16 v[84:87], a[152:155], a[36:39], v[84:87]\n"
  "ds_read_b64_tr_b16 a[140:141], v227 offset:12288\n"
  "v_mfma_f32_16x16x32_bf16 v[84:87], a[156:159], a[52:55], v[84:87]\n"
  "ds_read_b64_tr_b16 a[142:143], v227 offset:12800\n"
  "s_waitcnt lgkmcnt(4)\n"
  "v_mfma_f32_16x16x32_bf16 v[80:83], a[156:159], a[48:51], v[80:83]\n"
  "buffer_load_dwordx4 v217, s[36:39], s45 offen offset:3072 lds\n"
  "v_mfma_f32_16x16x32_bf16 v[80:83], a[152:155], a[32:35], v[80:83]\n"
  "s_add_u32 m0, s46, 4096\n"
  "v_mfma_f32_16x16x32_bf16 v[0:3], a[128:131], a[0:3], v[0:3]\n"
  "ds_read_b64_tr_b16 a[144:145], v226 offset:2048\n"
  "v_mfma_f32_16x16x32_bf16 v[0:3], a[132:135], a[16:19], v[0:3]\n"
  "ds_read_b64_tr_b16 a[146:147], v226 offset:2560\n"
  "v_mfma_f32_16x16x32_bf16 v[4:7], a[132:135], a[20:23], v[4:7]\n"
  "buffer_load_dwordx4 v218, s[36:39], s45 offen offset:0 lds\n"
  "v_mfma_f32_16x16x32_bf16 v[4:7], a[128:131], a[4:7], v[4:7]\n"
  "v_mfma_f32_16x16x32_bf16 v[8:11], a[128:131], a[8:11], v[8:11]\n"
  "ds_read_b64_tr_b16 a[148:149], v226 offset:14336\n"
  "v_mfma_f32_16x16x32_bf16 v[8:11], a[132:135], a[24:27], v[8:11]\n"
  "ds_read_b64_tr_b16 a[150:151], v226 offset:14848\n"
  "s_waitcnt lgkmcnt(4)\n"
  "v_mfma_f32_16x16x32_bf16 v[12:15], a[132:135], a[28:31], v[12:15]\n"
  "buffer_load_dwordx4 v219, s[36:39], s45 offen offset:1024 lds\n"
  "v_mfma_f32_16x16x32_bf16 v[12:15], a[128:131], a[12:15], v[12:15]\n"
  "s_xor_b32 s44, s44, 128\n"
  "s_mul_i32 s45, s44, 3072\n"
  "v_mfma_f32_16x16x32_bf16 v[28:31], a[136:139], a[12:15], v[28:31]\n"
  "s_waitcnt vmcnt(12)\n"
  "s_barrier\n"
  "s_add_u32 m0, s47, 32768\n"
  "v_mfma_f32_16x16x32_bf16 v[28:31], a[140:143], a[28:31], v[28:31]\n"
  "ds_read_b64_tr_b16 a[152:153], v227 offset:2048\n"
  "v_mfma_f32_16x16x32_bf16 v[24:27], a[140:143], a[24:27], v[24:27]\n"
  "ds_read_b64_tr_b16 a[154:155], v227 offset:2560\n"
  "v_mfma_f32_16x16x32_bf16 v[24:27], a[136:139], a[8:11], v[24:27]\n"
  "ds_read_b128 a[32:35], v228 offset:0\n"
  "v_mfma_f32_16x16x32_bf16 v[20:23], a[136:139], a[4:7], v[20:23]\n"
  "ds_read_b64_tr_b16 a[156:157], v227 offset:14336\n"
  "v_mfma_f32_16x16x32_bf16 v[20:23], a[140:143], a[20:23], v[20:23]\n"
  "ds_read_b64_tr_b16 a[158:159], v227 offset:14848\n"
  "v_mfma_f32_16x16x32_bf16 v[16:19], a[140:143], a[16:19], v[16:19]\n"
  "ds_read_b128 a[48:51], v229 offset:0\n"
  "s_waitcnt lgkmcnt(6)\n"
  "v_mfma_f32_16x16x32_bf16 v[16:19], a[136:139], a[0:3], v[16:19]\n"
  "buffer_load_dwordx4 v220, s[32:35], s44 offen offset:0 lds\n"
  "v_mfma_f32_16x16x32_bf16 v[32:35], a[0:3], a[144:147], v[32:35]\n"
  "ds_read_b64_tr_b16 a[128:129], v226 offset:4096\n"
  "v_mfma_f32_16x16x32_bf16 v[32:35], a[16:19], a[148:151], v[32:35]\n"
  "ds_read_b64_tr_b16 a[130:131], v226 offset:4608\n"
  "v_mfma_f32_16x16x32_bf16 v[36:39], a[20:23], a[148:151], v[36:39]\n"
  "ds_read_b128 a[36:39], v228 offset:2048\n"
  "v_mfma_f32_16x16x32_bf16 v[36:39], a[4:7], a[144:147], v[36:39]\n"
  "buffer_load_dwordx4 v221, s[32:35], s44 offen offset:1024 lds\n"
  "v_mfma_f32_16x16x32_bf16 v[40:43], a[8:11], a[144:147], v[40:43]\n"
  "ds_read_b64_tr_b16 a[132:133], v226 offset:16384\n"
  "v_mfma_f32_16x16x32_bf16 v[40:43], a[24:27], a[148:151], v[40:43]\n"
  "ds_read_b64_tr_b16 a[134:135], v226 offset:16896\n"
  "v_mfma_f32_16x16x32_bf16 v[44:47], a[28:31], a[148:151], v[44:47]\n"
  "ds_read_b128 a[52:55], v229 offset:2048\n"
  "s_waitcnt lgkmcnt(7)\n"
  "v_mfma_f32_16x16x32_bf16 v[44:47], a[12:15], a[144:147], v[44:47]\n"
  "buffer_load_dwordx4 v222, s[32:35], s44 offen offset:2048 lds\n"
  "v_mfma_f32_16x16x32_bf16 v[60:63], a[12:15], a[152:155], v[60:63]\n"
  "ds_read_b64_tr_b16 a[136:137], v227 offset:4096\n"
  "v_mfma_f32_16x16x32_bf16 v[60:63], a[28:31], a[156:159], v[60:63]\n"
  "ds_read_b64_tr_b16 a[138:139], v227 offset:4608\n"
  "v_mfma_f32_16x16x32_bf16 v[56:59], a[24:27], a[156:159], v[56:59]\n"
  "ds_read_b128 a[40:43], v228 offset:4096\n"
  "v_mfma_f32_16x16x32_bf16 v[56:59], a[8:11], a[152:155], v[56:59]\n"
  "buffer_load_dwordx4 v223, s[32:35], s44 offen offset:3072 lds\n"
  "v_mfma_f32_16x16x32_bf16 v[52:55], a[4:7], a[152:155], v[52:55]\n"
  "ds_read_b64_tr_b16 a[140:141], v227 offset:16384\n"
  "s_add_u32 m0, s46, 24576\n"
  "v_mfma_f32_16x16x32_bf16 v[52:55], a[20:23], a[156:159], v[52:55]\n"
  "ds_read_b64_tr_b16 a[142:143], v227 offset:16896\n"
  "v_mfma_f32_16x16x32_bf16 v[48:51], a[16:19], a[156:159], v[48:51]\n"
  "ds_read_b128 a[56:59], v229 offset:4096\n"
  "s_waitcnt lgkmcnt(7)\n"
  "v_mfma_f32_16x16x32_bf16 v[48:51], a[0:3], a[152:155], v[48:51]\n"
  "buffer_load_dwordx4 v216, s[36:39], s45 offen offset:0 lds\n"
  "v_mfma_f32_16x16x32_bf16 v[64:67], a[128:131], a[0:3], v[64:67]\n"
  "ds_read_b64_tr_b16 a[144:145], v226 offset:24576\n"
  "v_mfma_f32_16x16x32_bf16 v[64:67], a[132:135], a[16:19], v[64:67]\n"
  "ds_read_b64_tr_b16 a[146:147], v226 offset:25088\n"
  "v_mfma_f32_16x16x32_bf16 v[68:71], a[132:135], a[20:23], v[68:71]\n"
  "ds_read_b128 a[44:47], v228 offset:6144\n"
  "v_mfma_f32_16x16x32_bf16 v[68:71], a[128:131], a[4:7], v[68:71]\n"
  "buffer_load_dwordx4 v217, s[36:39], s45 offen offset:1024 lds\n"
  "v_mfma_f32_16x16x32_bf16 v[72:75], a[128:131], a[8:11], v[72:75]\n"
  "ds_read_b64_tr_b16 a[148:149], v226 offset:36864\n"
  "v_mfma_f32_16x16x32_bf16 v[72:75], a[132:135], a[24:27], v[72:75]\n"
  "ds_read_b64_tr_b16 a[150:151], v226 offset:37376\n"
  "v_mfma_f32_16x16x32_bf16 v[76:79], a[132:135], a[28:31], v[76:79]\n"
  "ds_read_b128 a[60:63], v229 offset:6144\n"
  "s_waitcnt lgkmcnt(7)\n"
  "v_mfma_f32_16x16x32_bf16 v[76:79], a[128:131], a[12:15], v[76:79]\n"
  "buffer_load_dwordx4 v216, s[36:39], s45 offen offset:2048 lds\n"
  "v_mfma_f32_16x16x32_bf16 v[92:95], a[136:139], a[12:15], v[92:95]\n"
  "ds_read_b64_tr_b16 a[152:153], v227 offset:24576\n"
  "v_mfma_f32_16x16x32_bf16 v[92:95], a[140:143], a[28:31], v[92:95]\n"
  "ds_read_b64_tr_b16 a[154:155], v227 offset:25088\n"
  "v_mfma_f32_16x16x32_bf16 v[88:91], a[140:143], a[24:27], v[88:91]\n"
  "buffer_load_dwordx4 v217, s[36:39], s45 offen offset:3072 lds\n"
  "v_mfma_f32_16x16x32_bf16 v[88:91], a[136:139], a[8:11], v[88:91]\n"
  "s_add_u32 m0, s46, 28672\n"
  "v_mfma_f32_16x16x32_bf16 v[84:87], a[136:139], a[4:7], v[84:87]\n"
  "ds_read_b64_tr_b16 a[156:157], v227 offset:36864\n"
  "v_mfma_f32_16x16x32_bf16 v[84:87], a[140:143], a[20:23], v[84:87]\n"
  "ds_read_b64_tr_b16 a[158:159], v227 offset:37376\n"
  "s_waitcnt lgkmcnt(4)\n"
  "v_mfma_f32_16x16x32_bf16 v[80:83], a[140:143], a[16:19], v[80:83]\n"
  "buffer_load_dwordx4 v218, s[36:39], s45 offen offset:0 lds\n"
  "v_mfma_f32_16x16x32_bf16 v[80:83], a[136:139], a[0:3], v[80:83]\n"
  "v_mfma_f32_16x16x32_bf16 v[0:3], a[144:147], a[32:35], v[0:3]\n"
  "ds_read_b64_tr_b16 a[128:129], v226 offset:26624\n"
  "v_mfma_f32_16x16x32_bf16 v[0:3], a[148:151], a[48:51], v[0:3]\n"
  "ds_read_b64_tr_b16 a[130:131], v226 offset:27136\n"
  "s_waitcnt vmcnt(16)\n"
  "buffer_store_dwordx4 v[248:251], v251, s[52:55], s68 offen sc1\n"
  "v_mfma_f32_16x16x32_bf16 v[4:7], a[148:151], a[52:55], v[4:7]\n"
  "buffer_load_dwordx4 v219, s[36:39], s45 offen offset:1024 lds\n"
  "v_mfma_f32_16x16x32_bf16 v[4:7], a[144:147], a[36:39], v[4:7]\n"
  "s_xor_b32 s44, s44, 128\n"
  "s_add_u32 s44, s44, 256\n"
  "s_and_b32 s44, s44, 0x7ff\n"
  "v_mfma_f32_16x16x32_bf16 v[8:11], a[144:147], a[40:43], v[8:11]\n"
  "ds_read_b64_tr_b16 a[132:133], v226 offset:38912\n"
  "s_mul_i32 s45, s44, 3072\n"
  "v_mfma_f32_16x16x32_bf16 v[8:11], a[148:151], a[56:59], v[8:11]\n"
  "ds_read_b64_tr_b16 a[134:135], v226 offset:39424\n"
  "s_waitcnt lgkmcnt(4)\n"
  "v_mfma_f32_16x16x32_bf16 v[12:15], a[148:151], a[60:63], v[12:15]\n"
  "v_mfma_f32_16x16x32_bf16 v[12:15], a[144:147], a[44:47], v[12:15]\n"
  "v_mfma_f32_16x16x32_bf16 v[28:31], a[152:155], a[44:47], v[28:31]\n"
  "s_waitcnt vmcnt(11)\n"
  "s_barrier\n"
  "s_add_u32 m0, s47, 0\n"
  "v_mfma_f32_16x16x32_bf16 v[28:31], a[156:159], a[60:63], v[28:31]\n"
  "ds_read_b64_tr_b16 a[136:137], v227 offset:26624\n"
  "v_mfma_f32_16x16x32_bf16 v[24:27], a[156:159], a[56:59], v[24:27]\n"
  "ds_read_b64_tr_b16 a[138:139], v227 offset:27136\n"
  "v_mfma_f32_16x16x32_bf16 v[24:27], a[152:155], a[40:43], v[24:27]\n"
  "ds_read_b128 a[0:3], v228 offset:16384\n"
  "v_mfma_f32_16x16x32_bf16 v[20:23], a[152:155], a[36:39], v[20:23]\n"
  "ds_read_b64_tr_b16 a[140:141], v227 offset:38912\n"
  "v_mfma_f32_16x16x32_bf16 v[20:23], a[156:159], a[52:55], v[20:23]\n"
  "ds_read_b64_tr_b16 a[142:143], v227 offset:39424\n"
  "v_mfma_f32_16x16x32_bf16 v[16:19], a[156:159], a[48:51], v[16:19]\n"
  "ds_read_b128 a[16:19], v229 offset:16384\n"
  "s_waitcnt lgkmcnt(6)\n"
  "v_mfma_f32_16x16x32_bf16 v[16:19], a[152:155], a[32:35], v[16:19]\n"
  "buffer_load_dwordx4 v220, s[32:35], s44 offen offset:0 lds\n"
  "v_mfma_f32_16x16x32_bf16 v[32:35], a[32:35], a[128:131], v[32:35]\n"
  "ds_read_b64_tr_b16 a[144:145], v226 offset:28672\n"
  "v_mfma_f32_16x16x32_bf16 v[32:35], a[48:51], a[132:135], v[32:35]\n"
  "ds_read_b64_tr_b16 a[146:147], v226 offset:29184\n"
  "v_mfma_f32_16x16x32_bf16 v[36:39], a[52:55], a[132:135], v[36:39]\n"
  "ds_read_b128 a[4:7], v228 offset:18432\n"
  "v_mfma_f32_16x16x32_bf16 v[36:39], a[36:39], a[128:131], v[36:39]\n"
  "buffer_load_dwordx4 v221, s[32:35], s44 offen offset:1024 lds\n"
  "v_mfma_f32_16x16x32_bf16 v[40:43], a[40:43], a[128:131], v[40:43]\n"
  "ds_read_b64_tr_b16 a[148:149], v226 offset:40960\n"
  "v_mfma_f32_16x16x32_bf16 v[40:43], a[56:59], a[132:135], v[40:43]\n"
  "ds_read_b64_tr_b16 a[150:151], v226 offset:41472\n"
  "v_mfma_f32_16x16x32_bf16 v[44:47], a[60:63], a[132:135], v[44:47]\n"
  "ds_read_b128 a[20:23], v229 offset:18432\n"
  "s_waitcnt lgkmcnt(7)\n"
  "v_mfma_f32_16x16x32_bf16 v[44:47], a[44:47], a[128:131], v[44:47]\n"
  "buffer_load_dwordx4 v222, s[32:35], s44 offen offset:2048 lds\n"
  "v_mfma_f32_16x16x32_bf16 v[60:63], a[44:47], a[136:139], v[60:63]\n"
  "ds_read_b64_tr_b16 a[152:153], v227 offset:28672\n"
  "v_mfma_f32_16x16x32_bf16 v[60:63], a[60:63], a[140:143], v[60:63]\n"
  "ds_read_b64_tr_b16 a[154:155], v227 offset:29184\n"
  "v_mfma_f32_16x16x32_bf16 v[56:59], a[56:59], a[140:143], v[56:59]\n"
  "ds_read_b128 a[8:11], v228 offset:20480\n"
  "v_mfma_f32_16x16x32_bf16 v[56:59], a[40:43], a[136:139], v[56:59]\n"
  "buffer_load_dwordx4 v223, s[32:35], s44 offen offset:3072 lds\n"
  "v_mfma_f32_16x16x32_bf16 v[52:55], a[36:39], a[136:139], v[52:55]\n"
  "ds_read_b64_tr_b16 a[156:157], v227 offset:40960\n"
  "s_add_u32 m0, s46, 49152\n"
  "v_mfma_f32_16x16x32_bf16 v[52:55], a[52:55], a[140:143], v[52:55]\n"
  "ds_read_b64_tr_b16 a[158:159], v227 offset:41472\n"
  "v_mfma_f32_16x16x32_bf16 v[48:51], a[48:51], a[140:143], v[48:51]\n"
  "ds_read_b128 a[24:27], v229 offset:20480\n"
  "s_waitcnt lgkmcnt(7)\n"
  "v_mfma_f32_16x16x32_bf16 v[48:51], a[32:35], a[136:139], v[48:51]\n"
  "buffer_load_dwordx4 v216, s[36:39], s45 offen offset:0 lds\n"
  "v_mfma_f32_16x16x32_bf16 v[64:67], a[144:147], a[32:35], v[64:67]\n"
  "ds_read_b64_tr_b16 a[128:129], v224 offset:0\n"
  "v_mfma_f32_16x16x32_bf16 v[64:67], a[148:151], a[48:51], v[64:67]\n"
  "ds_read_b64_tr_b16 a[130:131], v224 offset:512\n"
  "v_mfma_f32_16x16x32_bf16 v[68:71], a[148:151], a[52:55], v[68:71]\n"
  "ds_read_b128 a[12:15], v228 offset:22528\n"
  "v_mfma_f32_16x16x32_bf16 v[68:71], a[144:147], a[36:39], v[68:71]\n"
  "buffer_load_dwordx4 v217, s[36:39], s45 offen offset:1024 lds\n"
  "v_mfma_f32_16x16x32_bf16 v[72:75], a[144:147], a[40:43], v[72:75]\n"
  "ds_read_b64_tr_b16 a[132:133], v224 offset:12288\n"
  "v_mfma_f32_16x16x32_bf16 v[72:75], a[148:151], a[56:59], v[72:75]\n"
  "ds_read_b64_tr_b16 a[134:135], v224 offset:12800\n"
  "v_mfma_f32_16x16x32_bf16 v[76:79], a[148:151], a[60:63], v[76:79]\n"
  "ds_read_b128 a[28:31], v229 offset:22528\n"
  "s_waitcnt lgkmcnt(7)\n"
  "v_mfma_f32_16x16x32_bf16 v[76:79], a[144:147], a[44:47], v[76:79]\n"
  "buffer_load_dwordx4 v216, s[36:39], s45 offen offset:2048 lds\n"
  "v_mfma_f32_16x16x32_bf16 v[92:95], a[152:155], a[44:47], v[92:95]\n"
  "ds_read_b64_tr_b16 a[136:137], v225 offset:0\n"
  "v_mfma_f32_16x16x32_bf16 v[92:95], a[156:159], a[60:63], v[92:95]\n"
  "ds_read_b64_tr_b16 a[138:139], v225 offset:512\n"
  "v_mfma_f32_16x16x32_bf16 v[88:91], a[156:159], a[56:59], v[88:91]\n"
  "buffer_load_dwordx4 v217, s[36:39], s45 offen offset:3072 lds\n"
  "v_mfma_f32_16x16x32_bf16 v[88:91], a[152:155], a[40:43], v[88:91]\n"
  "s_add_u32 m0, s46, 53248\n"
  "v_mfma_f32_16x16x32_bf16 v[84:87], a[152:155], a[36:39], v[84:87]\n"
  "ds_read_b64_tr_b16 a[140:141], v225 offset:12288\n"
  "v_mfma_f32_16x16x32_bf16 v[84:87], a[156:159], a[52:55], v[84:87]\n"
  "ds_read_b64_tr_b16 a[142:143], v225 offset:12800\n"
  "s_waitcnt lgkmcnt(4)\n"
  "v_mfma_f32_16x16x32_bf16 v[80:83], a[156:159], a[48:51], v[80:83]\n"
  "buffer_load_dwordx4 v218, s[36:39], s45 offen offset:0 lds\n"
  "v_mfma_f32_16x16x32_bf16 v[80:83], a[152:155], a[32:35], v[80:83]\n"
  "v_mfma_f32_16x16x32_bf16 v[0:3], a[128:131], a[0:3], v[0:3]\n"
  "ds_read_b64_tr_b16 a[144:145], v224 offset:2048\n"
  "s_lshl_b32 s8, s91, 10\n"
  "v_mfma_f32_16x16x32_bf16 v[0:3], a[132:135], a[16:19], v[0:3]\n"
  "ds_read_b64_tr_b16 a[146:147], v224 offset:2560\n"
  "s_add_u32 s8, s8, 147456\n"
  "v_mfma_f32_16x16x32_bf16 v[4:7], a[132:135], a[20:23], v[4:7]\n"
  "buffer_load_dwordx4 v219, s[36:39], s45 offen offset:1024 lds\n"
  "v_mfma_f32_16x16x32_bf16 v[4:7], a[128:131], a[4:7], v[4:7]\n"
  "s_xor_b32 s44, s44, 128\n"
  "s_mul_i32 s45, s44, 3072\n"
  "v_and_b32 v242, 0xf0, v254\n"
  "v_mfma_f32_16x16x32_bf16 v[8:11], a[128:131], a[8:11], v[8:11]\n"
  "ds_read_b64_tr_b16 a[148:149], v224 offset:14336\n"
  "v_lshrrev_b32 v243, 5, v255\n"
  "v_mfma_f32_16x16x32_bf16 v[8:11], a[132:135], a[24:27], v[8:11]\n"
  "ds_read_b64_tr_b16 a[150:151], v224 offset:14848\n"
  "s_waitcnt lgkmcnt(4)\n"
  "v_mfma_f32_16x16x32_bf16 v[12:15], a[132:135], a[28:31], v[12:15]\n"
  "v_lshrrev_b32 v247, 2, v242\n"
  "v_lshl_add_u32 v247, v243, 6, v247\n"
  "v_mfma_f32_16x16x32_bf16 v[12:15], a[128:131], a[12:15], v[12:15]\n"
  "v_add_u32 v242, s8, v242\n"
  "v_and_b32 v241, 48, v255\n"
  "v_mfma_f32_16x16x32_bf16 v[28:31], a[136:139], a[12:15], v[28:31]\n"
  "s_waitcnt vmcnt(10)\n"
  "s_barrier\n"
  "s_add_u32 m0, s47, 16384\n"
  "v_mfma_f32_16x16x32_bf16 v[28:31], a[140:143], a[28:31], v[28:31]\n"
  "ds_read_b64_tr_b16 a[152:153], v225 offset:2048\n"
  "v_add_u32 v241, s8, v241\n"
  "v_mfma_f32_16x16x32_bf16 v[24:27], a[140:143], a[24:27], v[24:27]\n"
  "ds_read_b64_tr_b16 a[154:155], v225 offset:2560\n"
  "s_lshl_b32 s9, s90, 7\n"
  "v_mfma_f32_16x16x32_bf16 v[24:27], a[136:139], a[8:11], v[24:27]\n"
  "ds_read_b128 a[32:35], v228 offset:32768\n"
  "s_add_u32 s8, s8, s9\n"
  "v_mfma_f32_16x16x32_bf16 v[20:23], a[136:139], a[4:7], v[20:23]\n"
  "ds_read_b64_tr_b16 a[156:157], v225 offset:14336\n"
  "v_add_u32 v239, s8, v247\n"
  "v_mfma_f32_16x16x32_bf16 v[20:23], a[140:143], a[20:23], v[20:23]\n"
  "ds_read_b64_tr_b16 a[158:159], v225 offset:14848\n"
  "s_lshl_b32 s9, s90, 3\n"
  "v_mfma_f32_16x16x32_bf16 v[16:19], a[140:143], a[16:19], v[16:19]\n"
  "ds_read_b128 a[48:51], v229 offset:32768\n"
  "s_waitcnt lgkmcnt(6)\n"
  "v_mfma_f32_16x16x32_bf16 v[16:19], a[136:139], a[0:3], v[16:19]\n"
  "buffer_load_dwordx4 v220, s[32:35], s44 offen offset:0 lds\n"
  "v_mfma_f32_16x16x32_bf16 v[32:35], a[0:3], a[144:147], v[32:35]\n"
  "ds_read_b64_tr_b16 a[128:129], v224 offset:4096\n"
  "v_lshl_add_u32 v240, v243, 2, s9\n"
  "v_mfma_f32_16x16x32_bf16 v[32:35], a[16:19], a[148:151], v[32:35]\n"
  "ds_read_b64_tr_b16 a[130:131], v224 offset:4608\n"
  "v_add_u32 v240, v240, v242\n"
  "v_mfma_f32_16x16x32_bf16 v[36:39], a[20:23], a[148:151], v[36:39]\n"
  "ds_read_b128 a[36:39], v228 offset:34816\n"
  "s_mul_i32 s8, s90, 3072\n"
  "v_mfma_f32_16x16x32_bf16 v[36:39], a[4:7], a[144:147], v[36:39]\n"
  "buffer_load_dwordx4 v221, s[32:35], s44 offen offset:1024 lds\n"
  "v_mfma_f32_16x16x32_bf16 v[40:43], a[8:11], a[144:147], v[40:43]\n"
  "ds_read_b64_tr_b16 a[132:133], v224 offset:16384\n"
  "s_add_u32 s8, s8, 149504\n"
  "v_mfma_f32_16x16x32_bf16 v[40:43], a[24:27], a[148:151], v[40:43]\n"
  "ds_read_b64_tr_b16 a[134:135], v224 offset:16896\n"
  "s_mul_i32 s9, s91, 24\n"
  "v_mfma_f32_16x16x32_bf16 v[44:47], a[28:31], a[148:151], v[44:47]\n"
  "ds_read_b128 a[52:55], v229 offset:34816\n"
  "s_waitcnt lgkmcnt(7)\n"
  "v_mfma_f32_16x16x32_bf16 v[44:47], a[12:15], a[144:147], v[44:47]\n"
  "buffer_load_dwordx4 v222, s[32:35], s44 offen offset:2048 lds\n"
  "v_mfma_f32_16x16x32_bf16 v[60:63], a[12:15], a[152:155], v[60:63]\n"
  "ds_read_b64_tr_b16 a[136:137], v225 offset:4096\n"
  "v_mul_u32_u24 v238, 48, v255\n"
  "v_mfma_f32_16x16x32_bf16 v[60:63], a[28:31], a[156:159], v[60:63]\n"
  "ds_read_b64_tr_b16 a[138:139], v225 offset:4608\n"
  "v_add_u32 v238, s8, v238\n"
  "v_mfma_f32_16x16x32_bf16 v[56:59], a[24:27], a[156:159], v[56:59]\n"
  "ds_read_b128 a[40:43], v228 offset:36864\n"
  "v_add_u32 v243, s9, v238\n"
  "v_mfma_f32_16x16x32_bf16 v[56:59], a[8:11], a[152:155], v[56:59]\n"
  "buffer_load_dwordx4 v223, s[32:35], s44 offen offset:3072 lds\n"
  "v_mfma_f32_16x16x32_bf16 v[52:55], a[4:7], a[152:155], v[52:55]\n"
  "ds_read_b64_tr_b16 a[140:141], v225 offset:16384\n"
  "s_add_u32 m0, s46, 73728\n"
  "v_mfma_f32_16x16x32_bf16 v[52:55], a[20:23], a[156:159], v[52:55]\n"
  "ds_read_b64_tr_b16 a[142:143], v225 offset:16896\n"
  "v_mfma_f32_16x16x32_bf16 v[48:51], a[16:19], a[156:159], v[48:51]\n"
  "ds_read_b128 a[56:59], v229 offset:36864\n"
  "s_waitcnt lgkmcnt(7)\n"
  "v_mfma_f32_16x16x32_bf16 v[48:51], a[0:3], a[152:155], v[48:51]\n"
  "buffer_load_dwordx4 v216, s[36:39], s45 offen offset:0 lds\n"
  "v_mfma_f32_16x16x32_bf16 v[64:67], a[128:131], a[0:3], v[64:67]\n"
  "ds_read_b64_tr_b16 a[144:145], v224 offset:24576\n"
  "v_mfma_f32_16x16x32_bf16 v[64:67], a[132:135], a[16:19], v[64:67]\n"
  "ds_read_b64_tr_b16 a[146:147], v224 offset:25088\n"
  "v_mfma_f32_16x16x32_bf16 v[68:71], a[132:135], a[20:23], v[68:71]\n"
  "ds_read_b128 a[44:47], v228 offset:38912\n"
  "v_mfma_f32_16x16x32_bf16 v[68:71], a[128:131], a[4:7], v[68:71]\n"
  "buffer_load_dwordx4 v217, s[36:39], s45 offen offset:1024 lds\n"
  "v_mfma_f32_16x16x32_bf16 v[72:75], a[128:131], a[8:11], v[72:75]\n"
  "ds_read_b64_tr_b16 a[148:149], v224 offset:36864\n"
  "v_mfma_f32_16x16x32_bf16 v[72:75], a[132:135], a[24:27], v[72:75]\n"
  "ds_read_b64_tr_b16 a[150:151], v224 offset:37376\n"
  "v_mfma_f32_16x16x32_bf16 v[76:79], a[132:135], a[28:31], v[76:79]\n"
  "ds_read_b128 a[60:63], v229 offset:38912\n"
  "s_waitcnt lgkmcnt(7)\n"
  "v_mfma_f32_16x16x32_bf16 v[76:79], a[128:131], a[12:15], v[76:79]\n"
  "buffer_load_dwordx4 v216, s[36:39], s45 offen offset:2048 lds\n"
  "v_mfma_f32_16x16x32_bf16 v[92:95], a[136:139], a[12:15], v[92:95]\n"
  "ds_read_b64_tr_b16 a[152:153], v225 offset:24576\n"
  "v_mfma_f32_16x16x32_bf16 v[92:95], a[140:143], a[28:31], v[92:95]\n"
  "ds_read_b64_tr_b16 a[154:155], v225 offset:25088\n"
  "v_mfma_f32_16x16x32_bf16 v[88:91], a[140:143], a[24:27], v[88:91]\n"
  "buffer_load_dwordx4 v217, s[36:39], s45 offen offset:3072 lds\n"
  "v_mfma_f32_16x16x32_bf16 v[88:91], a[136:139], a[8:11], v[88:91]\n"
  "s_add_u32 m0, s46, 77824\n"
  "v_mfma_f32_16x16x32_bf16 v[84:87], a[136:139], a[4:7], v[84:87]\n"
  "ds_read_b64_tr_b16 a[156:157], v225 offset:36864\n"
  "v_mfma_f32_16x16x32_bf16 v[84:87], a[140:143], a[20:23], v[84:87]\n"
  "ds_read_b64_tr_b16 a[158:159], v225 offset:37376\n"
  "s_waitcnt lgkmcnt(4)\n"
  "v_mfma_f32_16x16x32_bf16 v[80:83], a[140:143], a[16:19], v[80:83]\n"
  "buffer_load_dwordx4 v218, s[36:39], s45 offen offset:0 lds\n"
  "v_mfma_f32_16x16x32_bf16 v[80:83], a[136:139], a[0:3], v[80:83]\n"
  "v_mfma_f32_16x16x32_bf16 v[0:3], a[144:147], a[32:35], v[0:3]\n"
  "ds_read_b64_tr_b16 a[128:129], v224 offset:26624\n"
  "v_mfma_f32_16x16x32_bf16 v[0:3], a[148:151], a[48:51], v[0:3]\n"
  "ds_read_b64_tr_b16 a[130:131], v224 offset:27136\n"
  "v_mfma_f32_16x16x32_bf16 v[4:7], a[148:151], a[52:55], v[4:7]\n"
  "buffer_load_dwordx4 v219, s[36:39], s45 offen offset:1024 lds\n"
  "v_mfma_f32_16x16x32_bf16 v[4:7], a[144:147], a[36:39], v[4:7]\n"
  "s_xor_b32 s44, s44, 128\n"
  "s_add_u32 s44, s44, 256\n"
  "s_and_b32 s44, s44, 0x7ff\n"
  "v_mfma_f32_16x16x32_bf16 v[8:11], a[144:147], a[40:43], v[8:11]\n"
  "ds_read_b64_tr_b16 a[132:133], v224 offset:38912\n"
  "s_mul_i32 s45, s44, 3072\n"
  "v_mfma_f32_16x16x32_bf16 v[8:11], a[148:151], a[56:59], v[8:11]\n"
  "ds_read_b64_tr_b16 a[134:135], v224 offset:39424\n"
  "s_waitcnt lgkmcnt(4)\n"
  "v_mfma_f32_16x16x32_bf16 v[12:15], a[148:151], a[60:63], v[12:15]\n"
  "v_mfma_f32_16x16x32_bf16 v[12:15], a[144:147], a[44:47], v[12:15]\n"
  "v_mfma_f32_16x16x32_bf16 v[28:31], a[152:155], a[44:47], v[28:31]\n"
  "s_waitcnt vmcnt(10)\n"
  "s_barrier\n"
  "s_add_u32 m0, s47, 32768\n"
  "v_mfma_f32_16x16x32_bf16 v[28:31], a[156:159], a[60:63], v[28:31]\n"
  "ds_read_b64_tr_b16 a[136:137], v225 offset:26624\n"
  "v_mfma_f32_16x16x32_bf16 v[24:27], a[156:159], a[56:59], v[24:27]\n"
  "ds_read_b64_tr_b16 a[138:139], v225 offset:27136\n"
  "v_mfma_f32_16x16x32_bf16 v[24:27], a[152:155], a[40:43], v[24:27]\n"
  "ds_read_b128 a[0:3], v228 offset:0\n"
  "v_mfma_f32_16x16x32_bf16 v[20:23], a[152:155], a[36:39], v[20:23]\n"
  "ds_read_b64_tr_b16 a[140:141], v225 offset:38912\n"
  "v_mfma_f32_16x16x32_bf16 v[20:23], a[156:159], a[52:55], v[20:23]\n"
  "ds_read_b64_tr_b16 a[142:143], v225 offset:39424\n"
  "v_mfma_f32_16x16x32_bf16 v[16:19], a[156:159], a[48:51], v[16:19]\n"
  "ds_read_b128 a[16:19], v229 offset:0\n"
  "s_waitcnt lgkmcnt(6)\n"
  "v_mfma_f32_16x16x32_bf16 v[16:19], a[152:155], a[32:35], v[16:19]\n"
  "buffer_load_dwordx4 v220, s[32:35], s44 offen offset:0 lds\n"
  "v_mfma_f32_16x16x32_bf16 v[32:35], a[32:35], a[128:131], v[32:35]\n"
  "ds_read_b64_tr_b16 a[144:145], v224 offset:28672\n"
  "v_mfma_f32_16x16x32_bf16 v[32:35], a[48:51], a[132:135], v[32:35]\n"
  "ds_read_b64_tr_b16 a[146:147], v224 offset:29184\n"
  "v_mfma_f32_16x16x32_bf16 v[36:39], a[52:55], a[132:135], v[36:39]\n"
  "ds_read_b128 a[4:7], v228 offset:2048\n"
  "v_mfma_f32_16x16x32_bf16 v[36:39], a[36:39], a[128:131], v[36:39]\n"
  "buffer_load_dwordx4 v221, s[32:35], s44 offen offset:1024 lds\n"
  "v_mfma_f32_16x16x32_bf16 v[40:43], a[40:43], a[128:131], v[40:43]\n"
  "ds_read_b64_tr_b16 a[148:149], v224 offset:40960\n"
  "v_mfma_f32_16x16x32_bf16 v[40:43], a[56:59], a[132:135], v[40:43]\n"
  "ds_read_b64_tr_b16 a[150:151], v224 offset:41472\n"
  "v_mfma_f32_16x16x32_bf16 v[44:47], a[60:63], a[132:135], v[44:47]\n"
  "ds_read_b128 a[20:23], v229 offset:2048\n"
  "s_waitcnt lgkmcnt(7)\n"
  "v_mfma_f32_16x16x32_bf16 v[44:47], a[44:47], a[128:131], v[44:47]\n"
  "buffer_load_dwordx4 v222, s[32:35], s44 offen offset:2048 lds\n"
  "v_mfma_f32_16x16x32_bf16 v[60:63], a[44:47], a[136:139], v[60:63]\n"
  "ds_read_b64_tr_b16 a[152:153], v225 offset:28672\n"
  "v_mfma_f32_16x16x32_bf16 v[60:63], a[60:63], a[140:143], v[60:63]\n"
  "ds_read_b64_tr_b16 a[154:155], v225 offset:29184\n"
  "v_mfma_f32_16x16x32_bf16 v[56:59], a[56:59], a[140:143], v[56:59]\n"
  "ds_read_b128 a[8:11], v228 offset:4096\n"
  "v_mfma_f32_16x16x32_bf16 v[56:59], a[40:43], a[136:139], v[56:59]\n"
  "buffer_load_dwordx4 v223, s[32:35], s44 offen offset:3072 lds\n"
  "v_mfma_f32_16x16x32_bf16 v[52:55], a[36:39], a[136:139], v[52:55]\n"
  "ds_read_b64_tr_b16 a[156:157], v225 offset:40960\n"
  "s_add_u32 m0, s46, 0\n"
  "v_mfma_f32_16x16x32_bf16 v[52:55], a[52:55], a[140:143], v[52:55]\n"
  "ds_read_b64_tr_b16 a[158:159], v225 offset:41472\n"
  "v_mfma_f32_16x16x32_bf16 v[48:51], a[48:51], a[140:143], v[48:51]\n"
  "ds_read_b128 a[24:27], v229 offset:4096\n"
  "s_waitcnt lgkmcnt(7)\n"
  "v_mfma_f32_16x16x32_bf16 v[48:51], a[32:35], a[136:139], v[48:51]\n"
  "buffer_load_dwordx4 v216, s[36:39], s45 offen offset:0 lds\n"
  "v_mfma_f32_16x16x32_bf16 v[64:67], a[144:147], a[32:35], v[64:67]\n"
  "ds_read_b64_tr_b16 a[128:129], v226 offset:0\n"
  "v_mfma_f32_16x16x32_bf16 v[64:67], a[148:151], a[48:51], v[64:67]\n"
  "ds_read_b64_tr_b16 a[130:131], v226 offset:512\n"
  "v_mfma_f32_16x16x32_bf16 v[68:71], a[148:151], a[52:55], v[68:71]\n"
  "ds_read_b128 a[12:15], v228 offset:6144\n"
  "v_mfma_f32_16x16x32_bf16 v[68:71], a[144:147], a[36:39], v[68:71]\n"
  "buffer_load_dwordx4 v217, s[36:39], s45 offen offset:1024 lds\n"
  "v_mfma_f32_16x16x32_bf16 v[72:75], a[144:147], a[40:43], v[72:75]\n"
  "ds_read_b64_tr_b16 a[132:133], v226 offset:12288\n"
  "v_mfma_f32_16x16x32_bf16 v[72:75], a[148:151], a[56:59], v[72:75]\n"
  "ds_read_b64_tr_b16 a[134:135], v226 offset:12800\n"
  "v_mfma_f32_16x16x32_bf16 v[76:79], a[148:151], a[60:63], v[76:79]\n"
  "ds_read_b128 a[28:31], v229 offset:6144\n"
  "s_waitcnt lgkmcnt(7)\n"
  "v_mfma_f32_16x16x32_bf16 v[76:79], a[144:147], a[44:47], v[76:79]\n"
  "buffer_load_dwordx4 v216, s[36:39], s45 offen offset:2048 lds\n"
  "v_mfma_f32_16x16x32_bf16 v[92:95], a[152:155], a[44:47], v[92:95]\n"
  "ds_read_b64_tr_b16 a[136:137], v227 offset:0\n"
  "v_mfma_f32_16x16x32_bf16 v[92:95], a[156:159], a[60:63], v[92:95]\n"
  "ds_read_b64_tr_b16 a[138:139], v227 offset:512\n"
  "v_mfma_f32_16x16x32_bf16 v[88:91], a[156:159], a[56:59], v[88:91]\n"
  "buffer_load_dwordx4 v217, s[36:39], s45 offen offset:3072 lds\n"
  "v_mfma_f32_16x16x32_bf16 v[88:91], a[152:155], a[40:43], v[88:91]\n"
  "s_add_u32 m0, s46, 4096\n"
  "v_mfma_f32_16x16x32_bf16 v[84:87], a[152:155], a[36:39], v[84:87]\n"
  "ds_read_b64_tr_b16 a[140:141], v227 offset:12288\n"
  "v_mfma_f32_16x16x32_bf16 v[84:87], a[156:159], a[52:55], v[84:87]\n"
  "ds_read_b64_tr_b16 a[142:143], v227 offset:12800\n"
  "s_waitcnt lgkmcnt(4)\n"
  "v_mfma_f32_16x16x32_bf16 v[80:83], a[156:159], a[48:51], v[80:83]\n"
  "buffer_load_dwordx4 v218, s[36:39], s45 offen offset:0 lds\n"
  "v_mfma_f32_16x16x32_bf16 v[80:83], a[152:155], a[32:35], v[80:83]\n"
  "v_mfma_f32_16x16x32_bf16 v[0:3], a[128:131], a[0:3], v[0:3]\n"
  "ds_read_b64_tr_b16 a[144:145], v226 offset:2048\n"
  "v_mfma_f32_16x16x32_bf16 v[0:3], a[132:135], a[16:19], v[0:3]\n"
  "ds_read_b64_tr_b16 a[146:147], v226 offset:2560\n"
  "v_mfma_f32_16x16x32_bf16 v[4:7], a[132:135], a[20:23], v[4:7]\n"
  "buffer_load_dwordx4 v219, s[36:39], s45 offen offset:1024 lds\n"
  "v_mfma_f32_16x16x32_bf16 v[4:7], a[128:131], a[4:7], v[4:7]\n"
  "s_xor_b32 s44, s44, 128\n"
  "s_mul_i32 s45, s44, 3072\n"
  "v_mfma_f32_16x16x32_bf16 v[8:11], a[128:131], a[8:11], v[8:11]\n"
  "ds_read_b64_tr_b16 a[148:149], v226 offset:14336\n"
  "v_mfma_f32_16x16x32_bf16 v[8:11], a[132:135], a[24:27], v[8:11]\n"
  "ds_read_b64_tr_b16 a[150:151], v226 offset:14848\n"
  "s_waitcnt lgkmcnt(4)\n"
  "v_mfma_f32_16x16x32_bf16 v[12:15], a[132:135], a[28:31], v[12:15]\n"
  "v_mfma_f32_16x16x32_bf16 v[12:15], a[128:131], a[12:15], v[12:15]\n"
  "v_mfma_f32_16x16x32_bf16 v[28:31], a[136:139], a[12:15], v[28:31]\n"
  "s_waitcnt vmcnt(10)\n"
  "s_barrier\n"
  "s_add_u32 m0, s47, 0\n"
  "v_mfma_f32_16x16x32_bf16 v[28:31], a[140:143], a[28:31], v[28:31]\n"
  "ds_read_b64_tr_b16 a[152:153], v227 offset:2048\n"
  "v_mfma_f32_16x16x32_bf16 v[24:27], a[140:143], a[24:27], v[24:27]\n"
  "ds_read_b64_tr_b16 a[154:155], v227 offset:2560\n"
  "v_mfma_f32_16x16x32_bf16 v[24:27], a[136:139], a[8:11], v[24:27]\n"
  "ds_read_b128 a[32:35], v228 offset:16384\n"
  "v_mfma_f32_16x16x32_bf16 v[20:23], a[136:139], a[4:7], v[20:23]\n"
  "ds_read_b64_tr_b16 a[156:157], v227 offset:14336\n"
  "v_mfma_f32_16x16x32_bf16 v[20:23], a[140:143], a[20:23], v[20:23]\n"
  "ds_read_b64_tr_b16 a[158:159], v227 offset:14848\n"
  "v_mfma_f32_16x16x32_bf16 v[16:19], a[140:143], a[16:19], v[16:19]\n"
  "ds_read_b128 a[48:51], v229 offset:16384\n"
  "s_waitcnt lgkmcnt(6)\n"
  "v_mfma_f32_16x16x32_bf16 v[16:19], a[136:139], a[0:3], v[16:19]\n"
  "buffer_load_dwordx4 v220, s[32:35], s44 offen offset:0 lds\n"
  "v_mfma_f32_16x16x32_bf16 v[32:35], a[0:3], a[144:147], v[32:35]\n"
  "ds_read_b64_tr_b16 a[128:129], v226 offset:4096\n"
  "v_mfma_f32_16x16x32_bf16 v[32:35], a[16:19], a[148:151], v[32:35]\n"
  "ds_read_b64_tr_b16 a[130:131], v226 offset:4608\n"
  "v_mfma_f32_16x16x32_bf16 v[36:39], a[20:23], a[148:151], v[36:39]\n"
  "ds_read_b128 a[36:39], v228 offset:18432\n"
  "v_mfma_f32_16x16x32_bf16 v[36:39], a[4:7], a[144:147], v[36:39]\n"
  "buffer_load_dwordx4 v221, s[32:35], s44 offen offset:1024 lds\n"
  "v_mfma_f32_16x16x32_bf16 v[40:43], a[8:11], a[144:147], v[40:43]\n"
  "ds_read_b64_tr_b16 a[132:133], v226 offset:16384\n"
  "v_mfma_f32_16x16x32_bf16 v[40:43], a[24:27], a[148:151], v[40:43]\n"
  "ds_read_b64_tr_b16 a[134:135], v226 offset:16896\n"
  "v_mfma_f32_16x16x32_bf16 v[44:47], a[28:31], a[148:151], v[44:47]\n"
  "ds_read_b128 a[52:55], v229 offset:18432\n"
  "s_waitcnt lgkmcnt(7)\n"
  "v_mfma_f32_16x16x32_bf16 v[44:47], a[12:15], a[144:147], v[44:47]\n"
  "buffer_load_dwordx4 v222, s[32:35], s44 offen offset:2048 lds\n"
  "v_mfma_f32_16x16x32_bf16 v[60:63], a[12:15], a[152:155], v[60:63]\n"
  "ds_read_b64_tr_b16 a[136:137], v227 offset:4096\n"
  "v_mfma_f32_16x16x32_bf16 v[60:63], a[28:31], a[156:159], v[60:63]\n"
  "ds_read_b64_tr_b16 a[138:139], v227 offset:4608\n"
  "v_mfma_f32_16x16x32_bf16 v[56:59], a[24:27], a[156:159], v[56:59]\n"
  "ds_read_b128 a[40:43], v228 offset:20480\n"
  "v_mfma_f32_16x16x32_bf16 v[56:59], a[8:11], a[152:155], v[56:59]\n"
  "buffer_load_dwordx4 v223, s[32:35], s44 offen offset:3072 lds\n"
  "v_mfma_f32_16x16x32_bf16 v[52:55], a[4:7], a[152:155], v[52:55]\n"
  "ds_read_b64_tr_b16 a[140:141], v227 offset:16384\n"
  "s_add_u32 m0, s46, 24576\n"
  "v_mfma_f32_16x16x32_bf16 v[52:55], a[20:23], a[156:159], v[52:55]\n"
  "ds_read_b64_tr_b16 a[142:143], v227 offset:16896\n"
  "v_mfma_f32_16x16x32_bf16 v[48:51], a[16:19], a[156:159], v[48:51]\n"
  "ds_read_b128 a[56:59], v229 offset:20480\n"
  "s_waitcnt lgkmcnt(7)\n"
  "v_mfma_f32_16x16x32_bf16 v[48:51], a[0:3], a[152:155], v[48:51]\n"
  "buffer_load_dwordx4 v216, s[36:39], s45 offen offset:0 lds\n"
  "v_mfma_f32_16x16x32_bf16 v[64:67], a[128:131], a[0:3], v[64:67]\n"
  "ds_read_b64_tr_b16 a[144:145], v226 offset:24576\n"
  "v_mfma_f32_16x16x32_bf16 v[64:67], a[132:135], a[16:19], v[64:67]\n"
  "ds_read_b64_tr_b16 a[146:147], v226 offset:25088\n"
  "v_mfma_f32_16x16x32_bf16 v[68:71], a[132:135], a[20:23], v[68:71]\n"
  "ds_read_b128 a[44:47], v228 offset:22528\n"
  "v_mfma_f32_16x16x32_bf16 v[68:71], a[128:131], a[4:7], v[68:71]\n"
  "buffer_load_dwordx4 v217, s[36:39], s45 offen offset:1024 lds\n"
  "v_mfma_f32_16x16x32_bf16 v[72:75], a[128:131], a[8:11], v[72:75]\n"
  "ds_read_b64_tr_b16 a[148:149], v226 offset:36864\n"
  "v_mfma_f32_16x16x32_bf16 v[72:75], a[132:135], a[24:27], v[72:75]\n"
  "ds_read_b64_tr_b16 a[150:151], v226 offset:37376\n"
  "v_mfma_f32_16x16x32_bf16 v[76:79], a[132:135], a[28:31], v[76:79]\n"
  "ds_read_b128 a[60:63], v229 offset:22528\n"
  "s_waitcnt lgkmcnt(7)\n"
  "v_mfma_f32_16x16x32_bf16 v[76:79], a[128:131], a[12:15], v[76:79]\n"
  "buffer_load_dwordx4 v216, s[36:39], s45 offen offset:2048 lds\n"
  "v_mfma_f32_16x16x32_bf16 v[92:95], a[136:139], a[12:15], v[92:95]\n"
  "ds_read_b64_tr_b16 a[152:153], v227 offset:24576\n"
  "v_mfma_f32_16x16x32_bf16 v[92:95], a[140:143], a[28:31], v[92:95]\n"
  "ds_read_b64_tr_b16 a[154:155], v227 offset:25088\n"
  "v_mfma_f32_16x16x32_bf16 v[88:91], a[140:143], a[24:27], v[88:91]\n"
  "buffer_load_dwordx4 v217, s[36:39], s45 offen offset:3072 lds\n"
  "v_mfma_f32_16x16x32_bf16 v[88:91], a[136:139], a[8:11], v[88:91]\n"
  "s_add_u32 m0, s46, 28672\n"
  "v_mfma_f32_16x16x32_bf16 v[84:87], a[136:139], a[4:7], v[84:87]\n"
  "ds_read_b64_tr_b16 a[156:157], v227 offset:36864\n"
  "v_mfma_f32_16x16x32_bf16 v[84:87], a[140:143], a[20:23], v[84:87]\n"
  "ds_read_b64_tr_b16 a[158:159], v227 offset:37376\n"
  "s_waitcnt lgkmcnt(4)\n"
  "v_mfma_f32_16x16x32_bf16 v[80:83], a[140:143], a[16:19], v[80:83]\n"
  "buffer_load_dwordx4 v218, s[36:39], s45 offen offset:0 lds\n"
  "v_mfma_f32_16x16x32_bf16 v[80:83], a[136:139], a[0:3], v[80:83]\n"
  "v_mfma_f32_16x16x32_bf16 v[0:3], a[144:147], a[32:35], v[0:3]\n"
  "ds_read_b64_tr_b16 a[128:129], v226 offset:26624\n"
  "v_mfma_f32_16x16x32_bf16 v[0:3], a[148:151], a[48:51], v[0:3]\n"
  "ds_read_b64_tr_b16 a[130:131], v226 offset:27136\n"
  "buffer_load_dwordx2 v[236:237], v235, s[52:55], s69 offen sc1\n"
  "v_mfma_f32_16x16x32_bf16 v[4:7], a[148:151], a[52:55], v[4:7]\n"
  "buffer_load_dwordx4 v219, s[36:39], s45 offen offset:1024 lds\n"
  "v_mfma_f32_16x16x32_bf16 v[4:7], a[144:147], a[36:39], v[4:7]\n"
  "s_xor_b32 s44, s44, 128\n"
  "s_add_u32 s44, s44, 256\n"
  "s_and_b32 s44, s44, 0x7ff\n"
  "v_mfma_f32_16x16x32_bf16 v[8:11], a[144:147], a[40:43], v[8:11]\n"
  "ds_read_b64_tr_b16 a[132:133], v226 offset:38912\n"
  "s_mul_i32 s45, s44, 3072\n"
  "v_mfma_f32_16x16x32_bf16 v[8:11], a[148:151], a[56:59], v[8:11]\n"
  "ds_read_b64_tr_b16 a[134:135], v226 offset:39424\n"
  "s_waitcnt lgkmcnt(4)\n"
  "v_mfma_f32_16x16x32_bf16 v[12:15], a[148:151], a[60:63], v[12:15]\n"
  "v_mfma_f32_16x16x32_bf16 v[12:15], a[144:147], a[44:47], v[12:15]\n"
  "v_mfma_f32_16x16x32_bf16 v[28:31], a[152:155], a[44:47], v[28:31]\n"
  "s_waitcnt vmcnt(11)\n"
  "s_barrier\n"
  "s_add_u32 m0, s47, 16384\n"
  "v_mfma_f32_16x16x32_bf16 v[28:31], a[156:159], a[60:63], v[28:31]\n"
  "ds_read_b64_tr_b16 a[136:137], v227 offset:26624\n"
  "v_mfma_f32_16x16x32_bf16 v[24:27], a[156:159], a[56:59], v[24:27]\n"
  "ds_read_b64_tr_b16 a[138:139], v227 offset:27136\n"
  "v_mfma_f32_16x16x32_bf16 v[24:27], a[152:155], a[40:43], v[24:27]\n"
  "ds_read_b128 a[0:3], v228 offset:32768\n"
  "v_mfma_f32_16x16x32_bf16 v[20:23], a[152:155], a[36:39], v[20:23]\n"
  "ds_read_b64_tr_b16 a[140:141], v227 offset:38912\n"
  "v_mfma_f32_16x16x32_bf16 v[20:23], a[156:159], a[52:55], v[20:23]\n"
  "ds_read_b64_tr_b16 a[142:143], v227 offset:39424\n"
  "v_mfma_f32_16x16x32_bf16 v[16:19], a[156:159], a[48:51], v[16:19]\n"
  "ds_read_b128 a[16:19], v229 offset:32768\n"
  "s_waitcnt lgkmcnt(6)\n"
  "v_mfma_f32_16x16x32_bf16 v[16:19], a[152:155], a[32:35], v[16:19]\n"
  "buffer_load_dwordx4 v220, s[32:35], s44 offen offset:0 lds\n"
  "v_mfma_f32_16x16x32_bf16 v[32:35], a[32:35], a[128:131], v[32:35]\n"
  "ds_read_b64_tr_b16 a[144:145], v226 offset:28672\n"
  "v_mfma_f32_16x16x32_bf16 v[32:35], a[48:51], a[132:135], v[32:35]\n"
  "ds_read_b64_tr_b16 a[146:147], v226 offset:29184\n"
  "v_mfma_f32_16x16x32_bf16 v[36:39], a[52:55], a[132:135], v[36:39]\n"
  "ds_read_b128 a[4:7], v228 offset:34816\n"
  "v_mfma_f32_16x16x32_bf16 v[36:39], a[36:39], a[128:131], v[36:39]\n"
  "buffer_load_dwordx4 v221, s[32:35], s44 offen offset:1024 lds\n"
  "v_mfma_f32_16x16x32_bf16 v[40:43], a[40:43], a[128:131], v[40:43]\n"
  "ds_read_b64_tr_b16 a[148:149], v226 offset:40960\n"
  "v_mfma_f32_16x16x32_bf16 v[40:43], a[56:59], a[132:135], v[40:43]\n"
  "ds_read_b64_tr_b16 a[150:151], v226 offset:41472\n"
  "v_mfma_f32_16x16x32_bf16 v[44:47], a[60:63], a[132:135], v[44:47]\n"
  "ds_read_b128 a[20:23], v229 offset:34816\n"
  "s_waitcnt lgkmcnt(7)\n"
  "v_mfma_f32_16x16x32_bf16 v[44:47], a[44:47], a[128:131], v[44:47]\n"
  "buffer_load_dwordx4 v222, s[32:35], s44 offen offset:2048 lds\n"
  "v_mfma_f32_16x16x32_bf16 v[60:63], a[44:47], a[136:139], v[60:63]\n"
  "ds_read_b64_tr_b16 a[152:153], v227 offset:28672\n"
  "v_mfma_f32_16x16x32_bf16 v[60:63], a[60:63], a[140:143], v[60:63]\n"
  "ds_read_b64_tr_b16 a[154:155], v227 offset:29184\n"
  "v_mfma_f32_16x16x32_bf16 v[56:59], a[56:59], a[140:143], v[56:59]\n"
  "ds_read_b128 a[8:11], v228 offset:36864\n"
  "v_mfma_f32_16x16x32_bf16 v[56:59], a[40:43], a[136:139], v[56:59]\n"
  "buffer_load_dwordx4 v223, s[32:35], s44 offen offset:3072 lds\n"
  "v_mfma_f32_16x16x32_bf16 v[52:55], a[36:39], a[136:139], v[52:55]\n"
  "ds_read_b64_tr_b16 a[156:157], v227 offset:40960\n"
  "s_add_u32 m0, s46, 49152\n"
  "v_mfma_f32_16x16x32_bf16 v[52:55], a[52:55], a[140:143], v[52:55]\n"
  "ds_read_b64_tr_b16 a[158:159], v227 offset:41472\n"
  "v_mfma_f32_16x16x32_bf16 v[48:51], a[48:51], a[140:143], v[48:51]\n"
  "ds_read_b128 a[24:27], v229 offset:36864\n"
  "s_waitcnt lgkmcnt(7)\n"
  "v_mfma_f32_16x16x32_bf16 v[48:51], a[32:35], a[136:139], v[48:51]\n"
  "buffer_load_dwordx4 v216, s[36:39], s45 offen offset:0 lds\n"
  "v_mfma_f32_16x16x32_bf16 v[64:67], a[144:147], a[32:35], v[64:67]\n"
  "ds_read_b64_tr_b16 a[128:129], v224 offset:0\n"
  "v_mfma_f32_16x16x32_bf16 v[64:67], a[148:151], a[48:51], v[64:67]\n"
  "ds_read_b64_tr_b16 a[130:131], v224 offset:512\n"
  "v_mfma_f32_16x16x32_bf16 v[68:71], a[148:151], a[52:55], v[68:71]\n"
  "ds_read_b128 a[12:15], v228 offset:38912\n"
  "v_mfma_f32_16x16x32_bf16 v[68:71], a[144:147], a[36:39], v[68:71]\n"
  "buffer_load_dwordx4 v217, s[36:39], s45 offen offset:1024 lds\n"
  "v_mfma_f32_16x16x32_bf16 v[72:75], a[144:147], a[40:43], v[72:75]\n"
  "ds_read_b64_tr_b16 a[132:133], v224 offset:12288\n"
  "v_mfma_f32_16x16x32_bf16 v[72:75], a[148:151], a[56:59], v[72:75]\n"
  "ds_read_b64_tr_b16 a[134:135], v224 offset:12800\n"
  "v_mfma_f32_16x16x32_bf16 v[76:79], a[148:151], a[60:63], v[76:79]\n"
  "ds_read_b128 a[28:31], v229 offset:38912\n"
  "s_waitcnt lgkmcnt(7)\n"
  "v_mfma_f32_16x16x32_bf16 v[76:79], a[144:147], a[44:47], v[76:79]\n"
  "buffer_load_dwordx4 v216, s[36:39], s45 offen offset:2048 lds\n"
  "v_mfma_f32_16x16x32_bf16 v[92:95], a[152:155], a[44:47], v[92:95]\n"
  "ds_read_b64_tr_b16 a[136:137], v225 offset:0\n"
  "v_mfma_f32_16x16x32_bf16 v[92:95], a[156:159], a[60:63], v[92:95]\n"
  "ds_read_b64_tr_b16 a[138:139], v225 offset:512\n"
  "v_mfma_f32_16x16x32_bf16 v[88:91], a[156:159], a[56:59], v[88:91]\n"
  "buffer_load_dwordx4 v217, s[36:39], s45 offen offset:3072 lds\n"
  "v_mfma_f32_16x16x32_bf16 v[88:91], a[152:155], a[40:43], v[88:91]\n"
  "s_add_u32 m0, s46, 53248\n"
  "v_mfma_f32_16x16x32_bf16 v[84:87], a[152:155], a[36:39], v[84:87]\n"
  "ds_read_b64_tr_b16 a[140:141], v225 offset:12288\n"
  "v_mfma_f32_16x16x32_bf16 v[84:87], a[156:159], a[52:55], v[84:87]\n"
  "ds_read_b64_tr_b16 a[142:143], v225 offset:12800\n"
  "s_waitcnt lgkmcnt(4)\n"
  "v_mfma_f32_16x16x32_bf16 v[80:83], a[156:159], a[48:51], v[80:83]\n"
  "buffer_load_dwordx4 v218, s[36:39], s45 offen offset:0 lds\n"
  "v_mfma_f32_16x16x32_bf16 v[80:83], a[152:155], a[32:35], v[80:83]\n"
  "v_mfma_f32_16x16x32_bf16 v[0:3], a[128:131], a[0:3], v[0:3]\n"
  "ds_read_b64_tr_b16 a[144:145], v224 offset:2048\n"
  "v_mfma_f32_16x16x32_bf16 v[0:3], a[132:135], a[16:19], v[0:3]\n"
  "ds_read_b64_tr_b16 a[146:147], v224 offset:2560\n"
  "s_waitcnt vmcnt(10)\n"
  "L_poll1_%=:\n"
  "v_cmp_ne_u64 vcc, s[10:11], v[236:237]\n"
  "s_cbranch_vccz L_ok1_%=\n"
  "s_sub_u32 s99, s99, 1\n"
  "s_cbranch_scc1 L_out1_%=\n"
  "buffer_load_dwordx2 v[236:237], v235, s[52:55], s69 offen sc1\n"
  "s_waitcnt vmcnt(0)\n"
  "s_branch L_poll1_%=\n"
  "L_out1_%=:\n"
  "s_mov_b32 s99, 0\n"
  "L_ok1_%=:\n"
  "v_mfma_f32_16x16x32_bf16 v[4:7], a[132:135], a[20:23], v[4:7]\n"
  "buffer_load_dwordx4 v219, s[36:39], s45 offen offset:1024 lds\n"
  "v_mfma_f32_16x16x32_bf16 v[4:7], a[128:131], a[4:7], v[4:7]\n"
  "s_xor_b32 s44, s44, 128\n"
  "s_mul_i32 s45, s44, 3072\n"
  "v_mfma_f32_16x16x32_bf16 v[8:11], a[128:131], a[8:11], v[8:11]\n"
  "ds_read_b64_tr_b16 a[148:149], v224 offset:14336\n"
  "v_mfma_f32_16x16x32_bf16 v[8:11], a[132:135], a[24:27], v[8:11]\n"
  "ds_read_b64_tr_b16 a[150:151], v224 offset:14848\n"
  "s_waitcnt lgkmcnt(4)\n"
  "v_mfma_f32_16x16x32_bf16 v[12:15], a[132:135], a[28:31], v[12:15]\n"
  "v_mfma_f32_16x16x32_bf16 v[12:15], a[128:131], a[12:15], v[12:15]\n"
  "v_mfma_f32_16x16x32_bf16 v[28:31], a[136:139], a[12:15], v[28:31]\n"
  "s_waitcnt vmcnt(10)\n"
  "s_barrier\n"
  "s_add_u32 m0, s47, 32768\n"
  "v_mfma_f32_16x16x32_bf16 v[28:31], a[140:143], a[28:31], v[28:31]\n"
  "ds_read_b64_tr_b16 a[152:153], v225 offset:2048\n"
  "buffer_load_dwordx4 v[96:99], v254, s[52:55], s58 offen offset:0 sc1\n"
  "v_mfma_f32_16x16x32_bf16 v[24:27], a[140:143], a[24:27], v[24:27]\n"
  "ds_read_b64_tr_b16 a[154:155], v225 offset:2560\n"
  "v_mfma_f32_16x16x32_bf16 v[24:27], a[136:139], a[8:11], v[24:27]\n"
  "ds_read_b128 a[32:35], v228 offset:0\n"
  "v_mfma_f32_16x16x32_bf16 v[20:23], a[136:139], a[4:7], v[20:23]\n"
  "ds_read_b64_tr_b16 a[156:157], v225 offset:14336\n"
  "v_mfma_f32_16x16x32_bf16 v[20:23], a[140:143], a[20:23], v[20:23]\n"
  "ds_read_b64_tr_b16 a[158:159], v225 offset:14848\n"
  "buffer_load_dwordx4 v[100:103], v254, s[52:55], s58 offen offset:1024 sc1\n"
  "v_mfma_f32_16x16x32_bf16 v[16:19], a[140:143], a[16:19], v[16:19]\n"
  "ds_read_b128 a[48:51], v229 offset:0\n"
  "s_waitcnt lgkmcnt(6)\n"
  "v_mfma_f32_16x16x32_bf16 v[16:19], a[136:139], a[0:3], v[16:19]\n"
  "buffer_load_dwordx4 v220, s[32:35], s44 offen offset:0 lds\n"
  "v_mfma_f32_16x16x32_bf16 v[32:35], a[0:3], a[144:147], v[32:35]\n"
  "ds_read_b64_tr_b16 a[128:129], v224 offset:4096\n"
  "v_mfma_f32_16x16x32_bf16 v[32:35], a[16:19], a[148:151], v[32:35]\n"
  "ds_read_b64_tr_b16 a[130:131], v224 offset:4608\n"
  "buffer_load_dwordx4 v[104:107], v254, s[52:55], s58 offen offset:2048 sc1\n"
  "v_mfma_f32_16x16x32_bf16 v[36:39], a[20:23], a[148:151], v[36:39]\n"
  "ds_read_b128 a[36:39], v228 offset:2048\n"
  "v_mfma_f32_16x16x32_bf16 v[36:39], a[4:7], a[144:147], v[36:39]\n"
  "buffer_load_dwordx4 v221, s[32:35], s44 offen offset:1024 lds\n"
  "v_mfma_f32_16x16x32_bf16 v[40:43], a[8:11], a[144:147], v[40:43]\n"
  "ds_read_b64_tr_b16 a[132:133], v224 offset:16384\n"
  "v_mfma_f32_16x16x32_bf16 v[40:43], a[24:27], a[148:151], v[40:43]\n"
  "ds_read_b64_tr_b16 a[134:135], v224 offset:16896\n"
  "buffer_load_dwordx4 v[108:111], v254, s[52:55], s58 offen offset:3072 sc1\n"
  "v_mfma_f32_16x16x32_bf16 v[44:47], a[28:31], a[148:151], v[44:47]\n"
  "ds_read_b128 a[52:55], v229 offset:2048\n"
  "s_waitcnt lgkmcnt(7)\n"
  "v_mfma_f32_16x16x32_bf16 v[44:47], a[12:15], a[144:147], v[44:47]\n"
  "buffer_load_dwordx4 v222, s[32:35], s44 offen offset:2048 lds\n"
  "v_mfma_f32_16x16x32_bf16 v[60:63], a[12:15], a[152:155], v[60:63]\n"
  "ds_read_b64_tr_b16 a[136:137], v225 offset:4096\n"
  "v_mfma_f32_16x16x32_bf16 v[60:63], a[28:31], a[156:159], v[60:63]\n"
  "ds_read_b64_tr_b16 a[138:139], v225 offset:4608\n"
  "buffer_load_dwordx3 v[112:114], v254, s[52:55], s66 offen offset:0 sc1\n"
  "v_mfma_f32_16x16x32_bf16 v[56:59], a[24:27], a[156:159], v[56:59]\n"
  "ds_read_b128 a[40:43], v228 offset:4096\n"
  "v_mfma_f32_16x16x32_bf16 v[56:59], a[8:11], a[152:155], v[56:59]\n"
  "buffer_load_dwordx4 v223, s[32:35], s44 offen offset:3072 lds\n"
  "v_mfma_f32_16x16x32_bf16 v[52:55], a[4:7], a[152:155], v[52:55]\n"
  "ds_read_b64_tr_b16 a[140:141], v225 offset:16384\n"
  "s_add_u32 m0, s46, 73728\n"
  "v_mfma_f32_16x16x32_bf16 v[52:55], a[20:23], a[156:159], v[52:55]\n"
  "ds_read_b64_tr_b16 a[142:143], v225 offset:16896\n"
  "buffer_load_dwordx3 v[116:118], v254, s[52:55], s66 offen offset:1024 sc1\n"
  "v_mfma_f32_16x16x32_bf16 v[48:51], a[16:19], a[156:159], v[48:51]\n"
  "ds_read_b128 a[56:59], v229 offset:4096\n"
  "s_waitcnt lgkmcnt(7)\n"
  "v_mfma_f32_16x16x32_bf16 v[48:51], a[0:3], a[152:155], v[48:51]\n"
  "buffer_load_dwordx4 v216, s[36:39], s45 offen offset:0 lds\n"
  "v_mfma_f32_16x16x32_bf16 v[64:67], a[128:131], a[0:3], v[64:67]\n"
  "ds_read_b64_tr_b16 a[144:145], v224 offset:24576\n"
  "v_mfma_f32_16x16x32_bf16 v[64:67], a[132:135], a[16:19], v[64:67]\n"
  "ds_read_b64_tr_b16 a[146:147], v224 offset:25088\n"
  "buffer_load_dwordx3 v[120:122], v254, s[52:55], s66 offen offset:2048 sc1\n"
  "v_mfma_f32_16x16x32_bf16 v[68:71], a[132:135], a[20:23], v[68:71]\n"
  "ds_read_b128 a[44:47], v228 offset:6144\n"
  "v_mfma_f32_16x16x32_bf16 v[68:71], a[128:131], a[4:7], v[68:71]\n"
  "buffer_load_dwordx4 v217, s[36:39], s45 offen offset:1024 lds\n"
  "v_mfma_f32_16x16x32_bf16 v[72:75], a[128:131], a[8:11], v[72:75]\n"
  "ds_read_b64_tr_b16 a[148:149], v224 offset:36864\n"
  "v_mfma_f32_16x16x32_bf16 v[72:75], a[132:135], a[24:27], v[72:75]\n"
  "ds_read_b64_tr_b16 a[150:151], v224 offset:37376\n"
  "buffer_load_dwordx3 v[124:126], v254, s[52:55], s66 offen offset:3072 sc1\n"
  "v_mfma_f32_16x16x32_bf16 v[76:79], a[132:135], a[28:31], v[76:79]\n"
  "ds_read_b128 a[60:63], v229 offset:6144\n"
  "s_waitcnt lgkmcnt(7)\n"
  "v_mfma_f32_16x16x32_bf16 v[76:79], a[128:131], a[12:15], v[76:79]\n"
  "buffer_load_dwordx4 v216, s[36:39], s45 offen offset:2048 lds\n"
  "v_mfma_f32_16x16x32_bf16 v[92:95], a[136:139], a[12:15], v[92:95]\n"
  "ds_read_b64_tr_b16 a[152:153], v225 offset:24576\n"
  "v_mfma_f32_16x16x32_bf16 v[92:95], a[140:143], a[28:31], v[92:95]\n"
  "ds_read_b64_tr_b16 a[154:155], v225 offset:25088\n"
  "v_mfma_f32_16x16x32_bf16 v[88:91], a[140:143], a[24:27], v[88:91]\n"
  "buffer_load_dwordx4 v217, s[36:39], s45 offen offset:3072 lds\n"
  "v_mfma_f32_16x16x32_bf16 v[88:91], a[136:139], a[8:11], v[88:91]\n"
  "s_add_u32 m0, s46, 77824\n"
  "v_mfma_f32_16x16x32_bf16 v[84:87], a[136:139], a[4:7], v[84:87]\n"
  "ds_read_b64_tr_b16 a[156:157], v225 offset:36864\n"
  "v_mfma_f32_16x16x32_bf16 v[84:87], a[140:143], a[20:23], v[84:87]\n"
  "ds_read_b64_tr_b16 a[158:159], v225 offset:37376\n"
  "s_waitcnt lgkmcnt(4)\n"
  "v_mfma_f32_16x16x32_bf16 v[80:83], a[140:143], a[16:19], v[80:83]\n"
  "buffer_load_dwordx4 v218, s[36:39], s45 offen offset:0 lds\n"
  "v_mfma_f32_16x16x32_bf16 v[80:83], a[136:139], a[0:3], v[80:83]\n"
  "v_mfma_f32_16x16x32_bf16 v[0:3], a[144:147], a[32:35], v[0:3]\n"
  "ds_read_b64_tr_b16 a[128:129], v224 offset:26624\n"
  "v_mfma_f32_16x16x32_bf16 v[0:3], a[148:151], a[48:51], v[0:3]\n"
  "ds_read_b64_tr_b16 a[130:131], v224 offset:27136\n"
  "v_mfma_f32_16x16x32_bf16 v[4:7], a[148:151], a[52:55], v[4:7]\n"
  "buffer_load_dwordx4 v219, s[36:39], s45 offen offset:1024 lds\n"
  "v_mfma_f32_16x16x32_bf16 v[4:7], a[144:147], a[36:39], v[4:7]\n"
  "s_xor_b32 s44, s44, 128\n"
  "s_add_u32 s44, s44, 256\n"
  "s_and_b32 s44, s44, 0x7ff\n"
  "v_mfma_f32_16x16x32_bf16 v[8:11], a[144:147], a[40:43], v[8:11]\n"
  "ds_read_b64_tr_b16 a[132:133], v224 offset:38912\n"
  "s_mul_i32 s45, s44, 3072\n"
  "v_mfma_f32_16x16x32_bf16 v[8:11], a[148:151], a[56:59], v[8:11]\n"
  "ds_read_b64_tr_b16 a[134:135], v224 offset:39424\n"
  "s_waitcnt lgkmcnt(4)\n"
  "v_mfma_f32_16x16x32_bf16 v[12:15], a[148:151], a[60:63], v[12:15]\n"
  "v_mfma_f32_16x16x32_bf16 v[12:15], a[144:147], a[44:47], v[12:15]\n"
  "v_mfma_f32_16x16x32_bf16 v[28:31], a[152:155], a[44:47], v[28:31]\n"
  "s_waitcnt vmcnt(18)\n"
  "s_barrier\n"
  "s_add_u32 m0, s47, 0\n"
  "v_mfma_f32_16x16x32_bf16 v[28:31], a[156:159], a[60:63], v[28:31]\n"
  "ds_read_b64_tr_b16 a[136:137], v225 offset:26624\n"
  "v_mfma_f32_16x16x32_bf16 v[24:27], a[156:159], a[56:59], v[24:27]\n"
  "ds_read_b64_tr_b16 a[138:139], v225 offset:27136\n"
  "v_mfma_f32_16x16x32_bf16 v[24:27], a[152:155], a[40:43], v[24:27]\n"
  "ds_read_b128 a[0:3], v228 offset:16384\n"
  "v_mfma_f32_16x16x32_bf16 v[20:23], a[152:155], a[36:39], v[20:23]\n"
  "ds_read_b64_tr_b16 a[140:141], v225 offset:38912\n"
  "v_mfma_f32_16x16x32_bf16 v[20:23], a[156:159], a[52:55], v[20:23]\n"
  "ds_read_b64_tr_b16 a[142:143], v225 offset:39424\n"
  "v_mfma_f32_16x16x32_bf16 v[16:19], a[156:159], a[48:51], v[16:19]\n"
  "ds_read_b128 a[16:19], v229 offset:16384\n"
  "s_waitcnt lgkmcnt(6)\n"
  "v_mfma_f32_16x16x32_bf16 v[16:19], a[152:155], a[32:35], v[16:19]\n"
  "buffer_load_dwordx4 v220, s[32:35], s44 offen offset:0 lds\n"
  "v_mfma_f32_16x16x32_bf16 v[32:35], a[32:35], a[128:131], v[32:35]\n"
  "ds_read_b64_tr_b16 a[144:145], v224 offset:28672\n"
  "v_mfma_f32_16x16x32_bf16 v[32:35], a[48:51], a[132:135], v[32:35]\n"
  "ds_read_b64_tr_b16 a[146:147], v224 offset:29184\n"
  "v_mfma_f32_16x16x32_bf16 v[36:39], a[52:55], a[132:135], v[36:39]\n"
  "ds_read_b128 a[4:7], v228 offset:18432\n"
  "v_mfma_f32_16x16x32_bf16 v[36:39], a[36:39], a[128:131], v[36:39]\n"
  "buffer_load_dwordx4 v221, s[32:35], s44 offen offset:1024 lds\n"
  "v_mfma_f32_16x16x32_bf16 v[40:43], a[40:43], a[128:131], v[40:43]\n"
  "ds_read_b64_tr_b16 a[148:149], v224 offset:40960\n"
  "v_mfma_f32_16x16x32_bf16 v[40:43], a[56:59], a[132:135], v[40:43]\n"
  "ds_read_b64_tr_b16 a[150:151], v224 offset:41472\n"
  "v_mfma_f32_16x16x32_bf16 v[44:47], a[60:63], a[132:135], v[44:47]\n"
  "ds_read_b128 a[20:23], v229 offset:18432\n"
  "s_waitcnt lgkmcnt(7)\n"
  "v_mfma_f32_16x16x32_bf16 v[44:47], a[44:47], a[128:131], v[44:47]\n"
  "buffer_load_dwordx4 v222, s[32:35], s44 offen offset:2048 lds\n"
  "v_mfma_f32_16x16x32_bf16 v[60:63], a[44:47], a[136:139], v[60:63]\n"
  "ds_read_b64_tr_b16 a[152:153], v225 offset:28672\n"
  "s_waitcnt vmcnt(15)\n"
  "v_mfma_f32_16x16x32_bf16 v[60:63], a[60:63], a[140:143], v[60:63]\n"
  "ds_read_b64_tr_b16 a[154:155], v225 offset:29184\n"
  "v_add_f32 v96, v96, v100\n"
  "v_mfma_f32_16x16x32_bf16 v[56:59], a[56:59], a[140:143], v[56:59]\n"
  "ds_read_b128 a[8:11], v228 offset:20480\n"
  "v_add_f32 v104, v104, v108\n"
  "v_mfma_f32_16x16x32_bf16 v[56:59], a[40:43], a[136:139], v[56:59]\n"
  "buffer_load_dwordx4 v223, s[32:35], s44 offen offset:3072 lds\n"
  "v_mfma_f32_16x16x32_bf16 v[52:55], a[36:39], a[136:139], v[52:55]\n"
  "ds_read_b64_tr_b16 a[156:157], v225 offset:40960\n"
  "s_add_u32 m0, s46, 0\n"
  "v_mfma_f32_16x16x32_bf16 v[52:55], a[52:55], a[140:143], v[52:55]\n"
  "ds_read_b64_tr_b16 a[158:159], v225 offset:41472\n"
  "v_add_f32 v97, v97, v101\n"
  "v_mfma_f32_16x16x32_bf16 v[48:51], a[48:51], a[140:143], v[48:51]\n"
  "ds_read_b128 a[24:27], v229 offset:20480\n"
  "s_waitcnt lgkmcnt(7)\n"
  "v_mfma_f32_16x16x32_bf16 v[48:51], a[32:35], a[136:139], v[48:51]\n"
  "buffer_load_dwordx4 v216, s[36:39], s45 offen offset:0 lds\n"
  "v_mfma_f32_16x16x32_bf16 v[64:67], a[144:147], a[32:35], v[64:67]\n"
  "ds_read_b64_tr_b16 a[128:129], v226 offset:0\n"
  "v_add_f32 v105, v105, v109\n"
  "v_mfma_f32_16x16x32_bf16 v[64:67], a[148:151], a[48:51], v[64:67]\n"
  "ds_read_b64_tr_b16 a[130:131], v226 offset:512\n"
  "v_add_f32 v98, v98, v102\n"
  "v_mfma_f32_16x16x32_bf16 v[68:71], a[148:151], a[52:55], v[68:71]\n"
  "ds_read_b128 a[12:15], v228 offset:22528\n"
  "v_add_f32 v106, v106, v110\n"
  "v_mfma_f32_16x16x32_bf16 v[68:71], a[144:147], a[36:39], v[68:71]\n"
  "buffer_load_dwordx4 v217, s[36:39], s45 offen offset:1024 lds\n"
  "v_mfma_f32_16x16x32_bf16 v[72:75], a[144:147], a[40:43], v[72:75]\n"
  "ds_read_b64_tr_b16 a[132:133], v226 offset:12288\n"
  "v_add_f32 v99, v99, v103\n"
  "v_mfma_f32_16x16x32_bf16 v[72:75], a[148:151], a[56:59], v[72:75]\n"
  "ds_read_b64_tr_b16 a[134:135], v226 offset:12800\n"
  "v_add_f32 v107, v107, v111\n"
  "v_mfma_f32_16x16x32_bf16 v[76:79], a[148:151], a[60:63], v[76:79]\n"
  "ds_read_b128 a[28:31], v229 offset:22528\n"
  "s_waitcnt lgkmcnt(7)\n"
  "v_mfma_f32_16x16x32_bf16 v[76:79], a[144:147], a[44:47], v[76:79]\n"
  "buffer_load_dwordx4 v216, s[36:39], s45 offen offset:2048 lds\n"
  "v_mfma_f32_16x16x32_bf16 v[92:95], a[152:155], a[44:47], v[92:95]\n"
  "ds_read_b64_tr_b16 a[136:137], v227 offset:0\n"
  "v_add_f32 v96, v96, v104\n"
  "v_mfma_f32_16x16x32_bf16 v[92:95], a[156:159], a[60:63], v[92:95]\n"
  "ds_read_b64_tr_b16 a[138:139], v227 offset:512\n"
  "v_add_f32 v97, v97, v105\n"
  "v_mfma_f32_16x16x32_bf16 v[88:91], a[156:159], a[56:59], v[88:91]\n"
  "buffer_load_dwordx4 v217, s[36:39], s45 offen offset:3072 lds\n"
  "v_mfma_f32_16x16x32_bf16 v[88:91], a[152:155], a[40:43], v[88:91]\n"
  "s_add_u32 m0, s46, 4096\n"
  "v_add_f32 v98, v98, v106\n"
  "v_add_f32 v99, v99, v107\n"
  "v_mfma_f32_16x16x32_bf16 v[84:87], a[152:155], a[36:39], v[84:87]\n"
  "ds_read_b64_tr_b16 a[140:141], v227 offset:12288\n"
  "v_permlane32_swap_b32 v96, v98\n"
  "v_mfma_f32_16x16x32_bf16 v[84:87], a[156:159], a[52:55], v[84:87]\n"
  "ds_read_b64_tr_b16 a[142:143], v227 offset:12800\n"
  "s_waitcnt lgkmcnt(4)\n"
  "v_mfma_f32_16x16x32_bf16 v[80:83], a[156:159], a[48:51], v[80:83]\n"
  "buffer_load_dwordx4 v218, s[36:39], s45 offen offset:0 lds\n"
  "v_mfma_f32_16x16x32_bf16 v[80:83], a[152:155], a[32:35], v[80:83]\n"
  "v_permlane32_swap_b32 v97, v99\n"
  "v_mfma_f32_16x16x32_bf16 v[0:3], a[128:131], a[0:3], v[0:3]\n"
  "ds_read_b64_tr_b16 a[144:145], v226 offset:2048\n"
  "v_add_f32 v96, v96, v98\n"
  "v_mfma_f32_16x16x32_bf16 v[0:3], a[132:135], a[16:19], v[0:3]\n"
  "ds_read_b64_tr_b16 a[146:147], v226 offset:2560\n"
  "v_add_f32 v97, v97, v99\n"
  "v_mfma_f32_16x16x32_bf16 v[4:7], a[132:135], a[20:23], v[4:7]\n"
  "buffer_load_dwordx4 v219, s[36:39], s45 offen offset:1024 lds\n"
  "v_mfma_f32_16x16x32_bf16 v[4:7], a[128:131], a[4:7], v[4:7]\n"
  "s_xor_b32 s44, s44, 128\n"
  "s_mul_i32 s45, s44, 3072\n"
  "v_mov_b32 v100, v96\n"
  "v_mfma_f32_16x16x32_bf16 v[8:11], a[128:131], a[8:11], v[8:11]\n"
  "ds_read_b64_tr_b16 a[148:149], v226 offset:14336\n"
  "v_mov_b32 v101, v97\n"
  "v_mfma_f32_16x16x32_bf16 v[8:11], a[132:135], a[24:27], v[8:11]\n"
  "ds_read_b64_tr_b16 a[150:151], v226 offset:14848\n"
  "s_waitcnt lgkmcnt(4)\n"
  "v_mfma_f32_16x16x32_bf16 v[12:15], a[132:135], a[28:31], v[12:15]\n"
  "v_permlane16_swap_b32 v100, v96\n"
  "v_mfma_f32_16x16x32_bf16 v[12:15], a[128:131], a[12:15], v[12:15]\n"
  "v_permlane16_swap_b32 v101, v97\n"
  "v_mfma_f32_16x16x32_bf16 v[28:31], a[136:139], a[12:15], v[28:31]\n"
  "s_waitcnt vmcnt(10)\n"
  "s_barrier\n"
  "s_add_u32 m0, s47, 16384\n"
  "v_mfma_f32_16x16x32_bf16 v[28:31], a[140:143], a[28:31], v[28:31]\n"
  "ds_read_b64_tr_b16 a[152:153], v227 offset:2048\n"
  "v_add_f32 v96, v100, v96\n"
  "v_mfma_f32_16x16x32_bf16 v[24:27], a[140:143], a[24:27], v[24:27]\n"
  "ds_read_b64_tr_b16 a[154:155], v227 offset:2560\n"
  "v_add_f32 v97, v101, v97\n"
  "v_mfma_f32_16x16x32_bf16 v[24:27], a[136:139], a[8:11], v[24:27]\n"
  "ds_read_b128 a[32:35], v228 offset:32768\n"
  "v_mul_f32 v96, 0x3a800000, v96\n"
  "v_mfma_f32_16x16x32_bf16 v[20:23], a[136:139], a[4:7], v[20:23]\n"
  "ds_read_b64_tr_b16 a[156:157], v227 offset:14336\n"
  "v_mul_f32 v97, 0x3a800000, v97\n"
  "v_mfma_f32_16x16x32_bf16 v[20:23], a[140:143], a[20:23], v[20:23]\n"
  "ds_read_b64_tr_b16 a[158:159], v227 offset:14848\n"
  "v_mfma_f32_16x16x32_bf16 v[16:19], a[140:143], a[16:19], v[16:19]\n"
  "ds_read_b128 a[48:51], v229 offset:32768\n"
  "s_waitcnt lgkmcnt(6)\n"
  "v_mfma_f32_16x16x32_bf16 v[16:19], a[136:139], a[0:3], v[16:19]\n"
  "buffer_load_dwordx4 v220, s[32:35], s44 offen offset:0 lds\n"
  "v_mfma_f32_16x16x32_bf16 v[32:35], a[0:3], a[144:147], v[32:35]\n"
  "ds_read_b64_tr_b16 a[128:129], v226 offset:4096\n"
  "v_fma_f32 v97, -v96, v96, v97\n"
  "v_mfma_f32_16x16x32_bf16 v[32:35], a[16:19], a[148:151], v[32:35]\n"
  "ds_read_b64_tr_b16 a[130:131], v226 offset:4608\n"
  "v_max_f32 v97, 0, v97\n"
  "v_mfma_f32_16x16x32_bf16 v[36:39], a[20:23], a[148:151], v[36:39]\n"
  "ds_read_b128 a[36:39], v228 offset:34816\n"
  "v_add_f32 v97, 0x3727c5ac, v97\n"
  "v_mfma_f32_16x16x32_bf16 v[36:39], a[4:7], a[144:147], v[36:39]\n"
  "buffer_load_dwordx4 v221, s[32:35], s44 offen offset:1024 lds\n"
  "v_mfma_f32_16x16x32_bf16 v[40:43], a[8:11], a[144:147], v[40:43]\n"
  "ds_read_b64_tr_b16 a[132:133], v226 offset:16384\n"
  "v_add_f32 v112, v112, v116\n"
  "v_mfma_f32_16x16x32_bf16 v[40:43], a[24:27], a[148:151], v[40:43]\n"
  "ds_read_b64_tr_b16 a[134:135], v226 offset:16896\n"
  "v_add_f32 v120, v120, v124\n"
  "v_mfma_f32_16x16x32_bf16 v[44:47], a[28:31], a[148:151], v[44:47]\n"
  "ds_read_b128 a[52:55], v229 offset:34816\n"
  "s_waitcnt lgkmcnt(7)\n"
  "v_mfma_f32_16x16x32_bf16 v[44:47], a[12:15], a[144:147], v[44:47]\n"
  "buffer_load_dwordx4 v222, s[32:35], s44 offen offset:2048 lds\n"
  "v_mfma_f32_16x16x32_bf16 v[60:63], a[12:15], a[152:155], v[60:63]\n"
  "ds_read_b64_tr_b16 a[136:137], v227 offset:4096\n"
  "v_add_f32 v113, v113, v117\n"
  "v_mfma_f32_16x16x32_bf16 v[60:63], a[28:31], a[156:159], v[60:63]\n"
  "ds_read_b64_tr_b16 a[138:139], v227 offset:4608\n"
  "v_add_f32 v121, v121, v125\n"
  "v_mfma_f32_16x16x32_bf16 v[56:59], a[24:27], a[156:159], v[56:59]\n"
  "ds_read_b128 a[40:43], v228 offset:36864\n"
  "v_add_f32 v114, v114, v118\n"
  "v_mfma_f32_16x16x32_bf16 v[56:59], a[8:11], a[152:155], v[56:59]\n"
  "buffer_load_dwordx4 v223, s[32:35], s44 offen offset:3072 lds\n"
  "v_mfma_f32_16x16x32_bf16 v[52:55], a[4:7], a[152:155], v[52:55]\n"
  "ds_read_b64_tr_b16 a[140:141], v227 offset:16384\n"
  "s_add_u32 m0, s46, 24576\n"
  "v_mfma_f32_16x16x32_bf16 v[52:55], a[20:23], a[156:159], v[52:55]\n"
  "ds_read_b64_tr_b16 a[142:143], v227 offset:16896\n"
  "v_add_f32 v122, v122, v126\n"
  "v_mfma_f32_16x16x32_bf16 v[48:51], a[16:19], a[156:159], v[48:51]\n"
  "ds_read_b128 a[56:59], v229 offset:36864\n"
  "s_waitcnt lgkmcnt(7)\n"
  "v_mfma_f32_16x16x32_bf16 v[48:51], a[0:3], a[152:155], v[48:51]\n"
  "buffer_load_dwordx4 v216, s[36:39], s45 offen offset:0 lds\n"
  "v_mfma_f32_16x16x32_bf16 v[64:67], a[128:131], a[0:3], v[64:67]\n"
  "ds_read_b64_tr_b16 a[144:145], v226 offset:24576\n"
  "v_add_f32 v112, v112, v120\n"
  "v_mfma_f32_16x16x32_bf16 v[64:67], a[132:135], a[16:19], v[64:67]\n"
  "ds_read_b64_tr_b16 a[146:147], v226 offset:25088\n"
  "v_cvt_pk_bf16_f32 v160, v112, v112\n"
  "v_mfma_f32_16x16x32_bf16 v[68:71], a[132:135], a[20:23], v[68:71]\n"
  "ds_read_b128 a[44:47], v228 offset:38912\n"
  "v_and_b32 v161, 0xffff0000, v160\n"
  "v_mfma_f32_16x16x32_bf16 v[68:71], a[128:131], a[4:7], v[68:71]\n"
  "buffer_load_dwordx4 v217, s[36:39], s45 offen offset:1024 lds\n"
  "v_mfma_f32_16x16x32_bf16 v[72:75], a[128:131], a[8:11], v[72:75]\n"
  "ds_read_b64_tr_b16 a[148:149], v226 offset:36864\n"
  "v_sub_f32 v112, v112, v161\n"
  "v_mfma_f32_16x16x32_bf16 v[72:75], a[132:135], a[24:27], v[72:75]\n"
  "ds_read_b64_tr_b16 a[150:151], v226 offset:37376\n"
  "v_cvt_pk_bf16_f32 v161, v112, 0\n"
  "v_mfma_f32_16x16x32_bf16 v[76:79], a[132:135], a[28:31], v[76:79]\n"
  "ds_read_b128 a[60:63], v229 offset:38912\n"
  "s_waitcnt lgkmcnt(7)\n"
  "v_mfma_f32_16x16x32_bf16 v[76:79], a[128:131], a[12:15], v[76:79]\n"
  "buffer_load_dwordx4 v216, s[36:39], s45 offen offset:2048 lds\n"
  "v_mfma_f32_16x16x32_bf16 v[92:95], a[136:139], a[12:15], v[92:95]\n"
  "ds_read_b64_tr_b16 a[152:153], v227 offset:24576\n"
  "v_add_f32 v113, v113, v121\n"
  "v_mfma_f32_16x16x32_bf16 v[92:95], a[140:143], a[28:31], v[92:95]\n"
  "ds_read_b64_tr_b16 a[154:155], v227 offset:25088\n"
  "v_cvt_pk_bf16_f32 v162, v113, v113\n"
  "v_mfma_f32_16x16x32_bf16 v[88:91], a[140:143], a[24:27], v[88:91]\n"
  "buffer_load_dwordx4 v217, s[36:39], s45 offen offset:3072 lds\n"
  "v_mfma_f32_16x16x32_bf16 v[88:91], a[136:139], a[8:11], v[88:91]\n"
  "s_add_u32 m0, s46, 28672\n"
  "v_and_b32 v163, 0xffff0000, v162\n"
  "v_sub_f32 v113, v113, v163\n"
  "v_mfma_f32_16x16x32_bf16 v[84:87], a[136:139], a[4:7], v[84:87]\n"
  "ds_read_b64_tr_b16 a[156:157], v227 offset:36864\n"
  "v_cvt_pk_bf16_f32 v163, v113, 0\n"
  "v_mfma_f32_16x16x32_bf16 v[84:87], a[140:143], a[20:23], v[84:87]\n"
  "ds_read_b64_tr_b16 a[158:159], v227 offset:37376\n"
  "s_waitcnt lgkmcnt(4)\n"
  "v_mfma_f32_16x16x32_bf16 v[80:83], a[140:143], a[16:19], v[80:83]\n"
  "buffer_load_dwordx4 v218, s[36:39], s45 offen offset:0 lds\n"
  "v_mfma_f32_16x16x32_bf16 v[80:83], a[136:139], a[0:3], v[80:83]\n"
  "v_add_f32 v114, v114, v122\n"
  "v_cvt_pk_bf16_f32 v164, v114, v114\n"
  "v_mfma_f32_16x16x32_bf16 v[0:3], a[144:147], a[32:35], v[0:3]\n"
  "ds_read_b64_tr_b16 a[128:129], v226 offset:26624\n"
  "v_and_b32 v165, 0xffff0000, v164\n"
  "v_mfma_f32_16x16x32_bf16 v[0:3], a[148:151], a[48:51], v[0:3]\n"
  "ds_read_b64_tr_b16 a[130:131], v226 offset:27136\n"
  "v_sub_f32 v114, v114, v165\n"
  "v_mfma_f32_16x16x32_bf16 v[4:7], a[148:151], a[52:55], v[4:7]\n"
  "buffer_load_dwordx4 v219, s[36:39], s45 offen offset:1024 lds\n"
  "v_mfma_f32_16x16x32_bf16 v[4:7], a[144:147], a[36:39], v[4:7]\n"
  "s_xor_b32 s44, s44, 128\n"
  "s_add_u32 s44, s44, 256\n"
  "s_and_b32 s44, s44, 0x7ff\n"
  "v_mfma_f32_16x16x32_bf16 v[8:11], a[144:147], a[40:43], v[8:11]\n"
  "ds_read_b64_tr_b16 a[132:133], v226 offset:38912\n"
  "s_mul_i32 s45, s44, 3072\n"
  "v_mfma_f32_16x16x32_bf16 v[8:11], a[148:151], a[56:59], v[8:11]\n"
  "ds_read_b64_tr_b16 a[134:135], v226 offset:39424\n"
  "s_waitcnt lgkmcnt(4)\n"
  "v_mfma_f32_16x16x32_bf16 v[12:15], a[148:151], a[60:63], v[12:15]\n"
  "v_cvt_pk_bf16_f32 v165, v114, 0\n"
  "v_mfma_f32_16x16x32_bf16 v[12:15], a[144:147], a[44:47], v[12:15]\n"
  "v_rsq_f32 v97, v97\n"
  "v_mfma_f32_16x16x32_bf16 v[28:31], a[152:155], a[44:47], v[28:31]\n"
  "s_waitcnt vmcnt(10)\n"
  "s_barrier\n"
  "s_add_u32 m0, s47, 32768\n"
  "v_mfma_f32_16x16x32_bf16 v[28:31], a[156:159], a[60:63], v[28:31]\n"
  "ds_read_b64_tr_b16 a[136:137], v227 offset:26624\n"
  "v_mfma_f32_16x16x32_bf16 v[24:27], a[156:159], a[56:59], v[24:27]\n"
  "ds_read_b64_tr_b16 a[138:139], v227 offset:27136\n"
  "v_mfma_f32_16x16x32_bf16 v[24:27], a[152:155], a[40:43], v[24:27]\n"
  "ds_read_b128 a[0:3], v228 offset:0\n"
  "v_mfma_f32_16x16x32_bf16 v[20:23], a[152:155], a[36:39], v[20:23]\n"
  "ds_read_b64_tr_b16 a[140:141], v227 offset:38912\n"
  "v_mfma_f32_16x16x32_bf16 v[20:23], a[156:159], a[52:55], v[20:23]\n"
  "ds_read_b64_tr_b16 a[142:143], v227 offset:39424\n"
  "v_mfma_f32_16x16x32_bf16 v[16:19], a[156:159], a[48:51], v[16:19]\n"
  "ds_read_b128 a[16:19], v229 offset:0\n"
  "s_waitcnt lgkmcnt(6)\n"
  "v_mfma_f32_16x16x32_bf16 v[16:19], a[152:155], a[32:35], v[16:19]\n"
  "buffer_load_dwordx4 v220, s[32:35], s44 offen offset:0 lds\n"
  "v_mfma_f32_16x16x32_bf16 v[32:35], a[32:35], a[128:131], v[32:35]\n"
  "ds_read_b64_tr_b16 a[144:145], v226 offset:28672\n"
  "v_mfma_f32_16x16x32_bf16 v[32:35], a[48:51], a[132:135], v[32:35]\n"
  "ds_read_b64_tr_b16 a[146:147], v226 offset:29184\n"
  "v_mfma_f32_16x16x32_bf16 v[36:39], a[52:55], a[132:135], v[36:39]\n"
  "ds_read_b128 a[4:7], v228 offset:2048\n"
  "v_mfma_f32_16x16x32_bf16 v[36:39], a[36:39], a[128:131], v[36:39]\n"
  "buffer_load_dwordx4 v221, s[32:35], s44 offen offset:1024 lds\n"
  "v_mfma_f32_16x16x32_bf16 v[40:43], a[40:43], a[128:131], v[40:43]\n"
  "ds_read_b64_tr_b16 a[148:149], v226 offset:40960\n"
  "v_mfma_f32_16x16x32_bf16 v[40:43], a[56:59], a[132:135], v[40:43]\n"
  "ds_read_b64_tr_b16 a[150:151], v226 offset:41472\n"
  "v_mfma_f32_16x16x32_bf16 v[44:47], a[60:63], a[132:135], v[44:47]\n"
  "ds_read_b128 a[20:23], v229 offset:2048\n"
  "s_waitcnt lgkmcnt(7)\n"
  "v_mfma_f32_16x16x32_bf16 v[44:47], a[44:47], a[128:131], v[44:47]\n"
  "buffer_load_dwordx4 v222, s[32:35], s44 offen offset:2048 lds\n"
  "v_mfma_f32_16x16x32_bf16 v[60:63], a[44:47], a[136:139], v[60:63]\n"
  "ds_read_b64_tr_b16 a[152:153], v227 offset:28672\n"
  "v_mfma_f32_16x16x32_bf16 v[60:63], a[60:63], a[140:143], v[60:63]\n"
  "ds_read_b64_tr_b16 a[154:155], v227 offset:29184\n"
  "v_mfma_f32_16x16x32_bf16 v[56:59], a[56:59], a[140:143], v[56:59]\n"
  "ds_read_b128 a[8:11], v228 offset:4096\n"
  "v_mfma_f32_16x16x32_bf16 v[56:59], a[40:43], a[136:139], v[56:59]\n"
  "buffer_load_dwordx4 v223, s[32:35], s44 offen offset:3072 lds\n"
  "v_mfma_f32_16x16x32_bf16 v[52:55], a[36:39], a[136:139], v[52:55]\n"
  "ds_read_b64_tr_b16 a[156:157], v227 offset:40960\n"
  "s_add_u32 m0, s46, 49152\n"
  "v_mfma_f32_16x16x32_bf16 v[52:55], a[52:55], a[140:143], v[52:55]\n"
  "ds_read_b64_tr_b16 a[158:159], v227 offset:41472\n"
  "v_mfma_f32_16x16x32_bf16 v[48:51], a[48:51], a[140:143], v[48:51]\n"
  "ds_read_b128 a[24:27], v229 offset:4096\n"
  "s_waitcnt lgkmcnt(7)\n"
  "v_mfma_f32_16x16x32_bf16 v[48:51], a[32:35], a[136:139], v[48:51]\n"
  "buffer_load_dwordx4 v216, s[36:39], s45 offen offset:0 lds\n"
  "v_mfma_f32_16x16x32_bf16 v[64:67], a[144:147], a[32:35], v[64:67]\n"
  "ds_read_b64_tr_b16 a[128:129], v224 offset:0\n"
  "v_mfma_f32_16x16x32_bf16 v[64:67], a[148:151], a[48:51], v[64:67]\n"
  "ds_read_b64_tr_b16 a[130:131], v224 offset:512\n"
  "v_mfma_f32_16x16x32_bf16 v[68:71], a[148:151], a[52:55], v[68:71]\n"
  "ds_read_b128 a[12:15], v228 offset:6144\n"
  "v_mfma_f32_16x16x32_bf16 v[68:71], a[144:147], a[36:39], v[68:71]\n"
  "buffer_load_dwordx4 v217, s[36:39], s45 offen offset:1024 lds\n"
  "v_mfma_f32_16x16x32_bf16 v[72:75], a[144:147], a[40:43], v[72:75]\n"
  "ds_read_b64_tr_b16 a[132:133], v224 offset:12288\n"
  "v_mfma_f32_16x16x32_bf16 v[72:75], a[148:151], a[56:59], v[72:75]\n"
  "ds_read_b64_tr_b16 a[134:135], v224 offset:12800\n"
  "v_mfma_f32_16x16x32_bf16 v[76:79], a[148:151], a[60:63], v[76:79]\n"
  "ds_read_b128 a[28:31], v229 offset:6144\n"
  "s_waitcnt lgkmcnt(7)\n"
  "v_mfma_f32_16x16x32_bf16 v[76:79], a[144:147], a[44:47], v[76:79]\n"
  "buffer_load_dwordx4 v216, s[36:39], s45 offen offset:2048 lds\n"
  "v_mfma_f32_16x16x32_bf16 v[92:95], a[152:155], a[44:47], v[92:95]\n"
  "ds_read_b64_tr_b16 a[136:137], v225 offset:0\n"
  "v_mfma_f32_16x16x32_bf16 v[92:95], a[156:159], a[60:63], v[92:95]\n"
  "ds_read_b64_tr_b16 a[138:139], v225 offset:512\n"
  "v_mfma_f32_16x16x32_bf16 v[88:91], a[156:159], a[56:59], v[88:91]\n"
  "buffer_load_dwordx4 v217, s[36:39], s45 offen offset:3072 lds\n"
  "v_mfma_f32_16x16x32_bf16 v[88:91], a[152:155], a[40:43], v[88:91]\n"
  "s_add_u32 m0, s46, 53248\n"
  "v_mfma_f32_16x16x32_bf16 v[84:87], a[152:155], a[36:39], v[84:87]\n"
  "ds_read_b64_tr_b16 a[140:141], v225 offset:12288\n"
  "v_mfma_f32_16x16x32_bf16 v[84:87], a[156:159], a[52:55], v[84:87]\n"
  "ds_read_b64_tr_b16 a[142:143], v225 offset:12800\n"
  "s_waitcnt lgkmcnt(4)\n"
  "v_mfma_f32_16x16x32_bf16 v[80:83], a[156:159], a[48:51], v[80:83]\n"
  "buffer_load_dwordx4 v218, s[36:39], s45 offen offset:0 lds\n"
  "v_mfma_f32_16x16x32_bf16 v[80:83], a[152:155], a[32:35], v[80:83]\n"
  "v_mfma_f32_16x16x32_bf16 v[0:3], a[128:131], a[0:3], v[0:3]\n"
  "ds_read_b64_tr_b16 a[144:145], v224 offset:2048\n"
  "v_mfma_f32_16x16x32_bf16 v[0:3], a[132:135], a[16:19], v[0:3]\n"
  "ds_read_b64_tr_b16 a[146:147], v224 offset:2560\n"
  "v_mfma_f32_16x16x32_bf16 v[4:7], a[132:135], a[20:23], v[4:7]\n"
  "buffer_load_dwordx4 v219, s[36:39], s45 offen offset:1024 lds\n"
  "v_mfma_f32_16x16x32_bf16 v[4:7], a[128:131], a[4:7], v[4:7]\n"
  "s_xor_b32 s44, s44, 128\n"
  "s_mul_i32 s45, s44, 3072\n"
  "v_mfma_f32_16x16x32_bf16 v[8:11], a[128:131], a[8:11], v[8:11]\n"
  "ds_read_b64_tr_b16 a[148:149], v224 offset:14336\n"
  "v_mfma_f32_16x16x32_bf16 v[8:11], a[132:135], a[24:27], v[8:11]\n"
  "ds_read_b64_tr_b16 a[150:151], v224 offset:14848\n"
  "s_waitcnt lgkmcnt(4)\n"
  "v_mfma_f32_16x16x32_bf16 v[12:15], a[132:135], a[28:31], v[12:15]\n"
  "v_mfma_f32_16x16x32_bf16 v[12:15], a[128:131], a[12:15], v[12:15]\n"
  "v_mfma_f32_16x16x32_bf16 v[28:31], a[136:139], a[12:15], v[28:31]\n"
  "s_waitcnt vmcnt(10)\n"
  "s_barrier\n"
  "s_add_u32 m0, s47, 0\n"
  "v_mfma_f32_16x16x32_bf16 v[28:31], a[140:143], a[28:31], v[28:31]\n"
  "ds_read_b64_tr_b16 a[152:153], v225 offset:2048\n"
  "v_mfma_f32_16x16x32_bf16 v[24:27], a[140:143], a[24:27], v[24:27]\n"
  "ds_read_b64_tr_b16 a[154:155], v225 offset:2560\n"
  "v_mfma_f32_16x16x32_bf16 v[24:27], a[136:139], a[8:11], v[24:27]\n"
  "ds_read_b128 a[32:35], v228 offset:16384\n"
  "v_mfma_f32_16x16x32_bf16 v[20:23], a[136:139], a[4:7], v[20:23]\n"
  "ds_read_b64_tr_b16 a[156:157], v225 offset:14336\n"
  "v_mfma_f32_16x16x32_bf16 v[20:23], a[140:143], a[20:23], v[20:23]\n"
  "ds_read_b64_tr_b16 a[158:159], v225 offset:14848\n"
  "v_mfma_f32_16x16x32_bf16 v[16:19], a[140:143], a[16:19], v[16:19]\n"
  "ds_read_b128 a[48:51], v229 offset:16384\n"
  "s_waitcnt lgkmcnt(6)\n"
  "v_mfma_f32_16x16x32_bf16 v[16:19], a[136:139], a[0:3], v[16:19]\n"
  "buffer_load_dwordx4 v220, s[32:35], s44 offen offset:0 lds\n"
  "v_mfma_f32_16x16x32_bf16 v[32:35], a[0:3], a[144:147], v[32:35]\n"
  "ds_read_b64_tr_b16 a[128:129], v224 offset:4096\n"
  "v_mfma_f32_16x16x32_bf16 v[32:35], a[16:19], a[148:151], v[32:35]\n"
  "ds_read_b64_tr_b16 a[130:131], v224 offset:4608\n"
  "v_mfma_f32_16x16x32_bf16 v[36:39], a[20:23], a[148:151], v[36:39]\n"
  "ds_read_b128 a[36:39], v228 offset:18432\n"
  "v_mfma_f32_16x16x32_bf16 v[36:39], a[4:7], a[144:147], v[36:39]\n"
  "buffer_load_dwordx4 v221, s[32:35], s44 offen offset:1024 lds\n"
  "v_mfma_f32_16x16x32_bf16 v[40:43], a[8:11], a[144:147], v[40:43]\n"
  "ds_read_b64_tr_b16 a[132:133], v224 offset:16384\n"
  "v_mfma_f32_16x16x32_bf16 v[40:43], a[24:27], a[148:151], v[40:43]\n"
  "ds_read_b64_tr_b16 a[134:135], v224 offset:16896\n"
  "v_mfma_f32_16x16x32_bf16 v[44:47], a[28:31], a[148:151], v[44:47]\n"
  "ds_read_b128 a[52:55], v229 offset:18432\n"
  "s_waitcnt lgkmcnt(7)\n"
  "v_mfma_f32_16x16x32_bf16 v[44:47], a[12:15], a[144:147], v[44:47]\n"
  "buffer_load_dwordx4 v222, s[32:35], s44 offen offset:2048 lds\n"
  "v_mfma_f32_16x16x32_bf16 v[60:63], a[12:15], a[152:155], v[60:63]\n"
  "ds_read_b64_tr_b16 a[136:137], v225 offset:4096\n"
  "v_mfma_f32_16x16x32_bf16 v[60:63], a[28:31], a[156:159], v[60:63]\n"
  "ds_read_b64_tr_b16 a[138:139], v225 offset:4608\n"
  "v_mfma_f32_16x16x32_bf16 v[56:59], a[24:27], a[156:159], v[56:59]\n"
  "ds_read_b128 a[40:43], v228 offset:20480\n"
  "v_mfma_f32_16x16x32_bf16 v[56:59], a[8:11], a[152:155], v[56:59]\n"
  "buffer_load_dwordx4 v223, s[32:35], s44 offen offset:3072 lds\n"
  "v_mfma_f32_16x16x32_bf16 v[52:55], a[4:7], a[152:155], v[52:55]\n"
  "ds_read_b64_tr_b16 a[140:141], v225 offset:16384\n"
  "s_add_u32 m0, s46, 73728\n"
  "v_mfma_f32_16x16x32_bf16 v[52:55], a[20:23], a[156:159], v[52:55]\n"
  "ds_read_b64_tr_b16 a[142:143], v225 offset:16896\n"
  "v_mfma_f32_16x16x32_bf16 v[48:51], a[16:19], a[156:159], v[48:51]\n"
  "ds_read_b128 a[56:59], v229 offset:20480\n"
  "s_waitcnt lgkmcnt(7)\n"
  "v_mfma_f32_16x16x32_bf16 v[48:51], a[0:3], a[152:155], v[48:51]\n"
  "buffer_load_dwordx4 v216, s[36:39], s45 offen offset:0 lds\n"
  "v_mfma_f32_16x16x32_bf16 v[64:67], a[128:131], a[0:3], v[64:67]\n"
  "ds_read_b64_tr_b16 a[144:145], v224 offset:24576\n"
  "v_mfma_f32_16x16x32_bf16 v[64:67], a[132:135], a[16:19], v[64:67]\n"
  "ds_read_b64_tr_b16 a[146:147], v224 offset:25088\n"
  "v_mfma_f32_16x16x32_bf16 v[68:71], a[132:135], a[20:23], v[68:71]\n"
  "ds_read_b128 a[44:47], v228 offset:22528\n"
  "v_mfma_f32_16x16x32_bf16 v[68:71], a[128:131], a[4:7], v[68:71]\n"
  "buffer_load_dwordx4 v217, s[36:39], s45 offen offset:1024 lds\n"
  "v_mfma_f32_16x16x32_bf16 v[72:75], a[128:131], a[8:11], v[72:75]\n"
  "ds_read_b64_tr_b16 a[148:149], v224 offset:36864\n"
  "v_mfma_f32_16x16x32_bf16 v[72:75], a[132:135], a[24:27], v[72:75]\n"
  "ds_read_b64_tr_b16 a[150:151], v224 offset:37376\n"
  "v_mfma_f32_16x16x32_bf16 v[76:79], a[132:135], a[28:31], v[76:79]\n"
  "ds_read_b128 a[60:63], v229 offset:22528\n"
  "s_waitcnt lgkmcnt(7)\n"
  "v_mfma_f32_16x16x32_bf16 v[76:79], a[128:131], a[12:15], v[76:79]\n"
  "buffer_load_dwordx4 v216, s[36:39], s45 offen offset:2048 lds\n"
  "v_mfma_f32_16x16x32_bf16 v[92:95], a[136:139], a[12:15], v[92:95]\n"
  "ds_read_b64_tr_b16 a[152:153], v225 offset:24576\n"
  "v_mfma_f32_16x16x32_bf16 v[92:95], a[140:143], a[28:31], v[92:95]\n"
  "ds_read_b64_tr_b16 a[154:155], v225 offset:25088\n"
  "v_mfma_f32_16x16x32_bf16 v[88:91], a[140:143], a[24:27], v[88:91]\n"
  "buffer_load_dwordx4 v217, s[36:39], s45 offen offset:3072 lds\n"
  "v_mfma_f32_16x16x32_bf16 v[88:91], a[136:139], a[8:11], v[88:91]\n"
  "s_add_u32 m0, s46, 77824\n"
  "v_mfma_f32_16x16x32_bf16 v[84:87], a[136:139], a[4:7], v[84:87]\n"
  "ds_read_b64_tr_b16 a[156:157], v225 offset:36864\n"
  "v_mfma_f32_16x16x32_bf16 v[84:87], a[140:143], a[20:23], v[84:87]\n"
  "ds_read_b64_tr_b16 a[158:159], v225 offset:37376\n"
  "s_waitcnt lgkmcnt(4)\n"
  "v_mfma_f32_16x16x32_bf16 v[80:83], a[140:143], a[16:19], v[80:83]\n"
  "buffer_load_dwordx4 v218, s[36:39], s45 offen offset:0 lds\n"
  "v_mfma_f32_16x16x32_bf16 v[80:83], a[136:139], a[0:3], v[80:83]\n"
  "v_mfma_f32_16x16x32_bf16 v[0:3], a[144:147], a[32:35], v[0:3]\n"
  "ds_read_b64_tr_b16 a[128:129], v224 offset:26624\n"
  "ds_write_b32 v240, v96 offset:512\n"
  "v_mfma_f32_16x16x32_bf16 v[0:3], a[148:151], a[48:51], v[0:3]\n"
  "ds_read_b64_tr_b16 a[130:131], v224 offset:27136\n"
  "ds_write_b64 v243, v[160:161] offset:0\n"
  "v_mfma_f32_16x16x32_bf16 v[4:7], a[148:151], a[52:55], v[4:7]\n"
  "buffer_load_dwordx4 v219, s[36:39], s45 offen offset:1024 lds\n"
  "v_mfma_f32_16x16x32_bf16 v[4:7], a[144:147], a[36:39], v[4:7]\n"
  "s_xor_b32 s44, s44, 128\n"
  "s_add_u32 s44, s44, 256\n"
  "s_and_b32 s44, s44, 0x7ff\n"
  "v_mfma_f32_16x16x32_bf16 v[8:11], a[144:147], a[40:43], v[8:11]\n"
  "ds_read_b64_tr_b16 a[132:133], v224 offset:38912\n"
  "s_mul_i32 s45, s44, 3072\n"
  "v_mfma_f32_16x16x32_bf16 v[8:11], a[148:151], a[56:59], v[8:11]\n"
  "ds_read_b64_tr_b16 a[134:135], v224 offset:39424\n"
  "s_waitcnt lgkmcnt(6)\n"
  "v_mfma_f32_16x16x32_bf16 v[12:15], a[148:151], a[60:63], v[12:15]\n"
  "ds_write_b64 v243, v[162:163] offset:8\n"
  "v_mfma_f32_16x16x32_bf16 v[12:15], a[144:147], a[44:47], v[12:15]\n"
  "ds_write_b64 v243, v[164:165] offset:16\n"
  "v_mfma_f32_16x16x32_bf16 v[28:31], a[152:155], a[44:47], v[28:31]\n"
  "s_waitcnt vmcnt(10)\n"
  "s_barrier\n"
  "ds_write_b32 v239, v97\n"
  "v_mfma_f32_16x16x32_bf16 v[28:31], a[156:159], a[60:63], v[28:31]\n"
  "ds_read_b64_tr_b16 a[136:137], v225 offset:26624\n"
  "ds_write_b32 v240, v97 offset:256\n"
  "v_mfma_f32_16x16x32_bf16 v[24:27], a[156:159], a[56:59], v[24:27]\n"
  "ds_read_b64_tr_b16 a[138:139], v225 offset:27136\n"
  "v_mfma_f32_16x16x32_bf16 v[24:27], a[152:155], a[40:43], v[24:27]\n"
  "ds_read_b128 a[0:3], v228 offset:32768\n"
  "v_mfma_f32_16x16x32_bf16 v[20:23], a[152:155], a[36:39], v[20:23]\n"
  "ds_read_b64_tr_b16 a[140:141], v225 offset:38912\n"
  "v_mfma_f32_16x16x32_bf16 v[20:23], a[156:159], a[52:55], v[20:23]\n"
  "ds_read_b64_tr_b16 a[142:143], v225 offset:39424\n"
  "s_waitcnt lgkmcnt(4)\n"
  "s_barrier\n"
  "v_mfma_f32_16x16x32_bf16 v[16:19], a[156:159], a[48:51], v[16:19]\n"
  "ds_read_b128 a[16:19], v229 offset:32768\n"
  "v_mfma_f32_16x16x32_bf16 v[16:19], a[152:155], a[32:35], v[16:19]\n"
  "ds_read_b128 v[180:183], v242 offset:512\n"
  "v_mfma_f32_16x16x32_bf16 v[32:35], a[32:35], a[128:131], v[32:35]\n"
  "ds_read_b64_tr_b16 a[144:145], v224 offset:28672\n"
  "ds_read_b128 v[192:195], v242 offset:256\n"
  "v_mfma_f32_16x16x32_bf16 v[32:35], a[48:51], a[132:135], v[32:35]\n"
  "ds_read_b64_tr_b16 a[146:147], v224 offset:29184\n"
  "ds_read_b128 v[160:163], v238 offset:0\n"
  "v_mfma_f32_16x16x32_bf16 v[36:39], a[52:55], a[132:135], v[36:39]\n"
  "ds_read_b128 a[4:7], v228 offset:34816\n"
  "ds_read_b128 v[164:167], v238 offset:16\n"
  "v_mfma_f32_16x16x32_bf16 v[36:39], a[36:39], a[128:131], v[36:39]\n"
  "ds_read_b128 v[168:171], v238 offset:32\n"
  "v_mfma_f32_16x16x32_bf16 v[40:43], a[40:43], a[128:131], v[40:43]\n"
  "ds_read_b64_tr_b16 a[148:149], v224 offset:40960\n"
  "s_waitcnt lgkmcnt(6)\n"
  "v_mfma_f32_16x16x32_bf16 v[40:43], a[56:59], a[132:135], v[40:43]\n"
  "ds_read_b64_tr_b16 a[150:151], v224 offset:41472\n"
  "v_cvt_pk_bf16_f32 v173, -v180, 0\n"
  "v_mfma_f32_16x16x32_bf16 v[44:47], a[60:63], a[132:135], v[44:47]\n"
  "ds_read_b128 a[20:23], v229 offset:34816\n"
  "v_mfma_f32_16x16x32_bf16 v[44:47], a[44:47], a[128:131], v[44:47]\n"
  "buffer_store_dwordx4 v[180:183], v250, s[52:55], s57 offen sc1\n"
  "v_mfma_f32_16x16x32_bf16 v[60:63], a[44:47], a[136:139], v[60:63]\n"
  "ds_read_b64_tr_b16 a[152:153], v225 offset:28672\n"
  "v_lshlrev_b32 v172, 16, v173\n"
  "v_mfma_f32_16x16x32_bf16 v[60:63], a[60:63], a[140:143], v[60:63]\n"
  "ds_read_b64_tr_b16 a[154:155], v225 offset:29184\n"
  "v_add_f32 v184, v180, v172\n"
  "v_mfma_f32_16x16x32_bf16 v[56:59], a[56:59], a[140:143], v[56:59]\n"
  "ds_read_b128 a[8:11], v228 offset:36864\n"
  "v_cvt_pk_bf16_f32 v172, v172, -v184\n"
  "v_mfma_f32_16x16x32_bf16 v[56:59], a[40:43], a[136:139], v[56:59]\n"
  "ds_read_b128 v[200:203], v241 offset:0\n"
  "v_mul_f32 v196, 0x3e8293ee, v192\n"
  "v_mfma_f32_16x16x32_bf16 v[52:55], a[36:39], a[136:139], v[52:55]\n"
  "ds_read_b64_tr_b16 a[156:157], v225 offset:40960\n"
  "v_cvt_pk_bf16_f32 v175, -v181, 0\n"
  "v_mfma_f32_16x16x32_bf16 v[52:55], a[52:55], a[140:143], v[52:55]\n"
  "ds_read_b64_tr_b16 a[158:159], v225 offset:41472\n"
  "v_lshlrev_b32 v174, 16, v175\n"
  "v_mfma_f32_16x16x32_bf16 v[48:51], a[48:51], a[140:143], v[48:51]\n"
  "ds_read_b128 a[24:27], v229 offset:36864\n"
  "s_waitcnt lgkmcnt(8)\n"
  "v_mfma_f32_16x16x32_bf16 v[48:51], a[32:35], a[136:139], v[48:51]\n"
  "ds_read_b128 v[204:207], v241 offset:64\n"
  "v_add_f32 v185, v181, v174\n"
  "v_mfma_f32_16x16x32_bf16 v[64:67], a[144:147], a[32:35], v[64:67]\n"
  "ds_read_b64_tr_b16 a[128:129], v226 offset:0\n"
  "v_cvt_pk_bf16_f32 v174, v174, -v185\n"
  "v_mfma_f32_16x16x32_bf16 v[64:67], a[148:151], a[48:51], v[64:67]\n"
  "ds_read_b64_tr_b16 a[130:131], v226 offset:512\n"
  "v_mul_f32 v197, 0x3e8293ee, v193\n"
  "v_mfma_f32_16x16x32_bf16 v[68:71], a[148:151], a[52:55], v[68:71]\n"
  "ds_read_b128 a[12:15], v228 offset:38912\n"
  "v_cvt_pk_bf16_f32 v177, -v182, 0\n"
  "v_mfma_f32_16x16x32_bf16 v[68:71], a[144:147], a[36:39], v[68:71]\n"
  "ds_read_b128 v[208:211], v241 offset:128\n"
  "v_lshlrev_b32 v176, 16, v177\n"
  "v_mfma_f32_16x16x32_bf16 v[72:75], a[144:147], a[40:43], v[72:75]\n"
  "ds_read_b64_tr_b16 a[132:133], v226 offset:12288\n"
  "v_add_f32 v186, v182, v176\n"
  "v_mfma_f32_16x16x32_bf16 v[72:75], a[148:151], a[56:59], v[72:75]\n"
  "ds_read_b64_tr_b16 a[134:135], v226 offset:12800\n"
  "v_cvt_pk_bf16_f32 v176, v176, -v186\n"
  "v_mfma_f32_16x16x32_bf16 v[76:79], a[148:151], a[60:63], v[76:79]\n"
  "s_waitcnt lgkmcnt(11)\n"
  "ds_read_b128 a[28:31], v229 offset:38912\n"
  "s_waitcnt lgkmcnt(9)\n"
  "v_mfma_f32_16x16x32_bf16 v[76:79], a[144:147], a[44:47], v[76:79]\n"
  "ds_read_b128 v[212:215], v241 offset:192\n"
  "v_mul_f32 v198, 0x3e8293ee, v194\n"
  "v_mfma_f32_16x16x32_bf16 v[92:95], a[152:155], a[44:47], v[92:95]\n"
  "s_waitcnt vmcnt(5)\n"
  "s_barrier\n"
  "ds_read_b64_tr_b16 a[136:137], v227 offset:0\n"
  "v_mfma_f32_16x16x32_bf16 v[92:95], a[156:159], a[60:63], v[92:95]\n"
  "ds_read_b64_tr_b16 a[138:139], v227 offset:512\n"
  "v_cvt_pk_bf16_f32 v179, -v183, 0\n"
  "v_mfma_f32_16x16x32_bf16 v[88:91], a[156:159], a[56:59], v[88:91]\n"
  "ds_read_b128 a[64:67], v228 offset:0\n"
  "v_lshlrev_b32 v178, 16, v179\n"
  "v_mfma_f32_16x16x32_bf16 v[88:91], a[152:155], a[40:43], v[88:91]\n"
  "ds_read_b128 a[80:83], v229 offset:0\n"
  "v_add_f32 v187, v183, v178\n"
  "v_mfma_f32_16x16x32_bf16 v[84:87], a[152:155], a[36:39], v[84:87]\n"
  "ds_read_b64_tr_b16 a[140:141], v227 offset:12288\n"
  "v_cvt_pk_bf16_f32 v178, v178, -v187\n"
  "v_mfma_f32_16x16x32_bf16 v[84:87], a[156:159], a[52:55], v[84:87]\n"
  "s_waitcnt lgkmcnt(11)\n"
  "ds_read_b64_tr_b16 a[142:143], v227 offset:12800\n"
  "v_mul_f32 v199, 0x3e8293ee, v195\n"
  "v_mfma_f32_16x16x32_bf16 v[80:83], a[156:159], a[48:51], v[80:83]\n"
  "ds_read_b128 a[68:71], v228 offset:2048\n"
  "v_mfma_f32_16x16x32_bf16 v[80:83], a[152:155], a[32:35], v[80:83]\n"
  "ds_read_b128 a[84:87], v229 offset:2048\n"
  "s_waitcnt lgkmcnt(9)\n"
  "v_mfma_f32_16x16x32_bf16 v[0:3], a[128:131], a[0:3], v[0:3]\n"
  "ds_read_b64_tr_b16 a[144:145], v226 offset:24576\n"
  "v_mfma_f32_16x16x32_bf16 v[0:3], a[132:135], a[16:19], v[0:3]\n"
  "ds_read_b64_tr_b16 a[146:147], v226 offset:25088\n"
  "v_mfma_f32_16x16x32_bf16 v[4:7], a[132:135], a[20:23], v[4:7]\n"
  "ds_read_b128 a[72:75], v228 offset:4096\n"
  "v_mfma_f32_16x16x32_bf16 v[4:7], a[128:131], a[4:7], v[4:7]\n"
  "ds_read_b128 a[88:91], v229 offset:4096\n"
  "v_mfma_f32_16x16x32_bf16 v[8:11], a[128:131], a[8:11], v[8:11]\n"
  "ds_read_b64_tr_b16 a[148:149], v226 offset:36864\n"
  "v_mfma_f32_16x16x32_bf16 v[8:11], a[132:135], a[24:27], v[8:11]\n"
  "ds_read_b64_tr_b16 a[150:151], v226 offset:37376\n"
  "v_mfma_f32_16x16x32_bf16 v[12:15], a[132:135], a[28:31], v[12:15]\n"
  "s_waitcnt lgkmcnt(11)\n"
  "ds_read_b128 a[76:79], v228 offset:6144\n"
  "v_mfma_f32_16x16x32_bf16 v[12:15], a[128:131], a[12:15], v[12:15]\n"
  "ds_read_b128 a[92:95], v229 offset:6144\n"
  "s_waitcnt lgkmcnt(10)\n"
  "v_mfma_f32_16x16x32_bf16 v[28:31], a[136:139], a[12:15], v[28:31]\n"
  "ds_read_b64_tr_b16 a[152:153], v227 offset:24576\n"
  "v_mfma_f32_16x16x32_bf16 v[28:31], a[140:143], a[28:31], v[28:31]\n"
  "ds_read_b64_tr_b16 a[154:155], v227 offset:25088\n"
  "v_mfma_f32_16x16x32_bf16 v[24:27], a[140:143], a[24:27], v[24:27]\n"
  "v_mfma_f32_16x16x32_bf16 v[24:27], a[136:139], a[8:11], v[24:27]\n"
  "v_mfma_f32_16x16x32_bf16 v[20:23], a[136:139], a[4:7], v[20:23]\n"
  "ds_read_b64_tr_b16 a[156:157], v227 offset:36864\n"
  "v_mfma_f32_16x16x32_bf16 v[20:23], a[140:143], a[20:23], v[20:23]\n"
  "ds_read_b64_tr_b16 a[158:159], v227 offset:37376\n"
  "s_waitcnt lgkmcnt(4)\n"
  "v_mfma_f32_16x16x32_bf16 v[16:19], a[140:143], a[16:19], v[16:19]\n"
  "v_mfma_f32_16x16x32_bf16 v[16:19], a[136:139], a[0:3], v[16:19]\n"
  "v_mfma_f32_16x16x32_bf16 v[0:3], a[144:147], a[64:67], v[0:3]\n"
  "v_mfma_f32_16x16x32_bf16 v[0:3], a[148:151], a[80:83], v[0:3]\n"
  "v_mfma_f32_16x16x32_bf16 v[4:7], a[148:151], a[84:87], v[4:7]\n"
  "v_mfma_f32_16x16x32_bf16 v[4:7], a[144:147], a[68:71], v[4:7]\n"
  "v_mfma_f32_16x16x32_bf16 v[8:11], a[144:147], a[72:75], v[8:11]\n"
  "v_mfma_f32_16x16x32_bf16 v[8:11], a[148:151], a[88:91], v[8:11]\n"
  "v_mfma_f32_16x16x32_bf16 v[12:15], a[148:151], a[92:95], v[12:15]\n"
  "v_mfma_f32_16x16x32_bf16 v[12:15], a[144:147], a[76:79], v[12:15]\n"
  "s_waitcnt lgkmcnt(0)\n"
  "v_mfma_f32_16x16x32_bf16 v[28:31], a[152:155], a[76:79], v[28:31]\n"
  "s_waitcnt vmcnt(1)\n"
  "s_barrier\n"
  "v_mfma_f32_16x16x32_bf16 v[28:31], a[156:159], a[92:95], v[28:31]\n"
  "ds_read_b64_tr_b16 a[136:137], v226 offset:2048\n"
  "v_mfma_f32_16x16x32_bf16 v[24:27], a[156:159], a[88:91], v[24:27]\n"
  "ds_read_b64_tr_b16 a[138:139], v226 offset:2560\n"
  "v_mfma_f32_16x16x32_bf16 v[24:27], a[152:155], a[72:75], v[24:27]\n"
  "v_mfma_f32_16x16x32_bf16 v[20:23], a[152:155], a[68:71], v[20:23]\n"
  "ds_read_b64_tr_b16 a[140:141], v226 offset:14336\n"
  "v_mfma_f32_16x16x32_bf16 v[20:23], a[156:159], a[84:87], v[20:23]\n"
  "ds_read_b64_tr_b16 a[142:143], v226 offset:14848\n"
  "v_mfma_f32_16x16x32_bf16 v[16:19], a[156:159], a[80:83], v[16:19]\n"
  "v_mfma_f32_16x16x32_bf16 v[16:19], a[152:155], a[64:67], v[16:19]\n"
  "v_mfma_f32_16x16x16_bf16 v[0:3], v[160:161], v[172:173], v[0:3]\n"
  "ds_read_b64_tr_b16 a[144:145], v227 offset:2048\n"
  "v_mfma_f32_16x16x16_bf16 v[4:7], v[160:161], v[174:175], v[4:7]\n"
  "ds_read_b64_tr_b16 a[146:147], v227 offset:2560\n"
  "v_mfma_f32_16x16x16_bf16 v[8:11], v[160:161], v[176:177], v[8:11]\n"
  "v_mfma_f32_16x16x16_bf16 v[12:15], v[160:161], v[178:179], v[12:15]\n"
  "v_mfma_f32_16x16x16_bf16 v[28:31], v[162:163], v[178:179], v[28:31]\n"
  "ds_read_b64_tr_b16 a[148:149], v227 offset:14336\n"
  "v_mfma_f32_16x16x16_bf16 v[24:27], v[162:163], v[176:177], v[24:27]\n"
  "ds_read_b64_tr_b16 a[150:151], v227 offset:14848\n"
  "s_waitcnt lgkmcnt(4)\n"
  "v_mfma_f32_16x16x16_bf16 v[20:23], v[162:163], v[174:175], v[20:23]\n"
  "v_mfma_f32_16x16x16_bf16 v[16:19], v[162:163], v[172:173], v[16:19]\n"
  "v_mfma_f32_16x16x32_bf16 v[44:47], a[12:15], a[136:139], v[44:47]\n"
  "ds_read_b64_tr_b16 a[152:153], v226 offset:26624\n"
  "v_mfma_f32_16x16x32_bf16 v[44:47], a[28:31], a[140:143], v[44:47]\n"
  "ds_read_b64_tr_b16 a[154:155], v226 offset:27136\n"
  "v_mfma_f32_16x16x32_bf16 v[40:43], a[24:27], a[140:143], v[40:43]\n"
  "v_mfma_f32_16x16x32_bf16 v[40:43], a[8:11], a[136:139], v[40:43]\n"
  "v_mfma_f32_16x16x32_bf16 v[36:39], a[4:7], a[136:139], v[36:39]\n"
  "ds_read_b64_tr_b16 a[156:157], v226 offset:38912\n"
  "v_mul_f32 v0, v0, v192\n"
  "v_mfma_f32_16x16x32_bf16 v[36:39], a[20:23], a[140:143], v[36:39]\n"
  "ds_read_b64_tr_b16 a[158:159], v226 offset:39424\n"
  "s_waitcnt lgkmcnt(4)\n"
  "v_mfma_f32_16x16x32_bf16 v[32:35], a[16:19], a[140:143], v[32:35]\n"
  "v_mul_f32 v1, v1, v192\n"
  "v_mul_f32 v2, v2, v192\n"
  "v_mfma_f32_16x16x32_bf16 v[32:35], a[0:3], a[136:139], v[32:35]\n"
  "v_mul_f32 v3, v3, v192\n"
  "v_mul_f32 v16, v16, v192\n"
  "v_mfma_f32_16x16x32_bf16 v[48:51], a[0:3], a[144:147], v[48:51]\n"
  "ds_read_b64_tr_b16 a[128:129], v227 offset:26624\n"
  "v_mul_f32 v17, v17, v192\n"
  "v_mfma_f32_16x16x32_bf16 v[48:51], a[16:19], a[148:151], v[48:51]\n"
  "ds_read_b64_tr_b16 a[130:131], v227 offset:27136\n"
  "v_mul_f32 v18, v18, v192\n"
  "v_mfma_f32_16x16x32_bf16 v[52:55], a[20:23], a[148:151], v[52:55]\n"
  "v_mul_f32 v19, v19, v192\n"
  "v_cvt_pk_bf16_f32 v0, v0, v1\n"
  "v_mfma_f32_16x16x32_bf16 v[52:55], a[4:7], a[144:147], v[52:55]\n"
  "v_cvt_pk_bf16_f32 v1, v2, v3\n"
  "v_cvt_pk_bf16_f32 v2, v16, v17\n"
  "v_mfma_f32_16x16x32_bf16 v[56:59], a[8:11], a[144:147], v[56:59]\n"
  "ds_read_b64_tr_b16 a[132:133], v227 offset:38912\n"
  "v_cvt_pk_bf16_f32 v3, v18, v19\n"
  "v_mfma_f32_16x16x32_bf16 v[56:59], a[24:27], a[148:151], v[56:59]\n"
  "ds_read_b64_tr_b16 a[134:135], v227 offset:39424\n"
  "s_waitcnt lgkmcnt(4)\n"
  "v_mfma_f32_16x16x32_bf16 v[60:63], a[28:31], a[148:151], v[60:63]\n"
  "ds_write_b128 v233, v[0:3] offset:0\n"
  "v_mfma_f32_16x16x32_bf16 v[60:63], a[12:15], a[144:147], v[60:63]\n"
  "buffer_store_dwordx4 v[0:3], v231, s[40:43], s48 offen offset:0 sc1\n"
  "v_mfma_f32_16x16x32_bf16 v[44:47], a[76:79], a[152:155], v[44:47]\n"
  "v_mul_f32 v4, v4, v193\n"
  "v_mul_f32 v5, v5, v193\n"
  "v_mfma_f32_16x16x32_bf16 v[44:47], a[92:95], a[156:159], v[44:47]\n"
  "v_mul_f32 v6, v6, v193\n"
  "v_mul_f32 v7, v7, v193\n"
  "v_mfma_f32_16x16x32_bf16 v[40:43], a[88:91], a[156:159], v[40:43]\n"
  "v_mul_f32 v20, v20, v193\n"
  "v_mul_f32 v21, v21, v193\n"
  "v_mfma_f32_16x16x32_bf16 v[40:43], a[72:75], a[152:155], v[40:43]\n"
  "v_mul_f32 v22, v22, v193\n"
  "v_mul_f32 v23, v23, v193\n"
  "v_mfma_f32_16x16x32_bf16 v[36:39], a[68:71], a[152:155], v[36:39]\n"
  "v_cvt_pk_bf16_f32 v4, v4, v5\n"
  "v_cvt_pk_bf16_f32 v5, v6, v7\n"
  "v_mfma_f32_16x16x32_bf16 v[36:39], a[84:87], a[156:159], v[36:39]\n"
  "v_cvt_pk_bf16_f32 v6, v20, v21\n"
  "v_cvt_pk_bf16_f32 v7, v22, v23\n"
  "v_mfma_f32_16x16x32_bf16 v[32:35], a[80:83], a[156:159], v[32:35]\n"
  "ds_write_b128 v233, v[4:7] offset:64\n"
  "v_mfma_f32_16x16x32_bf16 v[32:35], a[64:67], a[152:155], v[32:35]\n"
  "s_waitcnt lgkmcnt(2)\n"
  "v_mul_f32 v8, v8, v194\n"
  "v_mul_f32 v9, v9, v194\n"
  "v_mfma_f32_16x16x32_bf16 v[48:51], a[64:67], a[128:131], v[48:51]\n"
  "ds_read_b64_tr_b16 a[144:145], v226 offset:4096\n"
  "v_mul_f32 v10, v10, v194\n"
  "v_mfma_f32_16x16x32_bf16 v[48:51], a[80:83], a[132:135], v[48:51]\n"
  "ds_read_b64_tr_b16 a[146:147], v226 offset:4608\n"
  "v_mul_f32 v11, v11, v194\n"
  "v_mfma_f32_16x16x32_bf16 v[52:55], a[84:87], a[132:135], v[52:55]\n"
  "buffer_store_dwordx4 v[4:7], v231, s[40:43], s48 offen offset:64 sc1\n"
  "v_mfma_f32_16x16x32_bf16 v[52:55], a[68:71], a[128:131], v[52:55]\n"
  "v_mul_f32 v24, v24, v194\n"
  "v_mul_f32 v25, v25, v194\n"
  "v_mfma_f32_16x16x32_bf16 v[56:59], a[72:75], a[128:131], v[56:59]\n"
  "ds_read_b64_tr_b16 a[148:149], v226 offset:16384\n"
  "v_mul_f32 v26, v26, v194\n"
  "v_mfma_f32_16x16x32_bf16 v[56:59], a[88:91], a[132:135], v[56:59]\n"
  "ds_read_b64_tr_b16 a[150:151], v226 offset:16896\n"
  "v_mfma_f32_16x16x32_bf16 v[60:63], a[92:95], a[132:135], v[60:63]\n"
  "v_mul_f32 v27, v27, v194\n"
  "v_cvt_pk_bf16_f32 v8, v8, v9\n"
  "v_mfma_f32_16x16x32_bf16 v[60:63], a[76:79], a[128:131], v[60:63]\n"
  "v_cvt_pk_bf16_f32 v9, v10, v11\n"
  "v_cvt_pk_bf16_f32 v10, v24, v25\n"
  "v_mfma_f32_16x16x16_bf16 v[32:35], v[172:173], v[164:165], v[32:35]\n"
  "ds_read_b64_tr_b16 a[152:153], v227 offset:4096\n"
  "v_cvt_pk_bf16_f32 v11, v26, v27\n"
  "v_mfma_f32_16x16x16_bf16 v[36:39], v[174:175], v[164:165], v[36:39]\n"
  "ds_read_b64_tr_b16 a[154:155], v227 offset:4608\n"
  "v_mul_f32 v12, v12, v195\n"
  "v_mfma_f32_16x16x16_bf16 v[40:43], v[176:177], v[164:165], v[40:43]\n"
  "ds_write_b128 v233, v[8:11] offset:4096\n"
  "v_mfma_f32_16x16x16_bf16 v[44:47], v[178:179], v[164:165], v[44:47]\n"
  "buffer_store_dwordx4 v[8:11], v231, s[40:43], s49 offen offset:0 sc1\n"
  "v_mfma_f32_16x16x16_bf16 v[60:63], v[178:179], v[166:167], v[60:63]\n"
  "ds_read_b64_tr_b16 a[156:157], v227 offset:16384\n"
  "v_mul_f32 v13, v13, v195\n"
  "v_mfma_f32_16x16x16_bf16 v[56:59], v[176:177], v[166:167], v[56:59]\n"
  "ds_read_b64_tr_b16 a[158:159], v227 offset:16896\n"
  "s_waitcnt lgkmcnt(5)\n"
  "v_mfma_f32_16x16x16_bf16 v[52:55], v[174:175], v[166:167], v[52:55]\n"
  "v_mul_f32 v14, v14, v195\n"
  "v_mul_f32 v15, v15, v195\n"
  "v_mfma_f32_16x16x16_bf16 v[48:51], v[172:173], v[166:167], v[48:51]\n"
  "v_mul_f32 v28, v28, v195\n"
  "v_mul_f32 v29, v29, v195\n"
  "v_mfma_f32_16x16x32_bf16 v[64:67], a[144:147], a[0:3], v[64:67]\n"
  "ds_read_b64_tr_b16 a[128:129], v226 offset:28672\n"
  "v_mul_f32 v30, v30, v195\n"
  "v_mfma_f32_16x16x32_bf16 v[64:67], a[148:151], a[16:19], v[64:67]\n"
  "ds_read_b64_tr_b16 a[130:131], v226 offset:29184\n"
  "v_mul_f32 v31, v31, v195\n"
  "v_mfma_f32_16x16x32_bf16 v[68:71], a[148:151], a[20:23], v[68:71]\n"
  "v_cvt_pk_bf16_f32 v12, v12, v13\n"
  "v_cvt_pk_bf16_f32 v13, v14, v15\n"
  "v_mfma_f32_16x16x32_bf16 v[68:71], a[144:147], a[4:7], v[68:71]\n"
  "v_cvt_pk_bf16_f32 v14, v28, v29\n"
  "v_cvt_pk_bf16_f32 v15, v30, v31\n"
  "v_mfma_f32_16x16x32_bf16 v[72:75], a[144:147], a[8:11], v[72:75]\n"
  "ds_read_b64_tr_b16 a[132:133], v226 offset:40960\n"
  "v_mul_f32 v32, v32, v200\n"
  "v_mfma_f32_16x16x32_bf16 v[72:75], a[148:151], a[24:27], v[72:75]\n"
  "ds_read_b64_tr_b16 a[134:135], v226 offset:41472\n"
  "s_waitcnt lgkmcnt(4)\n"
  "v_mfma_f32_16x16x32_bf16 v[76:79], a[148:151], a[28:31], v[76:79]\n"
  "ds_write_b128 v233, v[12:15] offset:4160\n"
  "v_mfma_f32_16x16x32_bf16 v[76:79], a[144:147], a[12:15], v[76:79]\n"
  "buffer_store_dwordx4 v[12:15], v231, s[40:43], s49 offen offset:64 sc1\n"
  "v_mfma_f32_16x16x32_bf16 v[92:95], a[152:155], a[12:15], v[92:95]\n"
  "ds_read_b64_tr_b16 a[136:137], v227 offset:28672\n"
  "v_mul_f32 v33, v33, v201\n"
  "v_mfma_f32_16x16x32_bf16 v[92:95], a[156:159], a[28:31], v[92:95]\n"
  "ds_read_b64_tr_b16 a[138:139], v227 offset:29184\n"
  "v_mul_f32 v34, v34, v202\n"
  "v_mfma_f32_16x16x32_bf16 v[88:91], a[156:159], a[24:27], v[88:91]\n"
  "v_mul_f32 v35, v35, v203\n"
  "v_mul_f32 v36, v36, v204\n"
  "v_mfma_f32_16x16x32_bf16 v[88:91], a[152:155], a[8:11], v[88:91]\n"
  "v_mul_f32 v37, v37, v205\n"
  "v_mul_f32 v38, v38, v206\n"
  "v_mfma_f32_16x16x32_bf16 v[84:87], a[152:155], a[4:7], v[84:87]\n"
  "ds_read_b64_tr_b16 a[140:141], v227 offset:40960\n"
  "v_mul_f32 v39, v39, v207\n"
  "v_mfma_f32_16x16x32_bf16 v[84:87], a[156:159], a[20:23], v[84:87]\n"
  "ds_read_b64_tr_b16 a[142:143], v227 offset:41472\n"
  "s_waitcnt lgkmcnt(5)\n"
  "v_mfma_f32_16x16x32_bf16 v[80:83], a[156:159], a[16:19], v[80:83]\n"
  "v_cvt_pk_bf16_f32 v32, v32, v33\n"
  "v_cvt_pk_bf16_f32 v33, v34, v35\n"
  "v_mfma_f32_16x16x32_bf16 v[80:83], a[152:155], a[0:3], v[80:83]\n"
  "v_cvt_pk_bf16_f32 v34, v36, v37\n"
  "v_cvt_pk_bf16_f32 v35, v38, v39\n"
  "v_mfma_f32_16x16x32_bf16 v[64:67], a[128:131], a[64:67], v[64:67]\n"
  "ds_write_b128 v232, v[32:35] offset:2048\n"
  "v_mfma_f32_16x16x32_bf16 v[64:67], a[132:135], a[80:83], v[64:67]\n"
  "buffer_store_dwordx4 v[32:35], v230, s[40:43], s48 offen offset:2048 sc1\n"
  "v_mfma_f32_16x16x32_bf16 v[68:71], a[132:135], a[84:87], v[68:71]\n"
  "v_mul_f32 v48, v48, v200\n"
  "v_mul_f32 v49, v49, v201\n"
  "v_mfma_f32_16x16x32_bf16 v[68:71], a[128:131], a[68:71], v[68:71]\n"
  "v_mul_f32 v50, v50, v202\n"
  "v_mul_f32 v51, v51, v203\n"
  "v_mfma_f32_16x16x32_bf16 v[72:75], a[128:131], a[72:75], v[72:75]\n"
  "v_mul_f32 v52, v52, v204\n"
  "v_mul_f32 v53, v53, v205\n"
  "v_mfma_f32_16x16x32_bf16 v[72:75], a[132:135], a[88:91], v[72:75]\n"
  "v_mul_f32 v54, v54, v206\n"
  "v_mul_f32 v55, v55, v207\n"
  "v_mfma_f32_16x16x32_bf16 v[76:79], a[132:135], a[92:95], v[76:79]\n"
  "v_cvt_pk_bf16_f32 v48, v48, v49\n"
  "v_cvt_pk_bf16_f32 v49, v50, v51\n"
  "v_mfma_f32_16x16x32_bf16 v[76:79], a[128:131], a[76:79], v[76:79]\n"
  "s_waitcnt lgkmcnt(1)\n"
  "v_cvt_pk_bf16_f32 v50, v52, v53\n"
  "v_cvt_pk_bf16_f32 v51, v54, v55\n"
  "v_mfma_f32_16x16x32_bf16 v[92:95], a[136:139], a[76:79], v[92:95]\n"
  "ds_write_b128 v232, v[48:51] offset:2304\n"
  "v_mfma_f32_16x16x32_bf16 v[92:95], a[140:143], a[92:95], v[92:95]\n"
  "buffer_store_dwordx4 v[48:51], v230, s[40:43], s48 offen offset:2304 sc1\n"
  "v_mfma_f32_16x16x32_bf16 v[88:91], a[140:143], a[88:91], v[88:91]\n"
  "v_mul_f32 v40, v40, v208\n"
  "v_mul_f32 v41, v41, v209\n"
  "v_mfma_f32_16x16x32_bf16 v[88:91], a[136:139], a[72:75], v[88:91]\n"
  "v_mul_f32 v42, v42, v210\n"
  "v_mul_f32 v43, v43, v211\n"
  "v_mfma_f32_16x16x32_bf16 v[84:87], a[136:139], a[68:71], v[84:87]\n"
  "v_mul_f32 v44, v44, v212\n"
  "v_mul_f32 v45, v45, v213\n"
  "v_mfma_f32_16x16x32_bf16 v[84:87], a[140:143], a[84:87], v[84:87]\n"
  "v_mul_f32 v46, v46, v214\n"
  "v_mul_f32 v47, v47, v215\n"
  "v_mfma_f32_16x16x32_bf16 v[80:83], a[140:143], a[80:83], v[80:83]\n"
  "v_cvt_pk_bf16_f32 v40, v40, v41\n"
  "v_cvt_pk_bf16_f32 v41, v42, v43\n"
  "v_mfma_f32_16x16x32_bf16 v[80:83], a[136:139], a[64:67], v[80:83]\n"
  "v_cvt_pk_bf16_f32 v42, v44, v45\n"
  "v_cvt_pk_bf16_f32 v43, v46, v47\n"
  "v_mfma_f32_16x16x16_bf16 v[64:67], v[168:169], v[172:173], v[64:67]\n"
  "ds_write_b128 v232, v[40:43] offset:6144\n"
  "v_mfma_f32_16x16x16_bf16 v[68:71], v[168:169], v[174:175], v[68:71]\n"
  "buffer_store_dwordx4 v[40:43], v230, s[40:43], s49 offen offset:2048 sc1\n"
  "v_mfma_f32_16x16x16_bf16 v[72:75], v[168:169], v[176:177], v[72:75]\n"
  "v_mul_f32 v56, v56, v208\n"
  "v_mul_f32 v57, v57, v209\n"
  "v_mfma_f32_16x16x16_bf16 v[76:79], v[168:169], v[178:179], v[76:79]\n"
  "v_mul_f32 v58, v58, v210\n"
  "v_mul_f32 v59, v59, v211\n"
  "v_mfma_f32_16x16x16_bf16 v[92:95], v[170:171], v[178:179], v[92:95]\n"
  "v_mul_f32 v60, v60, v212\n"
  "v_mul_f32 v61, v61, v213\n"
  "v_mfma_f32_16x16x16_bf16 v[88:91], v[170:171], v[176:177], v[88:91]\n"
  "v_mul_f32 v62, v62, v214\n"
  "v_mul_f32 v63, v63, v215\n"
  "v_mfma_f32_16x16x16_bf16 v[84:87], v[170:171], v[174:175], v[84:87]\n"
  "v_cvt_pk_bf16_f32 v56, v56, v57\n"
  "v_cvt_pk_bf16_f32 v57, v58, v59\n"
  "v_mfma_f32_16x16x16_bf16 v[80:83], v[170:171], v[172:173], v[80:83]\n"
  "v_cvt_pk_bf16_f32 v58, v60, v61\n"
  "v_cvt_pk_bf16_f32 v59, v62, v63\n"
  "ds_write_b128 v232, v[56:59] offset:6400\n"
  "buffer_store_dwordx4 v[56:59], v230, s[40:43], s49 offen offset:2304 sc1\n"
  "v_mul_f32 v64, v64, v196\n"
  "v_mul_f32 v65, v65, v196\n"
  "v_mul_f32 v66, v66, v196\n"
  "v_mul_f32 v67, v67, v196\n"
  "v_mul_f32 v80, v80, v196\n"
  "v_mul_f32 v81, v81, v196\n"
  "v_mul_f32 v82, v82, v196\n"
  "v_mul_f32 v83, v83, v196\n"
  "v_cvt_pk_bf16_f32 v64, v64, v65\n"
  "v_cvt_pk_bf16_f32 v65, v66, v67\n"
  "v_cvt_pk_bf16_f32 v66, v80, v81\n"
  "v_cvt_pk_bf16_f32 v67, v82, v83\n"
  "ds_write_b128 v234, v[64:67] offset:0\n"
  "v_mul_f32 v68, v68, v197\n"
  "v_mul_f32 v69, v69, v197\n"
  "v_mul_f32 v70, v70, v197\n"
  "v_mul_f32 v71, v71, v197\n"
  "v_mul_f32 v84, v84, v197\n"
  "v_mul_f32 v85, v85, v197\n"
  "v_mul_f32 v86, v86, v197\n"
  "v_mul_f32 v87, v87, v197\n"
  "v_cvt_pk_bf16_f32 v68, v68, v69\n"
  "v_cvt_pk_bf16_f32 v69, v70, v71\n"
  "v_cvt_pk_bf16_f32 v70, v84, v85\n"
  "v_cvt_pk_bf16_f32 v71, v86, v87\n"
  "ds_write_b128 v234, v[68:71] offset:256\n"
  "v_mul_f32 v72, v72, v198\n"
  "v_mul_f32 v73, v73, v198\n"
  "v_mul_f32 v74, v74, v198\n"
  "v_mul_f32 v75, v75, v198\n"
  "v_mul_f32 v88, v88, v198\n"
  "v_mul_f32 v89, v89, v198\n"
  "v_mul_f32 v90, v90, v198\n"
  "v_mul_f32 v91, v91, v198\n"
  "v_cvt_pk_bf16_f32 v72, v72, v73\n"
  "v_cvt_pk_bf16_f32 v73, v74, v75\n"
  "v_cvt_pk_bf16_f32 v74, v88, v89\n"
  "v_cvt_pk_bf16_f32 v75, v90, v91\n"
  "ds_write_b128 v234, v[72:75] offset:2048\n"
  "v_mul_f32 v76, v76, v199\n"
  "v_mul_f32 v77, v77, v199\n"
  "v_mul_f32 v78, v78, v199\n"
  "v_mul_f32 v79, v79, v199\n"
  "v_mul_f32 v92, v92, v199\n"
  "v_mul_f32 v93, v93, v199\n"
  "v_mul_f32 v94, v94, v199\n"
  "v_mul_f32 v95, v95, v199\n"
  "v_cvt_pk_bf16_f32 v76, v76, v77\n"
  "v_cvt_pk_bf16_f32 v77, v78, v79\n"
  "v_cvt_pk_bf16_f32 v78, v92, v93\n"
  "v_cvt_pk_bf16_f32 v79, v94, v95\n"
  "ds_write_b128 v234, v[76:79] offset:2304\n"
  "; ---- phase 2: attention\n"
  "s_lshl_b32 s9, s90, 14\n"
  "v_and_b32 v240, 0x200, v254\n"
  "v_add_u32 v240, s9, v240\n"
  "s_lshl_b32 s9, s3, 13\n"
  "v_add_u32 v241, s9, v254\n"
  "s_lshl_b32 s9, s3, 12\n"
  "s_add_u32 s9, s9, 32768\n"
  "v_add_u32 v243, s9, v254\n"
  "v_xor_b32 v242, 0x1000, v243\n"
  "s_waitcnt lgkmcnt(0)\n"
  "s_barrier\n"
  "ds_read_b128 v[216:219], v240 offset:0\n"
  "ds_read_b128 v[220:223], v240 offset:1024\n"
  "ds_read_b128 a[0:3], v242 offset:0\n"
  "ds_read_b128 a[4:7], v242 offset:1024\n"
  "ds_read_b128 a[8:11], v242 offset:2048\n"
  "ds_read_b128 a[12:15], v242 offset:3072\n"
  "ds_read_b128 a[16:19], v243 offset:0\n"
  "ds_read_b128 a[20:23], v243 offset:1024\n"
  "ds_read_b128 a[24:27], v243 offset:2048\n"
  "ds_read_b128 a[28:31], v243 offset:3072\n"
  "s_lshl_b32 s8, s92, 1\n"
  "s_add_u32 s8, s8, s91\n"
  "s_mul_i32 s47, s8, 1024\n"
  "s_add_u32 s47, s47, 1196032\n"
  "s_mul_i32 s8, s6, 64\n"
  "s_add_u32 s46, s47, s8\n"
  "s_waitcnt lgkmcnt(8)\n"
  "v_xor_b32 v216, 0x80008000, v216\n"
  "v_xor_b32 v217, 0x80008000, v217\n"
  "v_xor_b32 v218, 0x80008000, v218\n"
  "v_xor_b32 v219, 0x80008000, v219\n"
  "v_xor_b32 v220, 0x80008000, v220\n"
  "v_xor_b32 v221, 0x80008000, v221\n"
  "v_xor_b32 v222, 0x80008000, v222\n"
  "v_xor_b32 v223, 0x80008000, v223\n"
  "s_waitcnt lgkmcnt(6)\n"
  "v_mfma_f32_32x32x16_bf16 v[144:159], v[216:219], a[0:3], 0\n"
  "ds_read_b128 a[32:35], v241 offset:0\n"
  "v_mov_b32 v128, 0\n"
  "v_mov_b32 v129, 0\n"
  "v_mov_b32 v130, 0\n"
  "v_mov_b32 v131, 0\n"
  "v_mov_b32 v132, 0\n"
  "v_mfma_f32_32x32x16_bf16 v[144:159], v[220:223], a[4:7], v[144:159]\n"
  "ds_read_b128 a[36:39], v241 offset:1024\n"
  "v_mov_b32 v133, 0\n"
  "v_mov_b32 v134, 0\n"
  "v_mov_b32 v135, 0\n"
  "v_mov_b32 v136, 0\n"
  "v_mov_b32 v137, 0\n"
  "s_waitcnt lgkmcnt(6)\n"
  "v_mfma_f32_32x32x16_bf16 v[160:175], v[216:219], a[8:11], 0\n"
  "ds_read_b128 a[40:43], v241 offset:2048\n"
  "v_mov_b32 v138, 0\n"
  "v_mov_b32 v139, 0\n"
  "v_mov_b32 v140, 0\n"
  "v_mov_b32 v141, 0\n"
  "v_mov_b32 v142, 0\n"
  "v_mfma_f32_32x32x16_bf16 v[160:175], v[220:223], a[12:15], v[160:175]\n"
  "ds_read_b128 a[44:47], v241 offset:3072\n"
  "v_mov_b32 v143, 0\n"
  "v_bfe_u32 v244, v255, 2, 1\n"
  "v_cmp_eq_u32 vcc, 0, v244\n"
  "s_add_u32 s93, s6, 1\n"
  "v_mov_b32 v245, 0x3f803f80\n"
  "s_waitcnt lgkmcnt(6)\n"
  "v_mfma_f32_32x32x16_bf16 v[176:191], v[216:219], a[16:19], 0\n"
  "ds_read_b128 a[48:51], v241 offset:4096\n"
  "v_cndmask_b32 v208, 0, v245, vcc\n"
  "v_mov_b32 v212, 0xdddd\n"
  "v_mov_b32 v209, v208\n"
  "v_xor_b32 v210, v245, v208\n"
  "v_mov_b32 v211, v210\n"
  "v_mfma_f32_32x32x16_bf16 v[176:191], v[220:223], a[20:23], v[176:191]\n"
  "ds_read_b128 a[52:55], v241 offset:5120\n"
  "v_add_u32 v244, s93, v255\n"
  "v_and_b32 v244, 15, v244\n"
  "v_mul_u32_u24 v214, 64, v244\n"
  "v_lshrrev_b32 v244, 3, v255\n"
  "v_mul_u32_u24 v244, 2048, v244\n"
  "s_waitcnt lgkmcnt(6)\n"
  "v_mfma_f32_32x32x16_bf16 v[192:207], v[216:219], a[24:27], 0\n"
  "ds_read_b128 a[56:59], v241 offset:6144\n"
  "v_lshrrev_b32 v245, 4, v255\n"
  "v_lshlrev_b32 v245, 1, v245\n"
  "v_and_b32 v246, 7, v255\n"
  "v_xor_b32 v245, v245, v246\n"
  "v_lshl_add_u32 v232, v245, 4, v244\n"
  "v_mfma_f32_32x32x16_bf16 v[192:207], v[220:223], a[28:31], v[192:207]\n"
  "ds_read_b128 a[60:63], v241 offset:7168\n"
  "v_add_u32 v233, 15360, v232\n"
  "v_add_u32 v234, 30720, v232\n"
  "v_add_u32 v235, 46080, v232\n"
  "s_waitcnt lgkmcnt(6)\n"
  "v_mfma_f32_32x32x16_bf16 v[0:15], a[32:35], a[0:3], v[144:159]\n"
  "v_mfma_f32_32x32x16_bf16 v[0:15], a[36:39], a[4:7], v[0:15]\n"
  "s_waitcnt lgkmcnt(0)\n"
  "v_mfma_f32_32x32x16_bf16 v[16:31], a[32:35], a[8:11], v[160:175]\n"
  "s_waitcnt vmcnt(0)\n"
  "buffer_store_dwordx4 v[248:251], v251, s[52:55], s46 offen sc1\n"
  "s_nop 7\n"
  "v_exp_f32 v0, v0\n"
  "v_exp_f32 v1, v1\n"
  "v_exp_f32 v2, v2\n"
  "v_mfma_f32_32x32x16_bf16 v[16:31], a[36:39], a[12:15], v[16:31]\n"
  "s_lshl_b32 s13, s91, 13\n"
  "s_lshl_b32 s9, s92, 18\n"
  "v_exp_f32 v3, v3\n"
  "v_exp_f32 v4, v4\n"
  "v_exp_f32 v5, v5\n"
  "v_mfma_f32_32x32x16_bf16 v[64:79], v[128:131], v[128:131], 0\n"
  "s_add_u32 s9, s9, s13\n"
  "s_sub_u32 s15, 0x40000, s13\n"
  "v_exp_f32 v6, v6\n"
  "v_exp_f32 v7, v7\n"
  "v_exp_f32 v8, v8\n"
  "v_mfma_f32_32x32x16_bf16 v[80:95], v[128:131], v[128:131], 0\n"
  "s_add_u32 s32, s30, 8388608\n"
  "s_addc_u32 s33, s31, 0\n"
  "v_exp_f32 v9, v9\n"
  "v_exp_f32 v10, v10\n"
  "v_exp_f32 v11, v11\n"
  "v_mfma_f32_32x32x16_bf16 v[96:111], v[128:131], v[128:131], 0\n"
  "s_add_u32 s32, s32, s9\n"
  "s_addc_u32 s33, s33, 0\n"
  "v_exp_f32 v12, v12\n"
  "v_exp_f32 v13, v13\n"
  "v_mfma_f32_32x32x16_bf16 v[112:127], v[128:131], v[128:131], 0\n"
  "s_pack_ll_b32_b16 s33, s33, 16\n"
  "s_mov_b32 s34, 0\n"
  "v_exp_f32 v14, v14\n"
  "v_exp_f32 v15, v15\n"
  "v_mfma_f32_32x32x16_bf16 v[32:47], a[32:35], a[16:19], v[176:191]\n"
  "s_mov_b32 s35, 0x800000\n"
  "s_and_b32 s8, s93, 15\n"
  "v_exp_f32 v16, v16\n"
  "v_exp_f32 v17, v17\n"
  "v_exp_f32 v18, v18\n"
  "v_mfma_f32_32x32x16_bf16 v[32:47], a[36:39], a[20:23], v[32:47]\n"
  "s_lshl_b32 s44, s8, 14\n"
  "s_mov_b64 s[56:57], s[22:23]\n"
  "v_exp_f32 v19, v19\n"
  "v_exp_f32 v20, v20\n"
  "v_exp_f32 v21, v21\n"
  "v_smfmac_f32_32x32x32_bf16 v[64:79], a[40:43], v[0:7], v212\n"
  "s_add_u32 s58, s22, 3670016\n"
  "s_addc_u32 s59, s23, 0\n"
  "v_exp_f32 v22, v22\n"
  "v_exp_f32 v23, v23\n"
  "v_exp_f32 v24, v24\n"
  "v_smfmac_f32_32x32x32_bf16 v[64:79], a[44:47], v[8:15], v212\n"
  "s_mov_b64 s[60:61], s[24:25]\n"
  "s_add_u32 s62, s24, 3670016\n"
  "s_addc_u32 s63, s25, 0\n"
  "v_exp_f32 v25, v25\n"
  "v_exp_f32 v26, v26\n"
  "v_exp_f32 v27, v27\n"
  "v_smfmac_f32_16x16x64_bf16 v[128:131], v[208:211], v[0:7], v212\n"
  "s_mov_b64 s[64:65], s[26:27]\n"
  "s_add_u32 s66, s26, 3670016\n"
  "s_addc_u32 s67, s27, 0\n"
  "v_exp_f32 v28, v28\n"
  "v_exp_f32 v29, v29\n"
  "v_smfmac_f32_16x16x64_bf16 v[128:131], v[208:211], v[8:15], v212\n"
  "s_mov_b32 s38, 3670016\n"
  "s_mov_b32 s39, 0x20000\n"
  "v_exp_f32 v30, v30\n"
  "v_exp_f32 v31, v31\n"
  "v_mfma_f32_32x32x16_bf16 v[48:63], a[32:35], a[24:27], v[192:207]\n"
  "s_mul_i32 s8, s12, 28\n"
  "v_min_u32 v215, 6, v255\n"
  "v_exp_f32 v32, v32\n"
  "v_exp_f32 v33, v33\n"
  "v_exp_f32 v34, v34\n"
  "v_mfma_f32_32x32x16_bf16 v[48:63], a[36:39], a[28:31], v[48:63]\n"
  "buffer_load_dwordx2 v[224:225], v214, s[52:55], s47 offen sc1\n"
  "v_exp_f32 v35, v35\n"
  "v_exp_f32 v36, v36\n"
  "v_exp_f32 v37, v37\n"
  "v_smfmac_f32_32x32x32_bf16 v[80:95], a[40:43], v[16:23], v212\n"
  "v_add_lshl_u32 v215, s8, v215, 7\n"
  "s_lshl_b32 s8, s4, 1\n"
  "v_exp_f32 v38, v38\n"
  "v_exp_f32 v39, v39\n"
  "v_exp_f32 v40, v40\n"
  "v_smfmac_f32_32x32x32_bf16 v[80:95], a[44:47], v[24:31], v212\n"
  "s_and_b32 s9, s5, 1\n"
  "s_add_u32 s8, s8, s9\n"
  "v_exp_f32 v41, v41\n"
  "v_exp_f32 v42, v42\n"
  "v_exp_f32 v43, v43\n"
  "v_smfmac_f32_16x16x64_bf16 v[132:135], v[208:211], v[16:23], v212\n"
  "s_lshl_b32 s8, s8, 2\n"
  "s_add_u32 s70, s8, s3\n"
  "v_exp_f32 v44, v44\n"
  "v_exp_f32 v45, v45\n"
  "v_smfmac_f32_16x16x64_bf16 v[132:135], v[208:211], v[24:31], v212\n"
  "s_lshr_b32 s71, s5, 1\n"
  "s_lshl_b32 s13, s71, 7\n"
  "v_exp_f32 v46, v46\n"
  "v_exp_f32 v47, v47\n"
  "v_mfma_f32_32x32x16_bf16 v[0:15], a[48:51], a[0:3], v[144:159]\n"
  "s_lshl_b32 s8, s3, 16\n"
  "s_add_u32 s8, s8, s13\n"
  "v_exp_f32 v48, v48\n"
  "v_exp_f32 v49, v49\n"
  "v_exp_f32 v50, v50\n"
  "v_mfma_f32_32x32x16_bf16 v[0:15], a[52:55], a[4:7], v[0:15]\n"
  "s_sub_u32 s15, 2097152, s8\n"
  "s_add_u32 s72, s20, s8\n"
  "s_addc_u32 s73, s21, 0\n"
  "v_exp_f32 v51, v51\n"
  "v_exp_f32 v52, v52\n"
  "v_exp_f32 v53, v53\n"
  "v_smfmac_f32_32x32x32_bf16 v[96:111], a[40:43], v[32:39], v212\n"
  "s_and_b32 s73, s73, 0xffff\n"
  "s_mov_b32 s74, s15\n"
  "v_exp_f32 v54, v54\n"
  "v_exp_f32 v55, v55\n"
  "v_exp_f32 v56, v56\n"
  "v_smfmac_f32_32x32x32_bf16 v[96:111], a[44:47], v[40:47], v212\n"
  "s_mov_b32 s75, 0x20000\n"
  "s_lshr_b32 s8, s71, 1\n"
  "v_exp_f32 v57, v57\n"
  "v_exp_f32 v58, v58\n"
  "v_exp_f32 v59, v59\n"
  "v_smfmac_f32_16x16x64_bf16 v[136:139], v[208:211], v[32:39], v212\n"
  "s_lshl_b32 s68, s8, 18\n"
  "s_lshl_b32 s69, s3, 12\n"
  "v_exp_f32 v60, v60\n"
  "v_exp_f32 v61, v61\n"
  "v_smfmac_f32_16x16x64_bf16 v[136:139], v[208:211], v[40:47], v212\n"
  "v_exp_f32 v62, v62\n"
  "v_exp_f32 v63, v63\n"
  "v_mfma_f32_32x32x16_bf16 v[16:31], a[48:51], a[8:11], v[160:175]\n"
  "s_waitcnt vmcnt(0)\n"
  "L_kp0_%=:\n"
  "v_cmp_ne_u64 vcc, s[10:11], v[224:225]\n"
  "s_and_b32 s89, vcc_lo, 7\n"
  "s_cbranch_scc0 L_kok0_%=\n"
  "s_sub_u32 s99, s99, 1\n"
  "s_cbranch_scc1 L_kout0_%=\n"
  "s_sleep 0\n"
  "buffer_load_dwordx2 v[224:225], v214, s[52:55], s47 offen sc1\n"
  "s_waitcnt vmcnt(0)\n"
  "s_branch L_kp0_%=\n"
  "L_kout0_%=:\n"
  "s_mov_b32 s99, 0\n"
  "L_kok0_%=:\n"
  "buffer_load_dwordx4 a[64:67], off, s[32:35], s44 offset:0 sc1\n"
  "v_exp_f32 v0, v0\n"
  "v_exp_f32 v1, v1\n"
  "v_exp_f32 v2, v2\n"
  "v_mfma_f32_32x32x16_bf16 v[16:31], a[52:55], a[12:15], v[16:31]\n"
  "buffer_load_dwordx4 a[68:71], off, s[32:35], s44 offset:1024 sc1\n"
  "v_exp_f32 v3, v3\n"
  "v_exp_f32 v4, v4\n"
  "v_exp_f32 v5, v5\n"
  "v_smfmac_f32_32x32x32_bf16 v[112:127], a[40:43], v[48:55], v212\n"
  "buffer_load_dwordx4 a[72:75], off, s[32:35], s44 offset:2048 sc1\n"
  "v_exp_f32 v6, v6\n"
  "v_exp_f32 v7, v7\n"
  "v_exp_f32 v8, v8\n"
  "v_smfmac_f32_32x32x32_bf16 v[112:127], a[44:47], v[56:63], v212\n"
  "buffer_load_dwordx4 a[76:79], off, s[32:35], s44 offset:3072 sc1\n"
  "s_add_u32 s44, s44, 0x1000\n"
  "v_exp_f32 v9, v9\n"
  "v_exp_f32 v10, v10\n"
  "v_exp_f32 v11, v11\n"
  "v_smfmac_f32_16x16x64_bf16 v[140:143], v[208:211], v[48:55], v212\n"
  "buffer_load_dwordx4 a[80:83], off, s[32:35], s44 offset:0 sc1\n"
  "v_exp_f32 v12, v12\n"
  "v_exp_f32 v13, v13\n"
  "v_smfmac_f32_16x16x64_bf16 v[140:143], v[208:211], v[56:63], v212\n"
  "v_exp_f32 v14, v14\n"
  "v_exp_f32 v15, v15\n"
  "v_mfma_f32_32x32x16_bf16 v[32:47], a[48:51], a[16:19], v[176:191]\n"
  "v_exp_f32 v16, v16\n"
  "v_exp_f32 v17, v17\n"
  "v_exp_f32 v18, v18\n"
  "v_mfma_f32_32x32x16_bf16 v[32:47], a[52:55], a[20:23], v[32:47]\n"
  "buffer_load_dwordx4 a[84:87], off, s[32:35], s44 offset:1024 sc1\n"
  "v_exp_f32 v19, v19\n"
  "v_exp_f32 v20, v20\n"
  "v_exp_f32 v21, v21\n"
  "v_smfmac_f32_32x32x32_bf16 v[64:79], a[56:59], v[0:7], v212\n"
  "buffer_load_dwordx4 a[88:91], off, s[32:35], s44 offset:2048 sc1\n"
  "v_exp_f32 v22, v22\n"
  "v_exp_f32 v23, v23\n"
  "v_exp_f32 v24, v24\n"
  "v_smfmac_f32_32x32x32_bf16 v[64:79], a[60:63], v[8:15], v212\n"
  "buffer_load_dwordx4 a[92:95], off, s[32:35], s44 offset:3072 sc1\n"
  "s_add_u32 s44, s44, 0x3000\n"
  "s_and_b32 s44, s44, 0x3ffff\n"
  "v_exp_f32 v25, v25\n"
  "v_exp_f32 v26, v26\n"
  "v_exp_f32 v27, v27\n"
  "v_smfmac_f32_16x16x64_bf16 v[128:131], v[208:211], v[0:7], v212\n"
  "v_exp_f32 v28, v28\n"
  "v_exp_f32 v29, v29\n"
  "v_smfmac_f32_16x16x64_bf16 v[128:131], v[208:211], v[8:15], v212\n"
  "v_exp_f32 v30, v30\n"
  "v_exp_f32 v31, v31\n"
  "v_mfma_f32_32x32x16_bf16 v[48:63], a[48:51], a[24:27], v[192:207]\n"
  "v_exp_f32 v32, v32\n"
  "v_exp_f32 v33, v33\n"
  "v_exp_f32 v34, v34\n"
  "v_mfma_f32_32x32x16_bf16 v[48:63], a[52:55], a[28:31], v[48:63]\n"
  "buffer_load_dwordx4 a[32:35], off, s[32:35], s44 offset:0 sc1\n"
  "v_exp_f32 v35, v35\n"
  "v_exp_f32 v36, v36\n"
  "v_exp_f32 v37, v37\n"
  "v_smfmac_f32_32x32x32_bf16 v[80:95], a[56:59], v[16:23], v212\n"
  "buffer_load_dwordx4 a[36:39], off, s[32:35], s44 offset:1024 sc1\n"
  "v_exp_f32 v38, v38\n"
  "v_exp_f32 v39, v39\n"
  "v_exp_f32 v40, v40\n"
  "v_smfmac_f32_32x32x32_bf16 v[80:95], a[60:63], v[24:31], v212\n"
  "v_exp_f32 v41, v41\n"
  "v_exp_f32 v42, v42\n"
  "v_exp_f32 v43, v43\n"
  "v_smfmac_f32_16x16x64_bf16 v[132:135], v[208:211], v[16:23], v212\n"
  "v_exp_f32 v44, v44\n"
  "v_exp_f32 v45, v45\n"
  "v_smfmac_f32_16x16x64_bf16 v[132:135], v[208:211], v[24:31], v212\n"
  "s_waitcnt vmcnt(8)\n"
  "v_exp_f32 v46, v46\n"
  "v_exp_f32 v47, v47\n"
  "v_mfma_f32_32x32x16_bf16 v[0:15], a[64:67], a[0:3], v[144:159]\n"
  "v_exp_f32 v48, v48\n"
  "v_exp_f32 v49, v49\n"
  "v_exp_f32 v50, v50\n"
  "v_mfma_f32_32x32x16_bf16 v[0:15], a[68:71], a[4:7], v[0:15]\n"
  "buffer_load_dwordx4 a[40:43], off, s[32:35], s44 offset:2048 sc1\n"
  "v_exp_f32 v51, v51\n"
  "v_exp_f32 v52, v52\n"
  "v_exp_f32 v53, v53\n"
  "v_smfmac_f32_32x32x32_bf16 v[96:111], a[56:59], v[32:39], v212\n"
  "buffer_load_dwordx4 a[44:47], off, s[32:35], s44 offset:3072 sc1\n"
  "s_add_u32 s44, s44, 0x1000\n"
  "v_exp_f32 v54, v54\n"
  "v_exp_f32 v55, v55\n"
  "v_exp_f32 v56, v56\n"
  "v_smfmac_f32_32x32x32_bf16 v[96:111], a[60:63], v[40:47], v212\n"
  "v_exp_f32 v57, v57\n"
  "v_exp_f32 v58, v58\n"
  "v_exp_f32 v59, v59\n"
  "v_smfmac_f32_16x16x64_bf16 v[136:139], v[208:211], v[32:39], v212\n"
  "v_exp_f32 v60, v60\n"
  "v_exp_f32 v61, v61\n"
  "v_smfmac_f32_16x16x64_bf16 v[136:139], v[208:211], v[40:47], v212\n"
  "v_exp_f32 v62, v62\n"
  "v_exp_f32 v63, v63\n"
  "v_smfmac_f32_16x16x64_bf16 v[140:143], v[208:211], v[48:55], v212\n"
  "v_exp_f32 v0, v0\n"
  "v_exp_f32 v1, v1\n"
  "v_smfmac_f32_16x16x64_bf16 v[140:143], v[208:211], v[56:63], v212\n"
  "buffer_load_dwordx4 a[48:51], off, s[32:35], s44 offset:0 sc1\n"
  "v_exp_f32 v2, v2\n"
  "v_exp_f32 v3, v3\n"
  "v_mfma_f32_32x32x16_bf16 v[16:31], a[64:67], a[8:11], v[160:175]\n"
  "v_exp_f32 v4, v4\n"
  "v_exp_f32 v5, v5\n"
  "v_exp_f32 v6, v6\n"
  "v_mfma_f32_32x32x16_bf16 v[16:31], a[68:71], a[12:15], v[16:31]\n"
  "buffer_load_dwordx4 a[96:99], v232, s[84:87], 0 offen offset:0\n"
  "v_exp_f32 v7, v7\n"
  "v_exp_f32 v8, v8\n"
  "v_exp_f32 v9, v9\n"
  "v_smfmac_f32_32x32x32_bf16 v[112:127], a[56:59], v[48:55], v212\n"
  "v_exp_f32 v10, v10\n"
  "v_exp_f32 v11, v11\n"
  "v_exp_f32 v12, v12\n"
  "v_smfmac_f32_32x32x32_bf16 v[112:127], a[60:63], v[56:63], v212\n"
  "v_exp_f32 v13, v13\n"
  "v_exp_f32 v14, v14\n"
  "v_exp_f32 v15, v15\n"
  "v_smfmac_f32_16x16x64_bf16 v[128:131], v[208:211], v[0:7], v212\n"
  "s_waitcnt vmcnt(10)\n"
  "s_barrier\n"
  "v_exp_f32 v16, v16\n"
  "v_exp_f32 v17, v17\n"
  "v_smfmac_f32_16x16x64_bf16 v[128:131], v[208:211], v[8:15], v212\n"
  "buffer_load_dwordx4 a[52:55], off, s[32:35], s44 offset:1024 sc1\n"
  "v_exp_f32 v18, v18\n"
  "v_exp_f32 v19, v19\n"
  "v_mfma_f32_32x32x16_bf16 v[32:47], a[64:67], a[16:19], v[176:191]\n"
  "v_exp_f32 v20, v20\n"
  "v_exp_f32 v21, v21\n"
  "v_exp_f32 v22, v22\n"
  "v_mfma_f32_32x32x16_bf16 v[32:47], a[68:71], a[20:23], v[32:47]\n"
  "buffer_load_dwordx4 a[100:103], v233, s[84:87], 0 offen offset:1024\n"
  "v_exp_f32 v23, v23\n"
  "v_exp_f32 v24, v24\n"
  "v_exp_f32 v25, v25\n"
  "v_smfmac_f32_32x32x32_bf16 v[64:79], a[72:75], v[0:7], v212\n"
  "v_exp_f32 v26, v26\n"
  "v_exp_f32 v27, v27\n"
  "v_exp_f32 v28, v28\n"
  "v_smfmac_f32_32x32x32_bf16 v[64:79], a[76:79], v[8:15], v212\n"
  "v_exp_f32 v29, v29\n"
  "v_exp_f32 v30, v30\n"
  "v_exp_f32 v31, v31\n"
  "v_smfmac_f32_16x16x64_bf16 v[132:135], v[208:211], v[16:23], v212\n"
  "v_exp_f32 v32, v32\n"
  "v_exp_f32 v33, v33\n"
  "v_smfmac_f32_16x16x64_bf16 v[132:135], v[208:211], v[24:31], v212\n"
  "buffer_load_dwordx4 a[56:59], off, s[32:35], s44 offset:2048 sc1\n"
  "v_exp_f32 v34, v34\n"
  "v_exp_f32 v35, v35\n"
  "v_mfma_f32_32x32x16_bf16 v[48:63], a[64:67], a[24:27], v[192:207]\n"
  "v_exp_f32 v36, v36\n"
  "v_exp_f32 v37, v37\n"
  "v_exp_f32 v38, v38\n"
  "v_mfma_f32_32x32x16_bf16 v[48:63], a[68:71], a[28:31], v[48:63]\n"
  "v_exp_f32 v39, v39\n"
  "v_exp_f32 v40, v40\n"
  "v_exp_f32 v41, v41\n"
  "v_smfmac_f32_32x32x32_bf16 v[80:95], a[72:75], v[16:23], v212\n"
  "v_exp_f32 v42, v42\n"
  "v_exp_f32 v43, v43\n"
  "v_exp_f32 v44, v44\n"
  "v_smfmac_f32_32x32x32_bf16 v[80:95], a[76:79], v[24:31], v212\n"
  "s_waitcnt vmcnt(11)\n"
  "v_exp_f32 v45, v45\n"
  "v_exp_f32 v46, v46\n"
  "v_exp_f32 v47, v47\n"
  "v_smfmac_f32_16x16x64_bf16 v[136:139], v[208:211], v[32:39], v212\n"
  "v_exp_f32 v48, v48\n"
  "v_exp_f32 v49, v49\n"
  "v_smfmac_f32_16x16x64_bf16 v[136:139], v[208:211], v[40:47], v212\n"
  "buffer_load_dwordx4 a[60:63], off, s[32:35], s44 offset:3072 sc1\n"
  "v_exp_f32 v50, v50\n"
  "v_exp_f32 v51, v51\n"
  "v_mfma_f32_32x32x16_bf16 v[0:15], a[80:83], a[0:3], v[144:159]\n"
  "s_add_u32 s44, s44, 0x3000\n"
  "v_exp_f32 v52, v52\n"
  "v_exp_f32 v53, v53\n"
  "v_exp_f32 v54, v54\n"
  "v_mfma_f32_32x32x16_bf16 v[0:15], a[84:87], a[4:7], v[0:15]\n"
  "s_and_b32 s44, s44, 0x3ffff\n"
  "v_exp_f32 v55, v55\n"
  "v_exp_f32 v56, v56\n"
  "v_exp_f32 v57, v57\n"
  "v_smfmac_f32_32x32x32_bf16 v[96:111], a[72:75], v[32:39], v212\n"
  "buffer_load_dwordx2 v[224:225], v214, s[52:55], s47 offen sc1\n"
  "v_exp_f32 v58, v58\n"
  "v_exp_f32 v59, v59\n"
  "v_exp_f32 v60, v60\n"
  "v_smfmac_f32_32x32x32_bf16 v[96:111], a[76:79], v[40:47], v212\n"
  "v_exp_f32 v61, v61\n"
  "v_exp_f32 v62, v62\n"
  "v_exp_f32 v63, v63\n"
  "v_smfmac_f32_16x16x64_bf16 v[140:143], v[208:211], v[48:55], v212\n"
  "v_exp_f32 v0, v0\n"
  "v_exp_f32 v1, v1\n"
  "v_smfmac_f32_16x16x64_bf16 v[140:143], v[208:211], v[56:63], v212\n"
  "buffer_load_dwordx4 a[64:67], off, s[32:35], s44 offset:0 sc1\n"
  "v_exp_f32 v2, v2\n"
  "v_exp_f32 v3, v3\n"
  "v_mfma_f32_32x32x16_bf16 v[16:31], a[80:83], a[8:11], v[160:175]\n"
  "v_exp_f32 v4, v4\n"
  "v_exp_f32 v5, v5\n"
  "v_exp_f32 v6, v6\n"
  "v_mfma_f32_32x32x16_bf16 v[16:31], a[84:87], a[12:15], v[16:31]\n"
  "buffer_load_dwordx4 a[104:107], v234, s[84:87], 0 offen offset:2048\n"
  "v_exp_f32 v7, v7\n"
  "v_exp_f32 v8, v8\n"
  "v_exp_f32 v9, v9\n"
  "v_smfmac_f32_32x32x32_bf16 v[112:127], a[72:75], v[48:55], v212\n"
  "v_exp_f32 v10, v10\n"
  "v_exp_f32 v11, v11\n"
  "v_exp_f32 v12, v12\n"
  "v_smfmac_f32_32x32x32_bf16 v[112:127], a[76:79], v[56:63], v212\n"
  "v_exp_f32 v13, v13\n"
  "v_exp_f32 v14, v14\n"
  "v_exp_f32 v15, v15\n"
  "v_smfmac_f32_16x16x64_bf16 v[128:131], v[208:211], v[0:7], v212\n"
  "s_waitcnt vmcnt(13)\n"
  "v_exp_f32 v16, v16\n"
  "v_exp_f32 v17, v17\n"
  "v_smfmac_f32_16x16x64_bf16 v[128:131], v[208:211], v[8:15], v212\n"
  "buffer_load_dwordx4 a[68:71], off, s[32:35], s44 offset:1024 sc1\n"
  "v_exp_f32 v18, v18\n"
  "v_exp_f32 v19, v19\n"
  "v_mfma_f32_32x32x16_bf16 v[32:47], a[80:83], a[16:19], v[176:191]\n"
  "v_exp_f32 v20, v20\n"
  "v_exp_f32 v21, v21\n"
  "v_exp_f32 v22, v22\n"
  "v_mfma_f32_32x32x16_bf16 v[32:47], a[84:87], a[20:23], v[32:47]\n"
  "buffer_load_dwordx4 a[108:111], v235, s[84:87], 0 offen offset:3072\n"
  "v_exp_f32 v23, v23\n"
  "v_exp_f32 v24, v24\n"
  "v_exp_f32 v25, v25\n"
  "v_smfmac_f32_32x32x32_bf16 v[64:79], a[88:91], v[0:7], v212\n"
  "s_waitcnt vmcnt(4)\n"
  "L_kp1_%=:\n"
  "v_cmp_ne_u64 vcc, s[10:11], v[224:225]\n"
  "s_and_b32 s89, vcc_lo, 0x7fff\n"
  "s_cbranch_scc0 L_kok1_%=\n"
  "s_sub_u32 s99, s99, 1\n"
  "s_cbranch_scc1 L_kout1_%=\n"
  "s_sleep 0\n"
  "buffer_load_dwordx2 v[224:225], v214, s[52:55], s47 offen sc1\n"
  "s_waitcnt vmcnt(0)\n"
  "s_branch L_kp1_%=\n"
  "L_kout1_%=:\n"
  "s_mov_b32 s99, 0\n"
  "L_kok1_%=:\n"
  "buffer_load_dwordx4 a[112:115], v254, s[80:83], 0 offen offset:0\n"
  "buffer_load_dwordx4 a[116:119], v254, s[80:83], 0 offen offset:1024\n"
  "v_exp_f32 v26, v26\n"
  "v_exp_f32 v27, v27\n"
  "v_exp_f32 v28, v28\n"
  "v_smfmac_f32_32x32x32_bf16 v[64:79], a[92:95], v[8:15], v212\n"
  "v_exp_f32 v29, v29\n"
  "v_exp_f32 v30, v30\n"
  "v_exp_f32 v31, v31\n"
  "s_mov_b32 s98, 7\n"
  "L_m_0_%=:\n"
  "v_smfmac_f32_16x16x64_bf16 v[132:135], v[208:211], v[16:23], v212\n"
  "s_lshl_b32 s8, s6, 2\n"
  "v_exp_f32 v32, v32\n"
  "v_exp_f32 v33, v33\n"
  "v_smfmac_f32_16x16x64_bf16 v[132:135], v[208:211], v[24:31], v212\n"
  "buffer_load_dwordx4 a[72:75], off, s[32:35], s44 offset:2048 sc1\n"
  "v_exp_f32 v34, v34\n"
  "v_exp_f32 v35, v35\n"
  "v_mfma_f32_32x32x16_bf16 v[48:63], a[80:83], a[24:27], v[192:207]\n"
  "s_lshl_b32 s9, s91, 1\n"
  "v_exp_f32 v36, v36\n"
  "v_exp_f32 v37, v37\n"
  "v_exp_f32 v38, v38\n"
  "v_mfma_f32_32x32x16_bf16 v[48:63], a[84:87], a[28:31], v[48:63]\n"
  "s_add_u32 s8, s8, s9\n"
  "v_exp_f32 v39, v39\n"
  "v_exp_f32 v40, v40\n"
  "v_exp_f32 v41, v41\n"
  "v_smfmac_f32_32x32x32_bf16 v[80:95], a[88:91], v[16:23], v212\n"
  "s_lshl_b32 s8, s8, 16\n"
  "v_exp_f32 v42, v42\n"
  "v_exp_f32 v43, v43\n"
  "v_exp_f32 v44, v44\n"
  "v_smfmac_f32_32x32x32_bf16 v[80:95], a[92:95], v[24:31], v212\n"
  "s_waitcnt vmcnt(15)\n"
  "v_exp_f32 v45, v45\n"
  "v_exp_f32 v46, v46\n"
  "v_exp_f32 v47, v47\n"
  "v_smfmac_f32_16x16x64_bf16 v[136:139], v[208:211], v[32:39], v212\n"
  "s_lshl_b32 s9, s92, 11\n"
  "v_exp_f32 v48, v48\n"
  "v_exp_f32 v49, v49\n"
  "v_smfmac_f32_16x16x64_bf16 v[136:139], v[208:211], v[40:47], v212\n"
  "buffer_load_dwordx4 a[76:79], off, s[32:35], s44 offset:3072 sc1\n"
  "v_exp_f32 v50, v50\n"
  "v_exp_f32 v51, v51\n"
  "v_mfma_f32_32x32x16_bf16 v[0:15], a[32:35], a[0:3], v[144:159]\n"
  "s_add_u32 s44, s44, 0x1000\n"
  "v_exp_f32 v52, v52\n"
  "v_exp_f32 v53, v53\n"
  "v_exp_f32 v54, v54\n"
  "v_mfma_f32_32x32x16_bf16 v[0:15], a[36:39], a[4:7], v[0:15]\n"
  "s_add_u32 s13, s8, s9\n"
  "v_exp_f32 v55, v55\n"
  "v_exp_f32 v56, v56\n"
  "v_exp_f32 v57, v57\n"
  "v_smfmac_f32_32x32x32_bf16 v[96:111], a[88:91], v[32:39], v212\n"
  "s_add_u32 s40, s30, 16777216\n"
  "s_addc_u32 s41, s31, 0\n"
  "v_exp_f32 v58, v58\n"
  "v_exp_f32 v59, v59\n"
  "v_exp_f32 v60, v60\n"
  "v_smfmac_f32_32x32x32_bf16 v[96:111], a[92:95], v[40:47], v212\n"
  "s_add_u32 s40, s40, s13\n"
  "s_addc_u32 s41, s41, 0\n"
  "v_exp_f32 v61, v61\n"
  "v_exp_f32 v62, v62\n"
  "v_exp_f32 v63, v63\n"
  "v_smfmac_f32_16x16x64_bf16 v[140:143], v[208:211], v[48:55], v212\n"
  "s_and_b32 s41, s41, 0xffff\n"
  "v_exp_f32 v0, v0\n"
  "v_exp_f32 v1, v1\n"
  "v_smfmac_f32_16x16x64_bf16 v[140:143], v[208:211], v[56:63], v212\n"
  "buffer_load_dwordx4 a[80:83], off, s[32:35], s44 offset:0 sc1\n"
  "v_exp_f32 v2, v2\n"
  "v_exp_f32 v3, v3\n"
  "v_mfma_f32_32x32x16_bf16 v[16:31], a[32:35], a[8:11], v[160:175]\n"
  "s_mov_b32 s36, s56\n"
  "v_exp_f32 v4, v4\n"
  "v_exp_f32 v5, v5\n"
  "v_exp_f32 v6, v6\n"
  "v_mfma_f32_32x32x16_bf16 v[16:31], a[36:39], a[12:15], v[16:31]\n"
  "s_and_b32 s37, s57, 0xffff\n"
  "v_exp_f32 v7, v7\n"
  "v_exp_f32 v8, v8\n"
  "v_exp_f32 v9, v9\n"
  "v_smfmac_f32_32x32x32_bf16 v[112:127], a[88:91], v[48:55], v212\n"
  "buffer_load_dword a124, v215, s[36:39], 0 offen offset:0\n"
  "v_exp_f32 v10, v10\n"
  "v_exp_f32 v11, v11\n"
  "v_exp_f32 v12, v12\n"
  "v_smfmac_f32_32x32x32_bf16 v[112:127], a[92:95], v[56:63], v212\n"
  "s_mov_b32 s42, 67584\n"
  "v_exp_f32 v13, v13\n"
  "v_exp_f32 v14, v14\n"
  "v_exp_f32 v15, v15\n"
  "v_smfmac_f32_16x16x64_bf16 v[128:131], v[208:211], v[0:7], v212\n"
  "s_waitcnt vmcnt(15)\n"
  "v_exp_f32 v16, v16\n"
  "v_exp_f32 v17, v17\n"
  "v_smfmac_f32_16x16x64_bf16 v[128:131], v[208:211], v[8:15], v212\n"
  "buffer_load_dwordx4 a[84:87], off, s[32:35], s44 offset:1024 sc1\n"
  "v_exp_f32 v18, v18\n"
  "v_exp_f32 v19, v19\n"
  "v_mfma_f32_32x32x16_bf16 v[32:47], a[32:35], a[16:19], v[176:191]\n"
  "s_mov_b64 s[56:57], s[58:59]\n"
  "s_mov_b64 s[58:59], s[60:61]\n"
  "v_exp_f32 v20, v20\n"
  "v_exp_f32 v21, v21\n"
  "v_exp_f32 v22, v22\n"
  "v_mfma_f32_32x32x16_bf16 v[32:47], a[36:39], a[20:23], v[32:47]\n"
  "s_mov_b64 s[60:61], s[62:63]\n"
  "v_exp_f32 v23, v23\n"
  "v_exp_f32 v24, v24\n"
  "v_exp_f32 v25, v25\n"
  "v_smfmac_f32_32x32x32_bf16 v[64:79], a[40:43], v[0:7], v212\n"
  "s_mov_b64 s[62:63], s[64:65]\n"
  "v_exp_f32 v26, v26\n"
  "v_exp_f32 v27, v27\n"
  "v_exp_f32 v28, v28\n"
  "v_smfmac_f32_32x32x32_bf16 v[64:79], a[44:47], v[8:15], v212\n"
  "s_mov_b32 s43, 0x20000\n"
  "v_exp_f32 v29, v29\n"
  "v_exp_f32 v30, v30\n"
  "v_exp_f32 v31, v31\n"
  "v_smfmac_f32_16x16x64_bf16 v[132:135], v[208:211], v[16:23], v212\n"
  "s_mov_b64 s[64:65], s[66:67]\n"
  "v_exp_f32 v32, v32\n"
  "v_exp_f32 v33, v33\n"
  "v_smfmac_f32_16x16x64_bf16 v[132:135], v[208:211], v[24:31], v212\n"
  "buffer_load_dwordx4 a[88:91], off, s[32:35], s44 offset:2048 sc1\n"
  "v_exp_f32 v34, v34\n"
  "v_exp_f32 v35, v35\n"
  "v_mfma_f32_32x32x16_bf16 v[48:63], a[32:35], a[24:27], v[192:207]\n"
  "s_mov_b32 s45, 0x10000\n"
  "v_exp_f32 v36, v36\n"
  "v_exp_f32 v37, v37\n"
  "v_exp_f32 v38, v38\n"
  "v_mfma_f32_32x32x16_bf16 v[48:63], a[36:39], a[28:31], v[48:63]\n"
  "s_mul_i32 s14, s3, 9216\n"
  "v_exp_f32 v39, v39\n"
  "v_exp_f32 v40, v40\n"
  "v_exp_f32 v41, v41\n"
  "v_smfmac_f32_32x32x32_bf16 v[80:95], a[40:43], v[16:23], v212\n"
  "s_add_u32 s14, s14, 114688\n"
  "v_exp_f32 v42, v42\n"
  "v_exp_f32 v43, v43\n"
  "v_exp_f32 v44, v44\n"
  "v_smfmac_f32_32x32x32_bf16 v[80:95], a[44:47], v[24:31], v212\n"
  "s_waitcnt vmcnt(13)\n"
  "v_exp_f32 v45, v45\n"
  "v_exp_f32 v46, v46\n"
  "v_exp_f32 v47, v47\n"
  "v_smfmac_f32_16x16x64_bf16 v[136:139], v[208:211], v[32:39], v212\n"
  "s_xor_b32 s15, s3, 1\n"
  "v_exp_f32 v48, v48\n"
  "v_exp_f32 v49, v49\n"
  "v_smfmac_f32_16x16x64_bf16 v[136:139], v[208:211], v[40:47], v212\n"
  "buffer_load_dwordx4 a[92:95], off, s[32:35], s44 offset:3072 sc1\n"
  "v_exp_f32 v50, v50\n"
  "v_exp_f32 v51, v51\n"
  "v_mfma_f32_32x32x16_bf16 v[0:15], a[48:51], a[0:3], v[144:159]\n"
  "s_add_u32 s44, s44, 0x3000\n"
  "v_exp_f32 v52, v52\n"
  "v_exp_f32 v53, v53\n"
  "v_exp_f32 v54, v54\n"
  "v_mfma_f32_32x32x16_bf16 v[0:15], a[52:55], a[4:7], v[0:15]\n"
  "s_and_b32 s44, s44, 0x3ffff\n"
  "v_exp_f32 v55, v55\n"
  "v_exp_f32 v56, v56\n"
  "v_exp_f32 v57, v57\n"
  "v_smfmac_f32_32x32x32_bf16 v[96:111], a[40:43], v[32:39], v212\n"
  "s_cmp_eq_u32 s98, 1\n"
  "s_cselect_b32 s35, 0x20000, s35\n"
  "v_exp_f32 v58, v58\n"
  "v_exp_f32 v59, v59\n"
  "v_exp_f32 v60, v60\n"
  "v_smfmac_f32_32x32x32_bf16 v[96:111], a[44:47], v[40:47], v212\n"
  "s_mul_i32 s15, s15, 9216\n"
  "v_exp_f32 v61, v61\n"
  "v_exp_f32 v62, v62\n"
  "v_exp_f32 v63, v63\n"
  "v_smfmac_f32_16x16x64_bf16 v[140:143], v[208:211], v[48:55], v212\n"
  "s_add_u32 s15, s15, 114688\n"
  "v_exp_f32 v0, v0\n"
  "v_exp_f32 v1, v1\n"
  "v_smfmac_f32_16x16x64_bf16 v[140:143], v[208:211], v[56:63], v212\n"
  "buffer_load_dwordx4 a[32:35], off, s[32:35], s44 offset:0 sc1\n"
  "v_exp_f32 v2, v2\n"
  "v_exp_f32 v3, v3\n"
  "v_mfma_f32_32x32x16_bf16 v[16:31], a[48:51], a[8:11], v[160:175]\n"
  "s_mov_b32 m0, s69\n"
  "v_exp_f32 v4, v4\n"
  "v_exp_f32 v5, v5\n"
  "v_exp_f32 v6, v6\n"
  "v_mfma_f32_32x32x16_bf16 v[16:31], a[52:55], a[12:15], v[16:31]\n"
  "s_add_u32 s69, s69, 0x4000\n"
  "v_exp_f32 v7, v7\n"
  "v_exp_f32 v8, v8\n"
  "v_exp_f32 v9, v9\n"
  "v_smfmac_f32_32x32x32_bf16 v[112:127], a[40:43], v[48:55], v212\n"
  "buffer_load_dwordx4 v232, s[72:75], s68 offen offset:0 lds\n"
  "v_exp_f32 v10, v10\n"
  "v_exp_f32 v11, v11\n"
  "v_exp_f32 v12, v12\n"
  "v_smfmac_f32_32x32x32_bf16 v[112:127], a[44:47], v[56:63], v212\n"
  "s_lshr_b32 s50, s70, 1\n"
  "v_exp_f32 v13, v13\n"
  "v_exp_f32 v14, v14\n"
  "v_exp_f32 v15, v15\n"
  "v_smfmac_f32_16x16x64_bf16 v[128:131], v[208:211], v[0:7], v212\n"
  "s_waitcnt vmcnt(13)\n"
  "v_exp_f32 v16, v16\n"
  "v_exp_f32 v17, v17\n"
  "v_smfmac_f32_16x16x64_bf16 v[128:131], v[208:211], v[8:15], v212\n"
  "buffer_load_dwordx4 a[36:39], off, s[32:35], s44 offset:1024 sc1\n"
  "v_exp_f32 v18, v18\n"
  "v_exp_f32 v19, v19\n"
  "v_mfma_f32_32x32x16_bf16 v[32:47], a[48:51], a[16:19], v[176:191]\n"
  "s_lshl_b32 s50, s50, 5\n"
  "v_exp_f32 v20, v20\n"
  "v_exp_f32 v21, v21\n"
  "v_exp_f32 v22, v22\n"
  "v_mfma_f32_32x32x16_bf16 v[32:47], a[52:55], a[20:23], v[32:47]\n"
  "s_add_u32 s50, s50, s91\n"
  "v_exp_f32 v23, v23\n"
  "v_exp_f32 v24, v24\n"
  "v_exp_f32 v25, v25\n"
  "v_smfmac_f32_32x32x32_bf16 v[64:79], a[56:59], v[0:7], v212\n"
  "buffer_load_dword a125, v215, s[36:39], 0 offen offset:896\n"
  "v_exp_f32 v26, v26\n"
  "v_exp_f32 v27, v27\n"
  "v_exp_f32 v28, v28\n"
  "v_smfmac_f32_32x32x32_bf16 v[64:79], a[60:63], v[8:15], v212\n"
  "s_lshl_b32 s50, s50, 3\n"
  "v_exp_f32 v29, v29\n"
  "v_exp_f32 v30, v30\n"
  "v_exp_f32 v31, v31\n"
  "v_smfmac_f32_16x16x64_bf16 v[132:135], v[208:211], v[16:23], v212\n"
  "s_lshl_b32 s8, s7, 13\n"
  "v_exp_f32 v32, v32\n"
  "v_exp_f32 v33, v33\n"
  "v_smfmac_f32_16x16x64_bf16 v[132:135], v[208:211], v[24:31], v212\n"
  "buffer_load_dwordx4 a[40:43], off, s[32:35], s44 offset:2048 sc1\n"
  "v_exp_f32 v34, v34\n"
  "v_exp_f32 v35, v35\n"
  "v_mfma_f32_32x32x16_bf16 v[48:63], a[48:51], a[24:27], v[192:207]\n"
  "s_add_u32 s50, s50, s8\n"
  "v_exp_f32 v36, v36\n"
  "v_exp_f32 v37, v37\n"
  "v_exp_f32 v38, v38\n"
  "v_mfma_f32_32x32x16_bf16 v[48:63], a[52:55], a[28:31], v[48:63]\n"
  "s_lshl_b32 s8, s3, 12\n"
  "v_exp_f32 v39, v39\n"
  "v_exp_f32 v40, v40\n"
  "v_exp_f32 v41, v41\n"
  "v_smfmac_f32_32x32x32_bf16 v[80:95], a[56:59], v[16:23], v212\n"
  "s_add_u32 s51, s8, 1179648\n"
  "v_exp_f32 v42, v42\n"
  "v_exp_f32 v43, v43\n"
  "v_exp_f32 v44, v44\n"
  "v_smfmac_f32_32x32x32_bf16 v[80:95], a[60:63], v[24:31], v212\n"
  "s_waitcnt vmcnt(13)\n"
  "v_exp_f32 v45, v45\n"
  "v_exp_f32 v46, v46\n"
  "v_exp_f32 v47, v47\n"
  "v_smfmac_f32_16x16x64_bf16 v[136:139], v[208:211], v[32:39], v212\n"
  "v_exp_f32 v48, v48\n"
  "v_exp_f32 v49, v49\n"
  "v_smfmac_f32_16x16x64_bf16 v[136:139], v[208:211], v[40:47], v212\n"
  "buffer_load_dwordx4 a[44:47], off, s[32:35], s44 offset:3072 sc1\n"
  "v_exp_f32 v50, v50\n"
  "v_exp_f32 v51, v51\n"
  "v_mfma_f32_32x32x16_bf16 v[0:15], a[64:67], a[0:3], v[144:159]\n"
  "s_add_u32 s44, s44, 0x1000\n"
  "v_exp_f32 v52, v52\n"
  "v_exp_f32 v53, v53\n"
  "v_exp_f32 v54, v54\n"
  "v_mfma_f32_32x32x16_bf16 v[0:15], a[68:71], a[4:7], v[0:15]\n"
  "v_exp_f32 v55, v55\n"
  "v_exp_f32 v56, v56\n"
  "v_exp_f32 v57, v57\n"
  "v_smfmac_f32_32x32x32_bf16 v[96:111], a[56:59], v[32:39], v212\n"
  "buffer_load_dwordx4 v233, s[72:75], s68 offen offset:1024 lds\n"
  "v_exp_f32 v58, v58\n"
  "v_exp_f32 v59, v59\n"
  "v_exp_f32 v60, v60\n"
  "v_smfmac_f32_32x32x32_bf16 v[96:111], a[60:63], v[40:47], v212\n"
  "v_exp_f32 v61, v61\n"
  "v_exp_f32 v62, v62\n"
  "v_exp_f32 v63, v63\n"
  "v_smfmac_f32_16x16x64_bf16 v[140:143], v[208:211], v[48:55], v212\n"
  "v_exp_f32 v0, v0\n"
  "v_exp_f32 v1, v1\n"
  "v_smfmac_f32_16x16x64_bf16 v[140:143], v[208:211], v[56:63], v212\n"
  "buffer_load_dwordx4 a[48:51], off, s[32:35], s44 offset:0 sc1\n"
  "v_exp_f32 v2, v2\n"
  "v_exp_f32 v3, v3\n"
  "v_mfma_f32_32x32x16_bf16 v[16:31], a[64:67], a[8:11], v[160:175]\n"
  "v_exp_f32 v4, v4\n"
  "v_exp_f32 v5, v5\n"
  "v_exp_f32 v6, v6\n"
  "v_mfma_f32_32x32x16_bf16 v[16:31], a[68:71], a[12:15], v[16:31]\n"
  "v_exp_f32 v7, v7\n"
  "v_exp_f32 v8, v8\n"
  "v_exp_f32 v9, v9\n"
  "v_smfmac_f32_32x32x32_bf16 v[112:127], a[56:59], v[48:55], v212\n"
  "buffer_load_dword a126, v215, s[36:39], 0 offen offset:1792\n"
  "v_exp_f32 v10, v10\n"
  "v_exp_f32 v11, v11\n"
  "v_exp_f32 v12, v12\n"
  "v_smfmac_f32_32x32x32_bf16 v[112:127], a[60:63], v[56:63], v212\n"
  "v_exp_f32 v13, v13\n"
  "v_exp_f32 v14, v14\n"
  "v_exp_f32 v15, v15\n"
  "v_smfmac_f32_16x16x64_bf16 v[128:131], v[208:211], v[0:7], v212\n"
  "s_waitcnt vmcnt(14)\n"
  "v_exp_f32 v16, v16\n"
  "v_exp_f32 v17, v17\n"
  "v_smfmac_f32_16x16x64_bf16 v[128:131], v[208:211], v[8:15], v212\n"
  "buffer_load_dwordx4 a[52:55], off, s[32:35], s44 offset:1024 sc1\n"
  "v_exp_f32 v18, v18\n"
  "v_exp_f32 v19, v19\n"
  "v_mfma_f32_32x32x16_bf16 v[32:47], a[64:67], a[16:19], v[176:191]\n"
  "v_exp_f32 v20, v20\n"
  "v_exp_f32 v21, v21\n"
  "v_exp_f32 v22, v22\n"
  "v_mfma_f32_32x32x16_bf16 v[32:47], a[68:71], a[20:23], v[32:47]\n"
  "v_exp_f32 v23, v23\n"
  "v_exp_f32 v24, v24\n"
  "v_exp_f32 v25, v25\n"
  "v_smfmac_f32_32x32x32_bf16 v[64:79], a[72:75], v[0:7], v212\n"
  "buffer_load_dwordx4 v234, s[72:75], s68 offen offset:2048 lds\n"
  "v_exp_f32 v26, v26\n"
  "v_exp_f32 v27, v27\n"
  "v_exp_f32 v28, v28\n"
  "v_smfmac_f32_32x32x32_bf16 v[64:79], a[76:79], v[8:15], v212\n"
  "v_exp_f32 v29, v29\n"
  "v_exp_f32 v30, v30\n"
  "v_exp_f32 v31, v31\n"
  "v_smfmac_f32_16x16x64_bf16 v[132:135], v[208:211], v[16:23], v212\n"
  "v_exp_f32 v32, v32\n"
  "v_exp_f32 v33, v33\n"
  "v_smfmac_f32_16x16x64_bf16 v[132:135], v[208:211], v[24:31], v212\n"
  "buffer_load_dwordx4 a[56:59], off, s[32:35], s44 offset:2048 sc1\n"
  "v_exp_f32 v34, v34\n"
  "v_exp_f32 v35, v35\n"
  "v_mfma_f32_32x32x16_bf16 v[48:63], a[64:67], a[24:27], v[192:207]\n"
  "v_exp_f32 v36, v36\n"
  "v_exp_f32 v37, v37\n"
  "v_exp_f32 v38, v38\n"
  "v_mfma_f32_32x32x16_bf16 v[48:63], a[68:71], a[28:31], v[48:63]\n"
  "v_exp_f32 v39, v39\n"
  "v_exp_f32 v40, v40\n"
  "v_exp_f32 v41, v41\n"
  "v_smfmac_f32_32x32x32_bf16 v[80:95], a[72:75], v[16:23], v212\n"
  "v_exp_f32 v42, v42\n"
  "v_exp_f32 v43, v43\n"
  "v_exp_f32 v44, v44\n"
  "v_smfmac_f32_32x32x32_bf16 v[80:95], a[76:79], v[24:31], v212\n"
  "s_waitcnt vmcnt(14)\n"
  "v_exp_f32 v45, v45\n"
  "v_exp_f32 v46, v46\n"
  "v_exp_f32 v47, v47\n"
  "v_smfmac_f32_16x16x64_bf16 v[136:139], v[208:211], v[32:39], v212\n"
  "v_exp_f32 v48, v48\n"
  "v_exp_f32 v49, v49\n"
  "v_smfmac_f32_16x16x64_bf16 v[136:139], v[208:211], v[40:47], v212\n"
  "buffer_load_dwordx4 a[60:63], off, s[32:35], s44 offset:3072 sc1\n"
  "v_exp_f32 v50, v50\n"
  "v_exp_f32 v51, v51\n"
  "v_mfma_f32_32x32x16_bf16 v[0:15], a[80:83], a[0:3], v[144:159]\n"
  "s_add_u32 s44, s44, 0x3000\n"
  "v_exp_f32 v52, v52\n"
  "v_exp_f32 v53, v53\n"
  "v_exp_f32 v54, v54\n"
  "v_mfma_f32_32x32x16_bf16 v[0:15], a[84:87], a[4:7], v[0:15]\n"
  "s_and_b32 s44, s44, 0x3ffff\n"
  "v_exp_f32 v55, v55\n"
  "v_exp_f32 v56, v56\n"
  "v_exp_f32 v57, v57\n"
  "v_smfmac_f32_32x32x32_bf16 v[96:111], a[72:75], v[32:39], v212\n"
  "s_cmp_eq_u32 s98, 7\n"
  "s_cbranch_scc1 L_wbjob_%=\n"
  "L_wbret_%=:\n"
  "v_exp_f32 v58, v58\n"
  "v_exp_f32 v59, v59\n"
  "v_exp_f32 v60, v60\n"
  "v_smfmac_f32_32x32x32_bf16 v[96:111], a[76:79], v[40:47], v212\n"
  "v_exp_f32 v61, v61\n"
  "v_exp_f32 v62, v62\n"
  "v_exp_f32 v63, v63\n"
  "v_smfmac_f32_16x16x64_bf16 v[140:143], v[208:211], v[48:55], v212\n"
  "v_exp_f32 v0, v0\n"
  "v_exp_f32 v1, v1\n"
  "v_smfmac_f32_16x16x64_bf16 v[140:143], v[208:211], v[56:63], v212\n"
  "buffer_load_dwordx4 a[64:67], off, s[32:35], s44 offset:0 sc1\n"
  "v_exp_f32 v2, v2\n"
  "v_exp_f32 v3, v3\n"
  "v_mfma_f32_32x32x16_bf16 v[16:31], a[80:83], a[8:11], v[160:175]\n"
  "v_exp_f32 v4, v4\n"
  "v_exp_f32 v5, v5\n"
  "v_exp_f32 v6, v6\n"
  "v_mfma_f32_32x32x16_bf16 v[16:31], a[84:87], a[12:15], v[16:31]\n"
  "v_exp_f32 v7, v7\n"
  "v_exp_f32 v8, v8\n"
  "v_exp_f32 v9, v9\n"
  "v_smfmac_f32_32x32x32_bf16 v[112:127], a[72:75], v[48:55], v212\n"
  "buffer_load_dwordx4 v235, s[72:75], s68 offen offset:3072 lds\n"
  "v_exp_f32 v10, v10\n"
  "v_exp_f32 v11, v11\n"
  "v_exp_f32 v12, v12\n"
  "v_smfmac_f32_32x32x32_bf16 v[112:127], a[76:79], v[56:63], v212\n"
  "v_exp_f32 v13, v13\n"
  "v_exp_f32 v14, v14\n"
  "v_exp_f32 v15, v15\n"
  "v_smfmac_f32_16x16x64_bf16 v[128:131], v[208:211], v[0:7], v212\n"
  "s_waitcnt vmcnt(15)\n"
  "v_exp_f32 v16, v16\n"
  "v_exp_f32 v17, v17\n"
  "v_smfmac_f32_16x16x64_bf16 v[128:131], v[208:211], v[8:15], v212\n"
  "buffer_load_dwordx4 a[68:71], off, s[32:35], s44 offset:1024 sc1\n"
  "v_exp_f32 v18, v18\n"
  "v_exp_f32 v19, v19\n"
  "v_mfma_f32_32x32x16_bf16 v[32:47], a[80:83], a[16:19], v[176:191]\n"
  "s_add_u32 s68, s68, 262144\n"
  "s_and_b32 s68, s68, 0x1fffff\n"
  "v_exp_f32 v20, v20\n"
  "v_exp_f32 v21, v21\n"
  "v_exp_f32 v22, v22\n"
  "v_mfma_f32_32x32x16_bf16 v[32:47], a[84:87], a[20:23], v[32:47]\n"
  "s_sub_u32 s98, s98, 1\n"
  "s_cmp_lg_u32 s98, 0\n"
  "v_exp_f32 v23, v23\n"
  "v_exp_f32 v24, v24\n"
  "v_exp_f32 v25, v25\n"
  "v_smfmac_f32_32x32x32_bf16 v[64:79], a[88:91], v[0:7], v212\n"
  "buffer_load_dword a127, v215, s[36:39], 0 offen offset:2688\n"
  "v_exp_f32 v26, v26\n"
  "v_exp_f32 v27, v27\n"
  "v_exp_f32 v28, v28\n"
  "v_smfmac_f32_32x32x32_bf16 v[64:79], a[92:95], v[8:15], v212\n"
  "v_exp_f32 v29, v29\n"
  "v_exp_f32 v30, v30\n"
  "v_exp_f32 v31, v31\n"
  "s_cbranch_scc1 L_m_0_%=\n"
  "v_mfma_f32_32x32x16_bf16 v[48:63], a[80:83], a[24:27], v[192:207]\n"
  "v_add_u32 v226, s14, v254\n"
  "v_lshrrev_b32 v216, 2, v254\n"
  "v_add_u32 v217, s14, v216\n"
  "v_add_u32 v218, s15, v216\n"
  "v_add_u32 v227, s15, v254\n"
  "v_exp_f32 v32, v32\n"
  "v_exp_f32 v33, v33\n"
  "v_exp_f32 v34, v34\n"
  "v_mfma_f32_32x32x16_bf16 v[48:63], a[84:87], a[28:31], v[48:63]\n"
  "ds_write_b128 v226, v[64:67] offset:0\n"
  "v_exp_f32 v35, v35\n"
  "v_exp_f32 v36, v36\n"
  "v_exp_f32 v37, v37\n"
  "v_smfmac_f32_32x32x32_bf16 v[80:95], a[88:91], v[16:23], v212\n"
  "ds_write_b128 v226, v[68:71] offset:1024\n"
  "v_exp_f32 v38, v38\n"
  "v_exp_f32 v39, v39\n"
  "v_exp_f32 v40, v40\n"
  "v_smfmac_f32_32x32x32_bf16 v[80:95], a[92:95], v[24:31], v212\n"
  "ds_write_b128 v226, v[72:75] offset:2048\n"
  "v_exp_f32 v41, v41\n"
  "v_exp_f32 v42, v42\n"
  "v_exp_f32 v43, v43\n"
  "v_smfmac_f32_16x16x64_bf16 v[132:135], v[208:211], v[16:23], v212\n"
  "ds_write_b128 v226, v[76:79] offset:3072\n"
  "v_exp_f32 v44, v44\n"
  "v_exp_f32 v45, v45\n"
  "v_smfmac_f32_16x16x64_bf16 v[132:135], v[208:211], v[24:31], v212\n"
  "v_exp_f32 v46, v46\n"
  "v_exp_f32 v47, v47\n"
  "s_nop 0\n"
  "ds_write_b128 v226, v[80:83] offset:4096\n"
  "ds_write_b128 v226, v[84:87] offset:5120\n"
  "v_exp_f32 v48, v48\n"
  "v_exp_f32 v49, v49\n"
  "v_exp_f32 v50, v50\n"
  "ds_write_b128 v226, v[88:91] offset:6144\n"
  "ds_write_b128 v226, v[92:95] offset:7168\n"
  "v_exp_f32 v51, v51\n"
  "v_exp_f32 v52, v52\n"
  "v_exp_f32 v53, v53\n"
  "v_smfmac_f32_32x32x32_bf16 v[96:111], a[88:91], v[32:39], v212\n"
  "ds_write_b32 v217, v128 offset:8192\n"
  "ds_write_b32 v217, v132 offset:8448\n"
  "v_exp_f32 v54, v54\n"
  "v_exp_f32 v55, v55\n"
  "v_exp_f32 v56, v56\n"
  "v_smfmac_f32_32x32x32_bf16 v[96:111], a[92:95], v[40:47], v212\n"
  "v_exp_f32 v57, v57\n"
  "v_exp_f32 v58, v58\n"
  "v_exp_f32 v59, v59\n"
  "v_smfmac_f32_16x16x64_bf16 v[136:139], v[208:211], v[32:39], v212\n"
  "s_waitcnt lgkmcnt(0)\n"
  "s_barrier\n"
  "ds_read_b32 v220, v218 offset:8192\n"
  "ds_read_b32 v221, v218 offset:8448\n"
  "ds_read_b128 v[64:67], v227 offset:0\n"
  "ds_read_b128 v[68:71], v227 offset:1024\n"
  "ds_read_b128 v[72:75], v227 offset:2048\n"
  "ds_read_b128 v[76:79], v227 offset:3072\n"
  "ds_read_b128 v[80:83], v227 offset:4096\n"
  "ds_read_b128 v[84:87], v227 offset:5120\n"
  "ds_read_b128 v[88:91], v227 offset:6144\n"
  "ds_read_b128 v[92:95], v227 offset:7168\n"
  "v_exp_f32 v60, v60\n"
  "v_exp_f32 v61, v61\n"
  "v_smfmac_f32_16x16x64_bf16 v[136:139], v[208:211], v[40:47], v212\n"
  "v_exp_f32 v62, v62\n"
  "v_exp_f32 v63, v63\n"
  "v_bfe_u32 v1, v255, 4, 1\n"
  "v_lshlrev_b32 v2, 10, v1\n"
  "v_smfmac_f32_32x32x32_bf16 v[112:127], a[88:91], v[48:55], v212\n"
  "v_lshrrev_b32 v3, 5, v255\n"
  "v_lshl_add_u32 v2, v3, 8, v2\n"
  "v_and_b32 v230, 0xf0, v254\n"
  "v_add_u32 v213, v230, v2\n"
  "v_mov_b32 v217, 0x71800000\n"
  "v_smfmac_f32_32x32x32_bf16 v[112:127], a[92:95], v[56:63], v212\n"
  "s_waitcnt lgkmcnt(8)\n"
  "v_mov_b32 v216, 0\n"
  "s_waitcnt lgkmcnt(7)\n"
  "v_add_f32 v96, v96, v64\n"
  "v_add_f32 v97, v97, v65\n"
  "v_add_f32 v98, v98, v66\n"
  "v_add_f32 v99, v99, v67\n"
  "v_max3_f32 v216, v216, abs(v96), abs(v97)\n"
  "v_max3_f32 v216, v216, abs(v98), abs(v99)\n"
  "v_smfmac_f32_16x16x64_bf16 v[140:143], v[208:211], v[48:55], v212\n"
  "v_smfmac_f32_16x16x64_bf16 v[140:143], v[208:211], v[56:63], v212\n"
  "s_waitcnt lgkmcnt(6)\n"
  "v_add_f32 v100, v100, v68\n"
  "v_add_f32 v101, v101, v69\n"
  "v_add_f32 v102, v102, v70\n"
  "v_add_f32 v103, v103, v71\n"
  "v_max3_f32 v216, v216, abs(v100), abs(v101)\n"
  "v_max3_f32 v216, v216, abs(v102), abs(v103)\n"
  "s_waitcnt lgkmcnt(5)\n"
  "v_pk_add_f32 v[104:105], v[104:105], v[72:73]\n"
  "v_pk_add_f32 v[106:107], v[106:107], v[74:75]\n"
  "v_max3_f32 v216, v216, abs(v104), abs(v105)\n"
  "v_max3_f32 v216, v216, abs(v106), abs(v107)\n"
  "s_waitcnt lgkmcnt(4)\n"
  "v_pk_add_f32 v[108:109], v[108:109], v[76:77]\n"
  "v_pk_add_f32 v[110:111], v[110:111], v[78:79]\n"
  "v_max3_f32 v216, v216, abs(v108), abs(v109)\n"
  "v_max3_f32 v216, v216, abs(v110), abs(v111)\n"
  "s_waitcnt lgkmcnt(3)\n"
  "v_pk_add_f32 v[112:113], v[112:113], v[80:81]\n"
  "v_pk_add_f32 v[114:115], v[114:115], v[82:83]\n"
  "v_max3_f32 v216, v216, abs(v112), abs(v113)\n"
  "v_max3_f32 v216, v216, abs(v114), abs(v115)\n"
  "s_waitcnt lgkmcnt(2)\n"
  "v_pk_add_f32 v[116:117], v[116:117], v[84:85]\n"
  "v_pk_add_f32 v[118:119], v[118:119], v[86:87]\n"
  "v_max3_f32 v216, v216, abs(v116), abs(v117)\n"
  "v_max3_f32 v216, v216, abs(v118), abs(v119)\n"
  "s_waitcnt lgkmcnt(1)\n"
  "v_pk_add_f32 v[120:121], v[120:121], v[88:89]\n"
  "v_pk_add_f32 v[122:123], v[122:123], v[90:91]\n"
  "v_max3_f32 v216, v216, abs(v120), abs(v121)\n"
  "v_max3_f32 v216, v216, abs(v122), abs(v123)\n"
  "s_waitcnt lgkmcnt(0)\n"
  "v_pk_add_f32 v[124:125], v[124:125], v[92:93]\n"
  "v_pk_add_f32 v[126:127], v[126:127], v[94:95]\n"
  "v_max3_f32 v216, v216, abs(v124), abs(v125)\n"
  "v_max3_f32 v216, v216, abs(v126), abs(v127)\n"
  "v_add_f32 v136, v136, v220\n"
  "v_add_f32 v140, v140, v221\n"
  "; run-time check of the softmax stand-in; on failure fall back to the true row maximum\n"
  "v_cmp_nlt_f32 s[8:9], v136, v217\n"
  "v_cmp_nlt_f32 s[14:15], v140, v217\n"
  "v_cmp_nlt_f32 vcc, v216, v217\n"
  "s_or_b64 s[8:9], s[8:9], s[14:15]\n"
  "s_or_b64 s[8:9], s[8:9], vcc\n"
  "s_cbranch_scc1 L_att_fb_%=\n"
  "L_att_norm_%=:\n"
  "v_rcp_f32 v216, v136\n"
  "v_rcp_f32 v217, v140\n"
  "s_nop 0\n"
  "v_pk_mul_f32 v[96:97], v[96:97], v[216:217] op_sel:[0,0] op_sel_hi:[1,0]\n"
  "v_pk_mul_f32 v[98:99], v[98:99], v[216:217] op_sel:[0,0] op_sel_hi:[1,0]\n"
  "v_pk_mul_f32 v[104:105], v[104:105], v[216:217] op_sel:[0,0] op_sel_hi:[1,0]\n"
  "v_pk_mul_f32 v[106:107], v[106:107], v[216:217] op_sel:[0,0] op_sel_hi:[1,0]\n"
  "v_cvt_pk_bf16_f32 v0, v96, v97\n"
  "v_cvt_pk_bf16_f32 v1, v98, v99\n"
  "v_cvt_pk_bf16_f32 v2, v104, v105\n"
  "v_cvt_pk_bf16_f32 v3, v106, v107\n"
  "buffer_store_dwordx4 v[0:3], v213, s[40:43], 0 offen offset:0 sc1\n"
  "v_pk_mul_f32 v[100:101], v[100:101], v[216:217] op_sel:[0,0] op_sel_hi:[1,0]\n"
  "v_pk_mul_f32 v[102:103], v[102:103], v[216:217] op_sel:[0,0] op_sel_hi:[1,0]\n"
  "v_pk_mul_f32 v[108:109], v[108:109], v[216:217] op_sel:[0,0] op_sel_hi:[1,0]\n"
  "v_pk_mul_f32 v[110:111], v[110:111], v[216:217] op_sel:[0,0] op_sel_hi:[1,0]\n"
  "v_cvt_pk_bf16_f32 v4, v100, v101\n"
  "v_cvt_pk_bf16_f32 v5, v102, v103\n"
  "v_cvt_pk_bf16_f32 v6, v108, v109\n"
  "v_cvt_pk_bf16_f32 v7, v110, v111\n"
  "buffer_store_dwordx4 v[4:7], v213, s[40:43], 0 offen offset:512 sc1\n"
  "v_pk_mul_f32 v[112:113], v[112:113], v[216:217] op_sel:[0,1] op_sel_hi:[1,1]\n"
  "v_pk_mul_f32 v[114:115], v[114:115], v[216:217] op_sel:[0,1] op_sel_hi:[1,1]\n"
  "v_pk_mul_f32 v[120:121], v[120:121], v[216:217] op_sel:[0,1] op_sel_hi:[1,1]\n"
  "v_pk_mul_f32 v[122:123], v[122:123], v[216:217] op_sel:[0,1] op_sel_hi:[1,1]\n"
  "v_cvt_pk_bf16_f32 v16, v112, v113\n"
  "v_cvt_pk_bf16_f32 v17, v114, v115\n"
  "v_cvt_pk_bf16_f32 v18, v120, v121\n"
  "v_cvt_pk_bf16_f32 v19, v122, v123\n"
  "buffer_store_dwordx4 v[16:19], v213, s[40:43], s45 offen offset:0 sc1\n"
  "v_pk_mul_f32 v[116:117], v[116:117], v[216:217] op_sel:[0,1] op_sel_hi:[1,1]\n"
  "v_pk_mul_f32 v[118:119], v[118:119], v[216:217] op_sel:[0,1] op_sel_hi:[1,1]\n"
  "v_pk_mul_f32 v[124:125], v[124:125], v[216:217] op_sel:[0,1] op_sel_hi:[1,1]\n"
  "v_pk_mul_f32 v[126:127], v[126:127], v[216:217] op_sel:[0,1] op_sel_hi:[1,1]\n"
  "v_cvt_pk_bf16_f32 v20, v116, v117\n"
  "v_cvt_pk_bf16_f32 v21, v118, v119\n"
  "v_cvt_pk_bf16_f32 v22, v124, v125\n"
  "v_cvt_pk_bf16_f32 v23, v126, v127\n"
  "buffer_store_dwordx4 v[20:23], v213, s[40:43], s45 offen offset:512 sc1\n"
  "; ---- phase 3: output projection + residual\n"
  "s_lshl_b32 s8, s6, 1\n"
  "s_add_u32 s8, s8, s91\n"
  "s_lshl_b32 s8, s8, 5\n"
  "s_add_u32 s8, s8, s92\n"
  "s_mul_i32 s15, s8, 64\n"
  "s_add_u32 s15, s15, 1327104\n"
  "s_andn2_b32 s8, 4, s4\n"
  "s_and_b32 s9, s5, 3\n"
  "s_add_u32 s6, s8, s9\n"
  "s_and_b32 s8, s4, 3\n"
  "s_lshl_b32 s8, s8, 3\n"
  "s_lshr_b32 s9, s5, 2\n"
  "s_add_u32 s7, s8, s9\n"
  "s_bitcmp1_b32 s7, 0\n"
  "s_cselect_b32 s14, 0, 12\n"
  "s_mul_i32 s46, s3, 7168\n"
  "s_mov_b32 s45, 0\n"
  "s_mul_i32 s13, s7, 224\n"
  "s_mul_i32 s8, s91, 458752\n"
  "s_add_u32 s13, s13, s8\n"
  "s_sub_u32 s12, 7340032, s13\n"
  "s_cmp_ge_u32 s3, 2\n"
  "s_cselect_b32 s8, s24, s22\n"
  "s_cselect_b32 s9, s25, s23\n"
  "s_add_u32 s36, s8, s13\n"
  "s_addc_u32 s37, s9, 0\n"
  "s_and_b32 s37, s37, 0xffff\n"
  "s_mov_b32 s38, s12\n"
  "s_mov_b32 s39, 0x20000\n"
  "v_add_u32 v101, 0, v255\n"
  "v_mul_u32_u24 v102, 4682, v101\n"
  "v_lshrrev_b32 v102, 16, v102\n"
  "v_mul_u32_u24 v103, 14, v102\n"
  "v_sub_u32 v101, v101, v103\n"
  "v_add_u32 v101, s14, v101\n"
  "v_subrev_u32 v103, 14, v101\n"
  "v_min_u32 v101, v101, v103\n"
  "v_mul_u32_u24 v102, 7168, v102\n"
  "v_lshl_add_u32 v101, v101, 4, v102\n"
  "v_subrev_u32 v240, 0, v101\n"
  "v_add_u32 v101, 64, v255\n"
  "v_mul_u32_u24 v102, 4682, v101\n"
  "v_lshrrev_b32 v102, 16, v102\n"
  "v_mul_u32_u24 v103, 14, v102\n"
  "v_sub_u32 v101, v101, v103\n"
  "v_add_u32 v101, s14, v101\n"
  "v_subrev_u32 v103, 14, v101\n"
  "v_min_u32 v101, v101, v103\n"
  "v_mul_u32_u24 v102, 7168, v102\n"
  "v_lshl_add_u32 v101, v101, 4, v102\n"
  "v_subrev_u32 v241, 1024, v101\n"
  "v_add_u32 v101, 128, v255\n"
  "v_mul_u32_u24 v102, 4682, v101\n"
  "v_lshrrev_b32 v102, 16, v102\n"
  "v_mul_u32_u24 v103, 14, v102\n"
  "v_sub_u32 v101, v101, v103\n"
  "v_add_u32 v101, s14, v101\n"
  "v_subrev_u32 v103, 14, v101\n"
  "v_min_u32 v101, v101, v103\n"
  "v_mul_u32_u24 v102, 7168, v102\n"
  "v_lshl_add_u32 v101, v101, 4, v102\n"
  "v_subrev_u32 v242, 2048, v101\n"
  "v_add_u32 v101, 192, v255\n"
  "v_mul_u32_u24 v102, 4682, v101\n"
  "v_lshrrev_b32 v102, 16, v102\n"
  "v_mul_u32_u24 v103, 14, v102\n"
  "v_sub_u32 v101, v101, v103\n"
  "v_add_u32 v101, s14, v101\n"
  "v_subrev_u32 v103, 14, v101\n"
  "v_min_u32 v101, v101, v103\n"
  "v_mul_u32_u24 v102, 7168, v102\n"
  "v_lshl_add_u32 v101, v101, 4, v102\n"
  "v_subrev_u32 v243, 3072, v101\n"
  "v_add_u32 v101, 256, v255\n"
  "v_mul_u32_u24 v102, 4682, v101\n"
  "v_lshrrev_b32 v102, 16, v102\n"
  "v_mul_u32_u24 v103, 14, v102\n"
  "v_sub_u32 v101, v101, v103\n"
  "v_add_u32 v101, s14, v101\n"
  "v_subrev_u32 v103, 14, v101\n"
  "v_min_u32 v101, v101, v103\n"
  "v_mul_u32_u24 v102, 7168, v102\n"
  "v_lshl_add_u32 v101, v101, 4, v102\n"
  "v_subrev_u32 v244, 0, v101\n"
  "v_add_u32 v101, 320, v255\n"
  "v_mul_u32_u24 v102, 4682, v101\n"
  "v_lshrrev_b32 v102, 16, v102\n"
  "v_mul_u32_u24 v103, 14, v102\n"
  "v_sub_u32 v101, v101, v103\n"
  "v_add_u32 v101, s14, v101\n"
  "v_subrev_u32 v103, 14, v101\n"
  "v_min_u32 v101, v101, v103\n"
  "v_mul_u32_u24 v102, 7168, v102\n"
  "v_lshl_add_u32 v101, v101, 4, v102\n"
  "v_subrev_u32 v245, 1024, v101\n"
  "v_add_u32 v101, 384, v255\n"
  "v_mul_u32_u24 v102, 4682, v101\n"
  "v_lshrrev_b32 v102, 16, v102\n"
  "v_mul_u32_u24 v103, 14, v102\n"
  "v_sub_u32 v101, v101, v103\n"
  "v_add_u32 v101, s14, v101\n"
  "v_subrev_u32 v103, 14, v101\n"
  "v_min_u32 v101, v101, v103\n"
  "v_mul_u32_u24 v102, 7168, v102\n"
  "v_lshl_add_u32 v101, v101, 4, v102\n"
  "v_subrev_u32 v246, 2048, v101\n"
  "v_lshrrev_b32 v101, 2, v255\n"
  "v_mul_u32_u24 v101, 224, v101\n"
  "v_and_b32 v102, 3, v255\n"
  "v_lshl_add_u32 v224, v102, 3, v101\n"
  "v_add_u32 v225, 57344, v224\n"
  "v_add_u32 v226, 114688, v224\n"
  "s_lshl_b32 s12, s6, 2\n"
  "s_add_u32 s12, s12, s3\n"
  "s_mul_i32 s93, s12, 2048\n"
  "s_add_u32 s93, s93, 1458176\n"
  "s_mov_b32 s44, 0\n"
  "s_lshl_b32 s13, s12, 17\n"
  "s_add_u32 s32, s30, 20971520\n"
  "s_addc_u32 s33, s31, 0\n"
  "s_add_u32 s32, s32, s13\n"
  "s_addc_u32 s33, s33, 0\n"
  "s_pack_ll_b32_b16 s33, s33, 16\n"
  "s_mov_b32 s34, 0\n"
  "s_mov_b32 s35, 0x800000\n"
  "s_lshr_b32 s8, s70, 1\n"
  "s_lshl_b32 s8, s8, 4\n"
  "s_add_u32 s8, s8, s71\n"
  "s_lshl_b32 s8, s8, 1\n"
  "s_and_b32 s9, s70, 1\n"
  "s_add_u32 s8, s8, s9\n"
  "s_mul_i32 s14, s8, 64\n"
  "s_add_u32 s14, s14, 1458176\n"
  "s_waitcnt vmcnt(0)\n"
  "buffer_store_dwordx4 v[248:251], v251, s[52:55], s15 offen sc1\n"
  "buffer_load_dwordx4 v[176:179], v254, s[52:55], s51 offen offset:0 sc1\n"
  "buffer_load_dwordx4 v[180:183], v254, s[52:55], s51 offen offset:1024 sc1\n"
  "buffer_load_dwordx4 v[184:187], v254, s[52:55], s51 offen offset:2048 sc1\n"
  "buffer_load_dwordx4 v[188:191], v254, s[52:55], s51 offen offset:3072 sc1\n"
  "buffer_load_dwordx2 v[228:229], v230, s[52:55], s50 offen sc1\n"
  "s_mov_b32 m0, s69\n"
  "s_barrier\n"
  "buffer_load_dwordx4 v232, s[72:75], s68 offen offset:0 lds\n"
  "buffer_load_dwordx4 v233, s[72:75], s68 offen offset:1024 lds\n"
  "buffer_load_dwordx4 v234, s[72:75], s68 offen offset:2048 lds\n"
  "buffer_load_dwordx4 v235, s[72:75], s68 offen offset:3072 lds\n"
  "s_getpc_b64 s[8:9]\n"
  "L_pc2_%=:\n"
  "s_mul_i32 s13, s5, 2560\n"
  "s_cmp_eq_u32 s3, 3\n"
  "s_cselect_b32 s15, L_end_%=-L_pc2_%=, 0\n"
  "s_sub_u32 s15, s15, s13\n"
  "s_cselect_b32 s15, 0, s15\n"
  "s_min_u32 s15, s15, 2560\n"
  "s_add_u32 s60, s8, s13\n"
  "s_addc_u32 s61, s9, 0\n"
  "s_and_b32 s61, s61, 0xffff\n"
  "s_mov_b32 s62, s15\n"
  "s_mov_b32 s63, 0x20000\n"
  "v_lshlrev_b32 v62, 7, v255\n"
  "buffer_load_dword v61, v62, s[60:63], 0 offen\n"
  "s_or_b32 s8, s2, s3\n"
  "s_cbranch_scc1 L_noep_%=\n"
  "s_mov_b32 s8, 917504\n"
  "buffer_store_dwordx4 v[248:251], v251, s[52:55], s8 offen sc1\n"
  "L_noep_%=:\n"
  "v_and_b32 v56, 31, v255\n"
  "v_mul_u32_u24 v51, 64, v56\n"
  "s_lshr_b32 s8, s70, 1\n"
  "s_mul_i32 s15, s8, 2048\n"
  "s_add_u32 s15, s15, 1327104\n"
  "s_lshl_b32 s13, s70, 16\n"
  "s_add_u32 s76, s30, 16777216\n"
  "s_addc_u32 s77, s31, 0\n"
  "s_add_u32 s76, s76, s13\n"
  "s_addc_u32 s77, s77, 0\n"
  "s_pack_ll_b32_b16 s77, s77, 16\n"
  "s_mov_b32 s78, 0\n"
  "s_mov_b32 s79, 0x800000\n"
  "s_add_u32 s40, s30, 20971520\n"
  "s_addc_u32 s41, s31, 0\n"
  "s_pack_ll_b32_b16 s41, s41, 16\n"
  "s_mov_b32 s42, 0\n"
  "s_mov_b32 s43, 0x800000\n"
  "s_lshr_b32 s8, s70, 1\n"
  "s_lshl_b32 s8, s8, 17\n"
  "s_and_b32 s9, s70, 1\n"
  "s_lshl_b32 s9, s9, 11\n"
  "s_add_u32 s8, s8, s9\n"
  "s_lshl_b32 s9, s71, 12\n"
  "s_add_u32 s48, s8, s9\n"
  "s_add_u32 s49, s48, 0x10000\n"
  "s_lshr_b32 s8, s71, 1\n"
  "s_lshl_b32 s47, s8, 13\n"
  "s_lshl_b32 s50, s70, 12\n"
  "s_lshl_b32 s8, s71, 8\n"
  "s_add_u32 s50, s50, s8\n"
  "s_add_u32 s50, s50, 655360\n"
  "v_lshrrev_b32 v56, 2, v255\n"
  "v_lshlrev_b32 v56, 7, v56\n"
  "v_and_b32 v57, 3, v255\n"
  "v_lshl_add_u32 v56, v57, 3, v56\n"
  "v_bfe_u32 v57, v255, 3, 2\n"
  "v_xor_b32 v58, 0, v57\n"
  "v_lshl_add_u32 v40, v58, 5, v56\n"
  "v_xor_b32 v58, 1, v57\n"
  "v_lshl_add_u32 v41, v58, 5, v56\n"
  "v_xor_b32 v58, 2, v57\n"
  "v_lshl_add_u32 v42, v58, 5, v56\n"
  "v_xor_b32 v58, 3, v57\n"
  "v_lshl_add_u32 v43, v58, 5, v56\n"
  "v_lshrrev_b32 v58, 4, v255\n"
  "v_lshlrev_b32 v56, 6, v58\n"
  "v_and_b32 v57, 3, v255\n"
  "v_lshl_add_u32 v56, v57, 4, v56\n"
  "v_add_u32 v44, 159744, v56\n"
  "v_and_b32 v57, 1, v255\n"
  "v_lshlrev_b32 v56, 1, v57\n"
  "v_mul_u32_u24 v57, 0xaaaa, v57\n"
  "v_add_u32 v45, 0x4444, v57\n"
  "v_bfe_u32 v57, v255, 5, 1\n"
  "v_lshl_add_u32 v56, v57, 7, v56\n"
  "v_bfe_u32 v57, v255, 3, 1\n"
  "v_lshl_add_u32 v56, v57, 6, v56\n"
  "v_bfe_u32 v57, v255, 1, 1\n"
  "v_lshl_add_u32 v56, v57, 4, v56\n"
  "v_bfe_u32 v57, v255, 2, 1\n"
  "v_lshl_add_u32 v56, v57, 3, v56\n"
  "v_bfe_u32 v57, v255, 4, 1\n"
  "v_lshl_add_u32 v56, v57, 2, v56\n"
  "s_lshl_b32 s8, s3, 10\n"
  "s_lshr_b32 s9, s71, 1\n"
  "s_lshl_b32 s9, s9, 9\n"
  "s_sub_u32 s8, s8, s9\n"
  "s_add_u32 s9, s8, 512\n"
  "s_and_b32 s8, s8, 0xfff\n"
  "s_and_b32 s9, s9, 0xfff\n"
  "s_add_u32 s8, s8, 159744\n"
  "s_add_u32 s9, s9, 159744\n"
  "v_add_u32 v59, s8, v56\n"
  "v_add_u32 v60, s9, v56\n"
  "s_sleep 8\n"
  "buffer_load_dwordx2 v[52:53], v51, s[52:55], s15 offen sc1\n"
  "s_lshl_b32 s8, s3, 12\n"
  "s_add_u32 s8, s8, 131072\n"
  "v_add_u32 v56, s8, v254\n"
  "ds_write_b128 v56, a[96:99] offset:0\n"
  "ds_write_b128 v56, a[100:103] offset:1024\n"
  "ds_write_b128 v56, a[104:107] offset:2048\n"
  "ds_write_b128 v56, a[108:111] offset:3072\n"
  "v_lshlrev_b32 v57, 3, v230\n"
  "v_lshl_add_u32 v56, v58, 3, v57\n"
  "v_bfe_u32 v57, v255, 1, 2\n"
  "v_lshl_add_u32 v56, v57, 5, v56\n"
  "v_add_u32 v56, s8, v56\n"
  "v_xor_b32 v57, 0, v56\n"
  "ds_read_b64 v[128:129], v57 offset:0\n"
  "ds_read_b64 v[136:137], v57 offset:2048\n"
  "v_xor_b32 v57, 32, v56\n"
  "ds_read_b64 v[130:131], v57 offset:0\n"
  "ds_read_b64 v[138:139], v57 offset:2048\n"
  "v_xor_b32 v57, 64, v56\n"
  "ds_read_b64 v[132:133], v57 offset:0\n"
  "ds_read_b64 v[140:141], v57 offset:2048\n"
  "v_xor_b32 v57, 96, v56\n"
  "ds_read_b64 v[134:135], v57 offset:0\n"
  "ds_read_b64 v[142:143], v57 offset:2048\n"
  "s_waitcnt vmcnt(0)\n"
  "L_poll3_%=:\n"
  "v_cmp_ne_u64 vcc, s[10:11], v[52:53]\n"
  "s_cbranch_vccz L_ok3_%=\n"
  "s_sub_u32 s99, s99, 1\n"
  "s_cbranch_scc1 L_out3_%=\n"
  "buffer_load_dwordx2 v[52:53], v51, s[52:55], s15 offen sc1\n"
  "s_waitcnt vmcnt(0)\n"
  "s_branch L_poll3_%=\n"
  "L_out3_%=:\n"
  "s_mov_b32 s99, 0\n"
  "L_ok3_%=:\n"
  "buffer_load_dwordx4 a[0:3], off, s[76:79], s47 offset:0 sc1\n"
  "buffer_load_dwordx4 a[8:11], off, s[76:79], s47 offset:1024 sc1\n"
  "buffer_load_dwordx4 a[4:7], off, s[76:79], s47 offset:2048 sc1\n"
  "buffer_load_dwordx4 a[12:15], off, s[76:79], s47 offset:3072 sc1\n"
  "s_add_u32 s47, s47, 4096\n"
  "buffer_load_dwordx4 a[16:19], off, s[76:79], s47 offset:0 sc1\n"
  "buffer_load_dwordx4 a[24:27], off, s[76:79], s47 offset:1024 sc1\n"
  "buffer_load_dwordx4 a[20:23], off, s[76:79], s47 offset:2048 sc1\n"
  "buffer_load_dwordx4 a[28:31], off, s[76:79], s47 offset:3072 sc1\n"
  "s_add_u32 s47, s47, 4096\n"
  "s_and_b32 s47, s47, 0xffff\n"
  "buffer_load_dwordx4 a[32:35], off, s[76:79], s47 offset:0 sc1\n"
  "buffer_load_dwordx4 a[40:43], off, s[76:79], s47 offset:1024 sc1\n"
  "buffer_load_dwordx4 a[36:39], off, s[76:79], s47 offset:2048 sc1\n"
  "buffer_load_dwordx4 a[44:47], off, s[76:79], s47 offset:3072 sc1\n"
  "s_add_u32 s47, s47, 4096\n"
  "L_wb_%=:\n"
  "v_xor_b32 v176, s10, v176\n"
  "v_xor_b32 v178, s11, v178\n"
  "v_xor_b32 v180, s10, v180\n"
  "v_xor_b32 v182, s11, v182\n"
  "v_xor_b32 v184, s10, v184\n"
  "v_xor_b32 v186, s11, v186\n"
  "v_xor_b32 v188, s10, v188\n"
  "v_xor_b32 v190, s11, v190\n"
  "v_or3_b32 v176, v176, v178, v180\n"
  "v_or3_b32 v182, v182, v184, v186\n"
  "v_or3_b32 v176, v176, v188, v190\n"
  "v_or_b32 v176, v176, v182\n"
  "v_cmp_ne_u32 vcc, 0, v176\n"
  "s_cbranch_vccz L_wbok_%=\n"
  "s_sub_u32 s99, s99, 1\n"
  "s_cbranch_scc1 L_wbout_%=\n"
  "s_sleep 4\n"
  "buffer_load_dwordx4 v[176:179], v254, s[52:55], s51 offen offset:0 sc1\n"
  "buffer_load_dwordx4 v[180:183], v254, s[52:55], s51 offen offset:1024 sc1\n"
  "buffer_load_dwordx4 v[184:187], v254, s[52:55], s51 offen offset:2048 sc1\n"
  "buffer_load_dwordx4 v[188:191], v254, s[52:55], s51 offen offset:3072 sc1\n"
  "s_waitcnt vmcnt(0)\n"
  "s_branch L_wb_%=\n"
  "L_wbout_%=:\n"
  "s_mov_b32 s99, 0\n"
  "L_wbok_%=:\n"
  "ds_write_b16 v59, v177 offset:0\n"
  "ds_write_b16_d16_hi v59, v177 offset:32\n"
  "ds_write_b16 v59, v181 offset:256\n"
  "s_waitcnt lgkmcnt(11)\n"
  "ds_write_b16_d16_hi v59, v181 offset:288\n"
  "ds_write_b16 v60, v185 offset:0\n"
  "ds_write_b16_d16_hi v60, v185 offset:32\n"
  "ds_write_b16 v60, v189 offset:256\n"
  "s_waitcnt lgkmcnt(11)\n"
  "ds_write_b16_d16_hi v60, v189 offset:288\n"
  "s_waitcnt lgkmcnt(0)\n"
  "s_barrier\n"
  "v_lshlrev_b32 v0, 16, v128\n"
  "v_and_b32 v1, 0xffff0000, v128\n"
  "v_lshlrev_b32 v2, 16, v129\n"
  "v_and_b32 v3, 0xffff0000, v129\n"
  "v_lshlrev_b32 v4, 16, v136\n"
  "v_and_b32 v5, 0xffff0000, v136\n"
  "v_lshlrev_b32 v6, 16, v137\n"
  "v_and_b32 v7, 0xffff0000, v137\n"
  "v_lshlrev_b32 v8, 16, v130\n"
  "v_and_b32 v9, 0xffff0000, v130\n"
  "v_lshlrev_b32 v10, 16, v131\n"
  "v_and_b32 v11, 0xffff0000, v131\n"
  "v_lshlrev_b32 v12, 16, v138\n"
  "v_and_b32 v13, 0xffff0000, v138\n"
  "v_lshlrev_b32 v14, 16, v139\n"
  "v_and_b32 v15, 0xffff0000, v139\n"
  "v_lshlrev_b32 v16, 16, v132\n"
  "v_and_b32 v17, 0xffff0000, v132\n"
  "v_lshlrev_b32 v18, 16, v133\n"
  "v_and_b32 v19, 0xffff0000, v133\n"
  "v_lshlrev_b32 v20, 16, v140\n"
  "v_and_b32 v21, 0xffff0000, v140\n"
  "v_lshlrev_b32 v22, 16, v141\n"
  "v_and_b32 v23, 0xffff0000, v141\n"
  "v_lshlrev_b32 v24, 16, v134\n"
  "v_and_b32 v25, 0xffff0000, v134\n"
  "v_lshlrev_b32 v26, 16, v135\n"
  "v_and_b32 v27, 0xffff0000, v135\n"
  "v_lshlrev_b32 v28, 16, v142\n"
  "v_and_b32 v29, 0xffff0000, v142\n"
  "v_lshlrev_b32 v30, 16, v143\n"
  "v_and_b32 v31, 0xffff0000, v143\n"
  "v_mov_b32 v46, 0\n"
  "v_mov_b32 v48, 0\n"
  "v_mov_b32 v32, 0\n"
  "v_mov_b32 v33, 0\n"
  "v_mov_b32 v34, 0\n"
  "v_mov_b32 v35, 0\n"
  "v_mov_b32 v36, 0\n"
  "v_mov_b32 v37, 0\n"
  "v_mov_b32 v38, 0\n"
  "v_mov_b32 v39, 0\n"
  "ds_read_b128 a[232:235], v44 offset:0\n"
  "ds_read_b128 a[236:239], v44 offset:256\n"
  "ds_read_b64_tr_b16 a[192:193], v40 offset:0\n"
  "ds_read_b64_tr_b16 a[194:195], v40 offset:2048\n"
  "ds_read_b64_tr_b16 a[196:197], v40 offset:4096\n"
  "ds_read_b64_tr_b16 a[198:199], v40 offset:6144\n"
  "ds_read_b64_tr_b16 a[200:201], v41 offset:0\n"
  "ds_read_b64_tr_b16 a[202:203], v41 offset:2048\n"
  "ds_read_b64_tr_b16 a[204:205], v41 offset:4096\n"
  "ds_read_b64_tr_b16 a[206:207], v41 offset:6144\n"
  "ds_read_b64_tr_b16 a[208:209], v42 offset:0\n"
  "ds_read_b64_tr_b16 a[210:211], v42 offset:2048\n"
  "ds_read_b64_tr_b16 a[212:213], v42 offset:4096\n"
  "ds_read_b64_tr_b16 a[214:215], v42 offset:6144\n"
  "ds_read_b64_tr_b16 a[216:217], v43 offset:0\n"
  "s_waitcnt lgkmcnt(11)\n"
  "ds_read_b64_tr_b16 a[218:219], v43 offset:2048\n"
  "s_waitcnt vmcnt(8) lgkmcnt(10)\n"
  "v_mfma_f32_16x16x32_bf16 v[0:3], a[192:195], a[0:3], v[0:3]\n"
  "ds_read_b64_tr_b16 a[220:221], v43 offset:4096\n"
  "v_mfma_f32_16x16x32_bf16 v[0:3], a[196:199], a[4:7], v[0:3]\n"
  "ds_read_b64_tr_b16 a[222:223], v43 offset:6144\n"
  "v_mfma_f32_16x16x32_bf16 v[4:7], a[196:199], a[12:15], v[4:7]\n"
  "ds_read_b64_tr_b16 a[224:225], v40 offset:8192\n"
  "buffer_load_dwordx4 a[48:51], off, s[76:79], s47 offset:0 sc1\n"
  "v_mfma_f32_16x16x32_bf16 v[4:7], a[192:195], a[8:11], v[4:7]\n"
  "ds_read_b64_tr_b16 a[226:227], v40 offset:10240\n"
  "s_waitcnt lgkmcnt(10)\n"
  "v_smfmac_f32_16x16x64_bf16 v[32:35], a[232:235], a[0:7], v45\n"
  "ds_read_b128 a[240:243], v44 offset:512\n"
  "v_smfmac_f32_16x16x64_bf16 v[36:39], a[232:235], a[8:15], v45\n"
  "v_mfma_f32_16x16x32_bf16 v[12:15], a[200:203], a[8:11], v[12:15]\n"
  "ds_read_b64_tr_b16 a[228:229], v40 offset:12288\n"
  "buffer_load_dwordx4 a[56:59], off, s[76:79], s47 offset:1024 sc1\n"
  "v_mfma_f32_16x16x32_bf16 v[12:15], a[204:207], a[12:15], v[12:15]\n"
  "ds_read_b64_tr_b16 a[230:231], v40 offset:14336\n"
  "v_mfma_f32_16x16x32_bf16 v[8:11], a[204:207], a[4:7], v[8:11]\n"
  "ds_read_b64_tr_b16 a[192:193], v41 offset:8192\n"
  "v_mfma_f32_16x16x32_bf16 v[8:11], a[200:203], a[0:3], v[8:11]\n"
  "ds_read_b64_tr_b16 a[194:195], v41 offset:10240\n"
  "s_waitcnt lgkmcnt(11)\n"
  "v_mfma_f32_16x16x32_bf16 v[16:19], a[208:211], a[0:3], v[16:19]\n"
  "ds_read_b64_tr_b16 a[196:197], v41 offset:12288\n"
  "buffer_load_dwordx4 a[52:55], off, s[76:79], s47 offset:2048 sc1\n"
  "v_mfma_f32_16x16x32_bf16 v[16:19], a[212:215], a[4:7], v[16:19]\n"
  "ds_read_b64_tr_b16 a[198:199], v41 offset:14336\n"
  "v_mfma_f32_16x16x32_bf16 v[20:23], a[212:215], a[12:15], v[20:23]\n"
  "ds_read_b64_tr_b16 a[200:201], v42 offset:8192\n"
  "buffer_load_dwordx4 a[60:63], off, s[76:79], s47 offset:3072 sc1\n"
  "v_mfma_f32_16x16x32_bf16 v[20:23], a[208:211], a[8:11], v[20:23]\n"
  "ds_read_b64_tr_b16 a[202:203], v42 offset:10240\n"
  "s_waitcnt lgkmcnt(11)\n"
  "v_mfma_f32_16x16x32_bf16 v[28:31], a[216:219], a[8:11], v[28:31]\n"
  "ds_read_b64_tr_b16 a[204:205], v42 offset:12288\n"
  "s_add_u32 s47, s47, 4096\n"
  "v_mfma_f32_16x16x32_bf16 v[28:31], a[220:223], a[12:15], v[28:31]\n"
  "ds_read_b64_tr_b16 a[206:207], v42 offset:14336\n"
  "s_and_b32 s47, s47, 0xffff\n"
  "v_mfma_f32_16x16x32_bf16 v[24:27], a[220:223], a[4:7], v[24:27]\n"
  "ds_read_b64_tr_b16 a[208:209], v43 offset:8192\n"
  "buffer_load_dwordx4 a[64:67], off, s[76:79], s47 offset:0 sc1\n"
  "v_mfma_f32_16x16x32_bf16 v[24:27], a[216:219], a[0:3], v[24:27]\n"
  "ds_read_b64_tr_b16 a[210:211], v43 offset:10240\n"
  "s_waitcnt vmcnt(9) lgkmcnt(10)\n"
  "v_mfma_f32_16x16x32_bf16 v[0:3], a[224:227], a[16:19], v[0:3]\n"
  "ds_read_b64_tr_b16 a[212:213], v43 offset:12288\n"
  "buffer_load_dwordx4 a[72:75], off, s[76:79], s47 offset:1024 sc1\n"
  "v_mfma_f32_16x16x32_bf16 v[0:3], a[228:231], a[20:23], v[0:3]\n"
  "ds_read_b64_tr_b16 a[214:215], v43 offset:14336\n"
  "v_mfma_f32_16x16x32_bf16 v[4:7], a[228:231], a[28:31], v[4:7]\n"
  "ds_read_b64_tr_b16 a[216:217], v40 offset:16384\n"
  "v_mfma_f32_16x16x32_bf16 v[4:7], a[224:227], a[24:27], v[4:7]\n"
  "ds_read_b64_tr_b16 a[218:219], v40 offset:18432\n"
  "s_waitcnt lgkmcnt(10)\n"
  "v_smfmac_f32_16x16x64_bf16 v[32:35], a[236:239], a[16:23], v45\n"
  "ds_read_b128 a[244:247], v44 offset:768\n"
  "v_smfmac_f32_16x16x64_bf16 v[36:39], a[236:239], a[24:31], v45\n"
  "v_mfma_f32_16x16x32_bf16 v[12:15], a[192:195], a[24:27], v[12:15]\n"
  "ds_read_b64_tr_b16 a[220:221], v40 offset:20480\n"
  "buffer_load_dwordx4 a[68:71], off, s[76:79], s47 offset:2048 sc1\n"
  "v_mfma_f32_16x16x32_bf16 v[12:15], a[196:199], a[28:31], v[12:15]\n"
  "ds_read_b64_tr_b16 a[222:223], v40 offset:22528\n"
  "v_mfma_f32_16x16x32_bf16 v[8:11], a[196:199], a[20:23], v[8:11]\n"
  "ds_read_b64_tr_b16 a[224:225], v41 offset:16384\n"
  "buffer_load_dwordx4 a[76:79], off, s[76:79], s47 offset:3072 sc1\n"
  "v_mfma_f32_16x16x32_bf16 v[8:11], a[192:195], a[16:19], v[8:11]\n"
  "ds_read_b64_tr_b16 a[226:227], v41 offset:18432\n"
  "s_waitcnt lgkmcnt(11)\n"
  "v_mfma_f32_16x16x32_bf16 v[16:19], a[200:203], a[16:19], v[16:19]\n"
  "ds_read_b64_tr_b16 a[228:229], v41 offset:20480\n"
  "s_add_u32 s47, s47, 4096\n"
  "v_mfma_f32_16x16x32_bf16 v[16:19], a[204:207], a[20:23], v[16:19]\n"
  "ds_read_b64_tr_b16 a[230:231], v41 offset:22528\n"
  "v_mfma_f32_16x16x32_bf16 v[20:23], a[204:207], a[28:31], v[20:23]\n"
  "ds_read_b64_tr_b16 a[192:193], v42 offset:16384\n"
  "buffer_load_dwordx4 a[80:83], off, s[76:79], s47 offset:0 sc1\n"
  "v_mfma_f32_16x16x32_bf16 v[20:23], a[200:203], a[24:27], v[20:23]\n"
  "ds_read_b64_tr_b16 a[194:195], v42 offset:18432\n"
  "s_waitcnt lgkmcnt(11)\n"
  "v_mfma_f32_16x16x32_bf16 v[28:31], a[208:211], a[24:27], v[28:31]\n"
  "ds_read_b64_tr_b16 a[196:197], v42 offset:20480\n"
  "buffer_load_dwordx4 a[88:91], off, s[76:79], s47 offset:1024 sc1\n"
  "v_mfma_f32_16x16x32_bf16 v[28:31], a[212:215], a[28:31], v[28:31]\n"
  "ds_read_b64_tr_b16 a[198:199], v42 offset:22528\n"
  "v_mfma_f32_16x16x32_bf16 v[24:27], a[212:215], a[20:23], v[24:27]\n"
  "ds_read_b64_tr_b16 a[200:201], v43 offset:16384\n"
  "v_mfma_f32_16x16x32_bf16 v[24:27], a[208:211], a[16:19], v[24:27]\n"
  "ds_read_b64_tr_b16 a[202:203], v43 offset:18432\n"
  "s_waitcnt vmcnt(10) lgkmcnt(10)\n"
  "v_mfma_f32_16x16x32_bf16 v[0:3], a[216:219], a[32:35], v[0:3]\n"
  "ds_read_b64_tr_b16 a[204:205], v43 offset:20480\n"
  "buffer_load_dwordx4 a[84:87], off, s[76:79], s47 offset:2048 sc1\n"
  "v_mfma_f32_16x16x32_bf16 v[0:3], a[220:223], a[36:39], v[0:3]\n"
  "ds_read_b64_tr_b16 a[206:207], v43 offset:22528\n"
  "v_mfma_f32_16x16x32_bf16 v[4:7], a[220:223], a[44:47], v[4:7]\n"
  "ds_read_b64_tr_b16 a[208:209], v40 offset:24576\n"
  "buffer_load_dwordx4 a[92:95], off, s[76:79], s47 offset:3072 sc1\n"
  "v_mfma_f32_16x16x32_bf16 v[4:7], a[216:219], a[40:43], v[4:7]\n"
  "ds_read_b64_tr_b16 a[210:211], v40 offset:26624\n"
  "s_waitcnt lgkmcnt(10)\n"
  "v_smfmac_f32_16x16x64_bf16 v[32:35], a[240:243], a[32:39], v45\n"
  "ds_read_b128 a[232:235], v44 offset:1024\n"
  "s_add_u32 s47, s47, 4096\n"
  "v_smfmac_f32_16x16x64_bf16 v[36:39], a[240:243], a[40:47], v45\n"
  "s_and_b32 s47, s47, 0xffff\n"
  "v_mfma_f32_16x16x32_bf16 v[12:15], a[224:227], a[40:43], v[12:15]\n"
  "ds_read_b64_tr_b16 a[212:213], v40 offset:28672\n"
  "v_mfma_f32_16x16x32_bf16 v[12:15], a[228:231], a[44:47], v[12:15]\n"
  "ds_read_b64_tr_b16 a[214:215], v40 offset:30720\n"
  "v_mfma_f32_16x16x32_bf16 v[8:11], a[228:231], a[36:39], v[8:11]\n"
  "ds_read_b64_tr_b16 a[216:217], v41 offset:24576\n"
  "buffer_load_dwordx4 a[96:99], off, s[76:79], s47 offset:0 sc1\n"
  "v_mfma_f32_16x16x32_bf16 v[8:11], a[224:227], a[32:35], v[8:11]\n"
  "ds_read_b64_tr_b16 a[218:219], v41 offset:26624\n"
  "s_waitcnt lgkmcnt(11)\n"
  "v_mfma_f32_16x16x32_bf16 v[16:19], a[192:195], a[32:35], v[16:19]\n"
  "ds_read_b64_tr_b16 a[220:221], v41 offset:28672\n"
  "buffer_load_dwordx4 a[104:107], off, s[76:79], s47 offset:1024 sc1\n"
  "v_mfma_f32_16x16x32_bf16 v[16:19], a[196:199], a[36:39], v[16:19]\n"
  "ds_read_b64_tr_b16 a[222:223], v41 offset:30720\n"
  "v_mfma_f32_16x16x32_bf16 v[20:23], a[196:199], a[44:47], v[20:23]\n"
  "ds_read_b64_tr_b16 a[224:225], v42 offset:24576\n"
  "v_mfma_f32_16x16x32_bf16 v[20:23], a[192:195], a[40:43], v[20:23]\n"
  "ds_read_b64_tr_b16 a[226:227], v42 offset:26624\n"
  "s_waitcnt lgkmcnt(11)\n"
  "v_mfma_f32_16x16x32_bf16 v[28:31], a[200:203], a[40:43], v[28:31]\n"
  "ds_read_b64_tr_b16 a[228:229], v42 offset:28672\n"
  "buffer_load_dwordx4 a[100:103], off, s[76:79], s47 offset:2048 sc1\n"
  "v_mfma_f32_16x16x32_bf16 v[28:31], a[204:207], a[44:47], v[28:31]\n"
  "ds_read_b64_tr_b16 a[230:231], v42 offset:30720\n"
  "v_mfma_f32_16x16x32_bf16 v[24:27], a[204:207], a[36:39], v[24:27]\n"
  "ds_read_b64_tr_b16 a[192:193], v43 offset:24576\n"
  "buffer_load_dwordx4 a[108:111], off, s[76:79], s47 offset:3072 sc1\n"
  "v_mfma_f32_16x16x32_bf16 v[24:27], a[200:203], a[32:35], v[24:27]\n"
  "ds_read_b64_tr_b16 a[194:195], v43 offset:26624\n"
  "s_waitcnt vmcnt(12) lgkmcnt(10)\n"
  "v_mfma_f32_16x16x32_bf16 v[0:3], a[208:211], a[48:51], v[0:3]\n"
  "ds_read_b64_tr_b16 a[196:197], v43 offset:28672\n"
  "s_add_u32 s47, s47, 4096\n"
  "v_mfma_f32_16x16x32_bf16 v[0:3], a[212:215], a[52:55], v[0:3]\n"
  "ds_read_b64_tr_b16 a[198:199], v43 offset:30720\n"
  "v_mfma_f32_16x16x32_bf16 v[4:7], a[212:215], a[60:63], v[4:7]\n"
  "ds_read_b64_tr_b16 a[200:201], v40 offset:32768\n"
  "buffer_load_dwordx4 a[112:115], off, s[76:79], s47 offset:0 sc1\n"
  "v_mfma_f32_16x16x32_bf16 v[4:7], a[208:211], a[56:59], v[4:7]\n"
  "ds_read_b64_tr_b16 a[202:203], v40 offset:34816\n"
  "s_waitcnt lgkmcnt(10)\n"
  "v_smfmac_f32_16x16x64_bf16 v[32:35], a[244:247], a[48:55], v45\n"
  "ds_read_b128 a[236:239], v44 offset:1280\n"
  "v_smfmac_f32_16x16x64_bf16 v[36:39], a[244:247], a[56:63], v45\n"
  "v_mfma_f32_16x16x32_bf16 v[12:15], a[216:219], a[56:59], v[12:15]\n"
  "ds_read_b64_tr_b16 a[204:205], v40 offset:36864\n"
  "buffer_load_dwordx4 a[120:123], off, s[76:79], s47 offset:1024 sc1\n"
  "v_mfma_f32_16x16x32_bf16 v[12:15], a[220:223], a[60:63], v[12:15]\n"
  "ds_read_b64_tr_b16 a[206:207], v40 offset:38912\n"
  "v_mfma_f32_16x16x32_bf16 v[8:11], a[220:223], a[52:55], v[8:11]\n"
  "ds_read_b64_tr_b16 a[208:209], v41 offset:32768\n"
  "v_mfma_f32_16x16x32_bf16 v[8:11], a[216:219], a[48:51], v[8:11]\n"
  "ds_read_b64_tr_b16 a[210:211], v41 offset:34816\n"
  "s_waitcnt lgkmcnt(11)\n"
  "v_mfma_f32_16x16x32_bf16 v[16:19], a[224:227], a[48:51], v[16:19]\n"
  "ds_read_b64_tr_b16 a[212:213], v41 offset:36864\n"
  "buffer_load_dwordx4 a[116:119], off, s[76:79], s47 offset:2048 sc1\n"
  "v_mfma_f32_16x16x32_bf16 v[16:19], a[228:231], a[52:55], v[16:19]\n"
  "ds_read_b64_tr_b16 a[214:215], v41 offset:38912\n"
  "v_mfma_f32_16x16x32_bf16 v[20:23], a[228:231], a[60:63], v[20:23]\n"
  "ds_read_b64_tr_b16 a[216:217], v42 offset:32768\n"
  "buffer_load_dwordx4 a[124:127], off, s[76:79], s47 offset:3072 sc1\n"
  "v_mfma_f32_16x16x32_bf16 v[20:23], a[224:227], a[56:59], v[20:23]\n"
  "ds_read_b64_tr_b16 a[218:219], v42 offset:34816\n"
  "s_waitcnt lgkmcnt(11)\n"
  "v_mfma_f32_16x16x32_bf16 v[28:31], a[192:195], a[56:59], v[28:31]\n"
  "ds_read_b64_tr_b16 a[220:221], v42 offset:36864\n"
  "s_add_u32 s47, s47, 4096\n"
  "v_mfma_f32_16x16x32_bf16 v[28:31], a[196:199], a[60:63], v[28:31]\n"
  "ds_read_b64_tr_b16 a[222:223], v42 offset:38912\n"
  "s_and_b32 s47, s47, 0xffff\n"
  "v_mfma_f32_16x16x32_bf16 v[24:27], a[196:199], a[52:55], v[24:27]\n"
  "ds_read_b64_tr_b16 a[224:225], v43 offset:32768\n"
  "buffer_load_dwordx4 a[128:131], off, s[76:79], s47 offset:0 sc1\n"
  "v_mfma_f32_16x16x32_bf16 v[24:27], a[192:195], a[48:51], v[24:27]\n"
  "ds_read_b64_tr_b16 a[226:227], v43 offset:34816\n"
  "s_waitcnt vmcnt(13) lgkmcnt(10)\n"
  "v_mfma_f32_16x16x32_bf16 v[0:3], a[200:203], a[64:67], v[0:3]\n"
  "ds_read_b64_tr_b16 a[228:229], v43 offset:36864\n"
  "buffer_load_dwordx4 a[136:139], off, s[76:79], s47 offset:1024 sc1\n"
  "v_mfma_f32_16x16x32_bf16 v[0:3], a[204:207], a[68:71], v[0:3]\n"
  "ds_read_b64_tr_b16 a[230:231], v43 offset:38912\n"
  "v_mfma_f32_16x16x32_bf16 v[4:7], a[204:207], a[76:79], v[4:7]\n"
  "ds_read_b64_tr_b16 a[192:193], v40 offset:40960\n"
  "v_mfma_f32_16x16x32_bf16 v[4:7], a[200:203], a[72:75], v[4:7]\n"
  "ds_read_b64_tr_b16 a[194:195], v40 offset:43008\n"
  "s_waitcnt lgkmcnt(10)\n"
  "v_smfmac_f32_16x16x64_bf16 v[32:35], a[232:235], a[64:71], v45\n"
  "ds_read_b128 a[240:243], v44 offset:1536\n"
  "v_smfmac_f32_16x16x64_bf16 v[36:39], a[232:235], a[72:79], v45\n"
  "v_mfma_f32_16x16x32_bf16 v[12:15], a[208:211], a[72:75], v[12:15]\n"
  "ds_read_b64_tr_b16 a[196:197], v40 offset:45056\n"
  "buffer_load_dwordx4 a[132:135], off, s[76:79], s47 offset:2048 sc1\n"
  "v_mfma_f32_16x16x32_bf16 v[12:15], a[212:215], a[76:79], v[12:15]\n"
  "ds_read_b64_tr_b16 a[198:199], v40 offset:47104\n"
  "v_mfma_f32_16x16x32_bf16 v[8:11], a[212:215], a[68:71], v[8:11]\n"
  "ds_read_b64_tr_b16 a[200:201], v41 offset:40960\n"
  "buffer_load_dwordx4 a[140:143], off, s[76:79], s47 offset:3072 sc1\n"
  "v_mfma_f32_16x16x32_bf16 v[8:11], a[208:211], a[64:67], v[8:11]\n"
  "ds_read_b64_tr_b16 a[202:203], v41 offset:43008\n"
  "s_waitcnt lgkmcnt(11)\n"
  "v_mfma_f32_16x16x32_bf16 v[16:19], a[216:219], a[64:67], v[16:19]\n"
  "ds_read_b64_tr_b16 a[204:205], v41 offset:45056\n"
  "s_add_u32 s47, s47, 4096\n"
  "v_mfma_f32_16x16x32_bf16 v[16:19], a[220:223], a[68:71], v[16:19]\n"
  "ds_read_b64_tr_b16 a[206:207], v41 offset:47104\n"
  "v_mfma_f32_16x16x32_bf16 v[20:23], a[220:223], a[76:79], v[20:23]\n"
  "ds_read_b64_tr_b16 a[208:209], v42 offset:40960\n"
  "buffer_load_dwordx4 a[144:147], off, s[76:79], s47 offset:0 sc1\n"
  "v_mfma_f32_16x16x32_bf16 v[20:23], a[216:219], a[72:75], v[20:23]\n"
  "ds_read_b64_tr_b16 a[210:211], v42 offset:43008\n"
  "s_waitcnt lgkmcnt(11)\n"
  "v_mfma_f32_16x16x32_bf16 v[28:31], a[224:227], a[72:75], v[28:31]\n"
  "ds_read_b64_tr_b16 a[212:213], v42 offset:45056\n"
  "buffer_load_dwordx4 a[152:155], off, s[76:79], s47 offset:1024 sc1\n"
  "v_mfma_f32_16x16x32_bf16 v[28:31], a[228:231], a[76:79], v[28:31]\n"
  "ds_read_b64_tr_b16 a[214:215], v42 offset:47104\n"
  "v_mfma_f32_16x16x32_bf16 v[24:27], a[228:231], a[68:71], v[24:27]\n"
  "ds_read_b64_tr_b16 a[216:217], v43 offset:40960\n"
  "v_mfma_f32_16x16x32_bf16 v[24:27], a[224:227], a[64:67], v[24:27]\n"
  "ds_read_b64_tr_b16 a[218:219], v43 offset:43008\n"
  "s_waitcnt vmcnt(14) lgkmcnt(10)\n"
  "v_mfma_f32_16x16x32_bf16 v[0:3], a[192:195], a[80:83], v[0:3]\n"
  "ds_read_b64_tr_b16 a[220:221], v43 offset:45056\n"
  "buffer_load_dwordx4 a[148:151], off, s[76:79], s47 offset:2048 sc1\n"
  "v_mfma_f32_16x16x32_bf16 v[0:3], a[196:199], a[84:87], v[0:3]\n"
  "ds_read_b64_tr_b16 a[222:223], v43 offset:47104\n"
  "v_mfma_f32_16x16x32_bf16 v[4:7], a[196:199], a[92:95], v[4:7]\n"
  "ds_read_b64_tr_b16 a[224:225], v40 offset:49152\n"
  "buffer_load_dwordx4 a[156:159], off, s[76:79], s47 offset:3072 sc1\n"
  "v_mfma_f32_16x16x32_bf16 v[4:7], a[192:195], a[88:91], v[4:7]\n"
  "ds_read_b64_tr_b16 a[226:227], v40 offset:51200\n"
  "s_waitcnt lgkmcnt(10)\n"
  "v_smfmac_f32_16x16x64_bf16 v[32:35], a[236:239], a[80:87], v45\n"
  "ds_read_b128 a[244:247], v44 offset:1792\n"
  "s_add_u32 s47, s47, 4096\n"
  "v_smfmac_f32_16x16x64_bf16 v[36:39], a[236:239], a[88:95], v45\n"
  "s_and_b32 s47, s47, 0xffff\n"
  "v_mfma_f32_16x16x32_bf16 v[12:15], a[200:203], a[88:91], v[12:15]\n"
  "ds_read_b64_tr_b16 a[228:229], v40 offset:53248\n"
  "v_mfma_f32_16x16x32_bf16 v[12:15], a[204:207], a[92:95], v[12:15]\n"
  "ds_read_b64_tr_b16 a[230:231], v40 offset:55296\n"
  "v_mfma_f32_16x16x32_bf16 v[8:11], a[204:207], a[84:87], v[8:11]\n"
  "ds_read_b64_tr_b16 a[192:193], v41 offset:49152\n"
  "buffer_load_dwordx4 a[160:163], off, s[76:79], s47 offset:0 sc1\n"
  "v_mfma_f32_16x16x32_bf16 v[8:11], a[200:203], a[80:83], v[8:11]\n"
  "ds_read_b64_tr_b16 a[194:195], v41 offset:51200\n"
  "s_waitcnt lgkmcnt(11)\n"
  "v_mfma_f32_16x16x32_bf16 v[16:19], a[208:211], a[80:83], v[16:19]\n"
  "ds_read_b64_tr_b16 a[196:197], v41 offset:53248\n"
  "buffer_load_dwordx4 a[168:171], off, s[76:79], s47 offset:1024 sc1\n"
  "v_mfma_f32_16x16x32_bf16 v[16:19], a[212:215], a[84:87], v[16:19]\n"
  "ds_read_b64_tr_b16 a[198:199], v41 offset:55296\n"
  "v_mfma_f32_16x16x32_bf16 v[20:23], a[212:215], a[92:95], v[20:23]\n"
  "ds_read_b64_tr_b16 a[200:201], v40 offset:57344\n"
  "v_mfma_f32_16x16x32_bf16 v[20:23], a[208:211], a[88:91], v[20:23]\n"
  "ds_read_b64_tr_b16 a[202:203], v40 offset:59392\n"
  "s_waitcnt lgkmcnt(11)\n"
  "v_mfma_f32_16x16x32_bf16 v[28:31], a[216:219], a[88:91], v[28:31]\n"
  "ds_read_b64_tr_b16 a[204:205], v40 offset:61440\n"
  "buffer_load_dwordx4 a[164:167], off, s[76:79], s47 offset:2048 sc1\n"
  "v_mfma_f32_16x16x32_bf16 v[28:31], a[220:223], a[92:95], v[28:31]\n"
  "ds_read_b64_tr_b16 a[206:207], v40 offset:63488\n"
  "v_add_u32 v40, 0x10000, v40\n"
  "v_mfma_f32_16x16x32_bf16 v[24:27], a[220:223], a[84:87], v[24:27]\n"
  "ds_read_b64_tr_b16 a[208:209], v41 offset:57344\n"
  "buffer_load_dwordx4 a[172:175], off, s[76:79], s47 offset:3072 sc1\n"
  "v_mfma_f32_16x16x32_bf16 v[24:27], a[216:219], a[80:83], v[24:27]\n"
  "ds_read_b64_tr_b16 a[210:211], v41 offset:59392\n"
  "s_waitcnt vmcnt(16) lgkmcnt(10)\n"
  "v_mfma_f32_16x16x32_bf16 v[0:3], a[224:227], a[96:99], v[0:3]\n"
  "ds_read_b64_tr_b16 a[212:213], v41 offset:61440\n"
  "s_add_u32 s47, s47, 4096\n"
  "v_mfma_f32_16x16x32_bf16 v[0:3], a[228:231], a[100:103], v[0:3]\n"
  "ds_read_b64_tr_b16 a[214:215], v41 offset:63488\n"
  "v_add_u32 v41, 0x10000, v41\n"
  "v_mfma_f32_16x16x32_bf16 v[4:7], a[228:231], a[108:111], v[4:7]\n"
  "ds_read_b64_tr_b16 a[216:217], v40 offset:0\n"
  "buffer_load_dwordx4 a[176:179], off, s[76:79], s47 offset:0 sc1\n"
  "v_mfma_f32_16x16x32_bf16 v[4:7], a[224:227], a[104:107], v[4:7]\n"
  "ds_read_b64_tr_b16 a[218:219], v40 offset:2048\n"
  "s_waitcnt lgkmcnt(10)\n"
  "v_smfmac_f32_16x16x64_bf16 v[32:35], a[240:243], a[96:103], v45\n"
  "ds_read_b128 a[232:235], v44 offset:2048\n"
  "v_smfmac_f32_16x16x64_bf16 v[36:39], a[240:243], a[104:111], v45\n"
  "v_mfma_f32_16x16x32_bf16 v[12:15], a[192:195], a[104:107], v[12:15]\n"
  "ds_read_b64_tr_b16 a[220:221], v40 offset:4096\n"
  "buffer_load_dwordx4 a[184:187], off, s[76:79], s47 offset:1024 sc1\n"
  "v_mfma_f32_16x16x32_bf16 v[12:15], a[196:199], a[108:111], v[12:15]\n"
  "ds_read_b64_tr_b16 a[222:223], v40 offset:6144\n"
  "v_mfma_f32_16x16x32_bf16 v[8:11], a[196:199], a[100:103], v[8:11]\n"
  "ds_read_b64_tr_b16 a[224:225], v41 offset:0\n"
  "v_mfma_f32_16x16x32_bf16 v[8:11], a[192:195], a[96:99], v[8:11]\n"
  "ds_read_b64_tr_b16 a[226:227], v41 offset:2048\n"
  "s_waitcnt vmcnt(14) lgkmcnt(11)\n"
  "v_mfma_f32_16x16x32_bf16 v[0:3], a[200:203], a[112:115], v[0:3]\n"
  "ds_read_b64_tr_b16 a[228:229], v41 offset:4096\n"
  "buffer_load_dwordx4 a[180:183], off, s[76:79], s47 offset:2048 sc1\n"
  "v_mfma_f32_16x16x32_bf16 v[0:3], a[204:207], a[116:119], v[0:3]\n"
  "ds_read_b64_tr_b16 a[230:231], v41 offset:6144\n"
  "v_mfma_f32_16x16x32_bf16 v[4:7], a[204:207], a[124:127], v[4:7]\n"
  "ds_read_b64_tr_b16 a[192:193], v40 offset:8192\n"
  "buffer_load_dwordx4 a[188:191], off, s[76:79], s47 offset:3072 sc1\n"
  "v_mfma_f32_16x16x32_bf16 v[4:7], a[200:203], a[120:123], v[4:7]\n"
  "ds_read_b64_tr_b16 a[194:195], v40 offset:10240\n"
  "s_waitcnt lgkmcnt(11)\n"
  "v_smfmac_f32_16x16x64_bf16 v[32:35], a[244:247], a[112:119], v45\n"
  "ds_read_b128 a[236:239], v44 offset:2304\n"
  "s_add_u32 s47, s47, 4096\n"
  "v_smfmac_f32_16x16x64_bf16 v[36:39], a[244:247], a[120:127], v45\n"
  "s_and_b32 s47, s47, 0xffff\n"
  "v_mfma_f32_16x16x32_bf16 v[12:15], a[208:211], a[120:123], v[12:15]\n"
  "ds_read_b64_tr_b16 a[196:197], v40 offset:12288\n"
  "v_mfma_f32_16x16x32_bf16 v[12:15], a[212:215], a[124:127], v[12:15]\n"
  "ds_read_b64_tr_b16 a[198:199], v40 offset:14336\n"
  "v_mfma_f32_16x16x32_bf16 v[8:11], a[212:215], a[116:119], v[8:11]\n"
  "ds_read_b64_tr_b16 a[200:201], v41 offset:8192\n"
  "buffer_load_dwordx4 a[0:3], off, s[76:79], s47 offset:0 sc1\n"
  "v_mfma_f32_16x16x32_bf16 v[8:11], a[208:211], a[112:115], v[8:11]\n"
  "s_waitcnt lgkmcnt(11)\n"
  "ds_read_b64_tr_b16 a[202:203], v41 offset:10240\n"
  "s_waitcnt vmcnt(13) lgkmcnt(11)\n"
  "v_mfma_f32_16x16x32_bf16 v[0:3], a[216:219], a[128:131], v[0:3]\n"
  "ds_read_b64_tr_b16 a[204:205], v41 offset:12288\n"
  "buffer_load_dwordx4 a[8:11], off, s[76:79], s47 offset:1024 sc1\n"
  "v_mfma_f32_16x16x32_bf16 v[0:3], a[220:223], a[132:135], v[0:3]\n"
  "ds_read_b64_tr_b16 a[206:207], v41 offset:14336\n"
  "v_mfma_f32_16x16x32_bf16 v[4:7], a[220:223], a[140:143], v[4:7]\n"
  "ds_read_b64_tr_b16 a[208:209], v40 offset:16384\n"
  "v_mfma_f32_16x16x32_bf16 v[4:7], a[216:219], a[136:139], v[4:7]\n"
  "ds_read_b64_tr_b16 a[210:211], v40 offset:18432\n"
  "s_waitcnt lgkmcnt(11)\n"
  "v_smfmac_f32_16x16x64_bf16 v[32:35], a[232:235], a[128:135], v45\n"
  "ds_read_b128 a[240:243], v44 offset:2560\n"
  "v_smfmac_f32_16x16x64_bf16 v[36:39], a[232:235], a[136:143], v45\n"
  "v_mfma_f32_16x16x32_bf16 v[12:15], a[224:227], a[136:139], v[12:15]\n"
  "ds_read_b64_tr_b16 a[212:213], v40 offset:20480\n"
  "buffer_load_dwordx4 a[4:7], off, s[76:79], s47 offset:2048 sc1\n"
  "v_mfma_f32_16x16x32_bf16 v[12:15], a[228:231], a[140:143], v[12:15]\n"
  "ds_read_b64_tr_b16 a[214:215], v40 offset:22528\n"
  "v_mfma_f32_16x16x32_bf16 v[8:11], a[228:231], a[132:135], v[8:11]\n"
  "ds_read_b64_tr_b16 a[216:217], v41 offset:16384\n"
  "buffer_load_dwordx4 a[12:15], off, s[76:79], s47 offset:3072 sc1\n"
  "v_mfma_f32_16x16x32_bf16 v[8:11], a[224:227], a[128:131], v[8:11]\n"
  "s_waitcnt lgkmcnt(11)\n"
  "ds_read_b64_tr_b16 a[218:219], v41 offset:18432\n"
  "s_waitcnt vmcnt(12) lgkmcnt(11)\n"
  "v_mfma_f32_16x16x32_bf16 v[0:3], a[192:195], a[144:147], v[0:3]\n"
  "ds_read_b64_tr_b16 a[220:221], v41 offset:20480\n"
  "s_add_u32 s47, s47, 4096\n"
  "v_mfma_f32_16x16x32_bf16 v[0:3], a[196:199], a[148:151], v[0:3]\n"
  "ds_read_b64_tr_b16 a[222:223], v41 offset:22528\n"
  "v_mfma_f32_16x16x32_bf16 v[4:7], a[196:199], a[156:159], v[4:7]\n"
  "ds_read_b64_tr_b16 a[224:225], v40 offset:24576\n"
  "buffer_load_dwordx4 a[16:19], off, s[76:79], s47 offset:0 sc1\n"
  "v_mfma_f32_16x16x32_bf16 v[4:7], a[192:195], a[152:155], v[4:7]\n"
  "ds_read_b64_tr_b16 a[226:227], v40 offset:26624\n"
  "s_waitcnt lgkmcnt(11)\n"
  "v_smfmac_f32_16x16x64_bf16 v[32:35], a[236:239], a[144:151], v45\n"
  "ds_read_b128 a[244:247], v44 offset:2816\n"
  "v_smfmac_f32_16x16x64_bf16 v[36:39], a[236:239], a[152:159], v45\n"
  "v_mfma_f32_16x16x32_bf16 v[12:15], a[200:203], a[152:155], v[12:15]\n"
  "ds_read_b64_tr_b16 a[228:229], v40 offset:28672\n"
  "buffer_load_dwordx4 a[24:27], off, s[76:79], s47 offset:1024 sc1\n"
  "v_mfma_f32_16x16x32_bf16 v[12:15], a[204:207], a[156:159], v[12:15]\n"
  "ds_read_b64_tr_b16 a[230:231], v40 offset:30720\n"
  "v_mfma_f32_16x16x32_bf16 v[8:11], a[204:207], a[148:151], v[8:11]\n"
  "ds_read_b64_tr_b16 a[192:193], v41 offset:24576\n"
  "v_mfma_f32_16x16x32_bf16 v[8:11], a[200:203], a[144:147], v[8:11]\n"
  "s_waitcnt lgkmcnt(11)\n"
  "ds_read_b64_tr_b16 a[194:195], v41 offset:26624\n"
  "s_waitcnt vmcnt(10) lgkmcnt(11)\n"
  "v_mfma_f32_16x16x32_bf16 v[0:3], a[208:211], a[160:163], v[0:3]\n"
  "ds_read_b64_tr_b16 a[196:197], v41 offset:28672\n"
  "buffer_load_dwordx4 a[20:23], off, s[76:79], s47 offset:2048 sc1\n"
  "v_mfma_f32_16x16x32_bf16 v[0:3], a[212:215], a[164:167], v[0:3]\n"
  "ds_read_b64_tr_b16 a[198:199], v41 offset:30720\n"
  "v_mfma_f32_16x16x32_bf16 v[4:7], a[212:215], a[172:175], v[4:7]\n"
  "ds_read_b64_tr_b16 a[200:201], v40 offset:32768\n"
  "buffer_load_dwordx4 a[28:31], off, s[76:79], s47 offset:3072 sc1\n"
  "v_mfma_f32_16x16x32_bf16 v[4:7], a[208:211], a[168:171], v[4:7]\n"
  "ds_read_b64_tr_b16 a[202:203], v40 offset:34816\n"
  "s_waitcnt lgkmcnt(11)\n"
  "v_smfmac_f32_16x16x64_bf16 v[32:35], a[240:243], a[160:167], v45\n"
  "ds_read_b128 a[232:235], v44 offset:3072\n"
  "s_add_u32 s47, s47, 4096\n"
  "v_smfmac_f32_16x16x64_bf16 v[36:39], a[240:243], a[168:175], v45\n"
  "s_and_b32 s47, s47, 0xffff\n"
  "v_mfma_f32_16x16x32_bf16 v[12:15], a[216:219], a[168:171], v[12:15]\n"
  "ds_read_b64_tr_b16 a[204:205], v40 offset:36864\n"
  "v_mfma_f32_16x16x32_bf16 v[12:15], a[220:223], a[172:175], v[12:15]\n"
  "ds_read_b64_tr_b16 a[206:207], v40 offset:38912\n"
  "v_mfma_f32_16x16x32_bf16 v[8:11], a[220:223], a[164:167], v[8:11]\n"
  "ds_read_b64_tr_b16 a[208:209], v41 offset:32768\n"
  "buffer_load_dwordx4 a[32:35], off, s[76:79], s47 offset:0 sc1\n"
  "v_mfma_f32_16x16x32_bf16 v[8:11], a[216:219], a[160:163], v[8:11]\n"
  "s_waitcnt lgkmcnt(11)\n"
  "ds_read_b64_tr_b16 a[210:211], v41 offset:34816\n"
  "s_waitcnt vmcnt(9) lgkmcnt(11)\n"
  "v_mfma_f32_16x16x32_bf16 v[0:3], a[224:227], a[176:179], v[0:3]\n"
  "ds_read_b64_tr_b16 a[212:213], v41 offset:36864\n"
  "buffer_load_dwordx4 a[40:43], off, s[76:79], s47 offset:1024 sc1\n"
  "v_mfma_f32_16x16x32_bf16 v[0:3], a[228:231], a[180:183], v[0:3]\n"
  "ds_read_b64_tr_b16 a[214:215], v41 offset:38912\n"
  "v_mfma_f32_16x16x32_bf16 v[4:7], a[228:231], a[188:191], v[4:7]\n"
  "ds_read_b64_tr_b16 a[216:217], v40 offset:40960\n"
  "v_mfma_f32_16x16x32_bf16 v[4:7], a[224:227], a[184:187], v[4:7]\n"
  "ds_read_b64_tr_b16 a[218:219], v40 offset:43008\n"
  "s_waitcnt lgkmcnt(11)\n"
  "v_smfmac_f32_16x16x64_bf16 v[32:35], a[244:247], a[176:183], v45\n"
  "ds_read_b128 a[236:239], v44 offset:3328\n"
  "v_smfmac_f32_16x16x64_bf16 v[36:39], a[244:247], a[184:191], v45\n"
  "v_mfma_f32_16x16x32_bf16 v[12:15], a[192:195], a[184:187], v[12:15]\n"
  "ds_read_b64_tr_b16 a[220:221], v40 offset:45056\n"
  "buffer_load_dwordx4 a[36:39], off, s[76:79], s47 offset:2048 sc1\n"
  "v_mfma_f32_16x16x32_bf16 v[12:15], a[196:199], a[188:191], v[12:15]\n"
  "ds_read_b64_tr_b16 a[222:223], v40 offset:47104\n"
  "v_mfma_f32_16x16x32_bf16 v[8:11], a[196:199], a[180:183], v[8:11]\n"
  "ds_read_b64_tr_b16 a[224:225], v41 offset:40960\n"
  "buffer_load_dwordx4 a[44:47], off, s[76:79], s47 offset:3072 sc1\n"
  "v_mfma_f32_16x16x32_bf16 v[8:11], a[192:195], a[176:179], v[8:11]\n"
  "s_waitcnt lgkmcnt(11)\n"
  "ds_read_b64_tr_b16 a[226:227], v41 offset:43008\n"
  "s_waitcnt vmcnt(8) lgkmcnt(11)\n"
  "v_mfma_f32_16x16x32_bf16 v[0:3], a[200:203], a[0:3], v[0:3]\n"
  "ds_read_b64_tr_b16 a[228:229], v41 offset:45056\n"
  "s_add_u32 s47, s47, 4096\n"
  "v_mfma_f32_16x16x32_bf16 v[0:3], a[204:207], a[4:7], v[0:3]\n"
  "ds_read_b64_tr_b16 a[230:231], v41 offset:47104\n"
  "v_mfma_f32_16x16x32_bf16 v[4:7], a[204:207], a[12:15], v[4:7]\n"
  "ds_read_b64_tr_b16 a[192:193], v40 offset:49152\n"
  "buffer_load_dwordx4 a[48:51], off, s[76:79], s47 offset:0 sc1\n"
  "v_mfma_f32_16x16x32_bf16 v[4:7], a[200:203], a[8:11], v[4:7]\n"
  "ds_read_b64_tr_b16 a[194:195], v40 offset:51200\n"
  "s_waitcnt lgkmcnt(11)\n"
  "v_smfmac_f32_16x16x64_bf16 v[32:35], a[232:235], a[0:7], v45\n"
  "ds_read_b128 a[240:243], v44 offset:3584\n"
  "v_smfmac_f32_16x16x64_bf16 v[36:39], a[232:235], a[8:15], v45\n"
  "v_mfma_f32_16x16x32_bf16 v[12:15], a[208:211], a[8:11], v[12:15]\n"
  "ds_read_b64_tr_b16 a[196:197], v40 offset:53248\n"
  "buffer_load_dwordx4 a[56:59], off, s[76:79], s47 offset:1024 sc1\n"
  "v_mfma_f32_16x16x32_bf16 v[12:15], a[212:215], a[12:15], v[12:15]\n"
  "ds_read_b64_tr_b16 a[198:199], v40 offset:55296\n"
  "v_mfma_f32_16x16x32_bf16 v[8:11], a[212:215], a[4:7], v[8:11]\n"
  "ds_read_b64_tr_b16 a[200:201], v41 offset:49152\n"
  "v_mfma_f32_16x16x32_bf16 v[8:11], a[208:211], a[0:3], v[8:11]\n"
  "s_waitcnt lgkmcnt(11)\n"
  "ds_read_b64_tr_b16 a[202:203], v41 offset:51200\n"
  "s_waitcnt vmcnt(6) lgkmcnt(11)\n"
  "v_mfma_f32_16x16x32_bf16 v[0:3], a[216:219], a[16:19], v[0:3]\n"
  "ds_read_b64_tr_b16 a[204:205], v41 offset:53248\n"
  "buffer_load_dwordx4 a[52:55], off, s[76:79], s47 offset:2048 sc1\n"
  "v_mfma_f32_16x16x32_bf16 v[0:3], a[220:223], a[20:23], v[0:3]\n"
  "ds_read_b64_tr_b16 a[206:207], v41 offset:55296\n"
  "v_mfma_f32_16x16x32_bf16 v[4:7], a[220:223], a[28:31], v[4:7]\n"
  "ds_read_b64_tr_b16 a[208:209], v40 offset:57344\n"
  "buffer_load_dwordx4 a[60:63], off, s[76:79], s47 offset:3072 sc1\n"
  "v_mfma_f32_16x16x32_bf16 v[4:7], a[216:219], a[24:27], v[4:7]\n"
  "ds_read_b64_tr_b16 a[210:211], v40 offset:59392\n"
  "s_waitcnt lgkmcnt(11)\n"
  "v_smfmac_f32_16x16x64_bf16 v[32:35], a[236:239], a[16:23], v45\n"
  "ds_read_b128 a[244:247], v44 offset:3840\n"
  "s_add_u32 s47, s47, 4096\n"
  "v_smfmac_f32_16x16x64_bf16 v[36:39], a[236:239], a[24:31], v45\n"
  "s_and_b32 s47, s47, 0xffff\n"
  "v_mfma_f32_16x16x32_bf16 v[12:15], a[224:227], a[24:27], v[12:15]\n"
  "ds_read_b64_tr_b16 a[212:213], v40 offset:61440\n"
  "s_barrier\n"
  "v_mfma_f32_16x16x32_bf16 v[12:15], a[228:231], a[28:31], v[12:15]\n"
  "ds_read_b64_tr_b16 a[214:215], v40 offset:63488\n"
  "s_add_u32 m0, s46, 0\n"
  "v_mfma_f32_16x16x32_bf16 v[8:11], a[228:231], a[20:23], v[8:11]\n"
  "ds_read_b64_tr_b16 a[216:217], v41 offset:57344\n"
  "buffer_load_dwordx4 v240, s[36:39], s45 offen offset:0 lds\n"
  "v_mfma_f32_16x16x32_bf16 v[8:11], a[224:227], a[16:19], v[8:11]\n"
  "s_waitcnt lgkmcnt(11)\n"
  "ds_read_b64_tr_b16 a[218:219], v41 offset:59392\n"
  "s_waitcnt vmcnt(5) lgkmcnt(11)\n"
  "v_mfma_f32_16x16x32_bf16 v[0:3], a[192:195], a[32:35], v[0:3]\n"
  "ds_read_b64_tr_b16 a[220:221], v41 offset:61440\n"
  "buffer_load_dwordx4 v241, s[36:39], s45 offen offset:1024 lds\n"
  "v_mfma_f32_16x16x32_bf16 v[0:3], a[196:199], a[36:39], v[0:3]\n"
  "ds_read_b64_tr_b16 a[222:223], v41 offset:63488\n"
  "v_mfma_f32_16x16x32_bf16 v[4:7], a[196:199], a[44:47], v[4:7]\n"
  "ds_read_b64_tr_b16 a[224:225], v42 offset:49152\n"
  "v_mfma_f32_16x16x32_bf16 v[4:7], a[192:195], a[40:43], v[4:7]\n"
  "ds_read_b64_tr_b16 a[226:227], v42 offset:51200\n"
  "s_waitcnt lgkmcnt(11)\n"
  "v_smfmac_f32_16x16x64_bf16 v[32:35], a[240:243], a[32:39], v45\n"
  "v_smfmac_f32_16x16x64_bf16 v[36:39], a[240:243], a[40:47], v45\n"
  "v_mfma_f32_16x16x32_bf16 v[12:15], a[200:203], a[40:43], v[12:15]\n"
  "ds_read_b64_tr_b16 a[228:229], v42 offset:53248\n"
  "buffer_load_dwordx4 v242, s[36:39], s45 offen offset:2048 lds\n"
  "v_mfma_f32_16x16x32_bf16 v[12:15], a[204:207], a[44:47], v[12:15]\n"
  "ds_read_b64_tr_b16 a[230:231], v42 offset:55296\n"
  "v_mfma_f32_16x16x32_bf16 v[8:11], a[204:207], a[36:39], v[8:11]\n"
  "ds_read_b64_tr_b16 a[192:193], v43 offset:49152\n"
  "buffer_load_dwordx4 v243, s[36:39], s45 offen offset:3072 lds\n"
  "v_mfma_f32_16x16x32_bf16 v[8:11], a[200:203], a[32:35], v[8:11]\n"
  "ds_read_b64_tr_b16 a[194:195], v43 offset:51200\n"
  "s_waitcnt vmcnt(4) lgkmcnt(10)\n"
  "v_mfma_f32_16x16x32_bf16 v[0:3], a[208:211], a[48:51], v[0:3]\n"
  "ds_read_b64_tr_b16 a[196:197], v43 offset:53248\n"
  "s_add_u32 m0, s46, 4096\n"
  "v_mfma_f32_16x16x32_bf16 v[0:3], a[212:215], a[52:55], v[0:3]\n"
  "ds_read_b64_tr_b16 a[198:199], v43 offset:55296\n"
  "v_mfma_f32_16x16x32_bf16 v[4:7], a[212:215], a[60:63], v[4:7]\n"
  "ds_read_b64_tr_b16 a[200:201], v42 offset:57344\n"
  "buffer_load_dwordx4 v244, s[36:39], s45 offen offset:0 lds\n"
  "v_mfma_f32_16x16x32_bf16 v[4:7], a[208:211], a[56:59], v[4:7]\n"
  "ds_read_b64_tr_b16 a[202:203], v42 offset:59392\n"
  "s_waitcnt lgkmcnt(10)\n"
  "v_smfmac_f32_16x16x64_bf16 v[32:35], a[244:247], a[48:55], v45\n"
  "v_smfmac_f32_16x16x64_bf16 v[36:39], a[244:247], a[56:63], v45\n"
  "v_mfma_f32_16x16x32_bf16 v[12:15], a[216:219], a[56:59], v[12:15]\n"
  "ds_read_b64_tr_b16 a[204:205], v42 offset:61440\n"
  "buffer_load_dwordx4 v245, s[36:39], s45 offen offset:1024 lds\n"
  "v_mfma_f32_16x16x32_bf16 v[12:15], a[220:223], a[60:63], v[12:15]\n"
  "ds_read_b64_tr_b16 a[206:207], v42 offset:63488\n"
  "v_add_u32 v42, 0x10000, v42\n"
  "v_mfma_f32_16x16x32_bf16 v[8:11], a[220:223], a[52:55], v[8:11]\n"
  "ds_read_b64_tr_b16 a[208:209], v43 offset:57344\n"
  "v_mfma_f32_16x16x32_bf16 v[8:11], a[216:219], a[48:51], v[8:11]\n"
  "ds_read_b64_tr_b16 a[210:211], v43 offset:59392\n"
  "s_waitcnt lgkmcnt(10)\n"
  "v_mfma_f32_16x16x32_bf16 v[16:19], a[224:227], a[96:99], v[16:19]\n"
  "ds_read_b64_tr_b16 a[212:213], v43 offset:61440\n"
  "buffer_load_dwordx4 v246, s[36:39], s45 offen offset:2048 lds\n"
  "v_add_f32 v32, v32, v33\n"
  "v_add_f32 v34, v34, v35\n"
  "v_mfma_f32_16x16x32_bf16 v[16:19], a[228:231], a[100:103], v[16:19]\n"
  "ds_read_b64_tr_b16 a[214:215], v43 offset:63488\n"
  "v_add_u32 v43, 0x10000, v43\n"
  "v_add_f32 v32, v32, v34\n"
  "v_add_f32 v228, v228, v32\n"
  "v_add_f32 v36, v36, v37\n"
  "v_add_f32 v38, v38, v39\n"
  "v_mfma_f32_16x16x32_bf16 v[20:23], a[228:231], a[108:111], v[20:23]\n"
  "ds_read_b64_tr_b16 a[216:217], v42 offset:0\n"
  "s_add_u32 s45, s45, 917504\n"
  "v_add_f32 v36, v36, v38\n"
  "v_add_f32 v229, v229, v36\n"
  "v_sub_f32 v64, v0, v228\n"
  "v_sub_f32 v65, v1, v228\n"
  "v_mfma_f32_16x16x32_bf16 v[20:23], a[224:227], a[104:107], v[20:23]\n"
  "ds_read_b64_tr_b16 a[218:219], v42 offset:2048\n"
  "s_waitcnt lgkmcnt(10)\n"
  "v_sub_f32 v66, v2, v228\n"
  "v_sub_f32 v67, v3, v228\n"
  "v_sub_f32 v72, v8, v228\n"
  "v_sub_f32 v73, v9, v228\n"
  "v_mfma_f32_16x16x32_bf16 v[28:31], a[192:195], a[104:107], v[28:31]\n"
  "ds_read_b64_tr_b16 a[220:221], v42 offset:4096\n"
  "v_sub_f32 v74, v10, v228\n"
  "v_sub_f32 v75, v11, v228\n"
  "v_cvt_pk_bf16_f32 v0, v64, v65\n"
  "v_cvt_pk_bf16_f32 v1, v66, v67\n"
  "v_cvt_pk_bf16_f32 v2, v72, v73\n"
  "v_mfma_f32_16x16x32_bf16 v[28:31], a[196:199], a[108:111], v[28:31]\n"
  "ds_read_b64_tr_b16 a[222:223], v42 offset:6144\n"
  "v_cvt_pk_bf16_f32 v3, v74, v75\n"
  "v_sub_f32 v68, v4, v229\n"
  "v_sub_f32 v69, v5, v229\n"
  "v_sub_f32 v70, v6, v229\n"
  "v_sub_f32 v71, v7, v229\n"
  "v_mfma_f32_16x16x32_bf16 v[24:27], a[196:199], a[100:103], v[24:27]\n"
  "ds_read_b64_tr_b16 a[224:225], v43 offset:0\n"
  "buffer_store_dwordx4 v[0:3], off, s[40:43], s48 offset:0 sc1\n"
  "v_sub_f32 v76, v12, v229\n"
  "v_sub_f32 v77, v13, v229\n"
  "v_mfma_f32_16x16x32_bf16 v[24:27], a[192:195], a[96:99], v[24:27]\n"
  "ds_read_b64_tr_b16 a[226:227], v43 offset:2048\n"
  "s_waitcnt lgkmcnt(10)\n"
  "v_sub_f32 v78, v14, v229\n"
  "v_sub_f32 v79, v15, v229\n"
  "v_cvt_pk_bf16_f32 v4, v68, v69\n"
  "v_cvt_pk_bf16_f32 v5, v70, v71\n"
  "v_mfma_f32_16x16x32_bf16 v[16:19], a[200:203], a[112:115], v[16:19]\n"
  "ds_read_b64_tr_b16 a[228:229], v43 offset:4096\n"
  "v_cvt_pk_bf16_f32 v6, v76, v77\n"
  "v_cvt_pk_bf16_f32 v7, v78, v79\n"
  "v_mfma_f32_16x16x32_bf16 v[16:19], a[204:207], a[116:119], v[16:19]\n"
  "ds_read_b64_tr_b16 a[230:231], v43 offset:6144\n"
  "buffer_store_dwordx4 v[4:7], off, s[40:43], s48 offset:1024 sc1\n"
  "v_mfma_f32_16x16x32_bf16 v[20:23], a[204:207], a[124:127], v[20:23]\n"
  "ds_read_b64_tr_b16 a[192:193], v42 offset:8192\n"
  "v_mfma_f32_16x16x32_bf16 v[20:23], a[200:203], a[120:123], v[20:23]\n"
  "ds_read_b64_tr_b16 a[194:195], v42 offset:10240\n"
  "s_waitcnt lgkmcnt(10)\n"
  "v_mfma_f32_16x16x32_bf16 v[28:31], a[208:211], a[120:123], v[28:31]\n"
  "ds_read_b64_tr_b16 a[196:197], v42 offset:12288\n"
  "v_mfma_f32_16x16x32_bf16 v[28:31], a[212:215], a[124:127], v[28:31]\n"
  "ds_read_b64_tr_b16 a[198:199], v42 offset:14336\n"
  "v_mfma_f32_16x16x32_bf16 v[24:27], a[212:215], a[116:119], v[24:27]\n"
  "ds_read_b64_tr_b16 a[200:201], v43 offset:8192\n"
  "v_mfma_f32_16x16x32_bf16 v[24:27], a[208:211], a[112:115], v[24:27]\n"
  "ds_read_b64_tr_b16 a[202:203], v43 offset:10240\n"
  "s_waitcnt lgkmcnt(10)\n"
  "v_mfma_f32_16x16x32_bf16 v[16:19], a[216:219], a[128:131], v[16:19]\n"
  "ds_read_b64_tr_b16 a[204:205], v43 offset:12288\n"
  "v_mfma_f32_16x16x32_bf16 v[16:19], a[220:223], a[132:135], v[16:19]\n"
  "ds_read_b64_tr_b16 a[206:207], v43 offset:14336\n"
  "v_mfma_f32_16x16x32_bf16 v[20:23], a[220:223], a[140:143], v[20:23]\n"
  "ds_read_b64_tr_b16 a[208:209], v42 offset:16384\n"
  "v_mfma_f32_16x16x32_bf16 v[20:23], a[216:219], a[136:139], v[20:23]\n"
  "ds_read_b64_tr_b16 a[210:211], v42 offset:18432\n"
  "s_waitcnt lgkmcnt(10)\n"
  "v_mfma_f32_16x16x32_bf16 v[28:31], a[224:227], a[136:139], v[28:31]\n"
  "ds_read_b64_tr_b16 a[212:213], v42 offset:20480\n"
  "v_mfma_f32_16x16x32_bf16 v[28:31], a[228:231], a[140:143], v[28:31]\n"
  "ds_read_b64_tr_b16 a[214:215], v42 offset:22528\n"
  "v_mfma_f32_16x16x32_bf16 v[24:27], a[228:231], a[132:135], v[24:27]\n"
  "ds_read_b64_tr_b16 a[216:217], v43 offset:16384\n"
  "v_mfma_f32_16x16x32_bf16 v[24:27], a[224:227], a[128:131], v[24:27]\n"
  "ds_read_b64_tr_b16 a[218:219], v43 offset:18432\n"
  "s_waitcnt lgkmcnt(10)\n"
  "v_mfma_f32_16x16x32_bf16 v[16:19], a[192:195], a[144:147], v[16:19]\n"
  "ds_read_b64_tr_b16 a[220:221], v43 offset:20480\n"
  "v_mfma_f32_16x16x32_bf16 v[16:19], a[196:199], a[148:151], v[16:19]\n"
  "ds_read_b64_tr_b16 a[222:223], v43 offset:22528\n"
  "v_mfma_f32_16x16x32_bf16 v[20:23], a[196:199], a[156:159], v[20:23]\n"
  "ds_read_b64_tr_b16 a[224:225], v42 offset:24576\n"
  "v_mfma_f32_16x16x32_bf16 v[20:23], a[192:195], a[152:155], v[20:23]\n"
  "ds_read_b64_tr_b16 a[226:227], v42 offset:26624\n"
  "s_waitcnt lgkmcnt(10)\n"
  "v_mfma_f32_16x16x32_bf16 v[28:31], a[200:203], a[152:155], v[28:31]\n"
  "ds_read_b64_tr_b16 a[228:229], v42 offset:28672\n"
  "v_mfma_f32_16x16x32_bf16 v[28:31], a[204:207], a[156:159], v[28:31]\n"
  "ds_read_b64_tr_b16 a[230:231], v42 offset:30720\n"
  "v_mfma_f32_16x16x32_bf16 v[24:27], a[204:207], a[148:151], v[24:27]\n"
  "ds_read_b64_tr_b16 a[192:193], v43 offset:24576\n"
  "v_mfma_f32_16x16x32_bf16 v[24:27], a[200:203], a[144:147], v[24:27]\n"
  "ds_read_b64_tr_b16 a[194:195], v43 offset:26624\n"
  "s_waitcnt lgkmcnt(10)\n"
  "v_mfma_f32_16x16x32_bf16 v[16:19], a[208:211], a[160:163], v[16:19]\n"
  "ds_read_b64_tr_b16 a[196:197], v43 offset:28672\n"
  "v_mfma_f32_16x16x32_bf16 v[16:19], a[212:215], a[164:167], v[16:19]\n"
  "ds_read_b64_tr_b16 a[198:199], v43 offset:30720\n"
  "v_mfma_f32_16x16x32_bf16 v[20:23], a[212:215], a[172:175], v[20:23]\n"
  "ds_read_b64_tr_b16 a[200:201], v42 offset:32768\n"
  "v_mfma_f32_16x16x32_bf16 v[20:23], a[208:211], a[168:171], v[20:23]\n"
  "ds_read_b64_tr_b16 a[202:203], v42 offset:34816\n"
  "s_waitcnt lgkmcnt(10)\n"
  "v_mfma_f32_16x16x32_bf16 v[28:31], a[216:219], a[168:171], v[28:31]\n"
  "ds_read_b64_tr_b16 a[204:205], v42 offset:36864\n"
  "s_waitcnt vmcnt(0)\n"
  "buffer_store_dwordx4 v[248:251], v251, s[52:55], s14 offen sc1\n"
  "v_mfma_f32_16x16x32_bf16 v[28:31], a[220:223], a[172:175], v[28:31]\n"
  "ds_read_b64_tr_b16 a[206:207], v42 offset:38912\n"
  "v_mfma_f32_16x16x32_bf16 v[24:27], a[220:223], a[164:167], v[24:27]\n"
  "ds_read_b64_tr_b16 a[208:209], v43 offset:32768\n"
  "v_mfma_f32_16x16x32_bf16 v[24:27], a[216:219], a[160:163], v[24:27]\n"
  "ds_read_b64_tr_b16 a[210:211], v43 offset:34816\n"
  "s_waitcnt lgkmcnt(10)\n"
  "v_mfma_f32_16x16x32_bf16 v[16:19], a[224:227], a[176:179], v[16:19]\n"
  "ds_read_b64_tr_b16 a[212:213], v43 offset:36864\n"
  "s_add_u32 m0, s46, 131072\n"
  "v_mfma_f32_16x16x32_bf16 v[16:19], a[228:231], a[180:183], v[16:19]\n"
  "ds_read_b64_tr_b16 a[214:215], v43 offset:38912\n"
  "v_mfma_f32_16x16x32_bf16 v[20:23], a[228:231], a[188:191], v[20:23]\n"
  "ds_read_b64_tr_b16 a[216:217], v42 offset:40960\n"
  "buffer_load_dwordx4 v240, s[36:39], s45 offen offset:0 lds\n"
  "v_mfma_f32_16x16x32_bf16 v[20:23], a[224:227], a[184:187], v[20:23]\n"
  "ds_read_b64_tr_b16 a[218:219], v42 offset:43008\n"
  "s_waitcnt lgkmcnt(10)\n"
  "v_mfma_f32_16x16x32_bf16 v[28:31], a[192:195], a[184:187], v[28:31]\n"
  "ds_read_b64_tr_b16 a[220:221], v42 offset:45056\n"
  "buffer_load_dwordx4 v241, s[36:39], s45 offen offset:1024 lds\n"
  "v_mfma_f32_16x16x32_bf16 v[28:31], a[196:199], a[188:191], v[28:31]\n"
  "ds_read_b64_tr_b16 a[222:223], v42 offset:47104\n"
  "v_mfma_f32_16x16x32_bf16 v[24:27], a[196:199], a[180:183], v[24:27]\n"
  "ds_read_b64_tr_b16 a[224:225], v43 offset:40960\n"
  "v_mfma_f32_16x16x32_bf16 v[24:27], a[192:195], a[176:179], v[24:27]\n"
  "ds_read_b64_tr_b16 a[226:227], v43 offset:43008\n"
  "s_waitcnt lgkmcnt(10)\n"
  "v_mfma_f32_16x16x32_bf16 v[16:19], a[200:203], a[0:3], v[16:19]\n"
  "ds_read_b64_tr_b16 a[228:229], v43 offset:45056\n"
  "buffer_load_dwordx4 v242, s[36:39], s45 offen offset:2048 lds\n"
  "v_mfma_f32_16x16x32_bf16 v[16:19], a[204:207], a[4:7], v[16:19]\n"
  "ds_read_b64_tr_b16 a[230:231], v43 offset:47104\n"
  "v_mfma_f32_16x16x32_bf16 v[20:23], a[204:207], a[12:15], v[20:23]\n"
  "ds_read_b64_tr_b16 a[192:193], v42 offset:49152\n"
  "buffer_load_dwordx4 v243, s[36:39], s45 offen offset:3072 lds\n"
  "v_mfma_f32_16x16x32_bf16 v[20:23], a[200:203], a[8:11], v[20:23]\n"
  "ds_read_b64_tr_b16 a[194:195], v42 offset:51200\n"
  "s_waitcnt lgkmcnt(10)\n"
  "v_mfma_f32_16x16x32_bf16 v[28:31], a[208:211], a[8:11], v[28:31]\n"
  "ds_read_b64_tr_b16 a[196:197], v42 offset:53248\n"
  "s_add_u32 m0, s46, 135168\n"
  "v_mfma_f32_16x16x32_bf16 v[28:31], a[212:215], a[12:15], v[28:31]\n"
  "ds_read_b64_tr_b16 a[198:199], v42 offset:55296\n"
  "v_mfma_f32_16x16x32_bf16 v[24:27], a[212:215], a[4:7], v[24:27]\n"
  "ds_read_b64_tr_b16 a[200:201], v43 offset:49152\n"
  "buffer_load_dwordx4 v244, s[36:39], s45 offen offset:0 lds\n"
  "v_mfma_f32_16x16x32_bf16 v[24:27], a[208:211], a[0:3], v[24:27]\n"
  "ds_read_b64_tr_b16 a[202:203], v43 offset:51200\n"
  "s_waitcnt lgkmcnt(10)\n"
  "v_mfma_f32_16x16x32_bf16 v[16:19], a[216:219], a[16:19], v[16:19]\n"
  "ds_read_b64_tr_b16 a[204:205], v43 offset:53248\n"
  "buffer_load_dwordx4 v245, s[36:39], s45 offen offset:1024 lds\n"
  "v_mfma_f32_16x16x32_bf16 v[16:19], a[220:223], a[20:23], v[16:19]\n"
  "ds_read_b64_tr_b16 a[206:207], v43 offset:55296\n"
  "v_mfma_f32_16x16x32_bf16 v[20:23], a[220:223], a[28:31], v[20:23]\n"
  "ds_read_b64_tr_b16 a[208:209], v42 offset:57344\n"
  "v_mfma_f32_16x16x32_bf16 v[20:23], a[216:219], a[24:27], v[20:23]\n"
  "ds_read_b64_tr_b16 a[210:211], v42 offset:59392\n"
  "s_waitcnt lgkmcnt(10)\n"
  "v_mfma_f32_16x16x32_bf16 v[28:31], a[224:227], a[24:27], v[28:31]\n"
  "ds_read_b64_tr_b16 a[212:213], v42 offset:61440\n"
  "buffer_load_dwordx4 v246, s[36:39], s45 offen offset:2048 lds\n"
  "v_mfma_f32_16x16x32_bf16 v[28:31], a[228:231], a[28:31], v[28:31]\n"
  "ds_read_b64_tr_b16 a[214:215], v42 offset:63488\n"
  "s_add_u32 s45, s45, 917504\n"
  "v_mfma_f32_16x16x32_bf16 v[24:27], a[228:231], a[20:23], v[24:27]\n"
  "ds_read_b64_tr_b16 a[216:217], v43 offset:57344\n"
  "v_mfma_f32_16x16x32_bf16 v[24:27], a[224:227], a[16:19], v[24:27]\n"
  "ds_read_b64_tr_b16 a[218:219], v43 offset:59392\n"
  "s_waitcnt lgkmcnt(10)\n"
  "v_mfma_f32_16x16x32_bf16 v[16:19], a[192:195], a[32:35], v[16:19]\n"
  "ds_read_b64_tr_b16 a[220:221], v43 offset:61440\n"
  "v_mfma_f32_16x16x32_bf16 v[16:19], a[196:199], a[36:39], v[16:19]\n"
  "ds_read_b64_tr_b16 a[222:223], v43 offset:63488\n"
  "v_mfma_f32_16x16x32_bf16 v[20:23], a[196:199], a[44:47], v[20:23]\n"
  "v_mfma_f32_16x16x32_bf16 v[20:23], a[192:195], a[40:43], v[20:23]\n"
  "s_waitcnt lgkmcnt(8)\n"
  "v_mfma_f32_16x16x32_bf16 v[28:31], a[200:203], a[40:43], v[28:31]\n"
  "v_mfma_f32_16x16x32_bf16 v[28:31], a[204:207], a[44:47], v[28:31]\n"
  "v_mfma_f32_16x16x32_bf16 v[24:27], a[204:207], a[36:39], v[24:27]\n"
  "v_mfma_f32_16x16x32_bf16 v[24:27], a[200:203], a[32:35], v[24:27]\n"
  "s_waitcnt lgkmcnt(4)\n"
  "v_mfma_f32_16x16x32_bf16 v[16:19], a[208:211], a[48:51], v[16:19]\n"
  "v_mfma_f32_16x16x32_bf16 v[16:19], a[212:215], a[52:55], v[16:19]\n"
  "v_mfma_f32_16x16x32_bf16 v[20:23], a[212:215], a[60:63], v[20:23]\n"
  "v_mfma_f32_16x16x32_bf16 v[20:23], a[208:211], a[56:59], v[20:23]\n"
  "s_waitcnt lgkmcnt(0)\n"
  "v_mfma_f32_16x16x32_bf16 v[28:31], a[216:219], a[56:59], v[28:31]\n"
  "v_mfma_f32_16x16x32_bf16 v[28:31], a[220:223], a[60:63], v[28:31]\n"
  "v_mfma_f32_16x16x32_bf16 v[24:27], a[220:223], a[52:55], v[24:27]\n"
  "buffer_load_dwordx2 v[52:53], v51, s[52:55], s93 offen sc1\n"
  "v_mfma_f32_16x16x32_bf16 v[24:27], a[216:219], a[48:51], v[24:27]\n"
  "v_sub_f32 v80, v16, v228\n"
  "v_sub_f32 v81, v17, v228\n"
  "v_sub_f32 v82, v18, v228\n"
  "v_sub_f32 v83, v19, v228\n"
  "s_nop 3\n"
  "v_sub_f32 v88, v24, v228\n"
  "v_sub_f32 v89, v25, v228\n"
  "v_sub_f32 v90, v26, v228\n"
  "v_sub_f32 v91, v27, v228\n"
  "v_cvt_pk_bf16_f32 v16, v80, v81\n"
  "v_cvt_pk_bf16_f32 v17, v82, v83\n"
  "v_cvt_pk_bf16_f32 v18, v88, v89\n"
  "v_cvt_pk_bf16_f32 v19, v90, v91\n"
  "buffer_store_dwordx4 v[16:19], off, s[40:43], s49 offset:0 sc1\n"
  "v_sub_f32 v84, v20, v229\n"
  "v_sub_f32 v85, v21, v229\n"
  "v_sub_f32 v86, v22, v229\n"
  "v_sub_f32 v87, v23, v229\n"
  "v_sub_f32 v92, v28, v229\n"
  "v_sub_f32 v93, v29, v229\n"
  "v_sub_f32 v94, v30, v229\n"
  "v_sub_f32 v95, v31, v229\n"
  "v_cvt_pk_bf16_f32 v20, v84, v85\n"
  "v_cvt_pk_bf16_f32 v21, v86, v87\n"
  "v_cvt_pk_bf16_f32 v22, v92, v93\n"
  "v_cvt_pk_bf16_f32 v23, v94, v95\n"
  "buffer_store_dwordx4 v[20:23], off, s[40:43], s49 offset:1024 sc1\n"
  "v_fmac_f32 v46, v64, v64\n"
  "v_fmac_f32 v46, v65, v65\n"
  "v_fmac_f32 v46, v66, v66\n"
  "v_fmac_f32 v46, v67, v67\n"
  "v_fmac_f32 v46, v72, v72\n"
  "v_fmac_f32 v46, v73, v73\n"
  "v_fmac_f32 v46, v74, v74\n"
  "v_fmac_f32 v46, v75, v75\n"
  "v_fmac_f32 v48, v68, v68\n"
  "v_fmac_f32 v48, v69, v69\n"
  "v_fmac_f32 v48, v70, v70\n"
  "v_fmac_f32 v48, v71, v71\n"
  "v_fmac_f32 v48, v76, v76\n"
  "v_fmac_f32 v48, v77, v77\n"
  "v_fmac_f32 v48, v78, v78\n"
  "v_fmac_f32 v48, v79, v79\n"
  "s_waitcnt vmcnt(2)\n"
  "L_poll4_%=:\n"
  "v_cmp_ne_u64 vcc, s[10:11], v[52:53]\n"
  "s_cbranch_vccz L_ok4_%=\n"
  "s_sub_u32 s99, s99, 1\n"
  "s_cbranch_scc1 L_out4_%=\n"
  "buffer_load_dwordx2 v[52:53], v51, s[52:55], s93 offen sc1\n"
  "s_waitcnt vmcnt(0)\n"
  "s_branch L_poll4_%=\n"
  "L_out4_%=:\n"
  "s_mov_b32 s99, 0\n"
  "L_ok4_%=:\n"
  "buffer_load_dwordx4 a[0:3], off, s[32:35], s44 offset:0 sc1\n"
  "buffer_load_dwordx4 a[4:7], off, s[32:35], s44 offset:1024 sc1\n"
  "buffer_load_dwordx4 a[8:11], off, s[32:35], s44 offset:2048 sc1\n"
  "buffer_load_dwordx4 a[12:15], off, s[32:35], s44 offset:3072 sc1\n"
  "s_add_u32 s44, s44, 4096\n"
  "buffer_load_dwordx4 a[16:19], off, s[32:35], s44 offset:0 sc1\n"
  "buffer_load_dwordx4 a[20:23], off, s[32:35], s44 offset:1024 sc1\n"
  "buffer_load_dwordx4 a[24:27], off, s[32:35], s44 offset:2048 sc1\n"
  "buffer_load_dwordx4 a[28:31], off, s[32:35], s44 offset:3072 sc1\n"
  "s_add_u32 s44, s44, 4096\n"
  "v_fmac_f32 v46, v80, v80\n"
  "v_fmac_f32 v46, v81, v81\n"
  "v_fmac_f32 v46, v82, v82\n"
  "v_fmac_f32 v46, v83, v83\n"
  "v_fmac_f32 v46, v88, v88\n"
  "v_fmac_f32 v46, v89, v89\n"
  "v_fmac_f32 v46, v90, v90\n"
  "v_fmac_f32 v46, v91, v91\n"
  "v_fmac_f32 v48, v84, v84\n"
  "v_fmac_f32 v48, v85, v85\n"
  "v_fmac_f32 v48, v86, v86\n"
  "v_fmac_f32 v48, v87, v87\n"
  "v_fmac_f32 v48, v92, v92\n"
  "v_fmac_f32 v48, v93, v93\n"
  "v_fmac_f32 v48, v94, v94\n"
  "v_fmac_f32 v48, v95, v95\n"
  "v_mov_b32 v56, v46\n"
  "v_mov_b32 v57, v48\n"
  "v_mov_b32 v47, v228\n"
  "v_permlane16_swap_b32 v56, v46\n"
  "v_permlane16_swap_b32 v57, v48\n"
  "v_add_f32 v46, v56, v46\n"
  "v_add_f32 v48, v57, v48\n"
  "v_mov_b32 v56, v46\n"
  "v_mov_b32 v57, v48\n"
  "v_mov_b32 v49, v229\n"
  "v_permlane32_swap_b32 v56, v46\n"
  "v_permlane32_swap_b32 v57, v48\n"
  "v_add_f32 v46, v56, v46\n"
  "v_add_f32 v48, v57, v48\n"
  "buffer_store_dwordx4 v[46:49], v250, s[52:55], s50 offen sc1\n"
  "s_lshl_b32 s8, s70, 4\n"
  "s_add_u32 s8, s8, s71\n"
  "s_mul_i32 s14, s8, 64\n"
  "s_add_u32 s14, s14, 1851392\n"
  "s_add_u32 s93, s93, 393216\n"
  "v_mov_b32 v227, v51\n"
  "; ---- phase 4: gate / up + ReGLU\n"
  "s_barrier\n"
  "ds_read_b64_tr_b16 a[160:161], v224 offset:0\n"
  "ds_read_b64_tr_b16 a[162:163], v224 offset:3584\n"
  "ds_read_b64_tr_b16 a[164:165], v224 offset:7168\n"
  "ds_read_b64_tr_b16 a[166:167], v224 offset:10752\n"
  "ds_read_b64_tr_b16 a[168:169], v224 offset:14336\n"
  "ds_read_b64_tr_b16 a[170:171], v224 offset:17920\n"
  "ds_read_b64_tr_b16 a[172:173], v224 offset:21504\n"
  "ds_read_b64_tr_b16 a[174:175], v224 offset:25088\n"
  "ds_read_b64_tr_b16 a[176:177], v224 offset:32\n"
  "ds_read_b64_tr_b16 a[178:179], v224 offset:3616\n"
  "s_waitcnt vmcnt(1) lgkmcnt(6)\n"
  "v_mfma_f32_16x16x32_bf16 v[0:3], a[160:163], a[0:3], 0\n"
  "ds_read_b64_tr_b16 a[180:181], v224 offset:7200\n"
  "v_mfma_f32_16x16x32_bf16 v[0:3], a[164:167], a[16:19], v[0:3]\n"
  "ds_read_b64_tr_b16 a[182:183], v224 offset:10784\n"
  "v_mfma_f32_16x16x32_bf16 v[4:7], a[164:167], a[20:23], 0\n"
  "buffer_load_dwordx4 a[32:35], off, s[32:35], s44 offset:0 sc1\n"
  "v_mfma_f32_16x16x32_bf16 v[4:7], a[160:163], a[4:7], v[4:7]\n"
  "v_mfma_f32_16x16x32_bf16 v[8:11], a[160:163], a[8:11], 0\n"
  "ds_read_b64_tr_b16 a[184:185], v224 offset:14368\n"
  "v_mfma_f32_16x16x32_bf16 v[8:11], a[164:167], a[24:27], v[8:11]\n"
  "ds_read_b64_tr_b16 a[186:187], v224 offset:17952\n"
  "s_waitcnt lgkmcnt(6)\n"
  "v_mfma_f32_16x16x32_bf16 v[12:15], a[164:167], a[28:31], 0\n"
  "buffer_load_dwordx4 a[36:39], off, s[32:35], s44 offset:1024 sc1\n"
  "v_mfma_f32_16x16x32_bf16 v[12:15], a[160:163], a[12:15], v[12:15]\n"
  "v_mfma_f32_16x16x32_bf16 v[28:31], a[168:171], a[12:15], 0\n"
  "ds_read_b64_tr_b16 a[188:189], v224 offset:21536\n"
  "v_mfma_f32_16x16x32_bf16 v[28:31], a[172:175], a[28:31], v[28:31]\n"
  "ds_read_b64_tr_b16 a[190:191], v224 offset:25120\n"
  "v_mfma_f32_16x16x32_bf16 v[24:27], a[172:175], a[24:27], 0\n"
  "buffer_load_dwordx4 a[40:43], off, s[32:35], s44 offset:2048 sc1\n"
  "v_mfma_f32_16x16x32_bf16 v[24:27], a[168:171], a[8:11], v[24:27]\n"
  "v_mfma_f32_16x16x32_bf16 v[20:23], a[168:171], a[4:7], 0\n"
  "ds_read_b64_tr_b16 a[160:161], v224 offset:64\n"
  "v_mfma_f32_16x16x32_bf16 v[20:23], a[172:175], a[20:23], v[20:23]\n"
  "ds_read_b64_tr_b16 a[162:163], v224 offset:3648\n"
  "s_waitcnt lgkmcnt(6)\n"
  "v_mfma_f32_16x16x32_bf16 v[16:19], a[172:175], a[16:19], 0\n"
  "buffer_load_dwordx4 a[44:47], off, s[32:35], s44 offset:3072 sc1\n"
  "v_mfma_f32_16x16x32_bf16 v[16:19], a[168:171], a[0:3], v[16:19]\n"
  "v_mfma_f32_16x16x32_bf16 v[32:35], a[176:179], a[0:3], 0\n"
  "ds_read_b64_tr_b16 a[164:165], v224 offset:7232\n"
  "s_add_u32 s44, s44, 4096\n"
  "v_mfma_f32_16x16x32_bf16 v[32:35], a[180:183], a[16:19], v[32:35]\n"
  "ds_read_b64_tr_b16 a[166:167], v224 offset:10816\n"
  "v_mfma_f32_16x16x32_bf16 v[36:39], a[180:183], a[20:23], 0\n"
  "v_mfma_f32_16x16x32_bf16 v[36:39], a[176:179], a[4:7], v[36:39]\n"
  "v_mfma_f32_16x16x32_bf16 v[40:43], a[176:179], a[8:11], 0\n"
  "ds_read_b64_tr_b16 a[168:169], v224 offset:14400\n"
  "v_mfma_f32_16x16x32_bf16 v[40:43], a[180:183], a[24:27], v[40:43]\n"
  "ds_read_b64_tr_b16 a[170:171], v224 offset:17984\n"
  "s_waitcnt lgkmcnt(6)\n"
  "v_mfma_f32_16x16x32_bf16 v[44:47], a[180:183], a[28:31], 0\n"
  "buffer_load_dwordx4 a[48:51], off, s[32:35], s44 offset:0 sc1\n"
  "v_mfma_f32_16x16x32_bf16 v[44:47], a[176:179], a[12:15], v[44:47]\n"
  "v_mfma_f32_16x16x32_bf16 v[60:63], a[184:187], a[12:15], 0\n"
  "ds_read_b64_tr_b16 a[172:173], v224 offset:21568\n"
  "v_mfma_f32_16x16x32_bf16 v[60:63], a[188:191], a[28:31], v[60:63]\n"
  "ds_read_b64_tr_b16 a[174:175], v224 offset:25152\n"
  "v_mfma_f32_16x16x32_bf16 v[56:59], a[188:191], a[24:27], 0\n"
  "v_mfma_f32_16x16x32_bf16 v[56:59], a[184:187], a[8:11], v[56:59]\n"
  "v_mfma_f32_16x16x32_bf16 v[52:55], a[184:187], a[4:7], 0\n"
  "ds_read_b64_tr_b16 a[176:177], v224 offset:96\n"
  "v_mfma_f32_16x16x32_bf16 v[52:55], a[188:191], a[20:23], v[52:55]\n"
  "ds_read_b64_tr_b16 a[178:179], v224 offset:3680\n"
  "s_waitcnt lgkmcnt(6)\n"
  "v_mfma_f32_16x16x32_bf16 v[48:51], a[188:191], a[16:19], 0\n"
  "buffer_load_dwordx4 a[52:55], off, s[32:35], s44 offset:1024 sc1\n"
  "v_mfma_f32_16x16x32_bf16 v[48:51], a[184:187], a[0:3], v[48:51]\n"
  "v_mfma_f32_16x16x32_bf16 v[64:67], a[160:163], a[0:3], 0\n"
  "ds_read_b64_tr_b16 a[180:181], v224 offset:7264\n"
  "v_mfma_f32_16x16x32_bf16 v[64:67], a[164:167], a[16:19], v[64:67]\n"
  "ds_read_b64_tr_b16 a[182:183], v224 offset:10848\n"
  "v_mfma_f32_16x16x32_bf16 v[68:71], a[164:167], a[20:23], 0\n"
  "buffer_load_dwordx4 a[56:59], off, s[32:35], s44 offset:2048 sc1\n"
  "v_mfma_f32_16x16x32_bf16 v[68:71], a[160:163], a[4:7], v[68:71]\n"
  "v_mfma_f32_16x16x32_bf16 v[72:75], a[160:163], a[8:11], 0\n"
  "ds_read_b64_tr_b16 a[184:185], v224 offset:14432\n"
  "v_mfma_f32_16x16x32_bf16 v[72:75], a[164:167], a[24:27], v[72:75]\n"
  "ds_read_b64_tr_b16 a[186:187], v224 offset:18016\n"
  "s_waitcnt lgkmcnt(6)\n"
  "v_mfma_f32_16x16x32_bf16 v[76:79], a[164:167], a[28:31], 0\n"
  "buffer_load_dwordx4 a[60:63], off, s[32:35], s44 offset:3072 sc1\n"
  "v_mfma_f32_16x16x32_bf16 v[76:79], a[160:163], a[12:15], v[76:79]\n"
  "v_mfma_f32_16x16x32_bf16 v[92:95], a[168:171], a[12:15], 0\n"
  "ds_read_b64_tr_b16 a[188:189], v224 offset:21600\n"
  "s_add_u32 s44, s44, 4096\n"
  "v_mfma_f32_16x16x32_bf16 v[92:95], a[172:175], a[28:31], v[92:95]\n"
  "ds_read_b64_tr_b16 a[190:191], v224 offset:25184\n"
  "s_add_u32 m0, s46, 86016\n"
  "v_mfma_f32_16x16x32_bf16 v[88:91], a[172:175], a[24:27], 0\n"
  "v_mfma_f32_16x16x32_bf16 v[88:91], a[168:171], a[8:11], v[88:91]\n"
  "v_mfma_f32_16x16x32_bf16 v[84:87], a[168:171], a[4:7], 0\n"
  "ds_read_b64_tr_b16 a[160:161], v224 offset:128\n"
  "v_mfma_f32_16x16x32_bf16 v[84:87], a[172:175], a[20:23], v[84:87]\n"
  "ds_read_b64_tr_b16 a[162:163], v224 offset:3712\n"
  "s_waitcnt lgkmcnt(6)\n"
  "v_mfma_f32_16x16x32_bf16 v[80:83], a[172:175], a[16:19], 0\n"
  "v_mfma_f32_16x16x32_bf16 v[80:83], a[168:171], a[0:3], v[80:83]\n"
  "v_mfma_f32_16x16x32_bf16 v[96:99], a[176:179], a[0:3], 0\n"
  "ds_read_b64_tr_b16 a[164:165], v224 offset:7296\n"
  "v_mfma_f32_16x16x32_bf16 v[96:99], a[180:183], a[16:19], v[96:99]\n"
  "ds_read_b64_tr_b16 a[166:167], v224 offset:10880\n"
  "v_mfma_f32_16x16x32_bf16 v[100:103], a[180:183], a[20:23], 0\n"
  "buffer_load_dwordx4 v240, s[36:39], s45 offen offset:0 lds\n"
  "v_mfma_f32_16x16x32_bf16 v[100:103], a[176:179], a[4:7], v[100:103]\n"
  "v_mfma_f32_16x16x32_bf16 v[104:107], a[176:179], a[8:11], 0\n"
  "ds_read_b64_tr_b16 a[168:169], v224 offset:14464\n"
  "v_mfma_f32_16x16x32_bf16 v[104:107], a[180:183], a[24:27], v[104:107]\n"
  "ds_read_b64_tr_b16 a[170:171], v224 offset:18048\n"
  "s_waitcnt lgkmcnt(6)\n"
  "v_mfma_f32_16x16x32_bf16 v[108:111], a[180:183], a[28:31], 0\n"
  "v_mfma_f32_16x16x32_bf16 v[108:111], a[176:179], a[12:15], v[108:111]\n"
  "v_mfma_f32_16x16x32_bf16 v[124:127], a[184:187], a[12:15], 0\n"
  "ds_read_b64_tr_b16 a[172:173], v224 offset:21632\n"
  "v_mfma_f32_16x16x32_bf16 v[124:127], a[188:191], a[28:31], v[124:127]\n"
  "ds_read_b64_tr_b16 a[174:175], v224 offset:25216\n"
  "v_mfma_f32_16x16x32_bf16 v[120:123], a[188:191], a[24:27], 0\n"
  "buffer_load_dwordx4 v241, s[36:39], s45 offen offset:1024 lds\n"
  "v_mfma_f32_16x16x32_bf16 v[120:123], a[184:187], a[8:11], v[120:123]\n"
  "v_mfma_f32_16x16x32_bf16 v[116:119], a[184:187], a[4:7], 0\n"
  "ds_read_b64_tr_b16 a[176:177], v224 offset:160\n"
  "v_mfma_f32_16x16x32_bf16 v[116:119], a[188:191], a[20:23], v[116:119]\n"
  "ds_read_b64_tr_b16 a[178:179], v224 offset:3744\n"
  "s_waitcnt lgkmcnt(6)\n"
  "v_mfma_f32_16x16x32_bf16 v[112:115], a[188:191], a[16:19], 0\n"
  "buffer_load_dwordx4 v242, s[36:39], s45 offen offset:2048 lds\n"
  "v_mfma_f32_16x16x32_bf16 v[112:115], a[184:187], a[0:3], v[112:115]\n"
  "v_mfma_f32_16x16x32_bf16 v[128:131], a[160:163], a[0:3], 0\n"
  "ds_read_b64_tr_b16 a[180:181], v224 offset:7328\n"
  "v_mfma_f32_16x16x32_bf16 v[128:131], a[164:167], a[16:19], v[128:131]\n"
  "ds_read_b64_tr_b16 a[182:183], v224 offset:10912\n"
  "v_mfma_f32_16x16x32_bf16 v[132:135], a[164:167], a[20:23], 0\n"
  "buffer_load_dwordx4 v243, s[36:39], s45 offen offset:3072 lds\n"
  "v_mfma_f32_16x16x32_bf16 v[132:135], a[160:163], a[4:7], v[132:135]\n"
  "v_mfma_f32_16x16x32_bf16 v[136:139], a[160:163], a[8:11], 0\n"
  "ds_read_b64_tr_b16 a[184:185], v224 offset:14496\n"
  "s_add_u32 m0, s46, 90112\n"
  "v_mfma_f32_16x16x32_bf16 v[136:139], a[164:167], a[24:27], v[136:139]\n"
  "ds_read_b64_tr_b16 a[186:187], v224 offset:18080\n"
  "s_waitcnt lgkmcnt(6)\n"
  "v_mfma_f32_16x16x32_bf16 v[140:143], a[164:167], a[28:31], 0\n"
  "v_mfma_f32_16x16x32_bf16 v[140:143], a[160:163], a[12:15], v[140:143]\n"
  "v_mfma_f32_16x16x32_bf16 v[156:159], a[168:171], a[12:15], 0\n"
  "ds_read_b64_tr_b16 a[188:189], v224 offset:21664\n"
  "v_mfma_f32_16x16x32_bf16 v[156:159], a[172:175], a[28:31], v[156:159]\n"
  "ds_read_b64_tr_b16 a[190:191], v224 offset:25248\n"
  "v_mfma_f32_16x16x32_bf16 v[152:155], a[172:175], a[24:27], 0\n"
  "buffer_load_dwordx4 v244, s[36:39], s45 offen offset:0 lds\n"
  "v_mfma_f32_16x16x32_bf16 v[152:155], a[168:171], a[8:11], v[152:155]\n"
  "v_mfma_f32_16x16x32_bf16 v[148:151], a[168:171], a[4:7], 0\n"
  "ds_read_b64_tr_b16 a[160:161], v224 offset:192\n"
  "v_mfma_f32_16x16x32_bf16 v[148:151], a[172:175], a[20:23], v[148:151]\n"
  "ds_read_b64_tr_b16 a[162:163], v224 offset:3776\n"
  "s_waitcnt lgkmcnt(6)\n"
  "v_mfma_f32_16x16x32_bf16 v[144:147], a[172:175], a[16:19], 0\n"
  "buffer_load_dwordx4 v245, s[36:39], s45 offen offset:1024 lds\n"
  "v_mfma_f32_16x16x32_bf16 v[144:147], a[168:171], a[0:3], v[144:147]\n"
  "v_mfma_f32_16x16x32_bf16 v[160:163], a[176:179], a[0:3], 0\n"
  "ds_read_b64_tr_b16 a[164:165], v224 offset:7360\n"
  "v_mfma_f32_16x16x32_bf16 v[160:163], a[180:183], a[16:19], v[160:163]\n"
  "ds_read_b64_tr_b16 a[166:167], v224 offset:10944\n"
  "v_mfma_f32_16x16x32_bf16 v[164:167], a[180:183], a[20:23], 0\n"
  "v_mfma_f32_16x16x32_bf16 v[164:167], a[176:179], a[4:7], v[164:167]\n"
  "v_mfma_f32_16x16x32_bf16 v[168:171], a[176:179], a[8:11], 0\n"
  "ds_read_b64_tr_b16 a[168:169], v224 offset:14528\n"
  "v_mfma_f32_16x16x32_bf16 v[168:171], a[180:183], a[24:27], v[168:171]\n"
  "ds_read_b64_tr_b16 a[170:171], v224 offset:18112\n"
  "s_waitcnt lgkmcnt(6)\n"
  "v_mfma_f32_16x16x32_bf16 v[172:175], a[180:183], a[28:31], 0\n"
  "buffer_load_dwordx4 v246, s[36:39], s45 offen offset:2048 lds\n"
  "v_mfma_f32_16x16x32_bf16 v[172:175], a[176:179], a[12:15], v[172:175]\n"
  "v_mfma_f32_16x16x32_bf16 v[188:191], a[184:187], a[12:15], 0\n"
  "ds_read_b64_tr_b16 a[172:173], v224 offset:21696\n"
  "s_add_u32 s45, s45, 917504\n"
  "v_mfma_f32_16x16x32_bf16 v[188:191], a[188:191], a[28:31], v[188:191]\n"
  "ds_read_b64_tr_b16 a[174:175], v224 offset:25280\n"
  "v_mfma_f32_16x16x32_bf16 v[184:187], a[188:191], a[24:27], 0\n"
  "v_mfma_f32_16x16x32_bf16 v[184:187], a[184:187], a[8:11], v[184:187]\n"
  "v_mfma_f32_16x16x32_bf16 v[180:183], a[184:187], a[4:7], 0\n"
  "s_barrier\n"
  "v_mfma_f32_16x16x32_bf16 v[180:183], a[188:191], a[20:23], v[180:183]\n"
  "ds_read_b64_tr_b16 a[176:177], v226 offset:16384\n"
  "v_mfma_f32_16x16x32_bf16 v[176:179], a[188:191], a[16:19], 0\n"
  "ds_read_b64_tr_b16 a[178:179], v226 offset:19968\n"
  "s_waitcnt lgkmcnt(6)\n"
  "v_mfma_f32_16x16x32_bf16 v[176:179], a[184:187], a[0:3], v[176:179]\n"
  "buffer_load_dwordx4 a[64:67], off, s[32:35], s44 offset:0 sc1\n"
  "v_mfma_f32_16x16x32_bf16 v[192:195], a[160:163], a[0:3], 0\n"
  "ds_read_b64_tr_b16 a[180:181], v226 offset:23552\n"
  "v_mfma_f32_16x16x32_bf16 v[192:195], a[164:167], a[16:19], v[192:195]\n"
  "ds_read_b64_tr_b16 a[182:183], v226 offset:27136\n"
  "v_mfma_f32_16x16x32_bf16 v[196:199], a[164:167], a[20:23], 0\n"
  "buffer_load_dwordx4 a[68:71], off, s[32:35], s44 offset:1024 sc1\n"
  "v_mfma_f32_16x16x32_bf16 v[196:199], a[160:163], a[4:7], v[196:199]\n"
  "v_mfma_f32_16x16x32_bf16 v[200:203], a[160:163], a[8:11], 0\n"
  "ds_read_b64_tr_b16 a[184:185], v226 offset:30720\n"
  "v_mfma_f32_16x16x32_bf16 v[200:203], a[164:167], a[24:27], v[200:203]\n"
  "ds_read_b64_tr_b16 a[186:187], v226 offset:34304\n"
  "s_waitcnt lgkmcnt(6)\n"
  "v_mfma_f32_16x16x32_bf16 v[204:207], a[164:167], a[28:31], 0\n"
  "buffer_load_dwordx4 a[72:75], off, s[32:35], s44 offset:2048 sc1\n"
  "v_mfma_f32_16x16x32_bf16 v[204:207], a[160:163], a[12:15], v[204:207]\n"
  "v_mfma_f32_16x16x32_bf16 v[220:223], a[168:171], a[12:15], 0\n"
  "ds_read_b64_tr_b16 a[188:189], v226 offset:37888\n"
  "v_mfma_f32_16x16x32_bf16 v[220:223], a[172:175], a[28:31], v[220:223]\n"
  "ds_read_b64_tr_b16 a[190:191], v226 offset:41472\n"
  "v_mfma_f32_16x16x32_bf16 v[216:219], a[172:175], a[24:27], 0\n"
  "s_waitcnt vmcnt(18)\n"
  "buffer_store_dwordx4 v[248:251], v251, s[52:55], s14 offen sc1\n"
  "v_mfma_f32_16x16x32_bf16 v[216:219], a[168:171], a[8:11], v[216:219]\n"
  "v_mfma_f32_16x16x32_bf16 v[212:215], a[168:171], a[4:7], 0\n"
  "ds_read_b64_tr_b16 a[160:161], v226 offset:16416\n"
  "v_mfma_f32_16x16x32_bf16 v[212:215], a[172:175], a[20:23], v[212:215]\n"
  "ds_read_b64_tr_b16 a[162:163], v226 offset:20000\n"
  "s_waitcnt vmcnt(11) lgkmcnt(6)\n"
  "v_mfma_f32_16x16x32_bf16 v[208:211], a[172:175], a[16:19], 0\n"
  "v_mfma_f32_16x16x32_bf16 v[208:211], a[168:171], a[0:3], v[208:211]\n"
  "v_mfma_f32_16x16x32_bf16 v[0:3], a[176:179], a[32:35], v[0:3]\n"
  "ds_read_b64_tr_b16 a[164:165], v226 offset:23584\n"
  "s_mul_i32 s13, s12, 458752\n"
  "v_mfma_f32_16x16x32_bf16 v[0:3], a[180:183], a[48:51], v[0:3]\n"
  "ds_read_b64_tr_b16 a[166:167], v226 offset:27168\n"
  "v_mfma_f32_16x16x32_bf16 v[4:7], a[180:183], a[52:55], v[4:7]\n"
  "buffer_load_dwordx4 a[76:79], off, s[32:35], s44 offset:3072 sc1\n"
  "v_mfma_f32_16x16x32_bf16 v[4:7], a[176:179], a[36:39], v[4:7]\n"
  "s_add_u32 s40, s30, 25165824\n"
  "s_addc_u32 s41, s31, 0\n"
  "v_mfma_f32_16x16x32_bf16 v[8:11], a[176:179], a[40:43], v[8:11]\n"
  "ds_read_b64_tr_b16 a[168:169], v226 offset:30752\n"
  "s_add_u32 s44, s44, 4096\n"
  "v_mfma_f32_16x16x32_bf16 v[8:11], a[180:183], a[56:59], v[8:11]\n"
  "ds_read_b64_tr_b16 a[170:171], v226 offset:34336\n"
  "s_waitcnt lgkmcnt(6)\n"
  "v_mfma_f32_16x16x32_bf16 v[12:15], a[180:183], a[60:63], v[12:15]\n"
  "s_add_u32 s40, s40, s13\n"
  "s_addc_u32 s41, s41, 0\n"
  "s_pack_ll_b32_b16 s41, s41, 16\n"
  "v_mfma_f32_16x16x32_bf16 v[12:15], a[176:179], a[44:47], v[12:15]\n"
  "s_mov_b32 s42, 0\n"
  "s_mov_b32 s43, 0x800000\n"
  "s_lshr_b32 s8, s7, 3\n"
  "v_mfma_f32_16x16x32_bf16 v[28:31], a[184:187], a[44:47], v[28:31]\n"
  "ds_read_b64_tr_b16 a[172:173], v226 offset:37920\n"
  "s_mul_i32 s8, s8, 114688\n"
  "v_mfma_f32_16x16x32_bf16 v[28:31], a[188:191], a[60:63], v[28:31]\n"
  "ds_read_b64_tr_b16 a[174:175], v226 offset:41504\n"
  "s_and_b32 s9, s7, 7\n"
  "v_mfma_f32_16x16x32_bf16 v[24:27], a[188:191], a[56:59], v[24:27]\n"
  "buffer_load_dwordx4 a[80:83], off, s[32:35], s44 offset:0 sc1\n"
  "v_mfma_f32_16x16x32_bf16 v[24:27], a[184:187], a[40:43], v[24:27]\n"
  "s_lshl_b32 s13, s9, 12\n"
  "s_add_u32 s13, s8, s13\n"
  "s_add_u32 s57, s13, 16384\n"
  "v_mfma_f32_16x16x32_bf16 v[20:23], a[184:187], a[36:39], v[20:23]\n"
  "ds_read_b64_tr_b16 a[176:177], v226 offset:16448\n"
  "s_add_u32 s58, s13, 49152\n"
  "v_mfma_f32_16x16x32_bf16 v[20:23], a[188:191], a[52:55], v[20:23]\n"
  "ds_read_b64_tr_b16 a[178:179], v226 offset:20032\n"
  "s_waitcnt lgkmcnt(6)\n"
  "v_mfma_f32_16x16x32_bf16 v[16:19], a[188:191], a[48:51], v[16:19]\n"
  "buffer_load_dwordx4 a[84:87], off, s[32:35], s44 offset:1024 sc1\n"
  "v_mfma_f32_16x16x32_bf16 v[16:19], a[184:187], a[32:35], v[16:19]\n"
  "s_add_u32 s59, s13, 81920\n"
  "s_lshr_b32 s9, s9, 1\n"
  "s_lshl_b32 s9, s9, 12\n"
  "v_mfma_f32_16x16x32_bf16 v[32:35], a[160:163], a[32:35], v[32:35]\n"
  "ds_read_b64_tr_b16 a[180:181], v226 offset:23616\n"
  "s_add_u32 s8, s8, s9\n"
  "v_mfma_f32_16x16x32_bf16 v[32:35], a[164:167], a[48:51], v[32:35]\n"
  "ds_read_b64_tr_b16 a[182:183], v226 offset:27200\n"
  "s_and_b32 s9, s7, 1\n"
  "v_mfma_f32_16x16x32_bf16 v[36:39], a[164:167], a[52:55], v[36:39]\n"
  "buffer_load_dwordx4 a[88:91], off, s[32:35], s44 offset:2048 sc1\n"
  "v_mfma_f32_16x16x32_bf16 v[36:39], a[160:163], a[36:39], v[36:39]\n"
  "s_lshl_b32 s9, s9, 10\n"
  "s_add_u32 s56, s8, s9\n"
  "s_lshl_b32 s8, s12, 7\n"
  "v_mfma_f32_16x16x32_bf16 v[40:43], a[160:163], a[40:43], v[40:43]\n"
  "ds_read_b64_tr_b16 a[184:185], v226 offset:30784\n"
  "s_add_u32 s8, s8, s7\n"
  "v_mfma_f32_16x16x32_bf16 v[40:43], a[164:167], a[56:59], v[40:43]\n"
  "ds_read_b64_tr_b16 a[186:187], v226 offset:34368\n"
  "s_waitcnt lgkmcnt(6)\n"
  "v_mfma_f32_16x16x32_bf16 v[44:47], a[164:167], a[60:63], v[44:47]\n"
  "buffer_load_dwordx4 a[92:95], off, s[32:35], s44 offset:3072 sc1\n"
  "v_mfma_f32_16x16x32_bf16 v[44:47], a[160:163], a[44:47], v[44:47]\n"
  "s_mul_i32 s8, s8, 64\n"
  "s_add_u32 s64, s8, 1982464\n"
  "s_add_u32 s65, s8, 1984512\n"
  "v_mfma_f32_16x16x32_bf16 v[60:63], a[168:171], a[44:47], v[60:63]\n"
  "ds_read_b64_tr_b16 a[188:189], v226 offset:37952\n"
  "s_add_u32 s44, s44, 4096\n"
  "v_mfma_f32_16x16x32_bf16 v[60:63], a[172:175], a[60:63], v[60:63]\n"
  "ds_read_b64_tr_b16 a[190:191], v226 offset:41536\n"
  "s_add_u32 m0, s46, 28672\n"
  "v_mfma_f32_16x16x32_bf16 v[56:59], a[172:175], a[56:59], v[56:59]\n"
  "s_add_u32 s66, s8, 1986560\n"
  "s_add_u32 s67, s8, 1988608\n"
  "s_and_b32 s8, s4, 4\n"
  "v_mfma_f32_16x16x32_bf16 v[56:59], a[168:171], a[40:43], v[56:59]\n"
  "s_and_b32 s9, s5, 3\n"
  "s_add_u32 s8, s8, s9\n"
  "s_lshl_b32 s8, s8, 2\n"
  "v_mfma_f32_16x16x32_bf16 v[52:55], a[168:171], a[36:39], v[52:55]\n"
  "ds_read_b64_tr_b16 a[160:161], v226 offset:16480\n"
  "s_add_u32 s87, s8, s3\n"
  "v_mfma_f32_16x16x32_bf16 v[52:55], a[172:175], a[52:55], v[52:55]\n"
  "ds_read_b64_tr_b16 a[162:163], v226 offset:20064\n"
  "s_waitcnt lgkmcnt(6)\n"
  "v_mfma_f32_16x16x32_bf16 v[48:51], a[172:175], a[48:51], v[48:51]\n"
  "s_and_b32 s89, s4, 3\n"
  "s_and_b32 s8, s5, 28\n"
  "s_add_u32 s88, s8, s89\n"
  "v_mfma_f32_16x16x32_bf16 v[48:51], a[168:171], a[32:35], v[48:51]\n"
  "s_lshl_b32 s8, s8, 6\n"
  "s_mul_i32 s9, s89, 1835008\n"
  "s_add_u32 s8, s8, s9\n"
  "v_mfma_f32_16x16x32_bf16 v[64:67], a[176:179], a[32:35], v[64:67]\n"
  "ds_read_b64_tr_b16 a[164:165], v226 offset:23648\n"
  "s_lshl_b32 s9, s91, 15\n"
  "v_mfma_f32_16x16x32_bf16 v[64:67], a[180:183], a[48:51], v[64:67]\n"
  "ds_read_b64_tr_b16 a[166:167], v226 offset:27232\n"
  "s_add_u32 s8, s8, s9\n"
  "v_mfma_f32_16x16x32_bf16 v[68:71], a[180:183], a[52:55], v[68:71]\n"
  "s_sub_u32 s13, 7342080, s8\n"
  "s_sub_u32 s76, s26, 2048\n"
  "s_subb_u32 s77, s27, 0\n"
  "v_mfma_f32_16x16x32_bf16 v[68:71], a[176:179], a[36:39], v[68:71]\n"
  "s_add_u32 s76, s76, s8\n"
  "s_addc_u32 s77, s77, 0\n"
  "s_and_b32 s77, s77, 0xffff\n"
  "v_mfma_f32_16x16x32_bf16 v[72:75], a[176:179], a[40:43], v[72:75]\n"
  "ds_read_b64_tr_b16 a[168:169], v226 offset:30816\n"
  "s_mov_b32 s78, s13\n"
  "v_mfma_f32_16x16x32_bf16 v[72:75], a[180:183], a[56:59], v[72:75]\n"
  "ds_read_b64_tr_b16 a[170:171], v226 offset:34400\n"
  "s_waitcnt lgkmcnt(6)\n"
  "v_mfma_f32_16x16x32_bf16 v[76:79], a[180:183], a[60:63], v[76:79]\n"
  "buffer_load_dwordx4 v240, s[36:39], s45 offen offset:0 lds\n"
  "v_mfma_f32_16x16x32_bf16 v[76:79], a[176:179], a[44:47], v[76:79]\n"
  "s_mov_b32 s79, 0x20000\n"
  "s_mul_i32 s8, s90, 458752\n"
  "s_add_u32 s81, s8, 196608\n"
  "v_mfma_f32_16x16x32_bf16 v[92:95], a[184:187], a[44:47], v[92:95]\n"
  "ds_read_b64_tr_b16 a[172:173], v226 offset:37984\n"
  "s_mul_i32 s8, s90, 196608\n"
  "v_mfma_f32_16x16x32_bf16 v[92:95], a[188:191], a[60:63], v[92:95]\n"
  "ds_read_b64_tr_b16 a[174:175], v226 offset:41568\n"
  "s_sub_u32 s86, -1114112, s8\n"
  "v_mfma_f32_16x16x32_bf16 v[88:91], a[188:191], a[56:59], v[88:91]\n"
  "buffer_load_dwordx4 v241, s[36:39], s45 offen offset:1024 lds\n"
  "v_mfma_f32_16x16x32_bf16 v[88:91], a[184:187], a[40:43], v[88:91]\n"
  "s_lshl_b32 s82, s3, 12\n"
  "s_mov_b32 s80, 0x3000\n"
  "s_mov_b32 s70, 0x1000\n"
  "v_mfma_f32_16x16x32_bf16 v[84:87], a[184:187], a[36:39], v[84:87]\n"
  "ds_read_b64_tr_b16 a[176:177], v226 offset:16512\n"
  "s_mov_b32 s71, 0x2000\n"
  "v_mfma_f32_16x16x32_bf16 v[84:87], a[188:191], a[52:55], v[84:87]\n"
  "ds_read_b64_tr_b16 a[178:179], v226 offset:20096\n"
  "s_waitcnt lgkmcnt(6)\n"
  "v_mfma_f32_16x16x32_bf16 v[80:83], a[188:191], a[48:51], v[80:83]\n"
  "buffer_load_dwordx4 v242, s[36:39], s45 offen offset:2048 lds\n"
  "v_mfma_f32_16x16x32_bf16 v[80:83], a[184:187], a[32:35], v[80:83]\n"
  "s_mul_i32 s8, s87, 458752\n"
  "s_mul_i32 s9, s89, 114688\n"
  "s_add_u32 s13, s8, s9\n"
  "v_mfma_f32_16x16x32_bf16 v[96:99], a[160:163], a[32:35], v[96:99]\n"
  "ds_read_b64_tr_b16 a[180:181], v226 offset:23680\n"
  "v_mfma_f32_16x16x32_bf16 v[96:99], a[164:167], a[48:51], v[96:99]\n"
  "ds_read_b64_tr_b16 a[182:183], v226 offset:27264\n"
  "v_mfma_f32_16x16x32_bf16 v[100:103], a[164:167], a[52:55], v[100:103]\n"
  "buffer_load_dwordx4 v243, s[36:39], s45 offen offset:3072 lds\n"
  "v_mfma_f32_16x16x32_bf16 v[100:103], a[160:163], a[36:39], v[100:103]\n"
  "s_add_u32 s72, s30, 25165824\n"
  "s_addc_u32 s73, s31, 0\n"
  "v_mfma_f32_16x16x32_bf16 v[104:107], a[160:163], a[40:43], v[104:107]\n"
  "ds_read_b64_tr_b16 a[184:185], v226 offset:30848\n"
  "v_mfma_f32_16x16x32_bf16 v[104:107], a[164:167], a[56:59], v[104:107]\n"
  "ds_read_b64_tr_b16 a[186:187], v226 offset:34432\n"
  "s_waitcnt lgkmcnt(6)\n"
  "v_mfma_f32_16x16x32_bf16 v[108:111], a[164:167], a[60:63], v[108:111]\n"
  "s_add_u32 s72, s72, s13\n"
  "s_addc_u32 s73, s73, 0\n"
  "s_pack_ll_b32_b16 s73, s73, 16\n"
  "v_mfma_f32_16x16x32_bf16 v[108:111], a[160:163], a[44:47], v[108:111]\n"
  "s_mov_b32 s74, 0\n"
  "s_mov_b32 s75, 0x800000\n"
  "s_lshl_b32 s8, s87, 4\n"
  "v_mfma_f32_16x16x32_bf16 v[124:127], a[168:171], a[44:47], v[124:127]\n"
  "ds_read_b64_tr_b16 a[188:189], v226 offset:38016\n"
  "s_add_u32 m0, s46, 32768\n"
  "v_mfma_f32_16x16x32_bf16 v[124:127], a[172:175], a[60:63], v[124:127]\n"
  "ds_read_b64_tr_b16 a[190:191], v226 offset:41600\n"
  "s_add_u32 s8, s8, s89\n"
  "v_mfma_f32_16x16x32_bf16 v[120:123], a[172:175], a[56:59], v[120:123]\n"
  "s_mul_i32 s8, s8, 512\n"
  "s_add_u32 s83, s8, 1982464\n"
  "s_mul_i32 s86, s90, 262144\n"
  "v_mfma_f32_16x16x32_bf16 v[120:123], a[168:171], a[40:43], v[120:123]\n"
  "s_lshr_b32 s8, s5, 2\n"
  "s_add_u32 s8, s8, s5\n"
  "s_add_u32 s9, s8, 0\n"
  "v_mfma_f32_16x16x32_bf16 v[116:119], a[168:171], a[36:39], v[116:119]\n"
  "ds_read_b64_tr_b16 a[160:161], v226 offset:16544\n"
  "s_and_b32 s9, s9, 3\n"
  "v_mfma_f32_16x16x32_bf16 v[116:119], a[172:175], a[52:55], v[116:119]\n"
  "ds_read_b64_tr_b16 a[162:163], v226 offset:20128\n"
  "s_waitcnt lgkmcnt(6)\n"
  "v_mfma_f32_16x16x32_bf16 v[112:115], a[172:175], a[48:51], v[112:115]\n"
  "buffer_load_dwordx4 v244, s[36:39], s45 offen offset:0 lds\n"
  "v_mfma_f32_16x16x32_bf16 v[112:115], a[168:171], a[32:35], v[112:115]\n"
  "s_lshl_b32 s16, s9, 13\n"
  "s_add_u32 s9, s8, 1\n"
  "s_and_b32 s9, s9, 3\n"
  "v_mfma_f32_16x16x32_bf16 v[128:131], a[176:179], a[32:35], v[128:131]\n"
  "ds_read_b64_tr_b16 a[164:165], v226 offset:23712\n"
  "s_lshl_b32 s17, s9, 13\n"
  "v_mfma_f32_16x16x32_bf16 v[128:131], a[180:183], a[48:51], v[128:131]\n"
  "ds_read_b64_tr_b16 a[166:167], v226 offset:27296\n"
  "s_add_u32 s9, s8, 2\n"
  "v_mfma_f32_16x16x32_bf16 v[132:135], a[180:183], a[52:55], v[132:135]\n"
  "buffer_load_dwordx4 v245, s[36:39], s45 offen offset:1024 lds\n"
  "v_mfma_f32_16x16x32_bf16 v[132:135], a[176:179], a[36:39], v[132:135]\n"
  "s_and_b32 s9, s9, 3\n"
  "s_lshl_b32 s18, s9, 13\n"
  "s_add_u32 s9, s8, 3\n"
  "v_mfma_f32_16x16x32_bf16 v[136:139], a[176:179], a[40:43], v[136:139]\n"
  "ds_read_b64_tr_b16 a[168:169], v226 offset:30880\n"
  "s_and_b32 s9, s9, 3\n"
  "v_mfma_f32_16x16x32_bf16 v[136:139], a[180:183], a[56:59], v[136:139]\n"
  "ds_read_b64_tr_b16 a[170:171], v226 offset:34464\n"
  "s_waitcnt lgkmcnt(6)\n"
  "v_mfma_f32_16x16x32_bf16 v[140:143], a[180:183], a[60:63], v[140:143]\n"
  "buffer_load_dwordx4 v246, s[36:39], s45 offen offset:2048 lds\n"
  "v_mfma_f32_16x16x32_bf16 v[140:143], a[176:179], a[44:47], v[140:143]\n"
  "s_lshl_b32 s19, s9, 13\n"
  "v_lshrrev_b32 v236, 3, v255\n"
  "v_mul_u32_u24 v236, 2048, v236\n"
  "v_mfma_f32_16x16x32_bf16 v[156:159], a[184:187], a[44:47], v[156:159]\n"
  "ds_read_b64_tr_b16 a[172:173], v226 offset:38048\n"
  "s_add_u32 s45, s45, 917504\n"
  "v_mfma_f32_16x16x32_bf16 v[156:159], a[188:191], a[60:63], v[156:159]\n"
  "ds_read_b64_tr_b16 a[174:175], v226 offset:41632\n"
  "v_lshrrev_b32 v237, 4, v255\n"
  "v_mfma_f32_16x16x32_bf16 v[152:155], a[188:191], a[56:59], v[152:155]\n"
  "v_lshlrev_b32 v237, 1, v237\n"
  "v_and_b32 v238, 7, v255\n"
  "v_mfma_f32_16x16x32_bf16 v[152:155], a[184:187], a[40:43], v[152:155]\n"
  "v_xor_b32 v237, v237, v238\n"
  "v_lshl_add_u32 v230, v237, 4, v236\n"
  "v_mfma_f32_16x16x32_bf16 v[148:151], a[184:187], a[36:39], v[148:151]\n"
  "ds_read_b64_tr_b16 a[176:177], v226 offset:16576\n"
  "s_and_b32 s8, s89, 2\n"
  "v_mfma_f32_16x16x32_bf16 v[148:151], a[188:191], a[52:55], v[148:151]\n"
  "ds_read_b64_tr_b16 a[178:179], v226 offset:20160\n"
  "s_waitcnt lgkmcnt(6)\n"
  "v_mfma_f32_16x16x32_bf16 v[144:147], a[188:191], a[48:51], v[144:147]\n"
  "buffer_load_dwordx4 a[96:99], off, s[32:35], s44 offset:0 sc1\n"
  "v_mfma_f32_16x16x32_bf16 v[144:147], a[184:187], a[32:35], v[144:147]\n"
  "s_lshl_b32 s8, s8, 6\n"
  "s_sub_u32 s9, 2176, s8\n"
  "v_add_u32 v228, s9, v230\n"
  "v_mfma_f32_16x16x32_bf16 v[160:163], a[160:163], a[32:35], v[160:163]\n"
  "ds_read_b64_tr_b16 a[180:181], v226 offset:23744\n"
  "v_add_u32 v230, s8, v230\n"
  "v_mfma_f32_16x16x32_bf16 v[160:163], a[164:167], a[48:51], v[160:163]\n"
  "ds_read_b64_tr_b16 a[182:183], v226 offset:27328\n"
  "v_add_u32 v229, 15360, v228\n"
  "v_mfma_f32_16x16x32_bf16 v[164:167], a[164:167], a[52:55], v[164:167]\n"
  "v_add_u32 v231, 15360, v230\n"
  "v_lshrrev_b32 v236, 2, v255\n"
  "v_mfma_f32_16x16x32_bf16 v[164:167], a[160:163], a[36:39], v[164:167]\n"
  "v_lshlrev_b32 v236, 7, v236\n"
  "v_and_b32 v237, 3, v255\n"
  "v_mfma_f32_16x16x32_bf16 v[168:171], a[160:163], a[40:43], v[168:171]\n"
  "ds_read_b64_tr_b16 a[184:185], v226 offset:30912\n"
  "v_lshl_add_u32 v236, v237, 3, v236\n"
  "v_mfma_f32_16x16x32_bf16 v[168:171], a[164:167], a[56:59], v[168:171]\n"
  "ds_read_b64_tr_b16 a[186:187], v226 offset:34496\n"
  "s_waitcnt lgkmcnt(6)\n"
  "v_mfma_f32_16x16x32_bf16 v[172:175], a[164:167], a[60:63], v[172:175]\n"
  "buffer_load_dwordx4 a[100:103], off, s[32:35], s44 offset:1024 sc1\n"
  "v_mfma_f32_16x16x32_bf16 v[172:175], a[160:163], a[44:47], v[172:175]\n"
  "v_bfe_u32 v237, v255, 3, 2\n"
  "v_xor_b32 v238, 0, v237\n"
  "v_mfma_f32_16x16x32_bf16 v[188:191], a[168:171], a[44:47], v[188:191]\n"
  "ds_read_b64_tr_b16 a[188:189], v226 offset:38080\n"
  "v_lshl_add_u32 v232, v238, 5, v236\n"
  "v_mfma_f32_16x16x32_bf16 v[188:191], a[172:175], a[60:63], v[188:191]\n"
  "ds_read_b64_tr_b16 a[190:191], v226 offset:41664\n"
  "v_xor_b32 v238, 1, v237\n"
  "v_mfma_f32_16x16x32_bf16 v[184:187], a[172:175], a[56:59], v[184:187]\n"
  "buffer_load_dwordx4 a[104:107], off, s[32:35], s44 offset:2048 sc1\n"
  "v_mfma_f32_16x16x32_bf16 v[184:187], a[168:171], a[40:43], v[184:187]\n"
  "v_lshl_add_u32 v233, v238, 5, v236\n"
  "v_xor_b32 v238, 2, v237\n"
  "v_mfma_f32_16x16x32_bf16 v[180:183], a[168:171], a[36:39], v[180:183]\n"
  "s_waitcnt vmcnt(19)\n"
  "s_barrier\n"
  "v_lshl_add_u32 v234, v238, 5, v236\n"
  "v_mfma_f32_16x16x32_bf16 v[180:183], a[172:175], a[52:55], v[180:183]\n"
  "ds_read_b64_tr_b16 a[160:161], v225 offset:28672\n"
  "v_xor_b32 v238, 3, v237\n"
  "v_mfma_f32_16x16x32_bf16 v[176:179], a[172:175], a[48:51], v[176:179]\n"
  "ds_read_b64_tr_b16 a[162:163], v225 offset:32256\n"
  "s_waitcnt lgkmcnt(6)\n"
  "v_mfma_f32_16x16x32_bf16 v[176:179], a[168:171], a[32:35], v[176:179]\n"
  "buffer_load_dwordx4 a[108:111], off, s[32:35], s44 offset:3072 sc1\n"
  "v_mfma_f32_16x16x32_bf16 v[192:195], a[176:179], a[32:35], v[192:195]\n"
  "ds_read_b64_tr_b16 a[164:165], v225 offset:35840\n"
  "s_add_u32 s44, s44, 4096\n"
  "v_mfma_f32_16x16x32_bf16 v[192:195], a[180:183], a[48:51], v[192:195]\n"
  "ds_read_b64_tr_b16 a[166:167], v225 offset:39424\n"
  "v_lshl_add_u32 v235, v238, 5, v236\n"
  "v_mfma_f32_16x16x32_bf16 v[196:199], a[180:183], a[52:55], v[196:199]\n"
  "s_andn2_b32 s8, 1, s89\n"
  "s_lshl_b32 s8, s8, 6\n"
  "s_or_b32 s8, s8, 0x18000\n"
  "v_mfma_f32_16x16x32_bf16 v[196:199], a[176:179], a[36:39], v[196:199]\n"
  "v_xor_b32 v232, s8, v232\n"
  "v_xor_b32 v233, s8, v233\n"
  "v_mfma_f32_16x16x32_bf16 v[200:203], a[176:179], a[40:43], v[200:203]\n"
  "ds_read_b64_tr_b16 a[168:169], v225 offset:43008\n"
  "v_xor_b32 v234, s8, v234\n"
  "v_mfma_f32_16x16x32_bf16 v[200:203], a[180:183], a[56:59], v[200:203]\n"
  "ds_read_b64_tr_b16 a[170:171], v225 offset:46592\n"
  "s_waitcnt lgkmcnt(6)\n"
  "v_mfma_f32_16x16x32_bf16 v[204:207], a[180:183], a[60:63], v[204:207]\n"
  "buffer_load_dwordx4 a[112:115], off, s[32:35], s44 offset:0 sc1\n"
  "v_mfma_f32_16x16x32_bf16 v[204:207], a[176:179], a[44:47], v[204:207]\n"
  "v_xor_b32 v235, s8, v235\n"
  "v_and_b32 v237, 7, v255\n"
  "v_mfma_f32_16x16x32_bf16 v[220:223], a[184:187], a[44:47], v[220:223]\n"
  "ds_read_b64_tr_b16 a[172:173], v225 offset:50176\n"
  "v_mul_u32_u24 v236, 64, v237\n"
  "v_mfma_f32_16x16x32_bf16 v[220:223], a[188:191], a[60:63], v[220:223]\n"
  "ds_read_b64_tr_b16 a[174:175], v225 offset:53760\n"
  "v_mfma_f32_16x16x32_bf16 v[216:219], a[188:191], a[56:59], v[216:219]\n"
  "buffer_load_dwordx4 a[116:119], off, s[32:35], s44 offset:1024 sc1\n"
  "v_mfma_f32_16x16x32_bf16 v[216:219], a[184:187], a[40:43], v[216:219]\n"
  "v_mfma_f32_16x16x32_bf16 v[212:215], a[184:187], a[36:39], v[212:215]\n"
  "ds_read_b64_tr_b16 a[176:177], v225 offset:28704\n"
  "v_mfma_f32_16x16x32_bf16 v[212:215], a[188:191], a[52:55], v[212:215]\n"
  "ds_read_b64_tr_b16 a[178:179], v225 offset:32288\n"
  "s_waitcnt vmcnt(13) lgkmcnt(6)\n"
  "v_mfma_f32_16x16x32_bf16 v[208:211], a[188:191], a[48:51], v[208:211]\n"
  "v_mfma_f32_16x16x32_bf16 v[208:211], a[184:187], a[32:35], v[208:211]\n"
  "v_mfma_f32_16x16x32_bf16 v[0:3], a[160:163], a[64:67], v[0:3]\n"
  "ds_read_b64_tr_b16 a[180:181], v225 offset:35872\n"
  "v_mfma_f32_16x16x32_bf16 v[0:3], a[164:167], a[80:83], v[0:3]\n"
  "ds_read_b64_tr_b16 a[182:183], v225 offset:39456\n"
  "v_mfma_f32_16x16x32_bf16 v[4:7], a[164:167], a[84:87], v[4:7]\n"
  "buffer_load_dwordx4 a[120:123], off, s[32:35], s44 offset:2048 sc1\n"
  "v_mfma_f32_16x16x32_bf16 v[4:7], a[160:163], a[68:71], v[4:7]\n"
  "v_mfma_f32_16x16x32_bf16 v[8:11], a[160:163], a[72:75], v[8:11]\n"
  "ds_read_b64_tr_b16 a[184:185], v225 offset:43040\n"
  "v_mfma_f32_16x16x32_bf16 v[8:11], a[164:167], a[88:91], v[8:11]\n"
  "ds_read_b64_tr_b16 a[186:187], v225 offset:46624\n"
  "s_waitcnt lgkmcnt(6)\n"
  "v_mfma_f32_16x16x32_bf16 v[12:15], a[164:167], a[92:95], v[12:15]\n"
  "buffer_load_dwordx4 a[124:127], off, s[32:35], s44 offset:3072 sc1\n"
  "v_mfma_f32_16x16x32_bf16 v[12:15], a[160:163], a[76:79], v[12:15]\n"
  "v_mfma_f32_16x16x32_bf16 v[28:31], a[168:171], a[76:79], v[28:31]\n"
  "ds_read_b64_tr_b16 a[188:189], v225 offset:50208\n"
  "s_add_u32 s44, s44, 4096\n"
  "v_mfma_f32_16x16x32_bf16 v[28:31], a[172:175], a[92:95], v[28:31]\n"
  "ds_read_b64_tr_b16 a[190:191], v225 offset:53792\n"
  "s_add_u32 m0, s46, 57344\n"
  "v_mfma_f32_16x16x32_bf16 v[24:27], a[172:175], a[88:91], v[24:27]\n"
  "v_mfma_f32_16x16x32_bf16 v[24:27], a[168:171], a[72:75], v[24:27]\n"
  "v_mfma_f32_16x16x32_bf16 v[20:23], a[168:171], a[68:71], v[20:23]\n"
  "ds_read_b64_tr_b16 a[160:161], v225 offset:28736\n"
  "v_mfma_f32_16x16x32_bf16 v[20:23], a[172:175], a[84:87], v[20:23]\n"
  "ds_read_b64_tr_b16 a[162:163], v225 offset:32320\n"
  "s_waitcnt lgkmcnt(6)\n"
  "v_mfma_f32_16x16x32_bf16 v[16:19], a[172:175], a[80:83], v[16:19]\n"
  "v_mfma_f32_16x16x32_bf16 v[16:19], a[168:171], a[64:67], v[16:19]\n"
  "v_mfma_f32_16x16x32_bf16 v[32:35], a[176:179], a[64:67], v[32:35]\n"
  "ds_read_b64_tr_b16 a[164:165], v225 offset:35904\n"
  "v_mfma_f32_16x16x32_bf16 v[32:35], a[180:183], a[80:83], v[32:35]\n"
  "ds_read_b64_tr_b16 a[166:167], v225 offset:39488\n"
  "v_mfma_f32_16x16x32_bf16 v[36:39], a[180:183], a[84:87], v[36:39]\n"
  "buffer_load_dwordx4 v240, s[36:39], s45 offen offset:0 lds\n"
  "v_mfma_f32_16x16x32_bf16 v[36:39], a[176:179], a[68:71], v[36:39]\n"
  "v_mfma_f32_16x16x32_bf16 v[40:43], a[176:179], a[72:75], v[40:43]\n"
  "ds_read_b64_tr_b16 a[168:169], v225 offset:43072\n"
  "v_mfma_f32_16x16x32_bf16 v[40:43], a[180:183], a[88:91], v[40:43]\n"
  "ds_read_b64_tr_b16 a[170:171], v225 offset:46656\n"
  "s_waitcnt lgkmcnt(6)\n"
  "v_mfma_f32_16x16x32_bf16 v[44:47], a[180:183], a[92:95], v[44:47]\n"
  "buffer_load_dwordx4 v241, s[36:39], s45 offen offset:1024 lds\n"
  "v_mfma_f32_16x16x32_bf16 v[44:47], a[176:179], a[76:79], v[44:47]\n"
  "v_mfma_f32_16x16x32_bf16 v[60:63], a[184:187], a[76:79], v[60:63]\n"
  "ds_read_b64_tr_b16 a[172:173], v225 offset:50240\n"
  "v_mfma_f32_16x16x32_bf16 v[60:63], a[188:191], a[92:95], v[60:63]\n"
  "ds_read_b64_tr_b16 a[174:175], v225 offset:53824\n"
  "v_mfma_f32_16x16x32_bf16 v[56:59], a[188:191], a[88:91], v[56:59]\n"
  "buffer_load_dwordx4 v242, s[36:39], s45 offen offset:2048 lds\n"
  "v_mfma_f32_16x16x32_bf16 v[56:59], a[184:187], a[72:75], v[56:59]\n"
  "v_mfma_f32_16x16x32_bf16 v[52:55], a[184:187], a[68:71], v[52:55]\n"
  "ds_read_b64_tr_b16 a[176:177], v225 offset:28768\n"
  "v_mfma_f32_16x16x32_bf16 v[52:55], a[188:191], a[84:87], v[52:55]\n"
  "ds_read_b64_tr_b16 a[178:179], v225 offset:32352\n"
  "s_waitcnt lgkmcnt(6)\n"
  "v_mfma_f32_16x16x32_bf16 v[48:51], a[188:191], a[80:83], v[48:51]\n"
  "buffer_load_dwordx4 v243, s[36:39], s45 offen offset:3072 lds\n"
  "v_mfma_f32_16x16x32_bf16 v[48:51], a[184:187], a[64:67], v[48:51]\n"
  "v_mfma_f32_16x16x32_bf16 v[64:67], a[160:163], a[64:67], v[64:67]\n"
  "ds_read_b64_tr_b16 a[180:181], v225 offset:35936\n"
  "s_add_u32 m0, s46, 61440\n"
  "v_mfma_f32_16x16x32_bf16 v[64:67], a[164:167], a[80:83], v[64:67]\n"
  "ds_read_b64_tr_b16 a[182:183], v225 offset:39520\n"
  "v_mfma_f32_16x16x32_bf16 v[68:71], a[164:167], a[84:87], v[68:71]\n"
  "v_mfma_f32_16x16x32_bf16 v[68:71], a[160:163], a[68:71], v[68:71]\n"
  "v_mfma_f32_16x16x32_bf16 v[72:75], a[160:163], a[72:75], v[72:75]\n"
  "ds_read_b64_tr_b16 a[184:185], v225 offset:43104\n"
  "v_mfma_f32_16x16x32_bf16 v[72:75], a[164:167], a[88:91], v[72:75]\n"
  "ds_read_b64_tr_b16 a[186:187], v225 offset:46688\n"
  "s_waitcnt lgkmcnt(6)\n"
  "v_mfma_f32_16x16x32_bf16 v[76:79], a[164:167], a[92:95], v[76:79]\n"
  "buffer_load_dwordx4 v244, s[36:39], s45 offen offset:0 lds\n"
  "v_mfma_f32_16x16x32_bf16 v[76:79], a[160:163], a[76:79], v[76:79]\n"
  "v_mfma_f32_16x16x32_bf16 v[92:95], a[168:171], a[76:79], v[92:95]\n"
  "ds_read_b64_tr_b16 a[188:189], v225 offset:50272\n"
  "v_mfma_f32_16x16x32_bf16 v[92:95], a[172:175], a[92:95], v[92:95]\n"
  "ds_read_b64_tr_b16 a[190:191], v225 offset:53856\n"
  "v_mfma_f32_16x16x32_bf16 v[88:91], a[172:175], a[88:91], v[88:91]\n"
  "buffer_load_dwordx4 v245, s[36:39], s45 offen offset:1024 lds\n"
  "v_mfma_f32_16x16x32_bf16 v[88:91], a[168:171], a[72:75], v[88:91]\n"
  "v_mfma_f32_16x16x32_bf16 v[84:87], a[168:171], a[68:71], v[84:87]\n"
  "ds_read_b64_tr_b16 a[160:161], v225 offset:28800\n"
  "v_mfma_f32_16x16x32_bf16 v[84:87], a[172:175], a[84:87], v[84:87]\n"
  "ds_read_b64_tr_b16 a[162:163], v225 offset:32384\n"
  "s_waitcnt lgkmcnt(6)\n"
  "v_mfma_f32_16x16x32_bf16 v[80:83], a[172:175], a[80:83], v[80:83]\n"
  "buffer_load_dwordx4 v246, s[36:39], s45 offen offset:2048 lds\n"
  "v_mfma_f32_16x16x32_bf16 v[80:83], a[168:171], a[64:67], v[80:83]\n"
  "v_mfma_f32_16x16x32_bf16 v[96:99], a[176:179], a[64:67], v[96:99]\n"
  "ds_read_b64_tr_b16 a[164:165], v225 offset:35968\n"
  "s_add_u32 s45, s45, 917504\n"
  "v_mfma_f32_16x16x32_bf16 v[96:99], a[180:183], a[80:83], v[96:99]\n"
  "ds_read_b64_tr_b16 a[166:167], v225 offset:39552\n"
  "v_mfma_f32_16x16x32_bf16 v[100:103], a[180:183], a[84:87], v[100:103]\n"
  "v_mfma_f32_16x16x32_bf16 v[100:103], a[176:179], a[68:71], v[100:103]\n"
  "v_mfma_f32_16x16x32_bf16 v[104:107], a[176:179], a[72:75], v[104:107]\n"
  "ds_read_b64_tr_b16 a[168:169], v225 offset:43136\n"
  "v_mfma_f32_16x16x32_bf16 v[104:107], a[180:183], a[88:91], v[104:107]\n"
  "ds_read_b64_tr_b16 a[170:171], v225 offset:46720\n"
  "s_waitcnt lgkmcnt(6)\n"
  "v_mfma_f32_16x16x32_bf16 v[108:111], a[180:183], a[92:95], v[108:111]\n"
  "v_mfma_f32_16x16x32_bf16 v[108:111], a[176:179], a[76:79], v[108:111]\n"
  "v_mfma_f32_16x16x32_bf16 v[124:127], a[184:187], a[76:79], v[124:127]\n"
  "ds_read_b64_tr_b16 a[172:173], v225 offset:50304\n"
  "v_mfma_f32_16x16x32_bf16 v[124:127], a[188:191], a[92:95], v[124:127]\n"
  "ds_read_b64_tr_b16 a[174:175], v225 offset:53888\n"
  "v_mfma_f32_16x16x32_bf16 v[120:123], a[188:191], a[88:91], v[120:123]\n"
  "buffer_load_dwordx4 a[128:131], off, s[32:35], s44 offset:0 sc1\n"
  "v_mfma_f32_16x16x32_bf16 v[120:123], a[184:187], a[72:75], v[120:123]\n"
  "v_mfma_f32_16x16x32_bf16 v[116:119], a[184:187], a[68:71], v[116:119]\n"
  "ds_read_b64_tr_b16 a[176:177], v225 offset:28832\n"
  "v_mfma_f32_16x16x32_bf16 v[116:119], a[188:191], a[84:87], v[116:119]\n"
  "ds_read_b64_tr_b16 a[178:179], v225 offset:32416\n"
  "s_waitcnt lgkmcnt(6)\n"
  "v_mfma_f32_16x16x32_bf16 v[112:115], a[188:191], a[80:83], v[112:115]\n"
  "buffer_load_dwordx4 a[132:135], off, s[32:35], s44 offset:1024 sc1\n"
  "v_mfma_f32_16x16x32_bf16 v[112:115], a[184:187], a[64:67], v[112:115]\n"
  "v_mfma_f32_16x16x32_bf16 v[128:131], a[160:163], a[64:67], v[128:131]\n"
  "ds_read_b64_tr_b16 a[180:181], v225 offset:36000\n"
  "v_mfma_f32_16x16x32_bf16 v[128:131], a[164:167], a[80:83], v[128:131]\n"
  "ds_read_b64_tr_b16 a[182:183], v225 offset:39584\n"
  "v_mfma_f32_16x16x32_bf16 v[132:135], a[164:167], a[84:87], v[132:135]\n"
  "buffer_load_dwordx4 a[136:139], off, s[32:35], s44 offset:2048 sc1\n"
  "v_mfma_f32_16x16x32_bf16 v[132:135], a[160:163], a[68:71], v[132:135]\n"
  "v_mfma_f32_16x16x32_bf16 v[136:139], a[160:163], a[72:75], v[136:139]\n"
  "ds_read_b64_tr_b16 a[184:185], v225 offset:43168\n"
  "v_mfma_f32_16x16x32_bf16 v[136:139], a[164:167], a[88:91], v[136:139]\n"
  "ds_read_b64_tr_b16 a[186:187], v225 offset:46752\n"
  "s_waitcnt lgkmcnt(6)\n"
  "v_mfma_f32_16x16x32_bf16 v[140:143], a[164:167], a[92:95], v[140:143]\n"
  "buffer_load_dwordx4 a[140:143], off, s[32:35], s44 offset:3072 sc1\n"
  "v_mfma_f32_16x16x32_bf16 v[140:143], a[160:163], a[76:79], v[140:143]\n"
  "v_mfma_f32_16x16x32_bf16 v[156:159], a[168:171], a[76:79], v[156:159]\n"
  "ds_read_b64_tr_b16 a[188:189], v225 offset:50336\n"
  "s_add_u32 s44, s44, 4096\n"
  "v_mfma_f32_16x16x32_bf16 v[156:159], a[172:175], a[92:95], v[156:159]\n"
  "ds_read_b64_tr_b16 a[190:191], v225 offset:53920\n"
  "v_mfma_f32_16x16x32_bf16 v[152:155], a[172:175], a[88:91], v[152:155]\n"
  "v_mfma_f32_16x16x32_bf16 v[152:155], a[168:171], a[72:75], v[152:155]\n"
  "v_mfma_f32_16x16x32_bf16 v[148:151], a[168:171], a[68:71], v[148:151]\n"
  "ds_read_b64_tr_b16 a[160:161], v225 offset:28864\n"
  "v_mfma_f32_16x16x32_bf16 v[148:151], a[172:175], a[84:87], v[148:151]\n"
  "ds_read_b64_tr_b16 a[162:163], v225 offset:32448\n"
  "s_waitcnt lgkmcnt(6)\n"
  "v_mfma_f32_16x16x32_bf16 v[144:147], a[172:175], a[80:83], v[144:147]\n"
  "buffer_load_dwordx4 a[144:147], off, s[32:35], s44 offset:0 sc1\n"
  "v_mfma_f32_16x16x32_bf16 v[144:147], a[168:171], a[64:67], v[144:147]\n"
  "v_mfma_f32_16x16x32_bf16 v[160:163], a[176:179], a[64:67], v[160:163]\n"
  "ds_read_b64_tr_b16 a[164:165], v225 offset:36032\n"
  "v_mfma_f32_16x16x32_bf16 v[160:163], a[180:183], a[80:83], v[160:163]\n"
  "ds_read_b64_tr_b16 a[166:167], v225 offset:39616\n"
  "v_mfma_f32_16x16x32_bf16 v[164:167], a[180:183], a[84:87], v[164:167]\n"
  "v_mfma_f32_16x16x32_bf16 v[164:167], a[176:179], a[68:71], v[164:167]\n"
  "v_mfma_f32_16x16x32_bf16 v[168:171], a[176:179], a[72:75], v[168:171]\n"
  "ds_read_b64_tr_b16 a[168:169], v225 offset:43200\n"
  "v_mfma_f32_16x16x32_bf16 v[168:171], a[180:183], a[88:91], v[168:171]\n"
  "ds_read_b64_tr_b16 a[170:171], v225 offset:46784\n"
  "s_waitcnt lgkmcnt(6)\n"
  "v_mfma_f32_16x16x32_bf16 v[172:175], a[180:183], a[92:95], v[172:175]\n"
  "buffer_load_dwordx4 a[148:151], off, s[32:35], s44 offset:1024 sc1\n"
  "v_mfma_f32_16x16x32_bf16 v[172:175], a[176:179], a[76:79], v[172:175]\n"
  "v_mfma_f32_16x16x32_bf16 v[188:191], a[184:187], a[76:79], v[188:191]\n"
  "ds_read_b64_tr_b16 a[172:173], v225 offset:50368\n"
  "v_mfma_f32_16x16x32_bf16 v[188:191], a[188:191], a[92:95], v[188:191]\n"
  "ds_read_b64_tr_b16 a[174:175], v225 offset:53952\n"
  "v_mfma_f32_16x16x32_bf16 v[184:187], a[188:191], a[88:91], v[184:187]\n"
  "buffer_load_dwordx4 a[152:155], off, s[32:35], s44 offset:2048 sc1\n"
  "v_mfma_f32_16x16x32_bf16 v[184:187], a[184:187], a[72:75], v[184:187]\n"
  "v_mfma_f32_16x16x32_bf16 v[180:183], a[184:187], a[68:71], v[180:183]\n"
  "s_waitcnt vmcnt(22)\n"
  "s_barrier\n"
  "v_mfma_f32_16x16x32_bf16 v[180:183], a[188:191], a[84:87], v[180:183]\n"
  "ds_read_b64_tr_b16 a[176:177], v224 offset:28672\n"
  "v_mfma_f32_16x16x32_bf16 v[176:179], a[188:191], a[80:83], v[176:179]\n"
  "ds_read_b64_tr_b16 a[178:179], v224 offset:32256\n"
  "s_waitcnt lgkmcnt(6)\n"
  "v_mfma_f32_16x16x32_bf16 v[176:179], a[184:187], a[64:67], v[176:179]\n"
  "buffer_load_dwordx4 a[156:159], off, s[32:35], s44 offset:3072 sc1\n"
  "v_mfma_f32_16x16x32_bf16 v[192:195], a[160:163], a[64:67], v[192:195]\n"
  "ds_read_b64_tr_b16 a[180:181], v224 offset:35840\n"
  "s_add_u32 s44, s44, 4096\n"
  "v_mfma_f32_16x16x32_bf16 v[192:195], a[164:167], a[80:83], v[192:195]\n"
  "ds_read_b64_tr_b16 a[182:183], v224 offset:39424\n"
  "s_add_u32 m0, s46, 0\n"
  "v_mfma_f32_16x16x32_bf16 v[196:199], a[164:167], a[84:87], v[196:199]\n"
  "v_mfma_f32_16x16x32_bf16 v[196:199], a[160:163], a[68:71], v[196:199]\n"
  "v_mfma_f32_16x16x32_bf16 v[200:203], a[160:163], a[72:75], v[200:203]\n"
  "ds_read_b64_tr_b16 a[184:185], v224 offset:43008\n"
  "v_mfma_f32_16x16x32_bf16 v[200:203], a[164:167], a[88:91], v[200:203]\n"
  "ds_read_b64_tr_b16 a[186:187], v224 offset:46592\n"
  "s_waitcnt lgkmcnt(6)\n"
  "v_mfma_f32_16x16x32_bf16 v[204:207], a[164:167], a[92:95], v[204:207]\n"
  "v_mfma_f32_16x16x32_bf16 v[204:207], a[160:163], a[76:79], v[204:207]\n"
  "v_mfma_f32_16x16x32_bf16 v[220:223], a[168:171], a[76:79], v[220:223]\n"
  "ds_read_b64_tr_b16 a[188:189], v224 offset:50176\n"
  "v_mfma_f32_16x16x32_bf16 v[220:223], a[172:175], a[92:95], v[220:223]\n"
  "ds_read_b64_tr_b16 a[190:191], v224 offset:53760\n"
  "v_mfma_f32_16x16x32_bf16 v[216:219], a[172:175], a[88:91], v[216:219]\n"
  "buffer_load_dwordx4 v240, s[36:39], s45 offen offset:0 lds\n"
  "v_mfma_f32_16x16x32_bf16 v[216:219], a[168:171], a[72:75], v[216:219]\n"
  "v_mfma_f32_16x16x32_bf16 v[212:215], a[168:171], a[68:71], v[212:215]\n"
  "ds_read_b64_tr_b16 a[160:161], v224 offset:28704\n"
  "v_mfma_f32_16x16x32_bf16 v[212:215], a[172:175], a[84:87], v[212:215]\n"
  "ds_read_b64_tr_b16 a[162:163], v224 offset:32288\n"
  "s_waitcnt vmcnt(16) lgkmcnt(6)\n"
  "v_mfma_f32_16x16x32_bf16 v[208:211], a[172:175], a[80:83], v[208:211]\n"
  "v_mfma_f32_16x16x32_bf16 v[208:211], a[168:171], a[64:67], v[208:211]\n"
  "v_mfma_f32_16x16x32_bf16 v[0:3], a[176:179], a[96:99], v[0:3]\n"
  "ds_read_b64_tr_b16 a[164:165], v224 offset:35872\n"
  "v_mfma_f32_16x16x32_bf16 v[0:3], a[180:183], a[112:115], v[0:3]\n"
  "ds_read_b64_tr_b16 a[166:167], v224 offset:39456\n"
  "v_mfma_f32_16x16x32_bf16 v[4:7], a[180:183], a[116:119], v[4:7]\n"
  "buffer_load_dwordx4 v241, s[36:39], s45 offen offset:1024 lds\n"
  "v_mfma_f32_16x16x32_bf16 v[4:7], a[176:179], a[100:103], v[4:7]\n"
  "v_mfma_f32_16x16x32_bf16 v[8:11], a[176:179], a[104:107], v[8:11]\n"
  "ds_read_b64_tr_b16 a[168:169], v224 offset:43040\n"
  "v_mfma_f32_16x16x32_bf16 v[8:11], a[180:183], a[120:123], v[8:11]\n"
  "ds_read_b64_tr_b16 a[170:171], v224 offset:46624\n"
  "s_waitcnt lgkmcnt(6)\n"
  "v_mfma_f32_16x16x32_bf16 v[12:15], a[180:183], a[124:127], v[12:15]\n"
  "buffer_load_dwordx4 v242, s[36:39], s45 offen offset:2048 lds\n"
  "v_mfma_f32_16x16x32_bf16 v[12:15], a[176:179], a[108:111], v[12:15]\n"
  "v_mfma_f32_16x16x32_bf16 v[28:31], a[184:187], a[108:111], v[28:31]\n"
  "ds_read_b64_tr_b16 a[172:173], v224 offset:50208\n"
  "v_mfma_f32_16x16x32_bf16 v[28:31], a[188:191], a[124:127], v[28:31]\n"
  "ds_read_b64_tr_b16 a[174:175], v224 offset:53792\n"
  "v_mfma_f32_16x16x32_bf16 v[24:27], a[188:191], a[120:123], v[24:27]\n"
  "buffer_load_dwordx4 v243, s[36:39], s45 offen offset:3072 lds\n"
  "v_mfma_f32_16x16x32_bf16 v[24:27], a[184:187], a[104:107], v[24:27]\n"
  "v_mfma_f32_16x16x32_bf16 v[20:23], a[184:187], a[100:103], v[20:23]\n"
  "ds_read_b64_tr_b16 a[176:177], v224 offset:28736\n"
  "s_add_u32 m0, s46, 4096\n"
  "v_mfma_f32_16x16x32_bf16 v[20:23], a[188:191], a[116:119], v[20:23]\n"
  "ds_read_b64_tr_b16 a[178:179], v224 offset:32320\n"
  "s_waitcnt lgkmcnt(6)\n"
  "v_mfma_f32_16x16x32_bf16 v[16:19], a[188:191], a[112:115], v[16:19]\n"
  "v_mfma_f32_16x16x32_bf16 v[16:19], a[184:187], a[96:99], v[16:19]\n"
  "v_mfma_f32_16x16x32_bf16 v[32:35], a[160:163], a[96:99], v[32:35]\n"
  "ds_read_b64_tr_b16 a[180:181], v224 offset:35904\n"
  "v_mfma_f32_16x16x32_bf16 v[32:35], a[164:167], a[112:115], v[32:35]\n"
  "ds_read_b64_tr_b16 a[182:183], v224 offset:39488\n"
  "v_mfma_f32_16x16x32_bf16 v[36:39], a[164:167], a[116:119], v[36:39]\n"
  "v_mfma_f32_16x16x32_bf16 v[36:39], a[160:163], a[100:103], v[36:39]\n"
  "v_mfma_f32_16x16x32_bf16 v[40:43], a[160:163], a[104:107], v[40:43]\n"
  "ds_read_b64_tr_b16 a[184:185], v224 offset:43072\n"
  "v_mfma_f32_16x16x32_bf16 v[40:43], a[164:167], a[120:123], v[40:43]\n"
  "ds_read_b64_tr_b16 a[186:187], v224 offset:46656\n"
  "s_waitcnt lgkmcnt(6)\n"
  "v_mfma_f32_16x16x32_bf16 v[44:47], a[164:167], a[124:127], v[44:47]\n"
  "buffer_load_dwordx4 v244, s[36:39], s45 offen offset:0 lds\n"
  "v_mfma_f32_16x16x32_bf16 v[44:47], a[160:163], a[108:111], v[44:47]\n"
  "v_mfma_f32_16x16x32_bf16 v[60:63], a[168:171], a[108:111], v[60:63]\n"
  "ds_read_b64_tr_b16 a[188:189], v224 offset:50240\n"
  "v_mfma_f32_16x16x32_bf16 v[60:63], a[172:175], a[124:127], v[60:63]\n"
  "ds_read_b64_tr_b16 a[190:191], v224 offset:53824\n"
  "v_mfma_f32_16x16x32_bf16 v[56:59], a[172:175], a[120:123], v[56:59]\n"
  "buffer_load_dwordx4 v245, s[36:39], s45 offen offset:1024 lds\n"
  "v_mfma_f32_16x16x32_bf16 v[56:59], a[168:171], a[104:107], v[56:59]\n"
  "v_mfma_f32_16x16x32_bf16 v[52:55], a[168:171], a[100:103], v[52:55]\n"
  "ds_read_b64_tr_b16 a[160:161], v224 offset:28768\n"
  "v_mfma_f32_16x16x32_bf16 v[52:55], a[172:175], a[116:119], v[52:55]\n"
  "ds_read_b64_tr_b16 a[162:163], v224 offset:32352\n"
  "s_waitcnt lgkmcnt(6)\n"
  "v_mfma_f32_16x16x32_bf16 v[48:51], a[172:175], a[112:115], v[48:51]\n"
  "buffer_load_dwordx4 v246, s[36:39], s45 offen offset:2048 lds\n"
  "v_mfma_f32_16x16x32_bf16 v[48:51], a[168:171], a[96:99], v[48:51]\n"
  "v_mfma_f32_16x16x32_bf16 v[64:67], a[176:179], a[96:99], v[64:67]\n"
  "ds_read_b64_tr_b16 a[164:165], v224 offset:35936\n"
  "s_add_u32 s45, s45, 917504\n"
  "v_mfma_f32_16x16x32_bf16 v[64:67], a[180:183], a[112:115], v[64:67]\n"
  "ds_read_b64_tr_b16 a[166:167], v224 offset:39520\n"
  "v_mfma_f32_16x16x32_bf16 v[68:71], a[180:183], a[116:119], v[68:71]\n"
  "v_mfma_f32_16x16x32_bf16 v[68:71], a[176:179], a[100:103], v[68:71]\n"
  "v_mfma_f32_16x16x32_bf16 v[72:75], a[176:179], a[104:107], v[72:75]\n"
  "ds_read_b64_tr_b16 a[168:169], v224 offset:43104\n"
  "v_mfma_f32_16x16x32_bf16 v[72:75], a[180:183], a[120:123], v[72:75]\n"
  "ds_read_b64_tr_b16 a[170:171], v224 offset:46688\n"
  "s_waitcnt lgkmcnt(6)\n"
  "v_mfma_f32_16x16x32_bf16 v[76:79], a[180:183], a[124:127], v[76:79]\n"
  "v_mfma_f32_16x16x32_bf16 v[76:79], a[176:179], a[108:111], v[76:79]\n"
  "v_mfma_f32_16x16x32_bf16 v[92:95], a[184:187], a[108:111], v[92:95]\n"
  "ds_read_b64_tr_b16 a[172:173], v224 offset:50272\n"
  "v_mfma_f32_16x16x32_bf16 v[92:95], a[188:191], a[124:127], v[92:95]\n"
  "ds_read_b64_tr_b16 a[174:175], v224 offset:53856\n"
  "v_mfma_f32_16x16x32_bf16 v[88:91], a[188:191], a[120:123], v[88:91]\n"
  "buffer_load_dwordx4 a[0:3], off, s[32:35], s44 offset:0 sc1\n"
  "v_mfma_f32_16x16x32_bf16 v[88:91], a[184:187], a[104:107], v[88:91]\n"
  "v_mfma_f32_16x16x32_bf16 v[84:87], a[184:187], a[100:103], v[84:87]\n"
  "ds_read_b64_tr_b16 a[176:177], v224 offset:28800\n"
  "v_mfma_f32_16x16x32_bf16 v[84:87], a[188:191], a[116:119], v[84:87]\n"
  "ds_read_b64_tr_b16 a[178:179], v224 offset:32384\n"
  "s_waitcnt lgkmcnt(6)\n"
  "v_mfma_f32_16x16x32_bf16 v[80:83], a[188:191], a[112:115], v[80:83]\n"
  "buffer_load_dwordx4 a[4:7], off, s[32:35], s44 offset:1024 sc1\n"
  "v_mfma_f32_16x16x32_bf16 v[80:83], a[184:187], a[96:99], v[80:83]\n"
  "v_mfma_f32_16x16x32_bf16 v[96:99], a[160:163], a[96:99], v[96:99]\n"
  "ds_read_b64_tr_b16 a[180:181], v224 offset:35968\n"
  "v_mfma_f32_16x16x32_bf16 v[96:99], a[164:167], a[112:115], v[96:99]\n"
  "ds_read_b64_tr_b16 a[182:183], v224 offset:39552\n"
  "v_mfma_f32_16x16x32_bf16 v[100:103], a[164:167], a[116:119], v[100:103]\n"
  "buffer_load_dwordx4 a[8:11], off, s[32:35], s44 offset:2048 sc1\n"
  "v_mfma_f32_16x16x32_bf16 v[100:103], a[160:163], a[100:103], v[100:103]\n"
  "v_mfma_f32_16x16x32_bf16 v[104:107], a[160:163], a[104:107], v[104:107]\n"
  "ds_read_b64_tr_b16 a[184:185], v224 offset:43136\n"
  "v_mfma_f32_16x16x32_bf16 v[104:107], a[164:167], a[120:123], v[104:107]\n"
  "ds_read_b64_tr_b16 a[186:187], v224 offset:46720\n"
  "s_waitcnt lgkmcnt(6)\n"
  "v_mfma_f32_16x16x32_bf16 v[108:111], a[164:167], a[124:127], v[108:111]\n"
  "v_mfma_f32_16x16x32_bf16 v[108:111], a[160:163], a[108:111], v[108:111]\n"
  "v_mfma_f32_16x16x32_bf16 v[124:127], a[168:171], a[108:111], v[124:127]\n"
  "ds_read_b64_tr_b16 a[188:189], v224 offset:50304\n"
  "v_mfma_f32_16x16x32_bf16 v[124:127], a[172:175], a[124:127], v[124:127]\n"
  "ds_read_b64_tr_b16 a[190:191], v224 offset:53888\n"
  "v_mfma_f32_16x16x32_bf16 v[120:123], a[172:175], a[120:123], v[120:123]\n"
  "buffer_load_dwordx4 a[12:15], off, s[32:35], s44 offset:3072 sc1\n"
  "v_mfma_f32_16x16x32_bf16 v[120:123], a[168:171], a[104:107], v[120:123]\n"
  "v_mfma_f32_16x16x32_bf16 v[116:119], a[168:171], a[100:103], v[116:119]\n"
  "ds_read_b64_tr_b16 a[160:161], v224 offset:28832\n"
  "s_add_u32 s44, s44, 4096\n"
  "v_mfma_f32_16x16x32_bf16 v[116:119], a[172:175], a[116:119], v[116:119]\n"
  "ds_read_b64_tr_b16 a[162:163], v224 offset:32416\n"
  "s_waitcnt lgkmcnt(6)\n"
  "v_mfma_f32_16x16x32_bf16 v[112:115], a[172:175], a[112:115], v[112:115]\n"
  "v_mfma_f32_16x16x32_bf16 v[112:115], a[168:171], a[96:99], v[112:115]\n"
  "v_mfma_f32_16x16x32_bf16 v[128:131], a[176:179], a[96:99], v[128:131]\n"
  "ds_read_b64_tr_b16 a[164:165], v224 offset:36000\n"
  "v_mfma_f32_16x16x32_bf16 v[128:131], a[180:183], a[112:115], v[128:131]\n"
  "ds_read_b64_tr_b16 a[166:167], v224 offset:39584\n"
  "v_mfma_f32_16x16x32_bf16 v[132:135], a[180:183], a[116:119], v[132:135]\n"
  "buffer_load_dwordx4 a[16:19], off, s[32:35], s44 offset:0 sc1\n"
  "v_mfma_f32_16x16x32_bf16 v[132:135], a[176:179], a[100:103], v[132:135]\n"
  "v_mfma_f32_16x16x32_bf16 v[136:139], a[176:179], a[104:107], v[136:139]\n"
  "ds_read_b64_tr_b16 a[168:169], v224 offset:43168\n"
  "v_mfma_f32_16x16x32_bf16 v[136:139], a[180:183], a[120:123], v[136:139]\n"
  "ds_read_b64_tr_b16 a[170:171], v224 offset:46752\n"
  "s_waitcnt lgkmcnt(6)\n"
  "v_mfma_f32_16x16x32_bf16 v[140:143], a[180:183], a[124:127], v[140:143]\n"
  "buffer_load_dwordx4 a[20:23], off, s[32:35], s44 offset:1024 sc1\n"
  "v_mfma_f32_16x16x32_bf16 v[140:143], a[176:179], a[108:111], v[140:143]\n"
  "v_mfma_f32_16x16x32_bf16 v[156:159], a[184:187], a[108:111], v[156:159]\n"
  "ds_read_b64_tr_b16 a[172:173], v224 offset:50336\n"
  "v_mfma_f32_16x16x32_bf16 v[156:159], a[188:191], a[124:127], v[156:159]\n"
  "ds_read_b64_tr_b16 a[174:175], v224 offset:53920\n"
  "v_mfma_f32_16x16x32_bf16 v[152:155], a[188:191], a[120:123], v[152:155]\n"
  "buffer_load_dwordx4 a[24:27], off, s[32:35], s44 offset:2048 sc1\n"
  "v_mfma_f32_16x16x32_bf16 v[152:155], a[184:187], a[104:107], v[152:155]\n"
  "v_mfma_f32_16x16x32_bf16 v[148:151], a[184:187], a[100:103], v[148:151]\n"
  "ds_read_b64_tr_b16 a[176:177], v224 offset:28864\n"
  "v_mfma_f32_16x16x32_bf16 v[148:151], a[188:191], a[116:119], v[148:151]\n"
  "ds_read_b64_tr_b16 a[178:179], v224 offset:32448\n"
  "s_waitcnt lgkmcnt(6)\n"
  "v_mfma_f32_16x16x32_bf16 v[144:147], a[188:191], a[112:115], v[144:147]\n"
  "buffer_load_dwordx4 a[28:31], off, s[32:35], s44 offset:3072 sc1\n"
  "v_mfma_f32_16x16x32_bf16 v[144:147], a[184:187], a[96:99], v[144:147]\n"
  "v_mfma_f32_16x16x32_bf16 v[160:163], a[160:163], a[96:99], v[160:163]\n"
  "ds_read_b64_tr_b16 a[180:181], v224 offset:36032\n"
  "s_add_u32 s44, s44, 4096\n"
  "v_mfma_f32_16x16x32_bf16 v[160:163], a[164:167], a[112:115], v[160:163]\n"
  "ds_read_b64_tr_b16 a[182:183], v224 offset:39616\n"
  "v_mfma_f32_16x16x32_bf16 v[164:167], a[164:167], a[116:119], v[164:167]\n"
  "v_mfma_f32_16x16x32_bf16 v[164:167], a[160:163], a[100:103], v[164:167]\n"
  "v_mfma_f32_16x16x32_bf16 v[168:171], a[160:163], a[104:107], v[168:171]\n"
  "ds_read_b64_tr_b16 a[184:185], v224 offset:43200\n"
  "v_mfma_f32_16x16x32_bf16 v[168:171], a[164:167], a[120:123], v[168:171]\n"
  "ds_read_b64_tr_b16 a[186:187], v224 offset:46784\n"
  "s_waitcnt lgkmcnt(6)\n"
  "v_mfma_f32_16x16x32_bf16 v[172:175], a[164:167], a[124:127], v[172:175]\n"
  "v_mfma_f32_16x16x32_bf16 v[172:175], a[160:163], a[108:111], v[172:175]\n"
  "v_mfma_f32_16x16x32_bf16 v[188:191], a[168:171], a[108:111], v[188:191]\n"
  "ds_read_b64_tr_b16 a[188:189], v224 offset:50368\n"
  "v_mfma_f32_16x16x32_bf16 v[188:191], a[172:175], a[124:127], v[188:191]\n"
  "ds_read_b64_tr_b16 a[190:191], v224 offset:53952\n"
  "v_mfma_f32_16x16x32_bf16 v[184:187], a[172:175], a[120:123], v[184:187]\n"
  "buffer_load_dwordx4 a[32:35], off, s[32:35], s44 offset:0 sc1\n"
  "v_mfma_f32_16x16x32_bf16 v[184:187], a[168:171], a[104:107], v[184:187]\n"
  "v_mfma_f32_16x16x32_bf16 v[180:183], a[168:171], a[100:103], v[180:183]\n"
  "s_waitcnt vmcnt(24)\n"
  "s_barrier\n"
  "v_mfma_f32_16x16x32_bf16 v[180:183], a[172:175], a[116:119], v[180:183]\n"
  "ds_read_b64_tr_b16 a[160:161], v225 offset:0\n"
  "v_mfma_f32_16x16x32_bf16 v[176:179], a[172:175], a[112:115], v[176:179]\n"
  "ds_read_b64_tr_b16 a[162:163], v225 offset:3584\n"
  "s_waitcnt lgkmcnt(6)\n"
  "v_mfma_f32_16x16x32_bf16 v[176:179], a[168:171], a[96:99], v[176:179]\n"
  "buffer_load_dwordx4 a[36:39], off, s[32:35], s44 offset:1024 sc1\n"
  "v_mfma_f32_16x16x32_bf16 v[192:195], a[176:179], a[96:99], v[192:195]\n"
  "ds_read_b64_tr_b16 a[164:165], v225 offset:7168\n"
  "v_mfma_f32_16x16x32_bf16 v[192:195], a[180:183], a[112:115], v[192:195]\n"
  "ds_read_b64_tr_b16 a[166:167], v225 offset:10752\n"
  "v_mfma_f32_16x16x32_bf16 v[196:199], a[180:183], a[116:119], v[196:199]\n"
  "buffer_load_dwordx4 a[40:43], off, s[32:35], s44 offset:2048 sc1\n"
  "v_mfma_f32_16x16x32_bf16 v[196:199], a[176:179], a[100:103], v[196:199]\n"
  "v_mfma_f32_16x16x32_bf16 v[200:203], a[176:179], a[104:107], v[200:203]\n"
  "ds_read_b64_tr_b16 a[168:169], v225 offset:14336\n"
  "v_mfma_f32_16x16x32_bf16 v[200:203], a[180:183], a[120:123], v[200:203]\n"
  "ds_read_b64_tr_b16 a[170:171], v225 offset:17920\n"
  "s_waitcnt lgkmcnt(6)\n"
  "v_mfma_f32_16x16x32_bf16 v[204:207], a[180:183], a[124:127], v[204:207]\n"
  "buffer_load_dwordx4 a[44:47], off, s[32:35], s44 offset:3072 sc1\n"
  "v_mfma_f32_16x16x32_bf16 v[204:207], a[176:179], a[108:111], v[204:207]\n"
  "v_mfma_f32_16x16x32_bf16 v[220:223], a[184:187], a[108:111], v[220:223]\n"
  "ds_read_b64_tr_b16 a[172:173], v225 offset:21504\n"
  "s_add_u32 s44, s44, 4096\n"
  "v_mfma_f32_16x16x32_bf16 v[220:223], a[188:191], a[124:127], v[220:223]\n"
  "ds_read_b64_tr_b16 a[174:175], v225 offset:25088\n"
  "v_mfma_f32_16x16x32_bf16 v[216:219], a[188:191], a[120:123], v[216:219]\n"
  "v_mfma_f32_16x16x32_bf16 v[216:219], a[184:187], a[104:107], v[216:219]\n"
  "v_mfma_f32_16x16x32_bf16 v[212:215], a[184:187], a[100:103], v[212:215]\n"
  "ds_read_b64_tr_b16 a[176:177], v225 offset:32\n"
  "v_mfma_f32_16x16x32_bf16 v[212:215], a[188:191], a[116:119], v[212:215]\n"
  "ds_read_b64_tr_b16 a[178:179], v225 offset:3616\n"
  "s_waitcnt vmcnt(19) lgkmcnt(6)\n"
  "v_mfma_f32_16x16x32_bf16 v[208:211], a[188:191], a[112:115], v[208:211]\n"
  "v_mfma_f32_16x16x32_bf16 v[208:211], a[184:187], a[96:99], v[208:211]\n"
  "v_mfma_f32_16x16x32_bf16 v[0:3], a[160:163], a[128:131], v[0:3]\n"
  "ds_read_b64_tr_b16 a[180:181], v225 offset:7200\n"
  "s_add_u32 m0, s46, 86016\n"
  "v_mfma_f32_16x16x32_bf16 v[0:3], a[164:167], a[144:147], v[0:3]\n"
  "ds_read_b64_tr_b16 a[182:183], v225 offset:10784\n"
  "v_mfma_f32_16x16x32_bf16 v[4:7], a[164:167], a[148:151], v[4:7]\n"
  "v_mfma_f32_16x16x32_bf16 v[4:7], a[160:163], a[132:135], v[4:7]\n"
  "v_mfma_f32_16x16x32_bf16 v[8:11], a[160:163], a[136:139], v[8:11]\n"
  "ds_read_b64_tr_b16 a[184:185], v225 offset:14368\n"
  "v_mfma_f32_16x16x32_bf16 v[8:11], a[164:167], a[152:155], v[8:11]\n"
  "ds_read_b64_tr_b16 a[186:187], v225 offset:17952\n"
  "s_waitcnt lgkmcnt(6)\n"
  "v_mfma_f32_16x16x32_bf16 v[12:15], a[164:167], a[156:159], v[12:15]\n"
  "buffer_load_dwordx4 v240, s[36:39], s45 offen offset:0 lds\n"
  "v_mfma_f32_16x16x32_bf16 v[12:15], a[160:163], a[140:143], v[12:15]\n"
  "v_mfma_f32_16x16x32_bf16 v[28:31], a[168:171], a[140:143], v[28:31]\n"
  "ds_read_b64_tr_b16 a[188:189], v225 offset:21536\n"
  "v_mfma_f32_16x16x32_bf16 v[28:31], a[172:175], a[156:159], v[28:31]\n"
  "ds_read_b64_tr_b16 a[190:191], v225 offset:25120\n"
  "v_mfma_f32_16x16x32_bf16 v[24:27], a[172:175], a[152:155], v[24:27]\n"
  "buffer_load_dwordx4 v241, s[36:39], s45 offen offset:1024 lds\n"
  "v_mfma_f32_16x16x32_bf16 v[24:27], a[168:171], a[136:139], v[24:27]\n"
  "v_mfma_f32_16x16x32_bf16 v[20:23], a[168:171], a[132:135], v[20:23]\n"
  "ds_read_b64_tr_b16 a[160:161], v225 offset:64\n"
  "v_mfma_f32_16x16x32_bf16 v[20:23], a[172:175], a[148:151], v[20:23]\n"
  "ds_read_b64_tr_b16 a[162:163], v225 offset:3648\n"
  "s_waitcnt lgkmcnt(6)\n"
  "v_mfma_f32_16x16x32_bf16 v[16:19], a[172:175], a[144:147], v[16:19]\n"
  "buffer_load_dwordx4 v242, s[36:39], s45 offen offset:2048 lds\n"
  "v_mfma_f32_16x16x32_bf16 v[16:19], a[168:171], a[128:131], v[16:19]\n"
  "v_mfma_f32_16x16x32_bf16 v[32:35], a[176:179], a[128:131], v[32:35]\n"
  "ds_read_b64_tr_b16 a[164:165], v225 offset:7232\n"
  "v_mfma_f32_16x16x32_bf16 v[32:35], a[180:183], a[144:147], v[32:35]\n"
  "ds_read_b64_tr_b16 a[166:167], v225 offset:10816\n"
  "v_mfma_f32_16x16x32_bf16 v[36:39], a[180:183], a[148:151], v[36:39]\n"
  "buffer_load_dwordx4 v243, s[36:39], s45 offen offset:3072 lds\n"
  "v_mfma_f32_16x16x32_bf16 v[36:39], a[176:179], a[132:135], v[36:39]\n"
  "v_mfma_f32_16x16x32_bf16 v[40:43], a[176:179], a[136:139], v[40:43]\n"
  "ds_read_b64_tr_b16 a[168:169], v225 offset:14400\n"
  "s_add_u32 m0, s46, 90112\n"
  "v_mfma_f32_16x16x32_bf16 v[40:43], a[180:183], a[152:155], v[40:43]\n"
  "ds_read_b64_tr_b16 a[170:171], v225 offset:17984\n"
  "s_waitcnt lgkmcnt(6)\n"
  "v_mfma_f32_16x16x32_bf16 v[44:47], a[180:183], a[156:159], v[44:47]\n"
  "v_mfma_f32_16x16x32_bf16 v[44:47], a[176:179], a[140:143], v[44:47]\n"
  "v_mfma_f32_16x16x32_bf16 v[60:63], a[184:187], a[140:143], v[60:63]\n"
  "ds_read_b64_tr_b16 a[172:173], v225 offset:21568\n"
  "v_mfma_f32_16x16x32_bf16 v[60:63], a[188:191], a[156:159], v[60:63]\n"
  "ds_read_b64_tr_b16 a[174:175], v225 offset:25152\n"
  "v_mfma_f32_16x16x32_bf16 v[56:59], a[188:191], a[152:155], v[56:59]\n"
  "v_mfma_f32_16x16x32_bf16 v[56:59], a[184:187], a[136:139], v[56:59]\n"
  "v_mfma_f32_16x16x32_bf16 v[52:55], a[184:187], a[132:135], v[52:55]\n"
  "ds_read_b64_tr_b16 a[176:177], v225 offset:96\n"
  "v_mfma_f32_16x16x32_bf16 v[52:55], a[188:191], a[148:151], v[52:55]\n"
  "ds_read_b64_tr_b16 a[178:179], v225 offset:3680\n"
  "s_waitcnt lgkmcnt(6)\n"
  "v_mfma_f32_16x16x32_bf16 v[48:51], a[188:191], a[144:147], v[48:51]\n"
  "buffer_load_dwordx4 v244, s[36:39], s45 offen offset:0 lds\n"
  "v_mfma_f32_16x16x32_bf16 v[48:51], a[184:187], a[128:131], v[48:51]\n"
  "v_mfma_f32_16x16x32_bf16 v[64:67], a[160:163], a[128:131], v[64:67]\n"
  "ds_read_b64_tr_b16 a[180:181], v225 offset:7264\n"
  "v_mfma_f32_16x16x32_bf16 v[64:67], a[164:167], a[144:147], v[64:67]\n"
  "ds_read_b64_tr_b16 a[182:183], v225 offset:10848\n"
  "v_mfma_f32_16x16x32_bf16 v[68:71], a[164:167], a[148:151], v[68:71]\n"
  "buffer_load_dwordx4 v245, s[36:39], s45 offen offset:1024 lds\n"
  "v_mfma_f32_16x16x32_bf16 v[68:71], a[160:163], a[132:135], v[68:71]\n"
  "v_mfma_f32_16x16x32_bf16 v[72:75], a[160:163], a[136:139], v[72:75]\n"
  "ds_read_b64_tr_b16 a[184:185], v225 offset:14432\n"
  "v_mfma_f32_16x16x32_bf16 v[72:75], a[164:167], a[152:155], v[72:75]\n"
  "ds_read_b64_tr_b16 a[186:187], v225 offset:18016\n"
  "s_waitcnt lgkmcnt(6)\n"
  "v_mfma_f32_16x16x32_bf16 v[76:79], a[164:167], a[156:159], v[76:79]\n"
  "buffer_load_dwordx4 v246, s[36:39], s45 offen offset:2048 lds\n"
  "v_mfma_f32_16x16x32_bf16 v[76:79], a[160:163], a[140:143], v[76:79]\n"
  "v_mfma_f32_16x16x32_bf16 v[92:95], a[168:171], a[140:143], v[92:95]\n"
  "ds_read_b64_tr_b16 a[188:189], v225 offset:21600\n"
  "s_add_u32 s45, s45, 917504\n"
  "v_mfma_f32_16x16x32_bf16 v[92:95], a[172:175], a[156:159], v[92:95]\n"
  "ds_read_b64_tr_b16 a[190:191], v225 offset:25184\n"
  "v_mfma_f32_16x16x32_bf16 v[88:91], a[172:175], a[152:155], v[88:91]\n"
  "v_mfma_f32_16x16x32_bf16 v[88:91], a[168:171], a[136:139], v[88:91]\n"
  "v_mfma_f32_16x16x32_bf16 v[84:87], a[168:171], a[132:135], v[84:87]\n"
  "ds_read_b64_tr_b16 a[160:161], v225 offset:128\n"
  "v_mfma_f32_16x16x32_bf16 v[84:87], a[172:175], a[148:151], v[84:87]\n"
  "ds_read_b64_tr_b16 a[162:163], v225 offset:3712\n"
  "s_waitcnt lgkmcnt(6)\n"
  "v_mfma_f32_16x16x32_bf16 v[80:83], a[172:175], a[144:147], v[80:83]\n"
  "buffer_load_dwordx2 v[238:239], v227, s[52:55], s93 offen sc1\n"
  "v_mfma_f32_16x16x32_bf16 v[80:83], a[168:171], a[128:131], v[80:83]\n"
  "v_mfma_f32_16x16x32_bf16 v[96:99], a[176:179], a[128:131], v[96:99]\n"
  "ds_read_b64_tr_b16 a[164:165], v225 offset:7296\n"
  "v_mfma_f32_16x16x32_bf16 v[96:99], a[180:183], a[144:147], v[96:99]\n"
  "ds_read_b64_tr_b16 a[166:167], v225 offset:10880\n"
  "v_mfma_f32_16x16x32_bf16 v[100:103], a[180:183], a[148:151], v[100:103]\n"
  "buffer_load_dwordx4 a[48:51], off, s[32:35], s44 offset:0 sc1\n"
  "v_mfma_f32_16x16x32_bf16 v[100:103], a[176:179], a[132:135], v[100:103]\n"
  "v_mfma_f32_16x16x32_bf16 v[104:107], a[176:179], a[136:139], v[104:107]\n"
  "ds_read_b64_tr_b16 a[168:169], v225 offset:14464\n"
  "v_mfma_f32_16x16x32_bf16 v[104:107], a[180:183], a[152:155], v[104:107]\n"
  "ds_read_b64_tr_b16 a[170:171], v225 offset:18048\n"
  "s_waitcnt lgkmcnt(6)\n"
  "v_mfma_f32_16x16x32_bf16 v[108:111], a[180:183], a[156:159], v[108:111]\n"
  "v_mfma_f32_16x16x32_bf16 v[108:111], a[176:179], a[140:143], v[108:111]\n"
  "v_mfma_f32_16x16x32_bf16 v[124:127], a[184:187], a[140:143], v[124:127]\n"
  "ds_read_b64_tr_b16 a[172:173], v225 offset:21632\n"
  "v_mfma_f32_16x16x32_bf16 v[124:127], a[188:191], a[156:159], v[124:127]\n"
  "ds_read_b64_tr_b16 a[174:175], v225 offset:25216\n"
  "v_mfma_f32_16x16x32_bf16 v[120:123], a[188:191], a[152:155], v[120:123]\n"
  "buffer_load_dwordx4 a[52:55], off, s[32:35], s44 offset:1024 sc1\n"
  "v_mfma_f32_16x16x32_bf16 v[120:123], a[184:187], a[136:139], v[120:123]\n"
  "v_mfma_f32_16x16x32_bf16 v[116:119], a[184:187], a[132:135], v[116:119]\n"
  "ds_read_b64_tr_b16 a[176:177], v225 offset:160\n"
  "v_mfma_f32_16x16x32_bf16 v[116:119], a[188:191], a[148:151], v[116:119]\n"
  "ds_read_b64_tr_b16 a[178:179], v225 offset:3744\n"
  "s_waitcnt lgkmcnt(6)\n"
  "v_mfma_f32_16x16x32_bf16 v[112:115], a[188:191], a[144:147], v[112:115]\n"
  "buffer_load_dwordx4 a[56:59], off, s[32:35], s44 offset:2048 sc1\n"
  "v_mfma_f32_16x16x32_bf16 v[112:115], a[184:187], a[128:131], v[112:115]\n"
  "v_mfma_f32_16x16x32_bf16 v[128:131], a[160:163], a[128:131], v[128:131]\n"
  "ds_read_b64_tr_b16 a[180:181], v225 offset:7328\n"
  "v_mfma_f32_16x16x32_bf16 v[128:131], a[164:167], a[144:147], v[128:131]\n"
  "ds_read_b64_tr_b16 a[182:183], v225 offset:10912\n"
  "v_mfma_f32_16x16x32_bf16 v[132:135], a[164:167], a[148:151], v[132:135]\n"
  "buffer_load_dwordx4 a[60:63], off, s[32:35], s44 offset:3072 sc1\n"
  "v_mfma_f32_16x16x32_bf16 v[132:135], a[160:163], a[132:135], v[132:135]\n"
  "v_mfma_f32_16x16x32_bf16 v[136:139], a[160:163], a[136:139], v[136:139]\n"
  "ds_read_b64_tr_b16 a[184:185], v225 offset:14496\n"
  "s_add_u32 s44, s44, 4096\n"
  "v_mfma_f32_16x16x32_bf16 v[136:139], a[164:167], a[152:155], v[136:139]\n"
  "ds_read_b64_tr_b16 a[186:187], v225 offset:18080\n"
  "s_waitcnt lgkmcnt(6)\n"
  "v_mfma_f32_16x16x32_bf16 v[140:143], a[164:167], a[156:159], v[140:143]\n"
  "v_mfma_f32_16x16x32_bf16 v[140:143], a[160:163], a[140:143], v[140:143]\n"
  "v_mfma_f32_16x16x32_bf16 v[156:159], a[168:171], a[140:143], v[156:159]\n"
  "ds_read_b64_tr_b16 a[188:189], v225 offset:21664\n"
  "s_add_u32 m0, s46, 114688\n"
  "v_mfma_f32_16x16x32_bf16 v[156:159], a[172:175], a[156:159], v[156:159]\n"
  "ds_read_b64_tr_b16 a[190:191], v225 offset:25248\n"
  "v_mfma_f32_16x16x32_bf16 v[152:155], a[172:175], a[152:155], v[152:155]\n"
  "v_mfma_f32_16x16x32_bf16 v[152:155], a[168:171], a[136:139], v[152:155]\n"
  "v_mfma_f32_16x16x32_bf16 v[148:151], a[168:171], a[132:135], v[148:151]\n"
  "ds_read_b64_tr_b16 a[160:161], v225 offset:192\n"
  "v_mfma_f32_16x16x32_bf16 v[148:151], a[172:175], a[148:151], v[148:151]\n"
  "ds_read_b64_tr_b16 a[162:163], v225 offset:3776\n"
  "s_waitcnt lgkmcnt(6)\n"
  "v_mfma_f32_16x16x32_bf16 v[144:147], a[172:175], a[144:147], v[144:147]\n"
  "buffer_load_dwordx4 v240, s[36:39], s45 offen offset:0 lds\n"
  "v_mfma_f32_16x16x32_bf16 v[144:147], a[168:171], a[128:131], v[144:147]\n"
  "v_mfma_f32_16x16x32_bf16 v[160:163], a[176:179], a[128:131], v[160:163]\n"
  "ds_read_b64_tr_b16 a[164:165], v225 offset:7360\n"
  "v_mfma_f32_16x16x32_bf16 v[160:163], a[180:183], a[144:147], v[160:163]\n"
  "ds_read_b64_tr_b16 a[166:167], v225 offset:10944\n"
  "v_mfma_f32_16x16x32_bf16 v[164:167], a[180:183], a[148:151], v[164:167]\n"
  "v_mfma_f32_16x16x32_bf16 v[164:167], a[176:179], a[132:135], v[164:167]\n"
  "v_mfma_f32_16x16x32_bf16 v[168:171], a[176:179], a[136:139], v[168:171]\n"
  "ds_read_b64_tr_b16 a[168:169], v225 offset:14528\n"
  "v_mfma_f32_16x16x32_bf16 v[168:171], a[180:183], a[152:155], v[168:171]\n"
  "ds_read_b64_tr_b16 a[170:171], v225 offset:18112\n"
  "s_waitcnt lgkmcnt(6)\n"
  "v_mfma_f32_16x16x32_bf16 v[172:175], a[180:183], a[156:159], v[172:175]\n"
  "buffer_load_dwordx4 v241, s[36:39], s45 offen offset:1024 lds\n"
  "v_mfma_f32_16x16x32_bf16 v[172:175], a[176:179], a[140:143], v[172:175]\n"
  "v_mfma_f32_16x16x32_bf16 v[188:191], a[184:187], a[140:143], v[188:191]\n"
  "ds_read_b64_tr_b16 a[172:173], v225 offset:21696\n"
  "v_mfma_f32_16x16x32_bf16 v[188:191], a[188:191], a[156:159], v[188:191]\n"
  "ds_read_b64_tr_b16 a[174:175], v225 offset:25280\n"
  "v_mfma_f32_16x16x32_bf16 v[184:187], a[188:191], a[152:155], v[184:187]\n"
  "buffer_load_dwordx4 v242, s[36:39], s45 offen offset:2048 lds\n"
  "v_mfma_f32_16x16x32_bf16 v[184:187], a[184:187], a[136:139], v[184:187]\n"
  "v_mfma_f32_16x16x32_bf16 v[180:183], a[184:187], a[132:135], v[180:183]\n"
  "s_waitcnt vmcnt(27)\n"
  "s_barrier\n"
  "v_mfma_f32_16x16x32_bf16 v[180:183], a[188:191], a[148:151], v[180:183]\n"
  "ds_read_b64_tr_b16 a[176:177], v224 offset:0\n"
  "v_mfma_f32_16x16x32_bf16 v[176:179], a[188:191], a[144:147], v[176:179]\n"
  "ds_read_b64_tr_b16 a[178:179], v224 offset:3584\n"
  "s_waitcnt lgkmcnt(6)\n"
  "v_mfma_f32_16x16x32_bf16 v[176:179], a[184:187], a[128:131], v[176:179]\n"
  "buffer_load_dwordx4 v243, s[36:39], s45 offen offset:3072 lds\n"
  "v_mfma_f32_16x16x32_bf16 v[192:195], a[160:163], a[128:131], v[192:195]\n"
  "ds_read_b64_tr_b16 a[180:181], v224 offset:7168\n"
  "s_add_u32 m0, s46, 118784\n"
  "v_mfma_f32_16x16x32_bf16 v[192:195], a[164:167], a[144:147], v[192:195]\n"
  "ds_read_b64_tr_b16 a[182:183], v224 offset:10752\n"
  "v_mfma_f32_16x16x32_bf16 v[196:199], a[164:167], a[148:151], v[196:199]\n"
  "v_mfma_f32_16x16x32_bf16 v[196:199], a[160:163], a[132:135], v[196:199]\n"
  "v_mfma_f32_16x16x32_bf16 v[200:203], a[160:163], a[136:139], v[200:203]\n"
  "ds_read_b64_tr_b16 a[184:185], v224 offset:14336\n"
  "v_mfma_f32_16x16x32_bf16 v[200:203], a[164:167], a[152:155], v[200:203]\n"
  "ds_read_b64_tr_b16 a[186:187], v224 offset:17920\n"
  "s_waitcnt lgkmcnt(6)\n"
  "v_mfma_f32_16x16x32_bf16 v[204:207], a[164:167], a[156:159], v[204:207]\n"
  "buffer_load_dwordx4 v244, s[36:39], s45 offen offset:0 lds\n"
  "v_mfma_f32_16x16x32_bf16 v[204:207], a[160:163], a[140:143], v[204:207]\n"
  "v_mfma_f32_16x16x32_bf16 v[220:223], a[168:171], a[140:143], v[220:223]\n"
  "ds_read_b64_tr_b16 a[188:189], v224 offset:21504\n"
  "v_mfma_f32_16x16x32_bf16 v[220:223], a[172:175], a[156:159], v[220:223]\n"
  "ds_read_b64_tr_b16 a[190:191], v224 offset:25088\n"
  "v_mfma_f32_16x16x32_bf16 v[216:219], a[172:175], a[152:155], v[216:219]\n"
  "buffer_load_dwordx4 v245, s[36:39], s45 offen offset:1024 lds\n"
  "v_mfma_f32_16x16x32_bf16 v[216:219], a[168:171], a[136:139], v[216:219]\n"
  "v_mfma_f32_16x16x32_bf16 v[212:215], a[168:171], a[132:135], v[212:215]\n"
  "ds_read_b64_tr_b16 a[160:161], v224 offset:32\n"
  "v_mfma_f32_16x16x32_bf16 v[212:215], a[172:175], a[148:151], v[212:215]\n"
  "ds_read_b64_tr_b16 a[162:163], v224 offset:3616\n"
  "s_waitcnt vmcnt(22) lgkmcnt(6)\n"
  "v_mfma_f32_16x16x32_bf16 v[208:211], a[172:175], a[144:147], v[208:211]\n"
  "v_mfma_f32_16x16x32_bf16 v[208:211], a[168:171], a[128:131], v[208:211]\n"
  "v_mfma_f32_16x16x32_bf16 v[0:3], a[176:179], a[0:3], v[0:3]\n"
  "ds_read_b64_tr_b16 a[164:165], v224 offset:7200\n"
  "v_mfma_f32_16x16x32_bf16 v[0:3], a[180:183], a[16:19], v[0:3]\n"
  "ds_read_b64_tr_b16 a[166:167], v224 offset:10784\n"
  "v_mfma_f32_16x16x32_bf16 v[4:7], a[180:183], a[20:23], v[4:7]\n"
  "buffer_load_dwordx4 v246, s[36:39], s45 offen offset:2048 lds\n"
  "v_mfma_f32_16x16x32_bf16 v[4:7], a[176:179], a[4:7], v[4:7]\n"
  "v_mfma_f32_16x16x32_bf16 v[8:11], a[176:179], a[8:11], v[8:11]\n"
  "ds_read_b64_tr_b16 a[168:169], v224 offset:14368\n"
  "s_add_u32 s45, s45, -6193152\n"
  "v_mfma_f32_16x16x32_bf16 v[8:11], a[180:183], a[24:27], v[8:11]\n"
  "ds_read_b64_tr_b16 a[170:171], v224 offset:17952\n"
  "s_waitcnt lgkmcnt(6)\n"
  "v_mfma_f32_16x16x32_bf16 v[12:15], a[180:183], a[28:31], v[12:15]\n"
  "v_mfma_f32_16x16x32_bf16 v[12:15], a[176:179], a[12:15], v[12:15]\n"
  "v_mfma_f32_16x16x32_bf16 v[28:31], a[184:187], a[12:15], v[28:31]\n"
  "ds_read_b64_tr_b16 a[172:173], v224 offset:21536\n"
  "v_mfma_f32_16x16x32_bf16 v[28:31], a[188:191], a[28:31], v[28:31]\n"
  "ds_read_b64_tr_b16 a[174:175], v224 offset:25120\n"
  "v_mfma_f32_16x16x32_bf16 v[24:27], a[188:191], a[24:27], v[24:27]\n"
  "buffer_load_dwordx4 a[64:67], off, s[32:35], s44 offset:0 sc1\n"
  "v_mfma_f32_16x16x32_bf16 v[24:27], a[184:187], a[8:11], v[24:27]\n"
  "v_mfma_f32_16x16x32_bf16 v[20:23], a[184:187], a[4:7], v[20:23]\n"
  "ds_read_b64_tr_b16 a[176:177], v224 offset:64\n"
  "v_mfma_f32_16x16x32_bf16 v[20:23], a[188:191], a[20:23], v[20:23]\n"
  "ds_read_b64_tr_b16 a[178:179], v224 offset:3648\n"
  "s_waitcnt lgkmcnt(6)\n"
  "v_mfma_f32_16x16x32_bf16 v[16:19], a[188:191], a[16:19], v[16:19]\n"
  "buffer_load_dwordx4 a[68:71], off, s[32:35], s44 offset:1024 sc1\n"
  "v_mfma_f32_16x16x32_bf16 v[16:19], a[184:187], a[0:3], v[16:19]\n"
  "v_mfma_f32_16x16x32_bf16 v[32:35], a[160:163], a[0:3], v[32:35]\n"
  "ds_read_b64_tr_b16 a[180:181], v224 offset:7232\n"
  "v_mfma_f32_16x16x32_bf16 v[32:35], a[164:167], a[16:19], v[32:35]\n"
  "ds_read_b64_tr_b16 a[182:183], v224 offset:10816\n"
  "v_mfma_f32_16x16x32_bf16 v[36:39], a[164:167], a[20:23], v[36:39]\n"
  "buffer_load_dwordx4 a[72:75], off, s[32:35], s44 offset:2048 sc1\n"
  "v_mfma_f32_16x16x32_bf16 v[36:39], a[160:163], a[4:7], v[36:39]\n"
  "v_mfma_f32_16x16x32_bf16 v[40:43], a[160:163], a[8:11], v[40:43]\n"
  "ds_read_b64_tr_b16 a[184:185], v224 offset:14400\n"
  "v_mfma_f32_16x16x32_bf16 v[40:43], a[164:167], a[24:27], v[40:43]\n"
  "ds_read_b64_tr_b16 a[186:187], v224 offset:17984\n"
  "s_waitcnt lgkmcnt(6)\n"
  "v_mfma_f32_16x16x32_bf16 v[44:47], a[164:167], a[28:31], v[44:47]\n"
  "buffer_load_dwordx4 a[76:79], off, s[32:35], s44 offset:3072 sc1\n"
  "v_mfma_f32_16x16x32_bf16 v[44:47], a[160:163], a[12:15], v[44:47]\n"
  "v_mfma_f32_16x16x32_bf16 v[60:63], a[168:171], a[12:15], v[60:63]\n"
  "ds_read_b64_tr_b16 a[188:189], v224 offset:21568\n"
  "s_add_u32 s44, s44, 4096\n"
  "v_mfma_f32_16x16x32_bf16 v[60:63], a[172:175], a[28:31], v[60:63]\n"
  "ds_read_b64_tr_b16 a[190:191], v224 offset:25152\n"
  "v_mfma_f32_16x16x32_bf16 v[56:59], a[172:175], a[24:27], v[56:59]\n"
  "v_mfma_f32_16x16x32_bf16 v[56:59], a[168:171], a[8:11], v[56:59]\n"
  "v_mfma_f32_16x16x32_bf16 v[52:55], a[168:171], a[4:7], v[52:55]\n"
  "ds_read_b64_tr_b16 a[160:161], v224 offset:96\n"
  "v_mfma_f32_16x16x32_bf16 v[52:55], a[172:175], a[20:23], v[52:55]\n"
  "ds_read_b64_tr_b16 a[162:163], v224 offset:3680\n"
  "s_waitcnt lgkmcnt(6)\n"
  "v_mfma_f32_16x16x32_bf16 v[48:51], a[172:175], a[16:19], v[48:51]\n"
  "v_mfma_f32_16x16x32_bf16 v[48:51], a[168:171], a[0:3], v[48:51]\n"
  "v_mfma_f32_16x16x32_bf16 v[64:67], a[176:179], a[0:3], v[64:67]\n"
  "ds_read_b64_tr_b16 a[164:165], v224 offset:7264\n"
  "v_mfma_f32_16x16x32_bf16 v[64:67], a[180:183], a[16:19], v[64:67]\n"
  "ds_read_b64_tr_b16 a[166:167], v224 offset:10848\n"
  "v_mfma_f32_16x16x32_bf16 v[68:71], a[180:183], a[20:23], v[68:71]\n"
  "buffer_load_dwordx4 a[80:83], off, s[32:35], s44 offset:0 sc1\n"
  "v_mfma_f32_16x16x32_bf16 v[68:71], a[176:179], a[4:7], v[68:71]\n"
  "v_mfma_f32_16x16x32_bf16 v[72:75], a[176:179], a[8:11], v[72:75]\n"
  "ds_read_b64_tr_b16 a[168:169], v224 offset:14432\n"
  "v_mfma_f32_16x16x32_bf16 v[72:75], a[180:183], a[24:27], v[72:75]\n"
  "ds_read_b64_tr_b16 a[170:171], v224 offset:18016\n"
  "s_waitcnt lgkmcnt(6)\n"
  "v_mfma_f32_16x16x32_bf16 v[76:79], a[180:183], a[28:31], v[76:79]\n"
  "buffer_load_dwordx4 a[84:87], off, s[32:35], s44 offset:1024 sc1\n"
  "v_mfma_f32_16x16x32_bf16 v[76:79], a[176:179], a[12:15], v[76:79]\n"
  "v_mfma_f32_16x16x32_bf16 v[92:95], a[184:187], a[12:15], v[92:95]\n"
  "ds_read_b64_tr_b16 a[172:173], v224 offset:21600\n"
  "v_mfma_f32_16x16x32_bf16 v[92:95], a[188:191], a[28:31], v[92:95]\n"
  "ds_read_b64_tr_b16 a[174:175], v224 offset:25184\n"
  "v_mfma_f32_16x16x32_bf16 v[88:91], a[188:191], a[24:27], v[88:91]\n"
  "buffer_load_dwordx4 a[88:91], off, s[32:35], s44 offset:2048 sc1\n"
  "v_mfma_f32_16x16x32_bf16 v[88:91], a[184:187], a[8:11], v[88:91]\n"
  "v_mfma_f32_16x16x32_bf16 v[84:87], a[184:187], a[4:7], v[84:87]\n"
  "ds_read_b64_tr_b16 a[176:177], v224 offset:128\n"
  "v_mfma_f32_16x16x32_bf16 v[84:87], a[188:191], a[20:23], v[84:87]\n"
  "ds_read_b64_tr_b16 a[178:179], v224 offset:3712\n"
  "s_waitcnt lgkmcnt(6)\n"
  "v_mfma_f32_16x16x32_bf16 v[80:83], a[188:191], a[16:19], v[80:83]\n"
  "buffer_load_dwordx4 a[92:95], off, s[32:35], s44 offset:3072 sc1\n"
  "v_mfma_f32_16x16x32_bf16 v[80:83], a[184:187], a[0:3], v[80:83]\n"
  "v_mfma_f32_16x16x32_bf16 v[96:99], a[160:163], a[0:3], v[96:99]\n"
  "ds_read_b64_tr_b16 a[180:181], v224 offset:7296\n"
  "s_add_u32 s44, s44, 4096\n"
  "v_mfma_f32_16x16x32_bf16 v[96:99], a[164:167], a[16:19], v[96:99]\n"
  "ds_read_b64_tr_b16 a[182:183], v224 offset:10880\n"
  "v_mfma_f32_16x16x32_bf16 v[100:103], a[164:167], a[20:23], v[100:103]\n"
  "v_mfma_f32_16x16x32_bf16 v[100:103], a[160:163], a[4:7], v[100:103]\n"
  "v_mfma_f32_16x16x32_bf16 v[104:107], a[160:163], a[8:11], v[104:107]\n"
  "ds_read_b64_tr_b16 a[184:185], v224 offset:14464\n"
  "v_mfma_f32_16x16x32_bf16 v[104:107], a[164:167], a[24:27], v[104:107]\n"
  "ds_read_b64_tr_b16 a[186:187], v224 offset:18048\n"
  "s_waitcnt lgkmcnt(6)\n"
  "v_mfma_f32_16x16x32_bf16 v[108:111], a[164:167], a[28:31], v[108:111]\n"
  "v_mfma_f32_16x16x32_bf16 v[108:111], a[160:163], a[12:15], v[108:111]\n"
  "v_mfma_f32_16x16x32_bf16 v[124:127], a[168:171], a[12:15], v[124:127]\n"
  "ds_read_b64_tr_b16 a[188:189], v224 offset:21632\n"
  "s_add_u32 m0, s46, 28672\n"
  "v_mfma_f32_16x16x32_bf16 v[124:127], a[172:175], a[28:31], v[124:127]\n"
  "ds_read_b64_tr_b16 a[190:191], v224 offset:25216\n"
  "v_mfma_f32_16x16x32_bf16 v[120:123], a[172:175], a[24:27], v[120:123]\n"
  "v_mfma_f32_16x16x32_bf16 v[120:123], a[168:171], a[8:11], v[120:123]\n"
  "v_mfma_f32_16x16x32_bf16 v[116:119], a[168:171], a[4:7], v[116:119]\n"
  "ds_read_b64_tr_b16 a[160:161], v224 offset:160\n"
  "v_mfma_f32_16x16x32_bf16 v[116:119], a[172:175], a[20:23], v[116:119]\n"
  "ds_read_b64_tr_b16 a[162:163], v224 offset:3744\n"
  "s_waitcnt lgkmcnt(6)\n"
  "v_mfma_f32_16x16x32_bf16 v[112:115], a[172:175], a[16:19], v[112:115]\n"
  "buffer_load_dwordx4 v240, s[36:39], s45 offen offset:0 lds\n"
  "v_mfma_f32_16x16x32_bf16 v[112:115], a[168:171], a[0:3], v[112:115]\n"
  "v_mfma_f32_16x16x32_bf16 v[128:131], a[176:179], a[0:3], v[128:131]\n"
  "ds_read_b64_tr_b16 a[164:165], v224 offset:7328\n"
  "v_mfma_f32_16x16x32_bf16 v[128:131], a[180:183], a[16:19], v[128:131]\n"
  "ds_read_b64_tr_b16 a[166:167], v224 offset:10912\n"
  "v_mfma_f32_16x16x32_bf16 v[132:135], a[180:183], a[20:23], v[132:135]\n"
  "buffer_load_dwordx4 v241, s[36:39], s45 offen offset:1024 lds\n"
  "v_mfma_f32_16x16x32_bf16 v[132:135], a[176:179], a[4:7], v[132:135]\n"
  "v_mfma_f32_16x16x32_bf16 v[136:139], a[176:179], a[8:11], v[136:139]\n"
  "ds_read_b64_tr_b16 a[168:169], v224 offset:14496\n"
  "v_mfma_f32_16x16x32_bf16 v[136:139], a[180:183], a[24:27], v[136:139]\n"
  "ds_read_b64_tr_b16 a[170:171], v224 offset:18080\n"
  "s_waitcnt lgkmcnt(6)\n"
  "v_mfma_f32_16x16x32_bf16 v[140:143], a[180:183], a[28:31], v[140:143]\n"
  "buffer_load_dwordx4 v242, s[36:39], s45 offen offset:2048 lds\n"
  "v_mfma_f32_16x16x32_bf16 v[140:143], a[176:179], a[12:15], v[140:143]\n"
  "v_mfma_f32_16x16x32_bf16 v[156:159], a[184:187], a[12:15], v[156:159]\n"
  "ds_read_b64_tr_b16 a[172:173], v224 offset:21664\n"
  "v_mfma_f32_16x16x32_bf16 v[156:159], a[188:191], a[28:31], v[156:159]\n"
  "ds_read_b64_tr_b16 a[174:175], v224 offset:25248\n"
  "v_mfma_f32_16x16x32_bf16 v[152:155], a[188:191], a[24:27], v[152:155]\n"
  "buffer_load_dwordx4 v243, s[36:39], s45 offen offset:3072 lds\n"
  "v_mfma_f32_16x16x32_bf16 v[152:155], a[184:187], a[8:11], v[152:155]\n"
  "v_mfma_f32_16x16x32_bf16 v[148:151], a[184:187], a[4:7], v[148:151]\n"
  "ds_read_b64_tr_b16 a[176:177], v224 offset:192\n"
  "s_add_u32 m0, s46, 32768\n"
  "v_mfma_f32_16x16x32_bf16 v[148:151], a[188:191], a[20:23], v[148:151]\n"
  "ds_read_b64_tr_b16 a[178:179], v224 offset:3776\n"
  "s_waitcnt lgkmcnt(6)\n"
  "v_mfma_f32_16x16x32_bf16 v[144:147], a[188:191], a[16:19], v[144:147]\n"
  "v_mfma_f32_16x16x32_bf16 v[144:147], a[184:187], a[0:3], v[144:147]\n"
  "v_mfma_f32_16x16x32_bf16 v[160:163], a[160:163], a[0:3], v[160:163]\n"
  "ds_read_b64_tr_b16 a[180:181], v224 offset:7360\n"
  "v_mfma_f32_16x16x32_bf16 v[160:163], a[164:167], a[16:19], v[160:163]\n"
  "ds_read_b64_tr_b16 a[182:183], v224 offset:10944\n"
  "v_mfma_f32_16x16x32_bf16 v[164:167], a[164:167], a[20:23], v[164:167]\n"
  "buffer_load_dwordx4 v244, s[36:39], s45 offen offset:0 lds\n"
  "v_mfma_f32_16x16x32_bf16 v[164:167], a[160:163], a[4:7], v[164:167]\n"
  "v_mfma_f32_16x16x32_bf16 v[168:171], a[160:163], a[8:11], v[168:171]\n"
  "ds_read_b64_tr_b16 a[184:185], v224 offset:14528\n"
  "v_mfma_f32_16x16x32_bf16 v[168:171], a[164:167], a[24:27], v[168:171]\n"
  "ds_read_b64_tr_b16 a[186:187], v224 offset:18112\n"
  "s_waitcnt lgkmcnt(6)\n"
  "v_mfma_f32_16x16x32_bf16 v[172:175], a[164:167], a[28:31], v[172:175]\n"
  "buffer_load_dwordx4 v245, s[36:39], s45 offen offset:1024 lds\n"
  "v_mfma_f32_16x16x32_bf16 v[172:175], a[160:163], a[12:15], v[172:175]\n"
  "v_mfma_f32_16x16x32_bf16 v[188:191], a[168:171], a[12:15], v[188:191]\n"
  "ds_read_b64_tr_b16 a[188:189], v224 offset:21696\n"
  "v_mfma_f32_16x16x32_bf16 v[188:191], a[172:175], a[28:31], v[188:191]\n"
  "ds_read_b64_tr_b16 a[190:191], v224 offset:25280\n"
  "v_mfma_f32_16x16x32_bf16 v[184:187], a[172:175], a[24:27], v[184:187]\n"
  "buffer_load_dwordx4 v246, s[36:39], s45 offen offset:2048 lds\n"
  "v_mfma_f32_16x16x32_bf16 v[184:187], a[168:171], a[8:11], v[184:187]\n"
  "v_mfma_f32_16x16x32_bf16 v[180:183], a[168:171], a[4:7], v[180:183]\n"
  "s_waitcnt vmcnt(27)\n"
  "s_barrier\n"
  "s_add_u32 s45, s45, 917504\n"
  "v_mfma_f32_16x16x32_bf16 v[180:183], a[172:175], a[20:23], v[180:183]\n"
  "ds_read_b64_tr_b16 a[160:161], v225 offset:28672\n"
  "v_mfma_f32_16x16x32_bf16 v[176:179], a[172:175], a[16:19], v[176:179]\n"
  "ds_read_b64_tr_b16 a[162:163], v225 offset:32256\n"
  "s_waitcnt lgkmcnt(6)\n"
  "v_mfma_f32_16x16x32_bf16 v[176:179], a[168:171], a[0:3], v[176:179]\n"
  "v_mfma_f32_16x16x32_bf16 v[192:195], a[176:179], a[0:3], v[192:195]\n"
  "ds_read_b64_tr_b16 a[164:165], v225 offset:35840\n"
  "v_mfma_f32_16x16x32_bf16 v[192:195], a[180:183], a[16:19], v[192:195]\n"
  "ds_read_b64_tr_b16 a[166:167], v225 offset:39424\n"
  "v_mfma_f32_16x16x32_bf16 v[196:199], a[180:183], a[20:23], v[196:199]\n"
  "s_waitcnt vmcnt(26)\n"
  "L_poll5_%=:\n"
  "v_cmp_ne_u64 vcc, s[10:11], v[238:239]\n"
  "s_cbranch_vccz L_ok5_%=\n"
  "s_sub_u32 s99, s99, 1\n"
  "s_cbranch_scc1 L_out5_%=\n"
  "buffer_load_dwordx2 v[238:239], v227, s[52:55], s93 offen sc1\n"
  "s_waitcnt vmcnt(0)\n"
  "s_branch L_poll5_%=\n"
  "L_out5_%=:\n"
  "s_mov_b32 s99, 0\n"
  "L_ok5_%=:\n"
  "v_mfma_f32_16x16x32_bf16 v[196:199], a[176:179], a[4:7], v[196:199]\n"
  "v_mfma_f32_16x16x32_bf16 v[200:203], a[176:179], a[8:11], v[200:203]\n"
  "ds_read_b64_tr_b16 a[168:169], v225 offset:43008\n"
  "v_mfma_f32_16x16x32_bf16 v[200:203], a[180:183], a[24:27], v[200:203]\n"
  "ds_read_b64_tr_b16 a[170:171], v225 offset:46592\n"
  "s_waitcnt lgkmcnt(6)\n"
  "v_mfma_f32_16x16x32_bf16 v[204:207], a[180:183], a[28:31], v[204:207]\n"
  "buffer_load_dwordx4 a[96:99], off, s[32:35], s44 offset:0 sc1\n"
  "v_mfma_f32_16x16x32_bf16 v[204:207], a[176:179], a[12:15], v[204:207]\n"
  "v_mfma_f32_16x16x32_bf16 v[220:223], a[184:187], a[12:15], v[220:223]\n"
  "ds_read_b64_tr_b16 a[172:173], v225 offset:50176\n"
  "v_mfma_f32_16x16x32_bf16 v[220:223], a[188:191], a[28:31], v[220:223]\n"
  "ds_read_b64_tr_b16 a[174:175], v225 offset:53760\n"
  "v_mfma_f32_16x16x32_bf16 v[216:219], a[188:191], a[24:27], v[216:219]\n"
  "buffer_load_dwordx4 a[100:103], off, s[32:35], s44 offset:1024 sc1\n"
  "v_mfma_f32_16x16x32_bf16 v[216:219], a[184:187], a[8:11], v[216:219]\n"
  "v_mfma_f32_16x16x32_bf16 v[212:215], a[184:187], a[4:7], v[212:215]\n"
  "ds_read_b64_tr_b16 a[176:177], v225 offset:28704\n"
  "v_mfma_f32_16x16x32_bf16 v[212:215], a[188:191], a[20:23], v[212:215]\n"
  "ds_read_b64_tr_b16 a[178:179], v225 offset:32288\n"
  "s_waitcnt vmcnt(24) lgkmcnt(6)\n"
  "v_mfma_f32_16x16x32_bf16 v[208:211], a[188:191], a[16:19], v[208:211]\n"
  "v_mfma_f32_16x16x32_bf16 v[208:211], a[184:187], a[0:3], v[208:211]\n"
  "v_mfma_f32_16x16x32_bf16 v[0:3], a[160:163], a[32:35], v[0:3]\n"
  "ds_read_b64_tr_b16 a[180:181], v225 offset:35872\n"
  "v_mfma_f32_16x16x32_bf16 v[0:3], a[164:167], a[48:51], v[0:3]\n"
  "ds_read_b64_tr_b16 a[182:183], v225 offset:39456\n"
  "v_mfma_f32_16x16x32_bf16 v[4:7], a[164:167], a[52:55], v[4:7]\n"
  "buffer_load_dwordx4 a[104:107], off, s[32:35], s44 offset:2048 sc1\n"
  "v_mfma_f32_16x16x32_bf16 v[4:7], a[160:163], a[36:39], v[4:7]\n"
  "v_mfma_f32_16x16x32_bf16 v[8:11], a[160:163], a[40:43], v[8:11]\n"
  "ds_read_b64_tr_b16 a[184:185], v225 offset:43040\n"
  "v_mfma_f32_16x16x32_bf16 v[8:11], a[164:167], a[56:59], v[8:11]\n"
  "ds_read_b64_tr_b16 a[186:187], v225 offset:46624\n"
  "s_waitcnt lgkmcnt(6)\n"
  "v_mfma_f32_16x16x32_bf16 v[12:15], a[164:167], a[60:63], v[12:15]\n"
  "buffer_load_dwordx4 a[108:111], off, s[32:35], s44 offset:3072 sc1\n"
  "v_mfma_f32_16x16x32_bf16 v[12:15], a[160:163], a[44:47], v[12:15]\n"
  "v_mfma_f32_16x16x32_bf16 v[28:31], a[168:171], a[44:47], v[28:31]\n"
  "ds_read_b64_tr_b16 a[188:189], v225 offset:50208\n"
  "s_add_u32 s44, s44, 4096\n"
  "v_mfma_f32_16x16x32_bf16 v[28:31], a[172:175], a[60:63], v[28:31]\n"
  "ds_read_b64_tr_b16 a[190:191], v225 offset:53792\n"
  "v_mfma_f32_16x16x32_bf16 v[24:27], a[172:175], a[56:59], v[24:27]\n"
  "v_mfma_f32_16x16x32_bf16 v[24:27], a[168:171], a[40:43], v[24:27]\n"
  "v_mfma_f32_16x16x32_bf16 v[20:23], a[168:171], a[36:39], v[20:23]\n"
  "ds_read_b64_tr_b16 a[160:161], v225 offset:28736\n"
  "v_mfma_f32_16x16x32_bf16 v[20:23], a[172:175], a[52:55], v[20:23]\n"
  "ds_read_b64_tr_b16 a[162:163], v225 offset:32320\n"
  "s_waitcnt lgkmcnt(6)\n"
  "v_mfma_f32_16x16x32_bf16 v[16:19], a[172:175], a[48:51], v[16:19]\n"
  "buffer_load_dwordx4 a[112:115], off, s[32:35], s44 offset:0 sc1\n"
  "v_mfma_f32_16x16x32_bf16 v[16:19], a[168:171], a[32:35], v[16:19]\n"
  "v_mfma_f32_16x16x32_bf16 v[32:35], a[176:179], a[32:35], v[32:35]\n"
  "ds_read_b64_tr_b16 a[164:165], v225 offset:35904\n"
  "v_mfma_f32_16x16x32_bf16 v[32:35], a[180:183], a[48:51], v[32:35]\n"
  "ds_read_b64_tr_b16 a[166:167], v225 offset:39488\n"
  "v_mfma_f32_16x16x32_bf16 v[36:39], a[180:183], a[52:55], v[36:39]\n"
  "buffer_load_dwordx4 a[116:119], off, s[32:35], s44 offset:1024 sc1\n"
  "v_mfma_f32_16x16x32_bf16 v[36:39], a[176:179], a[36:39], v[36:39]\n"
  "v_mfma_f32_16x16x32_bf16 v[40:43], a[176:179], a[40:43], v[40:43]\n"
  "ds_read_b64_tr_b16 a[168:169], v225 offset:43072\n"
  "v_mfma_f32_16x16x32_bf16 v[40:43], a[180:183], a[56:59], v[40:43]\n"
  "ds_read_b64_tr_b16 a[170:171], v225 offset:46656\n"
  "s_waitcnt lgkmcnt(6)\n"
  "v_mfma_f32_16x16x32_bf16 v[44:47], a[180:183], a[60:63], v[44:47]\n"
  "buffer_load_dwordx4 a[120:123], off, s[32:35], s44 offset:2048 sc1\n"
  "v_mfma_f32_16x16x32_bf16 v[44:47], a[176:179], a[44:47], v[44:47]\n"
  "v_mfma_f32_16x16x32_bf16 v[60:63], a[184:187], a[44:47], v[60:63]\n"
  "ds_read_b64_tr_b16 a[172:173], v225 offset:50240\n"
  "v_mfma_f32_16x16x32_bf16 v[60:63], a[188:191], a[60:63], v[60:63]\n"
  "ds_read_b64_tr_b16 a[174:175], v225 offset:53824\n"
  "v_mfma_f32_16x16x32_bf16 v[56:59], a[188:191], a[56:59], v[56:59]\n"
  "v_mfma_f32_16x16x32_bf16 v[56:59], a[184:187], a[40:43], v[56:59]\n"
  "v_mfma_f32_16x16x32_bf16 v[52:55], a[184:187], a[36:39], v[52:55]\n"
  "ds_read_b64_tr_b16 a[176:177], v225 offset:28768\n"
  "v_mfma_f32_16x16x32_bf16 v[52:55], a[188:191], a[52:55], v[52:55]\n"
  "ds_read_b64_tr_b16 a[178:179], v225 offset:32352\n"
  "s_waitcnt lgkmcnt(6)\n"
  "v_mfma_f32_16x16x32_bf16 v[48:51], a[188:191], a[48:51], v[48:51]\n"
  "buffer_load_dwordx4 a[124:127], off, s[32:35], s44 offset:3072 sc1\n"
  "v_mfma_f32_16x16x32_bf16 v[48:51], a[184:187], a[32:35], v[48:51]\n"
  "v_mfma_f32_16x16x32_bf16 v[64:67], a[160:163], a[32:35], v[64:67]\n"
  "ds_read_b64_tr_b16 a[180:181], v225 offset:35936\n"
  "s_add_u32 s44, s44, 4096\n"
  "v_mfma_f32_16x16x32_bf16 v[64:67], a[164:167], a[48:51], v[64:67]\n"
  "ds_read_b64_tr_b16 a[182:183], v225 offset:39520\n"
  "s_add_u32 m0, s46, 57344\n"
  "v_mfma_f32_16x16x32_bf16 v[68:71], a[164:167], a[52:55], v[68:71]\n"
  "v_mfma_f32_16x16x32_bf16 v[68:71], a[160:163], a[36:39], v[68:71]\n"
  "v_mfma_f32_16x16x32_bf16 v[72:75], a[160:163], a[40:43], v[72:75]\n"
  "ds_read_b64_tr_b16 a[184:185], v225 offset:43104\n"
  "v_mfma_f32_16x16x32_bf16 v[72:75], a[164:167], a[56:59], v[72:75]\n"
  "ds_read_b64_tr_b16 a[186:187], v225 offset:46688\n"
  "s_waitcnt lgkmcnt(6)\n"
  "v_mfma_f32_16x16x32_bf16 v[76:79], a[164:167], a[60:63], v[76:79]\n"
  "v_mfma_f32_16x16x32_bf16 v[76:79], a[160:163], a[44:47], v[76:79]\n"
  "v_mfma_f32_16x16x32_bf16 v[92:95], a[168:171], a[44:47], v[92:95]\n"
  "ds_read_b64_tr_b16 a[188:189], v225 offset:50272\n"
  "v_mfma_f32_16x16x32_bf16 v[92:95], a[172:175], a[60:63], v[92:95]\n"
  "ds_read_b64_tr_b16 a[190:191], v225 offset:53856\n"
  "v_mfma_f32_16x16x32_bf16 v[88:91], a[172:175], a[56:59], v[88:91]\n"
  "buffer_load_dwordx4 v240, s[36:39], s45 offen offset:0 lds\n"
  "v_mfma_f32_16x16x32_bf16 v[88:91], a[168:171], a[40:43], v[88:91]\n"
  "v_mfma_f32_16x16x32_bf16 v[84:87], a[168:171], a[36:39], v[84:87]\n"
  "ds_read_b64_tr_b16 a[160:161], v225 offset:28800\n"
  "v_mfma_f32_16x16x32_bf16 v[84:87], a[172:175], a[52:55], v[84:87]\n"
  "ds_read_b64_tr_b16 a[162:163], v225 offset:32384\n"
  "s_waitcnt lgkmcnt(6)\n"
  "v_mfma_f32_16x16x32_bf16 v[80:83], a[172:175], a[48:51], v[80:83]\n"
  "buffer_load_dwordx4 v241, s[36:39], s45 offen offset:1024 lds\n"
  "v_mfma_f32_16x16x32_bf16 v[80:83], a[168:171], a[32:35], v[80:83]\n"
  "v_mfma_f32_16x16x32_bf16 v[96:99], a[176:179], a[32:35], v[96:99]\n"
  "ds_read_b64_tr_b16 a[164:165], v225 offset:35968\n"
  "v_mfma_f32_16x16x32_bf16 v[96:99], a[180:183], a[48:51], v[96:99]\n"
  "ds_read_b64_tr_b16 a[166:167], v225 offset:39552\n"
  "v_mfma_f32_16x16x32_bf16 v[100:103], a[180:183], a[52:55], v[100:103]\n"
  "buffer_load_dwordx4 v242, s[36:39], s45 offen offset:2048 lds\n"
  "v_mfma_f32_16x16x32_bf16 v[100:103], a[176:179], a[36:39], v[100:103]\n"
  "v_mfma_f32_16x16x32_bf16 v[104:107], a[176:179], a[40:43], v[104:107]\n"
  "ds_read_b64_tr_b16 a[168:169], v225 offset:43136\n"
  "v_mfma_f32_16x16x32_bf16 v[104:107], a[180:183], a[56:59], v[104:107]\n"
  "ds_read_b64_tr_b16 a[170:171], v225 offset:46720\n"
  "s_waitcnt lgkmcnt(6)\n"
  "v_mfma_f32_16x16x32_bf16 v[108:111], a[180:183], a[60:63], v[108:111]\n"
  "v_mfma_f32_16x16x32_bf16 v[108:111], a[176:179], a[44:47], v[108:111]\n"
  "v_mfma_f32_16x16x32_bf16 v[124:127], a[184:187], a[44:47], v[124:127]\n"
  "ds_read_b64_tr_b16 a[172:173], v225 offset:50304\n"
  "v_mfma_f32_16x16x32_bf16 v[124:127], a[188:191], a[60:63], v[124:127]\n"
  "ds_read_b64_tr_b16 a[174:175], v225 offset:53888\n"
  "v_mfma_f32_16x16x32_bf16 v[120:123], a[188:191], a[56:59], v[120:123]\n"
  "buffer_load_dwordx4 v243, s[36:39], s45 offen offset:3072 lds\n"
  "v_mfma_f32_16x16x32_bf16 v[120:123], a[184:187], a[40:43], v[120:123]\n"
  "v_mfma_f32_16x16x32_bf16 v[116:119], a[184:187], a[36:39], v[116:119]\n"
  "ds_read_b64_tr_b16 a[176:177], v225 offset:28832\n"
  "s_add_u32 m0, s46, 61440\n"
  "v_mfma_f32_16x16x32_bf16 v[116:119], a[188:191], a[52:55], v[116:119]\n"
  "ds_read_b64_tr_b16 a[178:179], v225 offset:32416\n"
  "s_waitcnt lgkmcnt(6)\n"
  "v_mfma_f32_16x16x32_bf16 v[112:115], a[188:191], a[48:51], v[112:115]\n"
  "v_mfma_f32_16x16x32_bf16 v[112:115], a[184:187], a[32:35], v[112:115]\n"
  "v_mfma_f32_16x16x32_bf16 v[128:131], a[160:163], a[32:35], v[128:131]\n"
  "ds_read_b64_tr_b16 a[180:181], v225 offset:36000\n"
  "v_mfma_f32_16x16x32_bf16 v[128:131], a[164:167], a[48:51], v[128:131]\n"
  "ds_read_b64_tr_b16 a[182:183], v225 offset:39584\n"
  "v_mfma_f32_16x16x32_bf16 v[132:135], a[164:167], a[52:55], v[132:135]\n"
  "buffer_load_dwordx4 v244, s[36:39], s45 offen offset:0 lds\n"
  "v_mfma_f32_16x16x32_bf16 v[132:135], a[160:163], a[36:39], v[132:135]\n"
  "v_mfma_f32_16x16x32_bf16 v[136:139], a[160:163], a[40:43], v[136:139]\n"
  "ds_read_b64_tr_b16 a[184:185], v225 offset:43168\n"
  "v_mfma_f32_16x16x32_bf16 v[136:139], a[164:167], a[56:59], v[136:139]\n"
  "ds_read_b64_tr_b16 a[186:187], v225 offset:46752\n"
  "s_waitcnt lgkmcnt(6)\n"
  "v_mfma_f32_16x16x32_bf16 v[140:143], a[164:167], a[60:63], v[140:143]\n"
  "buffer_load_dwordx4 v245, s[36:39], s45 offen offset:1024 lds\n"
  "v_mfma_f32_16x16x32_bf16 v[140:143], a[160:163], a[44:47], v[140:143]\n"
  "v_mfma_f32_16x16x32_bf16 v[156:159], a[168:171], a[44:47], v[156:159]\n"
  "ds_read_b64_tr_b16 a[188:189], v225 offset:50336\n"
  "v_mfma_f32_16x16x32_bf16 v[156:159], a[172:175], a[60:63], v[156:159]\n"
  "ds_read_b64_tr_b16 a[190:191], v225 offset:53920\n"
  "v_mfma_f32_16x16x32_bf16 v[152:155], a[172:175], a[56:59], v[152:155]\n"
  "buffer_load_dwordx4 v246, s[36:39], s45 offen offset:2048 lds\n"
  "v_mfma_f32_16x16x32_bf16 v[152:155], a[168:171], a[40:43], v[152:155]\n"
  "v_mfma_f32_16x16x32_bf16 v[148:151], a[168:171], a[36:39], v[148:151]\n"
  "ds_read_b64_tr_b16 a[160:161], v225 offset:28864\n"
  "s_add_u32 s45, s45, 917504\n"
  "v_mfma_f32_16x16x32_bf16 v[148:151], a[172:175], a[52:55], v[148:151]\n"
  "ds_read_b64_tr_b16 a[162:163], v225 offset:32448\n"
  "s_waitcnt lgkmcnt(6)\n"
  "v_mfma_f32_16x16x32_bf16 v[144:147], a[172:175], a[48:51], v[144:147]\n"
  "v_mfma_f32_16x16x32_bf16 v[144:147], a[168:171], a[32:35], v[144:147]\n"
  "v_mfma_f32_16x16x32_bf16 v[160:163], a[176:179], a[32:35], v[160:163]\n"
  "ds_read_b64_tr_b16 a[164:165], v225 offset:36032\n"
  "v_mfma_f32_16x16x32_bf16 v[160:163], a[180:183], a[48:51], v[160:163]\n"
  "ds_read_b64_tr_b16 a[166:167], v225 offset:39616\n"
  "v_mfma_f32_16x16x32_bf16 v[164:167], a[180:183], a[52:55], v[164:167]\n"
  "v_mfma_f32_16x16x32_bf16 v[164:167], a[176:179], a[36:39], v[164:167]\n"
  "v_mfma_f32_16x16x32_bf16 v[168:171], a[176:179], a[40:43], v[168:171]\n"
  "ds_read_b64_tr_b16 a[168:169], v225 offset:43200\n"
  "v_mfma_f32_16x16x32_bf16 v[168:171], a[180:183], a[56:59], v[168:171]\n"
  "ds_read_b64_tr_b16 a[170:171], v225 offset:46784\n"
  "s_waitcnt lgkmcnt(6)\n"
  "v_mfma_f32_16x16x32_bf16 v[172:175], a[180:183], a[60:63], v[172:175]\n"
  "buffer_load_dwordx4 a[128:131], off, s[32:35], s44 offset:0 sc1\n"
  "v_mfma_f32_16x16x32_bf16 v[172:175], a[176:179], a[44:47], v[172:175]\n"
  "v_mfma_f32_16x16x32_bf16 v[188:191], a[184:187], a[44:47], v[188:191]\n"
  "ds_read_b64_tr_b16 a[172:173], v225 offset:50368\n"
  "v_mfma_f32_16x16x32_bf16 v[188:191], a[188:191], a[60:63], v[188:191]\n"
  "ds_read_b64_tr_b16 a[174:175], v225 offset:53952\n"
  "v_mfma_f32_16x16x32_bf16 v[184:187], a[188:191], a[56:59], v[184:187]\n"
  "buffer_load_dwordx4 a[132:135], off, s[32:35], s44 offset:1024 sc1\n"
  "v_mfma_f32_16x16x32_bf16 v[184:187], a[184:187], a[40:43], v[184:187]\n"
  "v_mfma_f32_16x16x32_bf16 v[180:183], a[184:187], a[36:39], v[180:183]\n"
  "s_waitcnt vmcnt(32)\n"
  "s_barrier\n"
  "v_mfma_f32_16x16x32_bf16 v[180:183], a[188:191], a[52:55], v[180:183]\n"
  "ds_read_b64_tr_b16 a[176:177], v226 offset:0\n"
  "v_mfma_f32_16x16x32_bf16 v[176:179], a[188:191], a[48:51], v[176:179]\n"
  "ds_read_b64_tr_b16 a[178:179], v226 offset:3584\n"
  "s_waitcnt lgkmcnt(6)\n"
  "v_mfma_f32_16x16x32_bf16 v[176:179], a[184:187], a[32:35], v[176:179]\n"
  "buffer_load_dwordx4 a[136:139], off, s[32:35], s44 offset:2048 sc1\n"
  "v_mfma_f32_16x16x32_bf16 v[192:195], a[160:163], a[32:35], v[192:195]\n"
  "ds_read_b64_tr_b16 a[180:181], v226 offset:7168\n"
  "v_mfma_f32_16x16x32_bf16 v[192:195], a[164:167], a[48:51], v[192:195]\n"
  "ds_read_b64_tr_b16 a[182:183], v226 offset:10752\n"
  "v_mfma_f32_16x16x32_bf16 v[196:199], a[164:167], a[52:55], v[196:199]\n"
  "buffer_load_dwordx4 a[140:143], off, s[32:35], s44 offset:3072 sc1\n"
  "v_mfma_f32_16x16x32_bf16 v[196:199], a[160:163], a[36:39], v[196:199]\n"
  "v_mfma_f32_16x16x32_bf16 v[200:203], a[160:163], a[40:43], v[200:203]\n"
  "ds_read_b64_tr_b16 a[184:185], v226 offset:14336\n"
  "s_add_u32 s44, s44, 4096\n"
  "v_mfma_f32_16x16x32_bf16 v[200:203], a[164:167], a[56:59], v[200:203]\n"
  "ds_read_b64_tr_b16 a[186:187], v226 offset:17920\n"
  "s_waitcnt lgkmcnt(6)\n"
  "v_mfma_f32_16x16x32_bf16 v[204:207], a[164:167], a[60:63], v[204:207]\n"
  "v_mfma_f32_16x16x32_bf16 v[204:207], a[160:163], a[44:47], v[204:207]\n"
  "v_mfma_f32_16x16x32_bf16 v[220:223], a[168:171], a[44:47], v[220:223]\n"
  "ds_read_b64_tr_b16 a[188:189], v226 offset:21504\n"
  "v_mfma_f32_16x16x32_bf16 v[220:223], a[172:175], a[60:63], v[220:223]\n"
  "ds_read_b64_tr_b16 a[190:191], v226 offset:25088\n"
  "v_mfma_f32_16x16x32_bf16 v[216:219], a[172:175], a[56:59], v[216:219]\n"
  "buffer_load_dwordx4 a[144:147], off, s[32:35], s44 offset:0 sc1\n"
  "v_mfma_f32_16x16x32_bf16 v[216:219], a[168:171], a[40:43], v[216:219]\n"
  "v_mfma_f32_16x16x32_bf16 v[212:215], a[168:171], a[36:39], v[212:215]\n"
  "ds_read_b64_tr_b16 a[160:161], v226 offset:32\n"
  "v_mfma_f32_16x16x32_bf16 v[212:215], a[172:175], a[52:55], v[212:215]\n"
  "ds_read_b64_tr_b16 a[162:163], v226 offset:3616\n"
  "s_waitcnt vmcnt(27) lgkmcnt(6)\n"
  "v_mfma_f32_16x16x32_bf16 v[208:211], a[172:175], a[48:51], v[208:211]\n"
  "v_mfma_f32_16x16x32_bf16 v[208:211], a[168:171], a[32:35], v[208:211]\n"
  "v_mfma_f32_16x16x32_bf16 v[0:3], a[176:179], a[64:67], v[0:3]\n"
  "ds_read_b64_tr_b16 a[164:165], v226 offset:7200\n"
  "v_mfma_f32_16x16x32_bf16 v[0:3], a[180:183], a[80:83], v[0:3]\n"
  "ds_read_b64_tr_b16 a[166:167], v226 offset:10784\n"
  "v_mfma_f32_16x16x32_bf16 v[4:7], a[180:183], a[84:87], v[4:7]\n"
  "buffer_load_dwordx4 a[148:151], off, s[32:35], s44 offset:1024 sc1\n"
  "v_mfma_f32_16x16x32_bf16 v[4:7], a[176:179], a[68:71], v[4:7]\n"
  "v_mfma_f32_16x16x32_bf16 v[8:11], a[176:179], a[72:75], v[8:11]\n"
  "ds_read_b64_tr_b16 a[168:169], v226 offset:14368\n"
  "v_mfma_f32_16x16x32_bf16 v[8:11], a[180:183], a[88:91], v[8:11]\n"
  "ds_read_b64_tr_b16 a[170:171], v226 offset:17952\n"
  "s_waitcnt lgkmcnt(6)\n"
  "v_mfma_f32_16x16x32_bf16 v[12:15], a[180:183], a[92:95], v[12:15]\n"
  "buffer_load_dwordx4 a[152:155], off, s[32:35], s44 offset:2048 sc1\n"
  "v_mfma_f32_16x16x32_bf16 v[12:15], a[176:179], a[76:79], v[12:15]\n"
  "v_mfma_f32_16x16x32_bf16 v[28:31], a[184:187], a[76:79], v[28:31]\n"
  "ds_read_b64_tr_b16 a[172:173], v226 offset:21536\n"
  "v_mfma_f32_16x16x32_bf16 v[28:31], a[188:191], a[92:95], v[28:31]\n"
  "ds_read_b64_tr_b16 a[174:175], v226 offset:25120\n"
  "v_mfma_f32_16x16x32_bf16 v[24:27], a[188:191], a[88:91], v[24:27]\n"
  "buffer_load_dwordx4 a[156:159], off, s[32:35], s44 offset:3072 sc1\n"
  "v_mfma_f32_16x16x32_bf16 v[24:27], a[184:187], a[72:75], v[24:27]\n"
  "v_mfma_f32_16x16x32_bf16 v[20:23], a[184:187], a[68:71], v[20:23]\n"
  "ds_read_b64_tr_b16 a[176:177], v226 offset:64\n"
  "s_add_u32 s44, s44, 4096\n"
  "v_mfma_f32_16x16x32_bf16 v[20:23], a[188:191], a[84:87], v[20:23]\n"
  "ds_read_b64_tr_b16 a[178:179], v226 offset:3648\n"
  "s_waitcnt lgkmcnt(6)\n"
  "v_mfma_f32_16x16x32_bf16 v[16:19], a[188:191], a[80:83], v[16:19]\n"
  "v_mfma_f32_16x16x32_bf16 v[16:19], a[184:187], a[64:67], v[16:19]\n"
  "v_mfma_f32_16x16x32_bf16 v[32:35], a[160:163], a[64:67], v[32:35]\n"
  "ds_read_b64_tr_b16 a[180:181], v226 offset:7232\n"
  "s_add_u32 m0, s46, 0\n"
  "v_mfma_f32_16x16x32_bf16 v[32:35], a[164:167], a[80:83], v[32:35]\n"
  "ds_read_b64_tr_b16 a[182:183], v226 offset:10816\n"
  "v_mfma_f32_16x16x32_bf16 v[36:39], a[164:167], a[84:87], v[36:39]\n"
  "v_mfma_f32_16x16x32_bf16 v[36:39], a[160:163], a[68:71], v[36:39]\n"
  "v_mfma_f32_16x16x32_bf16 v[40:43], a[160:163], a[72:75], v[40:43]\n"
  "ds_read_b64_tr_b16 a[184:185], v226 offset:14400\n"
  "v_mfma_f32_16x16x32_bf16 v[40:43], a[164:167], a[88:91], v[40:43]\n"
  "ds_read_b64_tr_b16 a[186:187], v226 offset:17984\n"
  "s_waitcnt lgkmcnt(6)\n"
  "v_mfma_f32_16x16x32_bf16 v[44:47], a[164:167], a[92:95], v[44:47]\n"
  "buffer_load_dwordx4 v240, s[36:39], s45 offen offset:0 lds\n"
  "v_mfma_f32_16x16x32_bf16 v[44:47], a[160:163], a[76:79], v[44:47]\n"
  "v_mfma_f32_16x16x32_bf16 v[60:63], a[168:171], a[76:79], v[60:63]\n"
  "ds_read_b64_tr_b16 a[188:189], v226 offset:21568\n"
  "v_mfma_f32_16x16x32_bf16 v[60:63], a[172:175], a[92:95], v[60:63]\n"
  "ds_read_b64_tr_b16 a[190:191], v226 offset:25152\n"
  "v_mfma_f32_16x16x32_bf16 v[56:59], a[172:175], a[88:91], v[56:59]\n"
  "v_mfma_f32_16x16x32_bf16 v[56:59], a[168:171], a[72:75], v[56:59]\n"
  "v_mfma_f32_16x16x32_bf16 v[52:55], a[168:171], a[68:71], v[52:55]\n"
  "ds_read_b64_tr_b16 a[160:161], v226 offset:96\n"
  "v_mfma_f32_16x16x32_bf16 v[52:55], a[172:175], a[84:87], v[52:55]\n"
  "ds_read_b64_tr_b16 a[162:163], v226 offset:3680\n"
  "s_waitcnt lgkmcnt(6)\n"
  "v_mfma_f32_16x16x32_bf16 v[48:51], a[172:175], a[80:83], v[48:51]\n"
  "buffer_load_dwordx4 v241, s[36:39], s45 offen offset:1024 lds\n"
  "v_mfma_f32_16x16x32_bf16 v[48:51], a[168:171], a[64:67], v[48:51]\n"
  "v_mfma_f32_16x16x32_bf16 v[64:67], a[176:179], a[64:67], v[64:67]\n"
  "ds_read_b64_tr_b16 a[164:165], v226 offset:7264\n"
  "v_mfma_f32_16x16x32_bf16 v[64:67], a[180:183], a[80:83], v[64:67]\n"
  "ds_read_b64_tr_b16 a[166:167], v226 offset:10848\n"
  "v_mfma_f32_16x16x32_bf16 v[68:71], a[180:183], a[84:87], v[68:71]\n"
  "buffer_load_dwordx4 v242, s[36:39], s45 offen offset:2048 lds\n"
  "v_mfma_f32_16x16x32_bf16 v[68:71], a[176:179], a[68:71], v[68:71]\n"
  "v_mfma_f32_16x16x32_bf16 v[72:75], a[176:179], a[72:75], v[72:75]\n"
  "ds_read_b64_tr_b16 a[168:169], v226 offset:14432\n"
  "v_mfma_f32_16x16x32_bf16 v[72:75], a[180:183], a[88:91], v[72:75]\n"
  "ds_read_b64_tr_b16 a[170:171], v226 offset:18016\n"
  "s_waitcnt lgkmcnt(6)\n"
  "v_mfma_f32_16x16x32_bf16 v[76:79], a[180:183], a[92:95], v[76:79]\n"
  "buffer_load_dwordx4 v243, s[36:39], s45 offen offset:3072 lds\n"
  "v_mfma_f32_16x16x32_bf16 v[76:79], a[176:179], a[76:79], v[76:79]\n"
  "v_mfma_f32_16x16x32_bf16 v[92:95], a[184:187], a[76:79], v[92:95]\n"
  "ds_read_b64_tr_b16 a[172:173], v226 offset:21600\n"
  "s_add_u32 m0, s46, 4096\n"
  "v_mfma_f32_16x16x32_bf16 v[92:95], a[188:191], a[92:95], v[92:95]\n"
  "ds_read_b64_tr_b16 a[174:175], v226 offset:25184\n"
  "v_mfma_f32_16x16x32_bf16 v[88:91], a[188:191], a[88:91], v[88:91]\n"
  "v_mfma_f32_16x16x32_bf16 v[88:91], a[184:187], a[72:75], v[88:91]\n"
  "v_mfma_f32_16x16x32_bf16 v[84:87], a[184:187], a[68:71], v[84:87]\n"
  "ds_read_b64_tr_b16 a[176:177], v226 offset:128\n"
  "v_mfma_f32_16x16x32_bf16 v[84:87], a[188:191], a[84:87], v[84:87]\n"
  "ds_read_b64_tr_b16 a[178:179], v226 offset:3712\n"
  "s_waitcnt lgkmcnt(6)\n"
  "v_mfma_f32_16x16x32_bf16 v[80:83], a[188:191], a[80:83], v[80:83]\n"
  "buffer_load_dwordx4 v244, s[36:39], s45 offen offset:0 lds\n"
  "v_mfma_f32_16x16x32_bf16 v[80:83], a[184:187], a[64:67], v[80:83]\n"
  "v_mfma_f32_16x16x32_bf16 v[96:99], a[160:163], a[64:67], v[96:99]\n"
  "ds_read_b64_tr_b16 a[180:181], v226 offset:7296\n"
  "v_mfma_f32_16x16x32_bf16 v[96:99], a[164:167], a[80:83], v[96:99]\n"
  "ds_read_b64_tr_b16 a[182:183], v226 offset:10880\n"
  "v_mfma_f32_16x16x32_bf16 v[100:103], a[164:167], a[84:87], v[100:103]\n"
  "buffer_load_dwordx4 v245, s[36:39], s45 offen offset:1024 lds\n"
  "v_mfma_f32_16x16x32_bf16 v[100:103], a[160:163], a[68:71], v[100:103]\n"
  "v_mfma_f32_16x16x32_bf16 v[104:107], a[160:163], a[72:75], v[104:107]\n"
  "ds_read_b64_tr_b16 a[184:185], v226 offset:14464\n"
  "v_mfma_f32_16x16x32_bf16 v[104:107], a[164:167], a[88:91], v[104:107]\n"
  "ds_read_b64_tr_b16 a[186:187], v226 offset:18048\n"
  "s_waitcnt lgkmcnt(6)\n"
  "v_mfma_f32_16x16x32_bf16 v[108:111], a[164:167], a[92:95], v[108:111]\n"
  "v_mfma_f32_16x16x32_bf16 v[108:111], a[160:163], a[76:79], v[108:111]\n"
  "v_mfma_f32_16x16x32_bf16 v[124:127], a[168:171], a[76:79], v[124:127]\n"
  "ds_read_b64_tr_b16 a[188:189], v226 offset:21632\n"
  "v_mfma_f32_16x16x32_bf16 v[124:127], a[172:175], a[92:95], v[124:127]\n"
  "ds_read_b64_tr_b16 a[190:191], v226 offset:25216\n"
  "v_mfma_f32_16x16x32_bf16 v[120:123], a[172:175], a[88:91], v[120:123]\n"
  "buffer_load_dwordx4 v246, s[36:39], s45 offen offset:2048 lds\n"
  "v_mfma_f32_16x16x32_bf16 v[120:123], a[168:171], a[72:75], v[120:123]\n"
  "v_mfma_f32_16x16x32_bf16 v[116:119], a[168:171], a[68:71], v[116:119]\n"
  "ds_read_b64_tr_b16 a[160:161], v226 offset:160\n"
  "s_add_u32 s45, s45, 917504\n"
  "v_mfma_f32_16x16x32_bf16 v[116:119], a[172:175], a[84:87], v[116:119]\n"
  "ds_read_b64_tr_b16 a[162:163], v226 offset:3744\n"
  "s_waitcnt lgkmcnt(6)\n"
  "v_mfma_f32_16x16x32_bf16 v[112:115], a[172:175], a[80:83], v[112:115]\n"
  "v_mfma_f32_16x16x32_bf16 v[112:115], a[168:171], a[64:67], v[112:115]\n"
  "v_mfma_f32_16x16x32_bf16 v[128:131], a[176:179], a[64:67], v[128:131]\n"
  "ds_read_b64_tr_b16 a[164:165], v226 offset:7328\n"
  "v_mfma_f32_16x16x32_bf16 v[128:131], a[180:183], a[80:83], v[128:131]\n"
  "ds_read_b64_tr_b16 a[166:167], v226 offset:10912\n"
  "v_mfma_f32_16x16x32_bf16 v[132:135], a[180:183], a[84:87], v[132:135]\n"
  "buffer_load_dwordx4 a[0:3], off, s[32:35], s44 offset:0 sc1\n"
  "v_mfma_f32_16x16x32_bf16 v[132:135], a[176:179], a[68:71], v[132:135]\n"
  "v_mfma_f32_16x16x32_bf16 v[136:139], a[176:179], a[72:75], v[136:139]\n"
  "ds_read_b64_tr_b16 a[168:169], v226 offset:14496\n"
  "v_mfma_f32_16x16x32_bf16 v[136:139], a[180:183], a[88:91], v[136:139]\n"
  "ds_read_b64_tr_b16 a[170:171], v226 offset:18080\n"
  "s_waitcnt lgkmcnt(6)\n"
  "v_mfma_f32_16x16x32_bf16 v[140:143], a[180:183], a[92:95], v[140:143]\n"
  "buffer_load_dwordx4 a[4:7], off, s[32:35], s44 offset:1024 sc1\n"
  "v_mfma_f32_16x16x32_bf16 v[140:143], a[176:179], a[76:79], v[140:143]\n"
  "v_mfma_f32_16x16x32_bf16 v[156:159], a[184:187], a[76:79], v[156:159]\n"
  "ds_read_b64_tr_b16 a[172:173], v226 offset:21664\n"
  "v_mfma_f32_16x16x32_bf16 v[156:159], a[188:191], a[92:95], v[156:159]\n"
  "ds_read_b64_tr_b16 a[174:175], v226 offset:25248\n"
  "v_mfma_f32_16x16x32_bf16 v[152:155], a[188:191], a[88:91], v[152:155]\n"
  "buffer_load_dwordx4 a[8:11], off, s[32:35], s44 offset:2048 sc1\n"
  "v_mfma_f32_16x16x32_bf16 v[152:155], a[184:187], a[72:75], v[152:155]\n"
  "v_mfma_f32_16x16x32_bf16 v[148:151], a[184:187], a[68:71], v[148:151]\n"
  "ds_read_b64_tr_b16 a[176:177], v226 offset:192\n"
  "v_mfma_f32_16x16x32_bf16 v[148:151], a[188:191], a[84:87], v[148:151]\n"
  "ds_read_b64_tr_b16 a[178:179], v226 offset:3776\n"
  "s_waitcnt lgkmcnt(6)\n"
  "v_mfma_f32_16x16x32_bf16 v[144:147], a[188:191], a[80:83], v[144:147]\n"
  "buffer_load_dwordx4 a[12:15], off, s[32:35], s44 offset:3072 sc1\n"
  "v_mfma_f32_16x16x32_bf16 v[144:147], a[184:187], a[64:67], v[144:147]\n"
  "v_mfma_f32_16x16x32_bf16 v[160:163], a[160:163], a[64:67], v[160:163]\n"
  "ds_read_b64_tr_b16 a[180:181], v226 offset:7360\n"
  "s_add_u32 s44, s44, 4096\n"
  "v_mfma_f32_16x16x32_bf16 v[160:163], a[164:167], a[80:83], v[160:163]\n"
  "ds_read_b64_tr_b16 a[182:183], v226 offset:10944\n"
  "v_mfma_f32_16x16x32_bf16 v[164:167], a[164:167], a[84:87], v[164:167]\n"
  "v_mfma_f32_16x16x32_bf16 v[164:167], a[160:163], a[68:71], v[164:167]\n"
  "v_mfma_f32_16x16x32_bf16 v[168:171], a[160:163], a[72:75], v[168:171]\n"
  "ds_read_b64_tr_b16 a[184:185], v226 offset:14528\n"
  "v_mfma_f32_16x16x32_bf16 v[168:171], a[164:167], a[88:91], v[168:171]\n"
  "ds_read_b64_tr_b16 a[186:187], v226 offset:18112\n"
  "s_waitcnt lgkmcnt(6)\n"
  "v_mfma_f32_16x16x32_bf16 v[172:175], a[164:167], a[92:95], v[172:175]\n"
  "v_mfma_f32_16x16x32_bf16 v[172:175], a[160:163], a[76:79], v[172:175]\n"
  "v_mfma_f32_16x16x32_bf16 v[188:191], a[168:171], a[76:79], v[188:191]\n"
  "ds_read_b64_tr_b16 a[188:189], v226 offset:21696\n"
  "v_mfma_f32_16x16x32_bf16 v[188:191], a[172:175], a[92:95], v[188:191]\n"
  "ds_read_b64_tr_b16 a[190:191], v226 offset:25280\n"
  "v_mfma_f32_16x16x32_bf16 v[184:187], a[172:175], a[88:91], v[184:187]\n"
  "buffer_load_dwordx4 a[16:19], off, s[32:35], s44 offset:0 sc1\n"
  "v_mfma_f32_16x16x32_bf16 v[184:187], a[168:171], a[72:75], v[184:187]\n"
  "v_mfma_f32_16x16x32_bf16 v[180:183], a[168:171], a[68:71], v[180:183]\n"
  "s_waitcnt vmcnt(35)\n"
  "s_barrier\n"
  "v_mfma_f32_16x16x32_bf16 v[180:183], a[172:175], a[84:87], v[180:183]\n"
  "ds_read_b64_tr_b16 a[160:161], v224 offset:28672\n"
  "v_mfma_f32_16x16x32_bf16 v[176:179], a[172:175], a[80:83], v[176:179]\n"
  "ds_read_b64_tr_b16 a[162:163], v224 offset:32256\n"
  "s_waitcnt lgkmcnt(6)\n"
  "v_mfma_f32_16x16x32_bf16 v[176:179], a[168:171], a[64:67], v[176:179]\n"
  "buffer_load_dwordx4 a[20:23], off, s[32:35], s44 offset:1024 sc1\n"
  "v_mfma_f32_16x16x32_bf16 v[192:195], a[176:179], a[64:67], v[192:195]\n"
  "ds_read_b64_tr_b16 a[164:165], v224 offset:35840\n"
  "v_mfma_f32_16x16x32_bf16 v[192:195], a[180:183], a[80:83], v[192:195]\n"
  "ds_read_b64_tr_b16 a[166:167], v224 offset:39424\n"
  "v_mfma_f32_16x16x32_bf16 v[196:199], a[180:183], a[84:87], v[196:199]\n"
  "buffer_load_dwordx4 a[24:27], off, s[32:35], s44 offset:2048 sc1\n"
  "v_mfma_f32_16x16x32_bf16 v[196:199], a[176:179], a[68:71], v[196:199]\n"
  "v_mfma_f32_16x16x32_bf16 v[200:203], a[176:179], a[72:75], v[200:203]\n"
  "ds_read_b64_tr_b16 a[168:169], v224 offset:43008\n"
  "v_mfma_f32_16x16x32_bf16 v[200:203], a[180:183], a[88:91], v[200:203]\n"
  "ds_read_b64_tr_b16 a[170:171], v224 offset:46592\n"
  "s_waitcnt lgkmcnt(6)\n"
  "v_mfma_f32_16x16x32_bf16 v[204:207], a[180:183], a[92:95], v[204:207]\n"
  "buffer_load_dwordx4 a[28:31], off, s[32:35], s44 offset:3072 sc1\n"
  "v_mfma_f32_16x16x32_bf16 v[204:207], a[176:179], a[76:79], v[204:207]\n"
  "v_mfma_f32_16x16x32_bf16 v[220:223], a[184:187], a[76:79], v[220:223]\n"
  "ds_read_b64_tr_b16 a[172:173], v224 offset:50176\n"
  "s_add_u32 s44, s44, 4096\n"
  "v_mfma_f32_16x16x32_bf16 v[220:223], a[188:191], a[92:95], v[220:223]\n"
  "ds_read_b64_tr_b16 a[174:175], v224 offset:53760\n"
  "v_mfma_f32_16x16x32_bf16 v[216:219], a[188:191], a[88:91], v[216:219]\n"
  "v_mfma_f32_16x16x32_bf16 v[216:219], a[184:187], a[72:75], v[216:219]\n"
  "v_mfma_f32_16x16x32_bf16 v[212:215], a[184:187], a[68:71], v[212:215]\n"
  "ds_read_b64_tr_b16 a[176:177], v224 offset:28704\n"
  "v_mfma_f32_16x16x32_bf16 v[212:215], a[188:191], a[84:87], v[212:215]\n"
  "ds_read_b64_tr_b16 a[178:179], v224 offset:32288\n"
  "s_waitcnt vmcnt(30) lgkmcnt(6)\n"
  "v_mfma_f32_16x16x32_bf16 v[208:211], a[188:191], a[80:83], v[208:211]\n"
  "v_mfma_f32_16x16x32_bf16 v[208:211], a[184:187], a[64:67], v[208:211]\n"
  "v_mfma_f32_16x16x32_bf16 v[0:3], a[160:163], a[96:99], v[0:3]\n"
  "ds_read_b64_tr_b16 a[180:181], v224 offset:35872\n"
  "s_add_u32 m0, s46, 86016\n"
  "v_mfma_f32_16x16x32_bf16 v[0:3], a[164:167], a[112:115], v[0:3]\n"
  "ds_read_b64_tr_b16 a[182:183], v224 offset:39456\n"
  "v_mfma_f32_16x16x32_bf16 v[4:7], a[164:167], a[116:119], v[4:7]\n"
  "v_mfma_f32_16x16x32_bf16 v[4:7], a[160:163], a[100:103], v[4:7]\n"
  "v_mfma_f32_16x16x32_bf16 v[8:11], a[160:163], a[104:107], v[8:11]\n"
  "ds_read_b64_tr_b16 a[184:185], v224 offset:43040\n"
  "v_mfma_f32_16x16x32_bf16 v[8:11], a[164:167], a[120:123], v[8:11]\n"
  "ds_read_b64_tr_b16 a[186:187], v224 offset:46624\n"
  "s_waitcnt lgkmcnt(6)\n"
  "v_mfma_f32_16x16x32_bf16 v[12:15], a[164:167], a[124:127], v[12:15]\n"
  "buffer_load_dwordx4 v240, s[36:39], s45 offen offset:0 lds\n"
  "v_mfma_f32_16x16x32_bf16 v[12:15], a[160:163], a[108:111], v[12:15]\n"
  "v_mfma_f32_16x16x32_bf16 v[28:31], a[168:171], a[108:111], v[28:31]\n"
  "ds_read_b64_tr_b16 a[188:189], v224 offset:50208\n"
  "v_mfma_f32_16x16x32_bf16 v[28:31], a[172:175], a[124:127], v[28:31]\n"
  "ds_read_b64_tr_b16 a[190:191], v224 offset:53792\n"
  "v_mfma_f32_16x16x32_bf16 v[24:27], a[172:175], a[120:123], v[24:27]\n"
  "buffer_load_dwordx4 v241, s[36:39], s45 offen offset:1024 lds\n"
  "v_mfma_f32_16x16x32_bf16 v[24:27], a[168:171], a[104:107], v[24:27]\n"
  "v_mfma_f32_16x16x32_bf16 v[20:23], a[168:171], a[100:103], v[20:23]\n"
  "ds_read_b64_tr_b16 a[160:161], v224 offset:28736\n"
  "v_mfma_f32_16x16x32_bf16 v[20:23], a[172:175], a[116:119], v[20:23]\n"
  "ds_read_b64_tr_b16 a[162:163], v224 offset:32320\n"
  "s_waitcnt lgkmcnt(6)\n"
  "v_mfma_f32_16x16x32_bf16 v[16:19], a[172:175], a[112:115], v[16:19]\n"
  "buffer_load_dwordx4 v242, s[36:39], s45 offen offset:2048 lds\n"
  "v_mfma_f32_16x16x32_bf16 v[16:19], a[168:171], a[96:99], v[16:19]\n"
  "v_mfma_f32_16x16x32_bf16 v[32:35], a[176:179], a[96:99], v[32:35]\n"
  "ds_read_b64_tr_b16 a[164:165], v224 offset:35904\n"
  "v_mfma_f32_16x16x32_bf16 v[32:35], a[180:183], a[112:115], v[32:35]\n"
  "ds_read_b64_tr_b16 a[166:167], v224 offset:39488\n"
  "v_mfma_f32_16x16x32_bf16 v[36:39], a[180:183], a[116:119], v[36:39]\n"
  "buffer_load_dwordx4 v243, s[36:39], s45 offen offset:3072 lds\n"
  "v_mfma_f32_16x16x32_bf16 v[36:39], a[176:179], a[100:103], v[36:39]\n"
  "v_mfma_f32_16x16x32_bf16 v[40:43], a[176:179], a[104:107], v[40:43]\n"
  "ds_read_b64_tr_b16 a[168:169], v224 offset:43072\n"
  "s_add_u32 m0, s46, 90112\n"
  "v_mfma_f32_16x16x32_bf16 v[40:43], a[180:183], a[120:123], v[40:43]\n"
  "ds_read_b64_tr_b16 a[170:171], v224 offset:46656\n"
  "s_waitcnt lgkmcnt(6)\n"
  "v_mfma_f32_16x16x32_bf16 v[44:47], a[180:183], a[124:127], v[44:47]\n"
  "v_mfma_f32_16x16x32_bf16 v[44:47], a[176:179], a[108:111], v[44:47]\n"
  "v_mfma_f32_16x16x32_bf16 v[60:63], a[184:187], a[108:111], v[60:63]\n"
  "ds_read_b64_tr_b16 a[172:173], v224 offset:50240\n"
  "v_mfma_f32_16x16x32_bf16 v[60:63], a[188:191], a[124:127], v[60:63]\n"
  "ds_read_b64_tr_b16 a[174:175], v224 offset:53824\n"
  "v_mfma_f32_16x16x32_bf16 v[56:59], a[188:191], a[120:123], v[56:59]\n"
  "v_mfma_f32_16x16x32_bf16 v[56:59], a[184:187], a[104:107], v[56:59]\n"
  "v_mfma_f32_16x16x32_bf16 v[52:55], a[184:187], a[100:103], v[52:55]\n"
  "ds_read_b64_tr_b16 a[176:177], v224 offset:28768\n"
  "v_mfma_f32_16x16x32_bf16 v[52:55], a[188:191], a[116:119], v[52:55]\n"
  "ds_read_b64_tr_b16 a[178:179], v224 offset:32352\n"
  "s_waitcnt lgkmcnt(6)\n"
  "v_mfma_f32_16x16x32_bf16 v[48:51], a[188:191], a[112:115], v[48:51]\n"
  "buffer_load_dwordx4 v244, s[36:39], s45 offen offset:0 lds\n"
  "v_mfma_f32_16x16x32_bf16 v[48:51], a[184:187], a[96:99], v[48:51]\n"
  "v_mfma_f32_16x16x32_bf16 v[64:67], a[160:163], a[96:99], v[64:67]\n"
  "ds_read_b64_tr_b16 a[180:181], v224 offset:35936\n"
  "v_mfma_f32_16x16x32_bf16 v[64:67], a[164:167], a[112:115], v[64:67]\n"
  "ds_read_b64_tr_b16 a[182:183], v224 offset:39520\n"
  "v_mfma_f32_16x16x32_bf16 v[68:71], a[164:167], a[116:119], v[68:71]\n"
  "buffer_load_dwordx4 v245, s[36:39], s45 offen offset:1024 lds\n"
  "v_mfma_f32_16x16x32_bf16 v[68:71], a[160:163], a[100:103], v[68:71]\n"
  "v_mfma_f32_16x16x32_bf16 v[72:75], a[160:163], a[104:107], v[72:75]\n"
  "ds_read_b64_tr_b16 a[184:185], v224 offset:43104\n"
  "v_mfma_f32_16x16x32_bf16 v[72:75], a[164:167], a[120:123], v[72:75]\n"
  "ds_read_b64_tr_b16 a[186:187], v224 offset:46688\n"
  "s_waitcnt lgkmcnt(6)\n"
  "v_mfma_f32_16x16x32_bf16 v[76:79], a[164:167], a[124:127], v[76:79]\n"
  "buffer_load_dwordx4 v246, s[36:39], s45 offen offset:2048 lds\n"
  "v_mfma_f32_16x16x32_bf16 v[76:79], a[160:163], a[108:111], v[76:79]\n"
  "v_mfma_f32_16x16x32_bf16 v[92:95], a[168:171], a[108:111], v[92:95]\n"
  "ds_read_b64_tr_b16 a[188:189], v224 offset:50272\n"
  "s_add_u32 s45, s45, 917504\n"
  "v_mfma_f32_16x16x32_bf16 v[92:95], a[172:175], a[124:127], v[92:95]\n"
  "ds_read_b64_tr_b16 a[190:191], v224 offset:53856\n"
  "v_mfma_f32_16x16x32_bf16 v[88:91], a[172:175], a[120:123], v[88:91]\n"
  "v_mfma_f32_16x16x32_bf16 v[88:91], a[168:171], a[104:107], v[88:91]\n"
  "v_mfma_f32_16x16x32_bf16 v[84:87], a[168:171], a[100:103], v[84:87]\n"
  "ds_read_b64_tr_b16 a[160:161], v224 offset:28800\n"
  "v_mfma_f32_16x16x32_bf16 v[84:87], a[172:175], a[116:119], v[84:87]\n"
  "ds_read_b64_tr_b16 a[162:163], v224 offset:32384\n"
  "s_waitcnt lgkmcnt(6)\n"
  "v_mfma_f32_16x16x32_bf16 v[80:83], a[172:175], a[112:115], v[80:83]\n"
  "buffer_load_dwordx4 a[32:35], off, s[32:35], s44 offset:0 sc1\n"
  "v_mfma_f32_16x16x32_bf16 v[80:83], a[168:171], a[96:99], v[80:83]\n"
  "v_mfma_f32_16x16x32_bf16 v[96:99], a[176:179], a[96:99], v[96:99]\n"
  "ds_read_b64_tr_b16 a[164:165], v224 offset:35968\n"
  "v_mfma_f32_16x16x32_bf16 v[96:99], a[180:183], a[112:115], v[96:99]\n"
  "ds_read_b64_tr_b16 a[166:167], v224 offset:39552\n"
  "v_mfma_f32_16x16x32_bf16 v[100:103], a[180:183], a[116:119], v[100:103]\n"
  "buffer_load_dwordx4 a[36:39], off, s[32:35], s44 offset:1024 sc1\n"
  "v_mfma_f32_16x16x32_bf16 v[100:103], a[176:179], a[100:103], v[100:103]\n"
  "v_mfma_f32_16x16x32_bf16 v[104:107], a[176:179], a[104:107], v[104:107]\n"
  "ds_read_b64_tr_b16 a[168:169], v224 offset:43136\n"
  "v_mfma_f32_16x16x32_bf16 v[104:107], a[180:183], a[120:123], v[104:107]\n"
  "ds_read_b64_tr_b16 a[170:171], v224 offset:46720\n"
  "s_waitcnt lgkmcnt(6)\n"
  "v_mfma_f32_16x16x32_bf16 v[108:111], a[180:183], a[124:127], v[108:111]\n"
  "v_mfma_f32_16x16x32_bf16 v[108:111], a[176:179], a[108:111], v[108:111]\n"
  "v_mfma_f32_16x16x32_bf16 v[124:127], a[184:187], a[108:111], v[124:127]\n"
  "ds_read_b64_tr_b16 a[172:173], v224 offset:50304\n"
  "v_mfma_f32_16x16x32_bf16 v[124:127], a[188:191], a[124:127], v[124:127]\n"
  "ds_read_b64_tr_b16 a[174:175], v224 offset:53888\n"
  "v_mfma_f32_16x16x32_bf16 v[120:123], a[188:191], a[120:123], v[120:123]\n"
  "buffer_load_dwordx4 a[40:43], off, s[32:35], s44 offset:2048 sc1\n"
  "v_mfma_f32_16x16x32_bf16 v[120:123], a[184:187], a[104:107], v[120:123]\n"
  "v_mfma_f32_16x16x32_bf16 v[116:119], a[184:187], a[100:103], v[116:119]\n"
  "ds_read_b64_tr_b16 a[176:177], v224 offset:28832\n"
  "v_mfma_f32_16x16x32_bf16 v[116:119], a[188:191], a[116:119], v[116:119]\n"
  "ds_read_b64_tr_b16 a[178:179], v224 offset:32416\n"
  "s_waitcnt lgkmcnt(6)\n"
  "v_mfma_f32_16x16x32_bf16 v[112:115], a[188:191], a[112:115], v[112:115]\n"
  "buffer_load_dwordx4 a[44:47], off, s[32:35], s44 offset:3072 sc1\n"
  "v_mfma_f32_16x16x32_bf16 v[112:115], a[184:187], a[96:99], v[112:115]\n"
  "v_mfma_f32_16x16x32_bf16 v[128:131], a[160:163], a[96:99], v[128:131]\n"
  "ds_read_b64_tr_b16 a[180:181], v224 offset:36000\n"
  "s_add_u32 s44, s44, 4096\n"
  "v_mfma_f32_16x16x32_bf16 v[128:131], a[164:167], a[112:115], v[128:131]\n"
  "ds_read_b64_tr_b16 a[182:183], v224 offset:39584\n"
  "v_mfma_f32_16x16x32_bf16 v[132:135], a[164:167], a[116:119], v[132:135]\n"
  "v_mfma_f32_16x16x32_bf16 v[132:135], a[160:163], a[100:103], v[132:135]\n"
  "v_mfma_f32_16x16x32_bf16 v[136:139], a[160:163], a[104:107], v[136:139]\n"
  "ds_read_b64_tr_b16 a[184:185], v224 offset:43168\n"
  "v_mfma_f32_16x16x32_bf16 v[136:139], a[164:167], a[120:123], v[136:139]\n"
  "ds_read_b64_tr_b16 a[186:187], v224 offset:46752\n"
  "s_waitcnt lgkmcnt(6)\n"
  "v_mfma_f32_16x16x32_bf16 v[140:143], a[164:167], a[124:127], v[140:143]\n"
  "buffer_load_dwordx4 a[48:51], off, s[32:35], s44 offset:0 sc1\n"
  "v_mfma_f32_16x16x32_bf16 v[140:143], a[160:163], a[108:111], v[140:143]\n"
  "v_mfma_f32_16x16x32_bf16 v[156:159], a[168:171], a[108:111], v[156:159]\n"
  "ds_read_b64_tr_b16 a[188:189], v224 offset:50336\n"
  "v_mfma_f32_16x16x32_bf16 v[156:159], a[172:175], a[124:127], v[156:159]\n"
  "ds_read_b64_tr_b16 a[190:191], v224 offset:53920\n"
  "v_mfma_f32_16x16x32_bf16 v[152:155], a[172:175], a[120:123], v[152:155]\n"
  "v_mfma_f32_16x16x32_bf16 v[152:155], a[168:171], a[104:107], v[152:155]\n"
  "v_mfma_f32_16x16x32_bf16 v[148:151], a[168:171], a[100:103], v[148:151]\n"
  "ds_read_b64_tr_b16 a[160:161], v224 offset:28864\n"
  "v_mfma_f32_16x16x32_bf16 v[148:151], a[172:175], a[116:119], v[148:151]\n"
  "ds_read_b64_tr_b16 a[162:163], v224 offset:32448\n"
  "s_waitcnt lgkmcnt(6)\n"
  "v_mfma_f32_16x16x32_bf16 v[144:147], a[172:175], a[112:115], v[144:147]\n"
  "buffer_load_dwordx4 a[52:55], off, s[32:35], s44 offset:1024 sc1\n"
  "v_mfma_f32_16x16x32_bf16 v[144:147], a[168:171], a[96:99], v[144:147]\n"
  "v_mfma_f32_16x16x32_bf16 v[160:163], a[176:179], a[96:99], v[160:163]\n"
  "ds_read_b64_tr_b16 a[164:165], v224 offset:36032\n"
  "v_mfma_f32_16x16x32_bf16 v[160:163], a[180:183], a[112:115], v[160:163]\n"
  "ds_read_b64_tr_b16 a[166:167], v224 offset:39616\n"
  "v_mfma_f32_16x16x32_bf16 v[164:167], a[180:183], a[116:119], v[164:167]\n"
  "buffer_load_dwordx4 a[56:59], off, s[32:35], s44 offset:2048 sc1\n"
  "v_mfma_f32_16x16x32_bf16 v[164:167], a[176:179], a[100:103], v[164:167]\n"
  "v_mfma_f32_16x16x32_bf16 v[168:171], a[176:179], a[104:107], v[168:171]\n"
  "ds_read_b64_tr_b16 a[168:169], v224 offset:43200\n"
  "v_mfma_f32_16x16x32_bf16 v[168:171], a[180:183], a[120:123], v[168:171]\n"
  "ds_read_b64_tr_b16 a[170:171], v224 offset:46784\n"
  "s_waitcnt lgkmcnt(6)\n"
  "v_mfma_f32_16x16x32_bf16 v[172:175], a[180:183], a[124:127], v[172:175]\n"
  "buffer_load_dwordx4 a[60:63], off, s[32:35], s44 offset:3072 sc1\n"
  "v_mfma_f32_16x16x32_bf16 v[172:175], a[176:179], a[108:111], v[172:175]\n"
  "v_mfma_f32_16x16x32_bf16 v[188:191], a[184:187], a[108:111], v[188:191]\n"
  "ds_read_b64_tr_b16 a[172:173], v224 offset:50368\n"
  "s_add_u32 s44, s44, 4096\n"
  "v_mfma_f32_16x16x32_bf16 v[188:191], a[188:191], a[124:127], v[188:191]\n"
  "ds_read_b64_tr_b16 a[174:175], v224 offset:53952\n"
  "v_mfma_f32_16x16x32_bf16 v[184:187], a[188:191], a[120:123], v[184:187]\n"
  "v_mfma_f32_16x16x32_bf16 v[184:187], a[184:187], a[104:107], v[184:187]\n"
  "v_mfma_f32_16x16x32_bf16 v[180:183], a[184:187], a[100:103], v[180:183]\n"
  "s_waitcnt vmcnt(38)\n"
  "s_barrier\n"
  "v_mfma_f32_16x16x32_bf16 v[180:183], a[188:191], a[116:119], v[180:183]\n"
  "ds_read_b64_tr_b16 a[176:177], v225 offset:0\n"
  "v_mfma_f32_16x16x32_bf16 v[176:179], a[188:191], a[112:115], v[176:179]\n"
  "ds_read_b64_tr_b16 a[178:179], v225 offset:3584\n"
  "s_waitcnt lgkmcnt(6)\n"
  "v_mfma_f32_16x16x32_bf16 v[176:179], a[184:187], a[96:99], v[176:179]\n"
  "v_mfma_f32_16x16x32_bf16 v[192:195], a[160:163], a[96:99], v[192:195]\n"
  "ds_read_b64_tr_b16 a[180:181], v225 offset:7168\n"
  "v_mfma_f32_16x16x32_bf16 v[192:195], a[164:167], a[112:115], v[192:195]\n"
  "ds_read_b64_tr_b16 a[182:183], v225 offset:10752\n"
  "v_mfma_f32_16x16x32_bf16 v[196:199], a[164:167], a[116:119], v[196:199]\n"
  "buffer_load_dwordx4 a[64:67], off, s[32:35], s44 offset:0 sc1\n"
  "v_mfma_f32_16x16x32_bf16 v[196:199], a[160:163], a[100:103], v[196:199]\n"
  "v_mfma_f32_16x16x32_bf16 v[200:203], a[160:163], a[104:107], v[200:203]\n"
  "ds_read_b64_tr_b16 a[184:185], v225 offset:14336\n"
  "v_mfma_f32_16x16x32_bf16 v[200:203], a[164:167], a[120:123], v[200:203]\n"
  "ds_read_b64_tr_b16 a[186:187], v225 offset:17920\n"
  "s_waitcnt lgkmcnt(6)\n"
  "v_mfma_f32_16x16x32_bf16 v[204:207], a[164:167], a[124:127], v[204:207]\n"
  "buffer_load_dwordx4 a[68:71], off, s[32:35], s44 offset:1024 sc1\n"
  "v_mfma_f32_16x16x32_bf16 v[204:207], a[160:163], a[108:111], v[204:207]\n"
  "v_mfma_f32_16x16x32_bf16 v[220:223], a[168:171], a[108:111], v[220:223]\n"
  "ds_read_b64_tr_b16 a[188:189], v225 offset:21504\n"
  "v_mfma_f32_16x16x32_bf16 v[220:223], a[172:175], a[124:127], v[220:223]\n"
  "ds_read_b64_tr_b16 a[190:191], v225 offset:25088\n"
  "v_mfma_f32_16x16x32_bf16 v[216:219], a[172:175], a[120:123], v[216:219]\n"
  "buffer_load_dwordx4 a[72:75], off, s[32:35], s44 offset:2048 sc1\n"
  "v_mfma_f32_16x16x32_bf16 v[216:219], a[168:171], a[104:107], v[216:219]\n"
  "v_mfma_f32_16x16x32_bf16 v[212:215], a[168:171], a[100:103], v[212:215]\n"
  "ds_read_b64_tr_b16 a[160:161], v225 offset:32\n"
  "v_mfma_f32_16x16x32_bf16 v[212:215], a[172:175], a[116:119], v[212:215]\n"
  "ds_read_b64_tr_b16 a[162:163], v225 offset:3616\n"
  "s_waitcnt vmcnt(33) lgkmcnt(6)\n"
  "v_mfma_f32_16x16x32_bf16 v[208:211], a[172:175], a[112:115], v[208:211]\n"
  "v_mfma_f32_16x16x32_bf16 v[208:211], a[168:171], a[96:99], v[208:211]\n"
  "v_mfma_f32_16x16x32_bf16 v[0:3], a[176:179], a[128:131], v[0:3]\n"
  "ds_read_b64_tr_b16 a[164:165], v225 offset:7200\n"
  "s_add_u32 m0, s46, 114688\n"
  "v_mfma_f32_16x16x32_bf16 v[0:3], a[180:183], a[144:147], v[0:3]\n"
  "ds_read_b64_tr_b16 a[166:167], v225 offset:10784\n"
  "v_mfma_f32_16x16x32_bf16 v[4:7], a[180:183], a[148:151], v[4:7]\n"
  "v_mfma_f32_16x16x32_bf16 v[4:7], a[176:179], a[132:135], v[4:7]\n"
  "v_mfma_f32_16x16x32_bf16 v[8:11], a[176:179], a[136:139], v[8:11]\n"
  "ds_read_b64_tr_b16 a[168:169], v225 offset:14368\n"
  "v_mfma_f32_16x16x32_bf16 v[8:11], a[180:183], a[152:155], v[8:11]\n"
  "ds_read_b64_tr_b16 a[170:171], v225 offset:17952\n"
  "s_waitcnt lgkmcnt(6)\n"
  "v_mfma_f32_16x16x32_bf16 v[12:15], a[180:183], a[156:159], v[12:15]\n"
  "buffer_load_dwordx4 v240, s[36:39], s45 offen offset:0 lds\n"
  "v_mfma_f32_16x16x32_bf16 v[12:15], a[176:179], a[140:143], v[12:15]\n"
  "v_mfma_f32_16x16x32_bf16 v[28:31], a[184:187], a[140:143], v[28:31]\n"
  "ds_read_b64_tr_b16 a[172:173], v225 offset:21536\n"
  "v_mfma_f32_16x16x32_bf16 v[28:31], a[188:191], a[156:159], v[28:31]\n"
  "ds_read_b64_tr_b16 a[174:175], v225 offset:25120\n"
  "v_mfma_f32_16x16x32_bf16 v[24:27], a[188:191], a[152:155], v[24:27]\n"
  "buffer_load_dwordx4 v241, s[36:39], s45 offen offset:1024 lds\n"
  "v_mfma_f32_16x16x32_bf16 v[24:27], a[184:187], a[136:139], v[24:27]\n"
  "v_mfma_f32_16x16x32_bf16 v[20:23], a[184:187], a[132:135], v[20:23]\n"
  "ds_read_b64_tr_b16 a[176:177], v225 offset:64\n"
  "v_mfma_f32_16x16x32_bf16 v[20:23], a[188:191], a[148:151], v[20:23]\n"
  "ds_read_b64_tr_b16 a[178:179], v225 offset:3648\n"
  "s_waitcnt lgkmcnt(6)\n"
  "v_mfma_f32_16x16x32_bf16 v[16:19], a[188:191], a[144:147], v[16:19]\n"
  "buffer_load_dwordx4 v242, s[36:39], s45 offen offset:2048 lds\n"
  "v_mfma_f32_16x16x32_bf16 v[16:19], a[184:187], a[128:131], v[16:19]\n"
  "v_mfma_f32_16x16x32_bf16 v[32:35], a[160:163], a[128:131], v[32:35]\n"
  "ds_read_b64_tr_b16 a[180:181], v225 offset:7232\n"
  "v_mfma_f32_16x16x32_bf16 v[32:35], a[164:167], a[144:147], v[32:35]\n"
  "ds_read_b64_tr_b16 a[182:183], v225 offset:10816\n"
  "v_mfma_f32_16x16x32_bf16 v[36:39], a[164:167], a[148:151], v[36:39]\n"
  "buffer_load_dwordx4 v243, s[36:39], s45 offen offset:3072 lds\n"
  "v_mfma_f32_16x16x32_bf16 v[36:39], a[160:163], a[132:135], v[36:39]\n"
  "v_mfma_f32_16x16x32_bf16 v[40:43], a[160:163], a[136:139], v[40:43]\n"
  "ds_read_b64_tr_b16 a[184:185], v225 offset:14400\n"
  "s_add_u32 m0, s46, 118784\n"
  "v_mfma_f32_16x16x32_bf16 v[40:43], a[164:167], a[152:155], v[40:43]\n"
  "ds_read_b64_tr_b16 a[186:187], v225 offset:17984\n"
  "s_waitcnt lgkmcnt(6)\n"
  "v_mfma_f32_16x16x32_bf16 v[44:47], a[164:167], a[156:159], v[44:47]\n"
  "v_mfma_f32_16x16x32_bf16 v[44:47], a[160:163], a[140:143], v[44:47]\n"
  "v_mfma_f32_16x16x32_bf16 v[60:63], a[168:171], a[140:143], v[60:63]\n"
  "ds_read_b64_tr_b16 a[188:189], v225 offset:21568\n"
  "v_mfma_f32_16x16x32_bf16 v[60:63], a[172:175], a[156:159], v[60:63]\n"
  "ds_read_b64_tr_b16 a[190:191], v225 offset:25152\n"
  "v_mfma_f32_16x16x32_bf16 v[56:59], a[172:175], a[152:155], v[56:59]\n"
  "buffer_load_dwordx4 v244, s[36:39], s45 offen offset:0 lds\n"
  "v_mfma_f32_16x16x32_bf16 v[56:59], a[168:171], a[136:139], v[56:59]\n"
  "v_mfma_f32_16x16x32_bf16 v[52:55], a[168:171], a[132:135], v[52:55]\n"
  "ds_read_b64_tr_b16 a[160:161], v225 offset:96\n"
  "v_mfma_f32_16x16x32_bf16 v[52:55], a[172:175], a[148:151], v[52:55]\n"
  "ds_read_b64_tr_b16 a[162:163], v225 offset:3680\n"
  "s_waitcnt lgkmcnt(6)\n"
  "v_mfma_f32_16x16x32_bf16 v[48:51], a[172:175], a[144:147], v[48:51]\n"
  "buffer_load_dwordx4 v245, s[36:39], s45 offen offset:1024 lds\n"
  "v_mfma_f32_16x16x32_bf16 v[48:51], a[168:171], a[128:131], v[48:51]\n"
  "v_mfma_f32_16x16x32_bf16 v[64:67], a[176:179], a[128:131], v[64:67]\n"
  "ds_read_b64_tr_b16 a[164:165], v225 offset:7264\n"
  "v_mfma_f32_16x16x32_bf16 v[64:67], a[180:183], a[144:147], v[64:67]\n"
  "ds_read_b64_tr_b16 a[166:167], v225 offset:10848\n"
  "v_mfma_f32_16x16x32_bf16 v[68:71], a[180:183], a[148:151], v[68:71]\n"
  "buffer_load_dwordx4 v246, s[36:39], s45 offen offset:2048 lds\n"
  "v_mfma_f32_16x16x32_bf16 v[68:71], a[176:179], a[132:135], v[68:71]\n"
  "v_mfma_f32_16x16x32_bf16 v[72:75], a[176:179], a[136:139], v[72:75]\n"
  "ds_read_b64_tr_b16 a[168:169], v225 offset:14432\n"
  "s_add_u32 s45, s45, 917504\n"
  "v_mfma_f32_16x16x32_bf16 v[72:75], a[180:183], a[152:155], v[72:75]\n"
  "ds_read_b64_tr_b16 a[170:171], v225 offset:18016\n"
  "s_waitcnt lgkmcnt(6)\n"
  "v_mfma_f32_16x16x32_bf16 v[76:79], a[180:183], a[156:159], v[76:79]\n"
  "v_mfma_f32_16x16x32_bf16 v[76:79], a[176:179], a[140:143], v[76:79]\n"
  "v_mfma_f32_16x16x32_bf16 v[92:95], a[184:187], a[140:143], v[92:95]\n"
  "ds_read_b64_tr_b16 a[172:173], v225 offset:21600\n"
  "v_mfma_f32_16x16x32_bf16 v[92:95], a[188:191], a[156:159], v[92:95]\n"
  "ds_read_b64_tr_b16 a[174:175], v225 offset:25184\n"
  "v_mfma_f32_16x16x32_bf16 v[88:91], a[188:191], a[152:155], v[88:91]\n"
  "buffer_load_dwordx4 a[76:79], off, s[32:35], s44 offset:3072 sc1\n"
  "v_mfma_f32_16x16x32_bf16 v[88:91], a[184:187], a[136:139], v[88:91]\n"
  "v_mfma_f32_16x16x32_bf16 v[84:87], a[184:187], a[132:135], v[84:87]\n"
  "ds_read_b64_tr_b16 a[176:177], v225 offset:128\n"
  "s_add_u32 s44, s44, 4096\n"
  "v_mfma_f32_16x16x32_bf16 v[84:87], a[188:191], a[148:151], v[84:87]\n"
  "ds_read_b64_tr_b16 a[178:179], v225 offset:3712\n"
  "s_waitcnt lgkmcnt(6)\n"
  "v_mfma_f32_16x16x32_bf16 v[80:83], a[188:191], a[144:147], v[80:83]\n"
  "v_mfma_f32_16x16x32_bf16 v[80:83], a[184:187], a[128:131], v[80:83]\n"
  "v_mfma_f32_16x16x32_bf16 v[96:99], a[160:163], a[128:131], v[96:99]\n"
  "ds_read_b64_tr_b16 a[180:181], v225 offset:7296\n"
  "v_mfma_f32_16x16x32_bf16 v[96:99], a[164:167], a[144:147], v[96:99]\n"
  "ds_read_b64_tr_b16 a[182:183], v225 offset:10880\n"
  "v_mfma_f32_16x16x32_bf16 v[100:103], a[164:167], a[148:151], v[100:103]\n"
  "buffer_load_dwordx4 a[80:83], off, s[32:35], s44 offset:0 sc1\n"
  "v_mfma_f32_16x16x32_bf16 v[100:103], a[160:163], a[132:135], v[100:103]\n"
  "v_mfma_f32_16x16x32_bf16 v[104:107], a[160:163], a[136:139], v[104:107]\n"
  "ds_read_b64_tr_b16 a[184:185], v225 offset:14464\n"
  "v_mfma_f32_16x16x32_bf16 v[104:107], a[164:167], a[152:155], v[104:107]\n"
  "ds_read_b64_tr_b16 a[186:187], v225 offset:18048\n"
  "s_waitcnt lgkmcnt(6)\n"
  "v_mfma_f32_16x16x32_bf16 v[108:111], a[164:167], a[156:159], v[108:111]\n"
  "v_mfma_f32_16x16x32_bf16 v[108:111], a[160:163], a[140:143], v[108:111]\n"
  "v_mfma_f32_16x16x32_bf16 v[124:127], a[168:171], a[140:143], v[124:127]\n"
  "ds_read_b64_tr_b16 a[188:189], v225 offset:21632\n"
  "v_mfma_f32_16x16x32_bf16 v[124:127], a[172:175], a[156:159], v[124:127]\n"
  "ds_read_b64_tr_b16 a[190:191], v225 offset:25216\n"
  "v_mfma_f32_16x16x32_bf16 v[120:123], a[172:175], a[152:155], v[120:123]\n"
  "buffer_load_dwordx4 a[84:87], off, s[32:35], s44 offset:1024 sc1\n"
  "v_mfma_f32_16x16x32_bf16 v[120:123], a[168:171], a[136:139], v[120:123]\n"
  "v_mfma_f32_16x16x32_bf16 v[116:119], a[168:171], a[132:135], v[116:119]\n"
  "ds_read_b64_tr_b16 a[160:161], v225 offset:160\n"
  "v_mfma_f32_16x16x32_bf16 v[116:119], a[172:175], a[148:151], v[116:119]\n"
  "ds_read_b64_tr_b16 a[162:163], v225 offset:3744\n"
  "s_waitcnt lgkmcnt(6)\n"
  "v_mfma_f32_16x16x32_bf16 v[112:115], a[172:175], a[144:147], v[112:115]\n"
  "buffer_load_dwordx4 a[88:91], off, s[32:35], s44 offset:2048 sc1\n"
  "v_mfma_f32_16x16x32_bf16 v[112:115], a[168:171], a[128:131], v[112:115]\n"
  "v_mfma_f32_16x16x32_bf16 v[128:131], a[176:179], a[128:131], v[128:131]\n"
  "ds_read_b64_tr_b16 a[164:165], v225 offset:7328\n"
  "v_mfma_f32_16x16x32_bf16 v[128:131], a[180:183], a[144:147], v[128:131]\n"
  "ds_read_b64_tr_b16 a[166:167], v225 offset:10912\n"
  "v_mfma_f32_16x16x32_bf16 v[132:135], a[180:183], a[148:151], v[132:135]\n"
  "buffer_load_dwordx4 a[92:95], off, s[32:35], s44 offset:3072 sc1\n"
  "v_mfma_f32_16x16x32_bf16 v[132:135], a[176:179], a[132:135], v[132:135]\n"
  "v_mfma_f32_16x16x32_bf16 v[136:139], a[176:179], a[136:139], v[136:139]\n"
  "ds_read_b64_tr_b16 a[168:169], v225 offset:14496\n"
  "s_add_u32 s44, s44, 4096\n"
  "v_mfma_f32_16x16x32_bf16 v[136:139], a[180:183], a[152:155], v[136:139]\n"
  "ds_read_b64_tr_b16 a[170:171], v225 offset:18080\n"
  "s_waitcnt lgkmcnt(6)\n"
  "v_mfma_f32_16x16x32_bf16 v[140:143], a[180:183], a[156:159], v[140:143]\n"
  "v_mfma_f32_16x16x32_bf16 v[140:143], a[176:179], a[140:143], v[140:143]\n"
  "v_mfma_f32_16x16x32_bf16 v[156:159], a[184:187], a[140:143], v[156:159]\n"
  "ds_read_b64_tr_b16 a[172:173], v225 offset:21664\n"
  "v_mfma_f32_16x16x32_bf16 v[156:159], a[188:191], a[156:159], v[156:159]\n"
  "ds_read_b64_tr_b16 a[174:175], v225 offset:25248\n"
  "v_mfma_f32_16x16x32_bf16 v[152:155], a[188:191], a[152:155], v[152:155]\n"
  "v_mfma_f32_16x16x32_bf16 v[152:155], a[184:187], a[136:139], v[152:155]\n"
  "v_mfma_f32_16x16x32_bf16 v[148:151], a[184:187], a[132:135], v[148:151]\n"
  "ds_read_b64_tr_b16 a[176:177], v225 offset:192\n"
  "v_mfma_f32_16x16x32_bf16 v[148:151], a[188:191], a[148:151], v[148:151]\n"
  "ds_read_b64_tr_b16 a[178:179], v225 offset:3776\n"
  "s_waitcnt lgkmcnt(6)\n"
  "v_mfma_f32_16x16x32_bf16 v[144:147], a[188:191], a[144:147], v[144:147]\n"
  "buffer_load_dwordx4 a[96:99], off, s[32:35], s44 offset:0 sc1\n"
  "v_mfma_f32_16x16x32_bf16 v[144:147], a[184:187], a[128:131], v[144:147]\n"
  "v_mfma_f32_16x16x32_bf16 v[160:163], a[160:163], a[128:131], v[160:163]\n"
  "ds_read_b64_tr_b16 a[180:181], v225 offset:7360\n"
  "v_mfma_f32_16x16x32_bf16 v[160:163], a[164:167], a[144:147], v[160:163]\n"
  "ds_read_b64_tr_b16 a[182:183], v225 offset:10944\n"
  "v_mfma_f32_16x16x32_bf16 v[164:167], a[164:167], a[148:151], v[164:167]\n"
  "v_mfma_f32_16x16x32_bf16 v[164:167], a[160:163], a[132:135], v[164:167]\n"
  "v_mfma_f32_16x16x32_bf16 v[168:171], a[160:163], a[136:139], v[168:171]\n"
  "ds_read_b64_tr_b16 a[184:185], v225 offset:14528\n"
  "v_mfma_f32_16x16x32_bf16 v[168:171], a[164:167], a[152:155], v[168:171]\n"
  "ds_read_b64_tr_b16 a[186:187], v225 offset:18112\n"
  "s_waitcnt lgkmcnt(6)\n"
  "v_mfma_f32_16x16x32_bf16 v[172:175], a[164:167], a[156:159], v[172:175]\n"
  "buffer_load_dwordx4 a[100:103], off, s[32:35], s44 offset:1024 sc1\n"
  "v_mfma_f32_16x16x32_bf16 v[172:175], a[160:163], a[140:143], v[172:175]\n"
  "v_mfma_f32_16x16x32_bf16 v[188:191], a[168:171], a[140:143], v[188:191]\n"
  "ds_read_b64_tr_b16 a[188:189], v225 offset:21696\n"
  "v_mfma_f32_16x16x32_bf16 v[188:191], a[172:175], a[156:159], v[188:191]\n"
  "ds_read_b64_tr_b16 a[190:191], v225 offset:25280\n"
  "v_mfma_f32_16x16x32_bf16 v[184:187], a[172:175], a[152:155], v[184:187]\n"
  "buffer_load_dwordx4 a[104:107], off, s[32:35], s44 offset:2048 sc1\n"
  "v_mfma_f32_16x16x32_bf16 v[184:187], a[168:171], a[136:139], v[184:187]\n"
  "v_mfma_f32_16x16x32_bf16 v[180:183], a[168:171], a[132:135], v[180:183]\n"
  "s_waitcnt vmcnt(41)\n"
  "s_barrier\n"
  "v_mfma_f32_16x16x32_bf16 v[180:183], a[172:175], a[148:151], v[180:183]\n"
  "ds_read_b64_tr_b16 a[160:161], v224 offset:0\n"
  "v_mfma_f32_16x16x32_bf16 v[176:179], a[172:175], a[144:147], v[176:179]\n"
  "ds_read_b64_tr_b16 a[162:163], v224 offset:3584\n"
  "s_waitcnt lgkmcnt(6)\n"
  "v_mfma_f32_16x16x32_bf16 v[176:179], a[168:171], a[128:131], v[176:179]\n"
  "buffer_load_dwordx4 a[108:111], off, s[32:35], s44 offset:3072 sc1\n"
  "v_mfma_f32_16x16x32_bf16 v[192:195], a[176:179], a[128:131], v[192:195]\n"
  "ds_read_b64_tr_b16 a[164:165], v224 offset:7168\n"
  "s_add_u32 s44, s44, 4096\n"
  "v_mfma_f32_16x16x32_bf16 v[192:195], a[180:183], a[144:147], v[192:195]\n"
  "ds_read_b64_tr_b16 a[166:167], v224 offset:10752\n"
  "v_mfma_f32_16x16x32_bf16 v[196:199], a[180:183], a[148:151], v[196:199]\n"
  "v_mfma_f32_16x16x32_bf16 v[196:199], a[176:179], a[132:135], v[196:199]\n"
  "v_mfma_f32_16x16x32_bf16 v[200:203], a[176:179], a[136:139], v[200:203]\n"
  "ds_read_b64_tr_b16 a[168:169], v224 offset:14336\n"
  "v_mfma_f32_16x16x32_bf16 v[200:203], a[180:183], a[152:155], v[200:203]\n"
  "ds_read_b64_tr_b16 a[170:171], v224 offset:17920\n"
  "s_waitcnt lgkmcnt(6)\n"
  "v_mfma_f32_16x16x32_bf16 v[204:207], a[180:183], a[156:159], v[204:207]\n"
  "v_mfma_f32_16x16x32_bf16 v[204:207], a[176:179], a[140:143], v[204:207]\n"
  "v_mfma_f32_16x16x32_bf16 v[220:223], a[184:187], a[140:143], v[220:223]\n"
  "ds_read_b64_tr_b16 a[172:173], v224 offset:21504\n"
  "v_mfma_f32_16x16x32_bf16 v[220:223], a[188:191], a[156:159], v[220:223]\n"
  "ds_read_b64_tr_b16 a[174:175], v224 offset:25088\n"
  "v_mfma_f32_16x16x32_bf16 v[216:219], a[188:191], a[152:155], v[216:219]\n"
  "buffer_load_dwordx4 a[112:115], off, s[32:35], s44 offset:0 sc1\n"
  "v_mfma_f32_16x16x32_bf16 v[216:219], a[184:187], a[136:139], v[216:219]\n"
  "v_mfma_f32_16x16x32_bf16 v[212:215], a[184:187], a[132:135], v[212:215]\n"
  "ds_read_b64_tr_b16 a[176:177], v224 offset:32\n"
  "v_mfma_f32_16x16x32_bf16 v[212:215], a[188:191], a[148:151], v[212:215]\n"
  "ds_read_b64_tr_b16 a[178:179], v224 offset:3616\n"
  "s_waitcnt vmcnt(35) lgkmcnt(6)\n"
  "v_mfma_f32_16x16x32_bf16 v[208:211], a[188:191], a[144:147], v[208:211]\n"
  "v_mfma_f32_16x16x32_bf16 v[208:211], a[184:187], a[128:131], v[208:211]\n"
  "v_mfma_f32_16x16x32_bf16 v[0:3], a[160:163], a[0:3], v[0:3]\n"
  "ds_read_b64_tr_b16 a[180:181], v224 offset:7200\n"
  "s_add_u32 m0, s46, 28672\n"
  "v_mfma_f32_16x16x32_bf16 v[0:3], a[164:167], a[16:19], v[0:3]\n"
  "ds_read_b64_tr_b16 a[182:183], v224 offset:10784\n"
  "v_mfma_f32_16x16x32_bf16 v[4:7], a[164:167], a[20:23], v[4:7]\n"
  "v_mfma_f32_16x16x32_bf16 v[4:7], a[160:163], a[4:7], v[4:7]\n"
  "v_mfma_f32_16x16x32_bf16 v[8:11], a[160:163], a[8:11], v[8:11]\n"
  "ds_read_b64_tr_b16 a[184:185], v224 offset:14368\n"
  "v_mfma_f32_16x16x32_bf16 v[8:11], a[164:167], a[24:27], v[8:11]\n"
  "ds_read_b64_tr_b16 a[186:187], v224 offset:17952\n"
  "s_waitcnt lgkmcnt(6)\n"
  "v_mfma_f32_16x16x32_bf16 v[12:15], a[164:167], a[28:31], v[12:15]\n"
  "buffer_load_dwordx4 v240, s[36:39], s45 offen offset:0 lds\n"
  "v_mfma_f32_16x16x32_bf16 v[12:15], a[160:163], a[12:15], v[12:15]\n"
  "v_mfma_f32_16x16x32_bf16 v[28:31], a[168:171], a[12:15], v[28:31]\n"
  "ds_read_b64_tr_b16 a[188:189], v224 offset:21536\n"
  "v_mfma_f32_16x16x32_bf16 v[28:31], a[172:175], a[28:31], v[28:31]\n"
  "ds_read_b64_tr_b16 a[190:191], v224 offset:25120\n"
  "v_mfma_f32_16x16x32_bf16 v[24:27], a[172:175], a[24:27], v[24:27]\n"
  "buffer_load_dwordx4 v241, s[36:39], s45 offen offset:1024 lds\n"
  "v_mfma_f32_16x16x32_bf16 v[24:27], a[168:171], a[8:11], v[24:27]\n"
  "v_mfma_f32_16x16x32_bf16 v[20:23], a[168:171], a[4:7], v[20:23]\n"
  "ds_read_b64_tr_b16 a[160:161], v224 offset:64\n"
  "v_mfma_f32_16x16x32_bf16 v[20:23], a[172:175], a[20:23], v[20:23]\n"
  "ds_read_b64_tr_b16 a[162:163], v224 offset:3648\n"
  "s_waitcnt lgkmcnt(6)\n"
  "v_mfma_f32_16x16x32_bf16 v[16:19], a[172:175], a[16:19], v[16:19]\n"
  "buffer_load_dwordx4 v242, s[36:39], s45 offen offset:2048 lds\n"
  "v_mfma_f32_16x16x32_bf16 v[16:19], a[168:171], a[0:3], v[16:19]\n"
  "v_mfma_f32_16x16x32_bf16 v[32:35], a[176:179], a[0:3], v[32:35]\n"
  "ds_read_b64_tr_b16 a[164:165], v224 offset:7232\n"
  "v_mfma_f32_16x16x32_bf16 v[32:35], a[180:183], a[16:19], v[32:35]\n"
  "ds_read_b64_tr_b16 a[166:167], v224 offset:10816\n"
  "v_mfma_f32_16x16x32_bf16 v[36:39], a[180:183], a[20:23], v[36:39]\n"
  "v_mfma_f32_16x16x32_bf16 v[36:39], a[176:179], a[4:7], v[36:39]\n"
  "v_mfma_f32_16x16x32_bf16 v[40:43], a[176:179], a[8:11], v[40:43]\n"
  "ds_read_b64_tr_b16 a[168:169], v224 offset:14400\n"
  "v_mfma_f32_16x16x32_bf16 v[40:43], a[180:183], a[24:27], v[40:43]\n"
  "ds_read_b64_tr_b16 a[170:171], v224 offset:17984\n"
  "s_waitcnt lgkmcnt(6)\n"
  "v_mfma_f32_16x16x32_bf16 v[44:47], a[180:183], a[28:31], v[44:47]\n"
  "buffer_load_dwordx4 v243, s[36:39], s45 offen offset:3072 lds\n"
  "v_mfma_f32_16x16x32_bf16 v[44:47], a[176:179], a[12:15], v[44:47]\n"
  "v_mfma_f32_16x16x32_bf16 v[60:63], a[184:187], a[12:15], v[60:63]\n"
  "ds_read_b64_tr_b16 a[172:173], v224 offset:21568\n"
  "s_add_u32 m0, s46, 32768\n"
  "v_mfma_f32_16x16x32_bf16 v[60:63], a[188:191], a[28:31], v[60:63]\n"
  "ds_read_b64_tr_b16 a[174:175], v224 offset:25152\n"
  "v_mfma_f32_16x16x32_bf16 v[56:59], a[188:191], a[24:27], v[56:59]\n"
  "v_mfma_f32_16x16x32_bf16 v[56:59], a[184:187], a[8:11], v[56:59]\n"
  "v_mfma_f32_16x16x32_bf16 v[52:55], a[184:187], a[4:7], v[52:55]\n"
  "ds_read_b64_tr_b16 a[176:177], v224 offset:96\n"
  "v_mfma_f32_16x16x32_bf16 v[52:55], a[188:191], a[20:23], v[52:55]\n"
  "ds_read_b64_tr_b16 a[178:179], v224 offset:3680\n"
  "s_waitcnt lgkmcnt(6)\n"
  "v_mfma_f32_16x16x32_bf16 v[48:51], a[188:191], a[16:19], v[48:51]\n"
  "buffer_load_dwordx4 v244, s[36:39], s45 offen offset:0 lds\n"
  "v_mfma_f32_16x16x32_bf16 v[48:51], a[184:187], a[0:3], v[48:51]\n"
  "v_mfma_f32_16x16x32_bf16 v[64:67], a[160:163], a[0:3], v[64:67]\n"
  "ds_read_b64_tr_b16 a[180:181], v224 offset:7264\n"
  "v_mfma_f32_16x16x32_bf16 v[64:67], a[164:167], a[16:19], v[64:67]\n"
  "ds_read_b64_tr_b16 a[182:183], v224 offset:10848\n"
  "v_mfma_f32_16x16x32_bf16 v[68:71], a[164:167], a[20:23], v[68:71]\n"
  "buffer_load_dwordx4 v245, s[36:39], s45 offen offset:1024 lds\n"
  "v_mfma_f32_16x16x32_bf16 v[68:71], a[160:163], a[4:7], v[68:71]\n"
  "v_mfma_f32_16x16x32_bf16 v[72:75], a[160:163], a[8:11], v[72:75]\n"
  "ds_read_b64_tr_b16 a[184:185], v224 offset:14432\n"
  "v_mfma_f32_16x16x32_bf16 v[72:75], a[164:167], a[24:27], v[72:75]\n"
  "ds_read_b64_tr_b16 a[186:187], v224 offset:18016\n"
  "s_waitcnt lgkmcnt(6)\n"
  "v_mfma_f32_16x16x32_bf16 v[76:79], a[164:167], a[28:31], v[76:79]\n"
  "v_mfma_f32_16x16x32_bf16 v[76:79], a[160:163], a[12:15], v[76:79]\n"
  "v_mfma_f32_16x16x32_bf16 v[92:95], a[168:171], a[12:15], v[92:95]\n"
  "ds_read_b64_tr_b16 a[188:189], v224 offset:21600\n"
  "v_mfma_f32_16x16x32_bf16 v[92:95], a[172:175], a[28:31], v[92:95]\n"
  "ds_read_b64_tr_b16 a[190:191], v224 offset:25184\n"
  "v_mfma_f32_16x16x32_bf16 v[88:91], a[172:175], a[24:27], v[88:91]\n"
  "buffer_load_dwordx4 v246, s[36:39], s45 offen offset:2048 lds\n"
  "v_mfma_f32_16x16x32_bf16 v[88:91], a[168:171], a[8:11], v[88:91]\n"
  "v_mfma_f32_16x16x32_bf16 v[84:87], a[168:171], a[4:7], v[84:87]\n"
  "ds_read_b64_tr_b16 a[160:161], v224 offset:128\n"
  "s_add_u32 s45, s45, 917504\n"
  "v_mfma_f32_16x16x32_bf16 v[84:87], a[172:175], a[20:23], v[84:87]\n"
  "ds_read_b64_tr_b16 a[162:163], v224 offset:3712\n"
  "s_waitcnt lgkmcnt(6)\n"
  "v_mfma_f32_16x16x32_bf16 v[80:83], a[172:175], a[16:19], v[80:83]\n"
  "v_mfma_f32_16x16x32_bf16 v[80:83], a[168:171], a[0:3], v[80:83]\n"
  "v_mfma_f32_16x16x32_bf16 v[96:99], a[176:179], a[0:3], v[96:99]\n"
  "ds_read_b64_tr_b16 a[164:165], v224 offset:7296\n"
  "v_mfma_f32_16x16x32_bf16 v[96:99], a[180:183], a[16:19], v[96:99]\n"
  "ds_read_b64_tr_b16 a[166:167], v224 offset:10880\n"
  "v_mfma_f32_16x16x32_bf16 v[100:103], a[180:183], a[20:23], v[100:103]\n"
  "buffer_load_dwordx4 a[116:119], off, s[32:35], s44 offset:1024 sc1\n"
  "v_mfma_f32_16x16x32_bf16 v[100:103], a[176:179], a[4:7], v[100:103]\n"
  "v_mfma_f32_16x16x32_bf16 v[104:107], a[176:179], a[8:11], v[104:107]\n"
  "ds_read_b64_tr_b16 a[168:169], v224 offset:14464\n"
  "v_mfma_f32_16x16x32_bf16 v[104:107], a[180:183], a[24:27], v[104:107]\n"
  "ds_read_b64_tr_b16 a[170:171], v224 offset:18048\n"
  "s_waitcnt lgkmcnt(6)\n"
  "v_mfma_f32_16x16x32_bf16 v[108:111], a[180:183], a[28:31], v[108:111]\n"
  "v_mfma_f32_16x16x32_bf16 v[108:111], a[176:179], a[12:15], v[108:111]\n"
  "v_mfma_f32_16x16x32_bf16 v[124:127], a[184:187], a[12:15], v[124:127]\n"
  "ds_read_b64_tr_b16 a[172:173], v224 offset:21632\n"
  "v_mfma_f32_16x16x32_bf16 v[124:127], a[188:191], a[28:31], v[124:127]\n"
  "ds_read_b64_tr_b16 a[174:175], v224 offset:25216\n"
  "v_mfma_f32_16x16x32_bf16 v[120:123], a[188:191], a[24:27], v[120:123]\n"
  "buffer_load_dwordx4 a[120:123], off, s[32:35], s44 offset:2048 sc1\n"
  "v_mfma_f32_16x16x32_bf16 v[120:123], a[184:187], a[8:11], v[120:123]\n"
  "v_mfma_f32_16x16x32_bf16 v[116:119], a[184:187], a[4:7], v[116:119]\n"
  "ds_read_b64_tr_b16 a[176:177], v224 offset:160\n"
  "v_mfma_f32_16x16x32_bf16 v[116:119], a[188:191], a[20:23], v[116:119]\n"
  "ds_read_b64_tr_b16 a[178:179], v224 offset:3744\n"
  "s_waitcnt lgkmcnt(6)\n"
  "v_mfma_f32_16x16x32_bf16 v[112:115], a[188:191], a[16:19], v[112:115]\n"
  "buffer_load_dwordx4 a[124:127], off, s[32:35], s44 offset:3072 sc1\n"
  "v_mfma_f32_16x16x32_bf16 v[112:115], a[184:187], a[0:3], v[112:115]\n"
  "v_mfma_f32_16x16x32_bf16 v[128:131], a[160:163], a[0:3], v[128:131]\n"
  "ds_read_b64_tr_b16 a[180:181], v224 offset:7328\n"
  "s_add_u32 s44, s44, 4096\n"
  "v_mfma_f32_16x16x32_bf16 v[128:131], a[164:167], a[16:19], v[128:131]\n"
  "ds_read_b64_tr_b16 a[182:183], v224 offset:10912\n"
  "v_mfma_f32_16x16x32_bf16 v[132:135], a[164:167], a[20:23], v[132:135]\n"
  "v_mfma_f32_16x16x32_bf16 v[132:135], a[160:163], a[4:7], v[132:135]\n"
  "v_mfma_f32_16x16x32_bf16 v[136:139], a[160:163], a[8:11], v[136:139]\n"
  "ds_read_b64_tr_b16 a[184:185], v224 offset:14496\n"
  "v_mfma_f32_16x16x32_bf16 v[136:139], a[164:167], a[24:27], v[136:139]\n"
  "ds_read_b64_tr_b16 a[186:187], v224 offset:18080\n"
  "s_waitcnt lgkmcnt(6)\n"
  "v_mfma_f32_16x16x32_bf16 v[140:143], a[164:167], a[28:31], v[140:143]\n"
  "v_mfma_f32_16x16x32_bf16 v[140:143], a[160:163], a[12:15], v[140:143]\n"
  "v_mfma_f32_16x16x32_bf16 v[156:159], a[168:171], a[12:15], v[156:159]\n"
  "ds_read_b64_tr_b16 a[188:189], v224 offset:21664\n"
  "v_mfma_f32_16x16x32_bf16 v[156:159], a[172:175], a[28:31], v[156:159]\n"
  "ds_read_b64_tr_b16 a[190:191], v224 offset:25248\n"
  "v_mfma_f32_16x16x32_bf16 v[152:155], a[172:175], a[24:27], v[152:155]\n"
  "buffer_load_dwordx4 a[128:131], off, s[32:35], s44 offset:0 sc1\n"
  "v_mfma_f32_16x16x32_bf16 v[152:155], a[168:171], a[8:11], v[152:155]\n"
  "v_mfma_f32_16x16x32_bf16 v[148:151], a[168:171], a[4:7], v[148:151]\n"
  "ds_read_b64_tr_b16 a[160:161], v224 offset:192\n"
  "v_mfma_f32_16x16x32_bf16 v[148:151], a[172:175], a[20:23], v[148:151]\n"
  "ds_read_b64_tr_b16 a[162:163], v224 offset:3776\n"
  "s_waitcnt lgkmcnt(6)\n"
  "v_mfma_f32_16x16x32_bf16 v[144:147], a[172:175], a[16:19], v[144:147]\n"
  "buffer_load_dwordx4 a[132:135], off, s[32:35], s44 offset:1024 sc1\n"
  "v_mfma_f32_16x16x32_bf16 v[144:147], a[168:171], a[0:3], v[144:147]\n"
  "v_mfma_f32_16x16x32_bf16 v[160:163], a[176:179], a[0:3], v[160:163]\n"
  "ds_read_b64_tr_b16 a[164:165], v224 offset:7360\n"
  "v_mfma_f32_16x16x32_bf16 v[160:163], a[180:183], a[16:19], v[160:163]\n"
  "ds_read_b64_tr_b16 a[166:167], v224 offset:10944\n"
  "v_mfma_f32_16x16x32_bf16 v[164:167], a[180:183], a[20:23], v[164:167]\n"
  "v_mfma_f32_16x16x32_bf16 v[164:167], a[176:179], a[4:7], v[164:167]\n"
  "v_mfma_f32_16x16x32_bf16 v[168:171], a[176:179], a[8:11], v[168:171]\n"
  "ds_read_b64_tr_b16 a[168:169], v224 offset:14528\n"
  "v_mfma_f32_16x16x32_bf16 v[168:171], a[180:183], a[24:27], v[168:171]\n"
  "ds_read_b64_tr_b16 a[170:171], v224 offset:18112\n"
  "s_waitcnt lgkmcnt(6)\n"
  "v_mfma_f32_16x16x32_bf16 v[172:175], a[180:183], a[28:31], v[172:175]\n"
  "buffer_load_dwordx4 a[136:139], off, s[32:35], s44 offset:2048 sc1\n"
  "v_mfma_f32_16x16x32_bf16 v[172:175], a[176:179], a[12:15], v[172:175]\n"
  "v_mfma_f32_16x16x32_bf16 v[188:191], a[184:187], a[12:15], v[188:191]\n"
  "ds_read_b64_tr_b16 a[172:173], v224 offset:21696\n"
  "v_mfma_f32_16x16x32_bf16 v[188:191], a[188:191], a[28:31], v[188:191]\n"
  "ds_read_b64_tr_b16 a[174:175], v224 offset:25280\n"
  "v_mfma_f32_16x16x32_bf16 v[184:187], a[188:191], a[24:27], v[184:187]\n"
  "buffer_load_dwordx4 a[140:143], off, s[32:35], s44 offset:3072 sc1\n"
  "v_mfma_f32_16x16x32_bf16 v[184:187], a[184:187], a[8:11], v[184:187]\n"
  "v_mfma_f32_16x16x32_bf16 v[180:183], a[184:187], a[4:7], v[180:183]\n"
  "s_waitcnt vmcnt(42)\n"
  "s_barrier\n"
  "s_add_u32 s44, s44, 4096\n"
  "v_mfma_f32_16x16x32_bf16 v[180:183], a[188:191], a[20:23], v[180:183]\n"
  "ds_read_b64_tr_b16 a[176:177], v225 offset:28672\n"
  "v_mfma_f32_16x16x32_bf16 v[176:179], a[188:191], a[16:19], v[176:179]\n"
  "ds_read_b64_tr_b16 a[178:179], v225 offset:32256\n"
  "s_waitcnt lgkmcnt(6)\n"
  "v_mfma_f32_16x16x32_bf16 v[176:179], a[184:187], a[0:3], v[176:179]\n"
  "v_mfma_f32_16x16x32_bf16 v[192:195], a[160:163], a[0:3], v[192:195]\n"
  "ds_read_b64_tr_b16 a[180:181], v225 offset:35840\n"
  "v_mfma_f32_16x16x32_bf16 v[192:195], a[164:167], a[16:19], v[192:195]\n"
  "ds_read_b64_tr_b16 a[182:183], v225 offset:39424\n"
  "v_mfma_f32_16x16x32_bf16 v[196:199], a[164:167], a[20:23], v[196:199]\n"
  "v_mfma_f32_16x16x32_bf16 v[196:199], a[160:163], a[4:7], v[196:199]\n"
  "v_mfma_f32_16x16x32_bf16 v[200:203], a[160:163], a[8:11], v[200:203]\n"
  "ds_read_b64_tr_b16 a[184:185], v225 offset:43008\n"
  "v_mfma_f32_16x16x32_bf16 v[200:203], a[164:167], a[24:27], v[200:203]\n"
  "ds_read_b64_tr_b16 a[186:187], v225 offset:46592\n"
  "s_waitcnt lgkmcnt(6)\n"
  "v_mfma_f32_16x16x32_bf16 v[204:207], a[164:167], a[28:31], v[204:207]\n"
  "buffer_load_dwordx4 a[144:147], off, s[32:35], s44 offset:0 sc1\n"
  "v_mfma_f32_16x16x32_bf16 v[204:207], a[160:163], a[12:15], v[204:207]\n"
  "v_mfma_f32_16x16x32_bf16 v[220:223], a[168:171], a[12:15], v[220:223]\n"
  "ds_read_b64_tr_b16 a[188:189], v225 offset:50176\n"
  "v_mfma_f32_16x16x32_bf16 v[220:223], a[172:175], a[28:31], v[220:223]\n"
  "ds_read_b64_tr_b16 a[190:191], v225 offset:53760\n"
  "v_mfma_f32_16x16x32_bf16 v[216:219], a[172:175], a[24:27], v[216:219]\n"
  "buffer_load_dwordx4 a[148:151], off, s[32:35], s44 offset:1024 sc1\n"
  "v_mfma_f32_16x16x32_bf16 v[216:219], a[168:171], a[8:11], v[216:219]\n"
  "v_mfma_f32_16x16x32_bf16 v[212:215], a[168:171], a[4:7], v[212:215]\n"
  "ds_read_b64_tr_b16 a[160:161], v225 offset:28704\n"
  "v_mfma_f32_16x16x32_bf16 v[212:215], a[172:175], a[20:23], v[212:215]\n"
  "ds_read_b64_tr_b16 a[162:163], v225 offset:32288\n"
  "s_waitcnt vmcnt(36) lgkmcnt(6)\n"
  "v_mfma_f32_16x16x32_bf16 v[208:211], a[172:175], a[16:19], v[208:211]\n"
  "v_mfma_f32_16x16x32_bf16 v[208:211], a[168:171], a[0:3], v[208:211]\n"
  "v_mfma_f32_16x16x32_bf16 v[0:3], a[176:179], a[32:35], v[0:3]\n"
  "ds_read_b64_tr_b16 a[164:165], v225 offset:35872\n"
  "s_add_u32 m0, s46, 57344\n"
  "v_mfma_f32_16x16x32_bf16 v[0:3], a[180:183], a[48:51], v[0:3]\n"
  "ds_read_b64_tr_b16 a[166:167], v225 offset:39456\n"
  "v_mfma_f32_16x16x32_bf16 v[4:7], a[180:183], a[52:55], v[4:7]\n"
  "v_mfma_f32_16x16x32_bf16 v[4:7], a[176:179], a[36:39], v[4:7]\n"
  "v_mfma_f32_16x16x32_bf16 v[8:11], a[176:179], a[40:43], v[8:11]\n"
  "ds_read_b64_tr_b16 a[168:169], v225 offset:43040\n"
  "v_mfma_f32_16x16x32_bf16 v[8:11], a[180:183], a[56:59], v[8:11]\n"
  "ds_read_b64_tr_b16 a[170:171], v225 offset:46624\n"
  "s_waitcnt lgkmcnt(6)\n"
  "v_mfma_f32_16x16x32_bf16 v[12:15], a[180:183], a[60:63], v[12:15]\n"
  "buffer_load_dwordx4 v240, s[36:39], s45 offen offset:0 lds\n"
  "v_mfma_f32_16x16x32_bf16 v[12:15], a[176:179], a[44:47], v[12:15]\n"
  "v_mfma_f32_16x16x32_bf16 v[28:31], a[184:187], a[44:47], v[28:31]\n"
  "ds_read_b64_tr_b16 a[172:173], v225 offset:50208\n"
  "v_mfma_f32_16x16x32_bf16 v[28:31], a[188:191], a[60:63], v[28:31]\n"
  "ds_read_b64_tr_b16 a[174:175], v225 offset:53792\n"
  "v_mfma_f32_16x16x32_bf16 v[24:27], a[188:191], a[56:59], v[24:27]\n"
  "buffer_load_dwordx4 v241, s[36:39], s45 offen offset:1024 lds\n"
  "v_mfma_f32_16x16x32_bf16 v[24:27], a[184:187], a[40:43], v[24:27]\n"
  "v_mfma_f32_16x16x32_bf16 v[20:23], a[184:187], a[36:39], v[20:23]\n"
  "ds_read_b64_tr_b16 a[176:177], v225 offset:28736\n"
  "v_mfma_f32_16x16x32_bf16 v[20:23], a[188:191], a[52:55], v[20:23]\n"
  "ds_read_b64_tr_b16 a[178:179], v225 offset:32320\n"
  "s_waitcnt lgkmcnt(6)\n"
  "v_mfma_f32_16x16x32_bf16 v[16:19], a[188:191], a[48:51], v[16:19]\n"
  "buffer_load_dwordx4 v242, s[36:39], s45 offen offset:2048 lds\n"
  "v_mfma_f32_16x16x32_bf16 v[16:19], a[184:187], a[32:35], v[16:19]\n"
  "v_mfma_f32_16x16x32_bf16 v[32:35], a[160:163], a[32:35], v[32:35]\n"
  "ds_read_b64_tr_b16 a[180:181], v225 offset:35904\n"
  "v_mfma_f32_16x16x32_bf16 v[32:35], a[164:167], a[48:51], v[32:35]\n"
  "ds_read_b64_tr_b16 a[182:183], v225 offset:39488\n"
  "v_mfma_f32_16x16x32_bf16 v[36:39], a[164:167], a[52:55], v[36:39]\n"
  "v_mfma_f32_16x16x32_bf16 v[36:39], a[160:163], a[36:39], v[36:39]\n"
  "v_mfma_f32_16x16x32_bf16 v[40:43], a[160:163], a[40:43], v[40:43]\n"
  "ds_read_b64_tr_b16 a[184:185], v225 offset:43072\n"
  "v_mfma_f32_16x16x32_bf16 v[40:43], a[164:167], a[56:59], v[40:43]\n"
  "ds_read_b64_tr_b16 a[186:187], v225 offset:46656\n"
  "s_waitcnt lgkmcnt(6)\n"
  "v_mfma_f32_16x16x32_bf16 v[44:47], a[164:167], a[60:63], v[44:47]\n"
  "buffer_load_dwordx4 v243, s[36:39], s45 offen offset:3072 lds\n"
  "v_mfma_f32_16x16x32_bf16 v[44:47], a[160:163], a[44:47], v[44:47]\n"
  "v_mfma_f32_16x16x32_bf16 v[60:63], a[168:171], a[44:47], v[60:63]\n"
  "ds_read_b64_tr_b16 a[188:189], v225 offset:50240\n"
  "s_add_u32 m0, s46, 61440\n"
  "v_mfma_f32_16x16x32_bf16 v[60:63], a[172:175], a[60:63], v[60:63]\n"
  "ds_read_b64_tr_b16 a[190:191], v225 offset:53824\n"
  "v_mfma_f32_16x16x32_bf16 v[56:59], a[172:175], a[56:59], v[56:59]\n"
  "v_mfma_f32_16x16x32_bf16 v[56:59], a[168:171], a[40:43], v[56:59]\n"
  "v_mfma_f32_16x16x32_bf16 v[52:55], a[168:171], a[36:39], v[52:55]\n"
  "ds_read_b64_tr_b16 a[160:161], v225 offset:28768\n"
  "v_mfma_f32_16x16x32_bf16 v[52:55], a[172:175], a[52:55], v[52:55]\n"
  "ds_read_b64_tr_b16 a[162:163], v225 offset:32352\n"
  "s_waitcnt lgkmcnt(6)\n"
  "v_mfma_f32_16x16x32_bf16 v[48:51], a[172:175], a[48:51], v[48:51]\n"
  "buffer_load_dwordx4 v244, s[36:39], s45 offen offset:0 lds\n"
  "v_mfma_f32_16x16x32_bf16 v[48:51], a[168:171], a[32:35], v[48:51]\n"
  "v_mfma_f32_16x16x32_bf16 v[64:67], a[176:179], a[32:35], v[64:67]\n"
  "ds_read_b64_tr_b16 a[164:165], v225 offset:35936\n"
  "v_mfma_f32_16x16x32_bf16 v[64:67], a[180:183], a[48:51], v[64:67]\n"
  "ds_read_b64_tr_b16 a[166:167], v225 offset:39520\n"
  "v_mfma_f32_16x16x32_bf16 v[68:71], a[180:183], a[52:55], v[68:71]\n"
  "buffer_load_dwordx4 v245, s[36:39], s45 offen offset:1024 lds\n"
  "v_mfma_f32_16x16x32_bf16 v[68:71], a[176:179], a[36:39], v[68:71]\n"
  "v_mfma_f32_16x16x32_bf16 v[72:75], a[176:179], a[40:43], v[72:75]\n"
  "ds_read_b64_tr_b16 a[168:169], v225 offset:43104\n"
  "v_mfma_f32_16x16x32_bf16 v[72:75], a[180:183], a[56:59], v[72:75]\n"
  "ds_read_b64_tr_b16 a[170:171], v225 offset:46688\n"
  "s_waitcnt lgkmcnt(6)\n"
  "v_mfma_f32_16x16x32_bf16 v[76:79], a[180:183], a[60:63], v[76:79]\n"
  "v_mfma_f32_16x16x32_bf16 v[76:79], a[176:179], a[44:47], v[76:79]\n"
  "v_mfma_f32_16x16x32_bf16 v[92:95], a[184:187], a[44:47], v[92:95]\n"
  "ds_read_b64_tr_b16 a[172:173], v225 offset:50272\n"
  "v_mfma_f32_16x16x32_bf16 v[92:95], a[188:191], a[60:63], v[92:95]\n"
  "ds_read_b64_tr_b16 a[174:175], v225 offset:53856\n"
  "v_mfma_f32_16x16x32_bf16 v[88:91], a[188:191], a[56:59], v[88:91]\n"
  "buffer_load_dwordx4 v246, s[36:39], s45 offen offset:2048 lds\n"
  "v_mfma_f32_16x16x32_bf16 v[88:91], a[184:187], a[40:43], v[88:91]\n"
  "v_mfma_f32_16x16x32_bf16 v[84:87], a[184:187], a[36:39], v[84:87]\n"
  "ds_read_b64_tr_b16 a[176:177], v225 offset:28800\n"
  "s_add_u32 s45, s45, 917504\n"
  "v_mfma_f32_16x16x32_bf16 v[84:87], a[188:191], a[52:55], v[84:87]\n"
  "ds_read_b64_tr_b16 a[178:179], v225 offset:32384\n"
  "s_waitcnt lgkmcnt(6)\n"
  "v_mfma_f32_16x16x32_bf16 v[80:83], a[188:191], a[48:51], v[80:83]\n"
  "v_mfma_f32_16x16x32_bf16 v[80:83], a[184:187], a[32:35], v[80:83]\n"
  "v_mfma_f32_16x16x32_bf16 v[96:99], a[160:163], a[32:35], v[96:99]\n"
  "ds_read_b64_tr_b16 a[180:181], v225 offset:35968\n"
  "v_mfma_f32_16x16x32_bf16 v[96:99], a[164:167], a[48:51], v[96:99]\n"
  "ds_read_b64_tr_b16 a[182:183], v225 offset:39552\n"
  "v_mfma_f32_16x16x32_bf16 v[100:103], a[164:167], a[52:55], v[100:103]\n"
  "buffer_load_dwordx4 a[152:155], off, s[32:35], s44 offset:2048 sc1\n"
  "v_mfma_f32_16x16x32_bf16 v[100:103], a[160:163], a[36:39], v[100:103]\n"
  "v_mfma_f32_16x16x32_bf16 v[104:107], a[160:163], a[40:43], v[104:107]\n"
  "ds_read_b64_tr_b16 a[184:185], v225 offset:43136\n"
  "v_mfma_f32_16x16x32_bf16 v[104:107], a[164:167], a[56:59], v[104:107]\n"
  "ds_read_b64_tr_b16 a[186:187], v225 offset:46720\n"
  "s_waitcnt lgkmcnt(6)\n"
  "v_mfma_f32_16x16x32_bf16 v[108:111], a[164:167], a[60:63], v[108:111]\n"
  "v_mfma_f32_16x16x32_bf16 v[108:111], a[160:163], a[44:47], v[108:111]\n"
  "v_mfma_f32_16x16x32_bf16 v[124:127], a[168:171], a[44:47], v[124:127]\n"
  "ds_read_b64_tr_b16 a[188:189], v225 offset:50304\n"
  "v_mfma_f32_16x16x32_bf16 v[124:127], a[172:175], a[60:63], v[124:127]\n"
  "ds_read_b64_tr_b16 a[190:191], v225 offset:53888\n"
  "v_mfma_f32_16x16x32_bf16 v[120:123], a[172:175], a[56:59], v[120:123]\n"
  "buffer_load_dwordx4 a[156:159], off, s[32:35], s44 offset:3072 sc1\n"
  "v_mfma_f32_16x16x32_bf16 v[120:123], a[168:171], a[40:43], v[120:123]\n"
  "v_mfma_f32_16x16x32_bf16 v[116:119], a[168:171], a[36:39], v[116:119]\n"
  "ds_read_b64_tr_b16 a[160:161], v225 offset:28832\n"
  "s_add_u32 s44, s44, 4096\n"
  "v_mfma_f32_16x16x32_bf16 v[116:119], a[172:175], a[52:55], v[116:119]\n"
  "ds_read_b64_tr_b16 a[162:163], v225 offset:32416\n"
  "s_waitcnt lgkmcnt(6)\n"
  "v_mfma_f32_16x16x32_bf16 v[112:115], a[172:175], a[48:51], v[112:115]\n"
  "v_mfma_f32_16x16x32_bf16 v[112:115], a[168:171], a[32:35], v[112:115]\n"
  "v_mfma_f32_16x16x32_bf16 v[128:131], a[176:179], a[32:35], v[128:131]\n"
  "ds_read_b64_tr_b16 a[164:165], v225 offset:36000\n"
  "v_mfma_f32_16x16x32_bf16 v[128:131], a[180:183], a[48:51], v[128:131]\n"
  "ds_read_b64_tr_b16 a[166:167], v225 offset:39584\n"
  "v_mfma_f32_16x16x32_bf16 v[132:135], a[180:183], a[52:55], v[132:135]\n"
  "buffer_load_dwordx4 a[0:3], off, s[32:35], s44 offset:0 sc1\n"
  "v_mfma_f32_16x16x32_bf16 v[132:135], a[176:179], a[36:39], v[132:135]\n"
  "v_mfma_f32_16x16x32_bf16 v[136:139], a[176:179], a[40:43], v[136:139]\n"
  "ds_read_b64_tr_b16 a[168:169], v225 offset:43168\n"
  "v_mfma_f32_16x16x32_bf16 v[136:139], a[180:183], a[56:59], v[136:139]\n"
  "ds_read_b64_tr_b16 a[170:171], v225 offset:46752\n"
  "s_waitcnt lgkmcnt(6)\n"
  "v_mfma_f32_16x16x32_bf16 v[140:143], a[180:183], a[60:63], v[140:143]\n"
  "v_mfma_f32_16x16x32_bf16 v[140:143], a[176:179], a[44:47], v[140:143]\n"
  "v_mfma_f32_16x16x32_bf16 v[156:159], a[184:187], a[44:47], v[156:159]\n"
  "ds_read_b64_tr_b16 a[172:173], v225 offset:50336\n"
  "v_mfma_f32_16x16x32_bf16 v[156:159], a[188:191], a[60:63], v[156:159]\n"
  "ds_read_b64_tr_b16 a[174:175], v225 offset:53920\n"
  "v_mfma_f32_16x16x32_bf16 v[152:155], a[188:191], a[56:59], v[152:155]\n"
  "buffer_load_dwordx4 a[4:7], off, s[32:35], s44 offset:1024 sc1\n"
  "v_mfma_f32_16x16x32_bf16 v[152:155], a[184:187], a[40:43], v[152:155]\n"
  "v_mfma_f32_16x16x32_bf16 v[148:151], a[184:187], a[36:39], v[148:151]\n"
  "ds_read_b64_tr_b16 a[176:177], v225 offset:28864\n"
  "v_mfma_f32_16x16x32_bf16 v[148:151], a[188:191], a[52:55], v[148:151]\n"
  "ds_read_b64_tr_b16 a[178:179], v225 offset:32448\n"
  "s_waitcnt lgkmcnt(6)\n"
  "v_mfma_f32_16x16x32_bf16 v[144:147], a[188:191], a[48:51], v[144:147]\n"
  "buffer_load_dwordx4 a[8:11], off, s[32:35], s44 offset:2048 sc1\n"
  "v_mfma_f32_16x16x32_bf16 v[144:147], a[184:187], a[32:35], v[144:147]\n"
  "v_mfma_f32_16x16x32_bf16 v[160:163], a[160:163], a[32:35], v[160:163]\n"
  "ds_read_b64_tr_b16 a[180:181], v225 offset:36032\n"
  "v_mfma_f32_16x16x32_bf16 v[160:163], a[164:167], a[48:51], v[160:163]\n"
  "ds_read_b64_tr_b16 a[182:183], v225 offset:39616\n"
  "v_mfma_f32_16x16x32_bf16 v[164:167], a[164:167], a[52:55], v[164:167]\n"
  "v_mfma_f32_16x16x32_bf16 v[164:167], a[160:163], a[36:39], v[164:167]\n"
  "v_mfma_f32_16x16x32_bf16 v[168:171], a[160:163], a[40:43], v[168:171]\n"
  "ds_read_b64_tr_b16 a[184:185], v225 offset:43200\n"
  "v_mfma_f32_16x16x32_bf16 v[168:171], a[164:167], a[56:59], v[168:171]\n"
  "ds_read_b64_tr_b16 a[186:187], v225 offset:46784\n"
  "s_waitcnt lgkmcnt(6)\n"
  "v_mfma_f32_16x16x32_bf16 v[172:175], a[164:167], a[60:63], v[172:175]\n"
  "buffer_load_dwordx4 a[12:15], off, s[32:35], s44 offset:3072 sc1\n"
  "v_mfma_f32_16x16x32_bf16 v[172:175], a[160:163], a[44:47], v[172:175]\n"
  "v_mfma_f32_16x16x32_bf16 v[188:191], a[168:171], a[44:47], v[188:191]\n"
  "ds_read_b64_tr_b16 a[188:189], v225 offset:50368\n"
  "s_add_u32 s44, s44, 4096\n"
  "v_mfma_f32_16x16x32_bf16 v[188:191], a[172:175], a[60:63], v[188:191]\n"
  "ds_read_b64_tr_b16 a[190:191], v225 offset:53952\n"
  "v_mfma_f32_16x16x32_bf16 v[184:187], a[172:175], a[56:59], v[184:187]\n"
  "v_mfma_f32_16x16x32_bf16 v[184:187], a[168:171], a[40:43], v[184:187]\n"
  "v_mfma_f32_16x16x32_bf16 v[180:183], a[168:171], a[36:39], v[180:183]\n"
  "s_waitcnt vmcnt(39)\n"
  "s_barrier\n"
  "v_mfma_f32_16x16x32_bf16 v[180:183], a[172:175], a[52:55], v[180:183]\n"
  "ds_read_b64_tr_b16 a[160:161], v226 offset:0\n"
  "v_mfma_f32_16x16x32_bf16 v[176:179], a[172:175], a[48:51], v[176:179]\n"
  "ds_read_b64_tr_b16 a[162:163], v226 offset:3584\n"
  "s_waitcnt lgkmcnt(6)\n"
  "v_mfma_f32_16x16x32_bf16 v[176:179], a[168:171], a[32:35], v[176:179]\n"
  "buffer_load_dwordx4 a[16:19], off, s[32:35], s44 offset:0 sc1\n"
  "v_mfma_f32_16x16x32_bf16 v[192:195], a[176:179], a[32:35], v[192:195]\n"
  "ds_read_b64_tr_b16 a[164:165], v226 offset:7168\n"
  "v_mfma_f32_16x16x32_bf16 v[192:195], a[180:183], a[48:51], v[192:195]\n"
  "ds_read_b64_tr_b16 a[166:167], v226 offset:10752\n"
  "v_mfma_f32_16x16x32_bf16 v[196:199], a[180:183], a[52:55], v[196:199]\n"
  "v_mfma_f32_16x16x32_bf16 v[196:199], a[176:179], a[36:39], v[196:199]\n"
  "v_mfma_f32_16x16x32_bf16 v[200:203], a[176:179], a[40:43], v[200:203]\n"
  "ds_read_b64_tr_b16 a[168:169], v226 offset:14336\n"
  "v_mfma_f32_16x16x32_bf16 v[200:203], a[180:183], a[56:59], v[200:203]\n"
  "ds_read_b64_tr_b16 a[170:171], v226 offset:17920\n"
  "s_waitcnt lgkmcnt(6)\n"
  "v_mfma_f32_16x16x32_bf16 v[204:207], a[180:183], a[60:63], v[204:207]\n"
  "buffer_load_dwordx4 a[20:23], off, s[32:35], s44 offset:1024 sc1\n"
  "v_mfma_f32_16x16x32_bf16 v[204:207], a[176:179], a[44:47], v[204:207]\n"
  "v_mfma_f32_16x16x32_bf16 v[220:223], a[184:187], a[44:47], v[220:223]\n"
  "ds_read_b64_tr_b16 a[172:173], v226 offset:21504\n"
  "v_mfma_f32_16x16x32_bf16 v[220:223], a[188:191], a[60:63], v[220:223]\n"
  "ds_read_b64_tr_b16 a[174:175], v226 offset:25088\n"
  "v_mfma_f32_16x16x32_bf16 v[216:219], a[188:191], a[56:59], v[216:219]\n"
  "buffer_load_dwordx4 a[24:27], off, s[32:35], s44 offset:2048 sc1\n"
  "v_mfma_f32_16x16x32_bf16 v[216:219], a[184:187], a[40:43], v[216:219]\n"
  "v_mfma_f32_16x16x32_bf16 v[212:215], a[184:187], a[36:39], v[212:215]\n"
  "ds_read_b64_tr_b16 a[176:177], v226 offset:32\n"
  "v_mfma_f32_16x16x32_bf16 v[212:215], a[188:191], a[52:55], v[212:215]\n"
  "ds_read_b64_tr_b16 a[178:179], v226 offset:3616\n"
  "s_waitcnt vmcnt(37) lgkmcnt(6)\n"
  "v_mfma_f32_16x16x32_bf16 v[208:211], a[188:191], a[48:51], v[208:211]\n"
  "v_mfma_f32_16x16x32_bf16 v[208:211], a[184:187], a[32:35], v[208:211]\n"
  "v_mfma_f32_16x16x32_bf16 v[0:3], a[160:163], a[64:67], v[0:3]\n"
  "ds_read_b64_tr_b16 a[180:181], v226 offset:7200\n"
  "s_add_u32 m0, s46, 0\n"
  "v_mfma_f32_16x16x32_bf16 v[0:3], a[164:167], a[80:83], v[0:3]\n"
  "ds_read_b64_tr_b16 a[182:183], v226 offset:10784\n"
  "v_mfma_f32_16x16x32_bf16 v[4:7], a[164:167], a[84:87], v[4:7]\n"
  "v_mfma_f32_16x16x32_bf16 v[4:7], a[160:163], a[68:71], v[4:7]\n"
  "v_mfma_f32_16x16x32_bf16 v[8:11], a[160:163], a[72:75], v[8:11]\n"
  "ds_read_b64_tr_b16 a[184:185], v226 offset:14368\n"
  "v_mfma_f32_16x16x32_bf16 v[8:11], a[164:167], a[88:91], v[8:11]\n"
  "ds_read_b64_tr_b16 a[186:187], v226 offset:17952\n"
  "s_waitcnt lgkmcnt(6)\n"
  "v_mfma_f32_16x16x32_bf16 v[12:15], a[164:167], a[92:95], v[12:15]\n"
  "buffer_load_dwordx4 v240, s[36:39], s45 offen offset:0 lds\n"
  "v_mfma_f32_16x16x32_bf16 v[12:15], a[160:163], a[76:79], v[12:15]\n"
  "v_mfma_f32_16x16x32_bf16 v[28:31], a[168:171], a[76:79], v[28:31]\n"
  "ds_read_b64_tr_b16 a[188:189], v226 offset:21536\n"
  "v_mfma_f32_16x16x32_bf16 v[28:31], a[172:175], a[92:95], v[28:31]\n"
  "ds_read_b64_tr_b16 a[190:191], v226 offset:25120\n"
  "v_mfma_f32_16x16x32_bf16 v[24:27], a[172:175], a[88:91], v[24:27]\n"
  "buffer_load_dwordx4 v241, s[36:39], s45 offen offset:1024 lds\n"
  "v_mfma_f32_16x16x32_bf16 v[24:27], a[168:171], a[72:75], v[24:27]\n"
  "v_mfma_f32_16x16x32_bf16 v[20:23], a[168:171], a[68:71], v[20:23]\n"
  "ds_read_b64_tr_b16 a[160:161], v226 offset:64\n"
  "v_mfma_f32_16x16x32_bf16 v[20:23], a[172:175], a[84:87], v[20:23]\n"
  "ds_read_b64_tr_b16 a[162:163], v226 offset:3648\n"
  "s_waitcnt lgkmcnt(6)\n"
  "v_mfma_f32_16x16x32_bf16 v[16:19], a[172:175], a[80:83], v[16:19]\n"
  "buffer_load_dwordx4 v242, s[36:39], s45 offen offset:2048 lds\n"
  "v_mfma_f32_16x16x32_bf16 v[16:19], a[168:171], a[64:67], v[16:19]\n"
  "v_mfma_f32_16x16x32_bf16 v[32:35], a[176:179], a[64:67], v[32:35]\n"
  "ds_read_b64_tr_b16 a[164:165], v226 offset:7232\n"
  "v_mfma_f32_16x16x32_bf16 v[32:35], a[180:183], a[80:83], v[32:35]\n"
  "ds_read_b64_tr_b16 a[166:167], v226 offset:10816\n"
  "v_mfma_f32_16x16x32_bf16 v[36:39], a[180:183], a[84:87], v[36:39]\n"
  "buffer_load_dwordx4 v243, s[36:39], s45 offen offset:3072 lds\n"
  "v_mfma_f32_16x16x32_bf16 v[36:39], a[176:179], a[68:71], v[36:39]\n"
  "v_mfma_f32_16x16x32_bf16 v[40:43], a[176:179], a[72:75], v[40:43]\n"
  "ds_read_b64_tr_b16 a[168:169], v226 offset:14400\n"
  "s_add_u32 m0, s46, 4096\n"
  "v_mfma_f32_16x16x32_bf16 v[40:43], a[180:183], a[88:91], v[40:43]\n"
  "ds_read_b64_tr_b16 a[170:171], v226 offset:17984\n"
  "s_waitcnt lgkmcnt(6)\n"
  "v_mfma_f32_16x16x32_bf16 v[44:47], a[180:183], a[92:95], v[44:47]\n"
  "v_mfma_f32_16x16x32_bf16 v[44:47], a[176:179], a[76:79], v[44:47]\n"
  "v_mfma_f32_16x16x32_bf16 v[60:63], a[184:187], a[76:79], v[60:63]\n"
  "ds_read_b64_tr_b16 a[172:173], v226 offset:21568\n"
  "v_mfma_f32_16x16x32_bf16 v[60:63], a[188:191], a[92:95], v[60:63]\n"
  "ds_read_b64_tr_b16 a[174:175], v226 offset:25152\n"
  "v_mfma_f32_16x16x32_bf16 v[56:59], a[188:191], a[88:91], v[56:59]\n"
  "v_mfma_f32_16x16x32_bf16 v[56:59], a[184:187], a[72:75], v[56:59]\n"
  "v_mfma_f32_16x16x32_bf16 v[52:55], a[184:187], a[68:71], v[52:55]\n"
  "ds_read_b64_tr_b16 a[176:177], v226 offset:96\n"
  "v_mfma_f32_16x16x32_bf16 v[52:55], a[188:191], a[84:87], v[52:55]\n"
  "ds_read_b64_tr_b16 a[178:179], v226 offset:3680\n"
  "s_waitcnt lgkmcnt(6)\n"
  "v_mfma_f32_16x16x32_bf16 v[48:51], a[188:191], a[80:83], v[48:51]\n"
  "buffer_load_dwordx4 v244, s[36:39], s45 offen offset:0 lds\n"
  "v_mfma_f32_16x16x32_bf16 v[48:51], a[184:187], a[64:67], v[48:51]\n"
  "v_mfma_f32_16x16x32_bf16 v[64:67], a[160:163], a[64:67], v[64:67]\n"
  "ds_read_b64_tr_b16 a[180:181], v226 offset:7264\n"
  "v_mfma_f32_16x16x32_bf16 v[64:67], a[164:167], a[80:83], v[64:67]\n"
  "ds_read_b64_tr_b16 a[182:183], v226 offset:10848\n"
  "v_mfma_f32_16x16x32_bf16 v[68:71], a[164:167], a[84:87], v[68:71]\n"
  "buffer_load_dwordx4 v245, s[36:39], s45 offen offset:1024 lds\n"
  "v_mfma_f32_16x16x32_bf16 v[68:71], a[160:163], a[68:71], v[68:71]\n"
  "v_mfma_f32_16x16x32_bf16 v[72:75], a[160:163], a[72:75], v[72:75]\n"
  "ds_read_b64_tr_b16 a[184:185], v226 offset:14432\n"
  "v_mfma_f32_16x16x32_bf16 v[72:75], a[164:167], a[88:91], v[72:75]\n"
  "ds_read_b64_tr_b16 a[186:187], v226 offset:18016\n"
  "s_waitcnt lgkmcnt(6)\n"
  "v_mfma_f32_16x16x32_bf16 v[76:79], a[164:167], a[92:95], v[76:79]\n"
  "buffer_load_dwordx4 v246, s[36:39], s45 offen offset:2048 lds\n"
  "v_mfma_f32_16x16x32_bf16 v[76:79], a[160:163], a[76:79], v[76:79]\n"
  "v_mfma_f32_16x16x32_bf16 v[92:95], a[168:171], a[76:79], v[92:95]\n"
  "ds_read_b64_tr_b16 a[188:189], v226 offset:21600\n"
  "s_add_u32 s45, s45, 917504\n"
  "v_mfma_f32_16x16x32_bf16 v[92:95], a[172:175], a[92:95], v[92:95]\n"
  "ds_read_b64_tr_b16 a[190:191], v226 offset:25184\n"
  "v_mfma_f32_16x16x32_bf16 v[88:91], a[172:175], a[88:91], v[88:91]\n"
  "v_mfma_f32_16x16x32_bf16 v[88:91], a[168:171], a[72:75], v[88:91]\n"
  "v_mfma_f32_16x16x32_bf16 v[84:87], a[168:171], a[68:71], v[84:87]\n"
  "ds_read_b64_tr_b16 a[160:161], v226 offset:128\n"
  "v_mfma_f32_16x16x32_bf16 v[84:87], a[172:175], a[84:87], v[84:87]\n"
  "ds_read_b64_tr_b16 a[162:163], v226 offset:3712\n"
  "s_waitcnt lgkmcnt(6)\n"
  "v_mfma_f32_16x16x32_bf16 v[80:83], a[172:175], a[80:83], v[80:83]\n"
  "buffer_load_dwordx4 a[28:31], off, s[32:35], s44 offset:3072 sc1\n"
  "v_mfma_f32_16x16x32_bf16 v[80:83], a[168:171], a[64:67], v[80:83]\n"
  "v_mfma_f32_16x16x32_bf16 v[96:99], a[176:179], a[64:67], v[96:99]\n"
  "ds_read_b64_tr_b16 a[164:165], v226 offset:7296\n"
  "s_add_u32 s44, s44, 4096\n"
  "v_mfma_f32_16x16x32_bf16 v[96:99], a[180:183], a[80:83], v[96:99]\n"
  "ds_read_b64_tr_b16 a[166:167], v226 offset:10880\n"
  "v_mfma_f32_16x16x32_bf16 v[100:103], a[180:183], a[84:87], v[100:103]\n"
  "v_mfma_f32_16x16x32_bf16 v[100:103], a[176:179], a[68:71], v[100:103]\n"
  "v_mfma_f32_16x16x32_bf16 v[104:107], a[176:179], a[72:75], v[104:107]\n"
  "ds_read_b64_tr_b16 a[168:169], v226 offset:14464\n"
  "v_mfma_f32_16x16x32_bf16 v[104:107], a[180:183], a[88:91], v[104:107]\n"
  "ds_read_b64_tr_b16 a[170:171], v226 offset:18048\n"
  "s_waitcnt lgkmcnt(6)\n"
  "v_mfma_f32_16x16x32_bf16 v[108:111], a[180:183], a[92:95], v[108:111]\n"
  "v_mfma_f32_16x16x32_bf16 v[108:111], a[176:179], a[76:79], v[108:111]\n"
  "v_mfma_f32_16x16x32_bf16 v[124:127], a[184:187], a[76:79], v[124:127]\n"
  "ds_read_b64_tr_b16 a[172:173], v226 offset:21632\n"
  "v_mfma_f32_16x16x32_bf16 v[124:127], a[188:191], a[92:95], v[124:127]\n"
  "ds_read_b64_tr_b16 a[174:175], v226 offset:25216\n"
  "v_mfma_f32_16x16x32_bf16 v[120:123], a[188:191], a[88:91], v[120:123]\n"
  "s_add_u32 s60, s30, 20971520\n"
  "s_addc_u32 s61, s31, 0\n"
  "s_pack_ll_b32_b16 s61, s61, 16\n"
  "v_mfma_f32_16x16x32_bf16 v[120:123], a[184:187], a[72:75], v[120:123]\n"
  "s_mov_b32 s62, 0\n"
  "s_mov_b32 s63, 0x800000\n"
  "s_lshl_b32 s8, s87, 17\n"
  "v_mfma_f32_16x16x32_bf16 v[116:119], a[184:187], a[68:71], v[116:119]\n"
  "ds_read_b64_tr_b16 a[176:177], v226 offset:160\n"
  "s_lshr_b32 s9, s88, 1\n"
  "v_mfma_f32_16x16x32_bf16 v[116:119], a[188:191], a[84:87], v[116:119]\n"
  "ds_read_b64_tr_b16 a[178:179], v226 offset:3744\n"
  "s_waitcnt lgkmcnt(6)\n"
  "v_mfma_f32_16x16x32_bf16 v[112:115], a[188:191], a[80:83], v[112:115]\n"
  "s_lshl_b32 s9, s9, 12\n"
  "s_add_u32 s8, s8, s9\n"
  "s_and_b32 s9, s88, 1\n"
  "v_mfma_f32_16x16x32_bf16 v[112:115], a[184:187], a[64:67], v[112:115]\n"
  "s_lshl_b32 s9, s9, 16\n"
  "s_add_u32 s48, s8, s9\n"
  "s_lshl_b32 s49, s87, 13\n"
  "v_mfma_f32_16x16x32_bf16 v[128:131], a[160:163], a[64:67], v[128:131]\n"
  "ds_read_b64_tr_b16 a[180:181], v226 offset:7328\n"
  "s_add_u32 s49, s49, 655360\n"
  "v_mfma_f32_16x16x32_bf16 v[128:131], a[164:167], a[80:83], v[128:131]\n"
  "ds_read_b64_tr_b16 a[182:183], v226 offset:10912\n"
  "s_add_u32 s50, s49, 4096\n"
  "v_mfma_f32_16x16x32_bf16 v[132:135], a[164:167], a[84:87], v[132:135]\n"
  "s_mul_i32 s8, s87, 2048\n"
  "s_mov_b32 s51, 1851392\n"
  "v_cmp_gt_u32 vcc, 32, v255\n"
  "v_mfma_f32_16x16x32_bf16 v[132:135], a[160:163], a[68:71], v[132:135]\n"
  "v_lshlrev_b32 v247, 5, v227\n"
  "v_add_u32 v227, s8, v227\n"
  "v_mfma_f32_16x16x32_bf16 v[136:139], a[160:163], a[72:75], v[136:139]\n"
  "ds_read_b64_tr_b16 a[184:185], v226 offset:14496\n"
  "v_cndmask_b32 v227, v247, v227, vcc\n"
  "v_mfma_f32_16x16x32_bf16 v[136:139], a[164:167], a[88:91], v[136:139]\n"
  "ds_read_b64_tr_b16 a[186:187], v226 offset:18080\n"
  "s_waitcnt lgkmcnt(6)\n"
  "v_mfma_f32_16x16x32_bf16 v[140:143], a[164:167], a[92:95], v[140:143]\n"
  "buffer_load_dwordx2 v[238:239], v227, s[52:55], s51 offen sc1\n"
  "v_mfma_f32_16x16x32_bf16 v[140:143], a[160:163], a[76:79], v[140:143]\n"
  "v_mfma_f32_16x16x32_bf16 v[156:159], a[168:171], a[76:79], v[156:159]\n"
  "ds_read_b64_tr_b16 a[188:189], v226 offset:21664\n"
  "v_mfma_f32_16x16x32_bf16 v[156:159], a[172:175], a[92:95], v[156:159]\n"
  "ds_read_b64_tr_b16 a[190:191], v226 offset:25248\n"
  "v_mfma_f32_16x16x32_bf16 v[152:155], a[172:175], a[88:91], v[152:155]\n"
  "v_mfma_f32_16x16x32_bf16 v[152:155], a[168:171], a[72:75], v[152:155]\n"
  "v_mfma_f32_16x16x32_bf16 v[148:151], a[168:171], a[68:71], v[148:151]\n"
  "ds_read_b64_tr_b16 a[160:161], v226 offset:192\n"
  "v_mfma_f32_16x16x32_bf16 v[148:151], a[172:175], a[84:87], v[148:151]\n"
  "ds_read_b64_tr_b16 a[162:163], v226 offset:3776\n"
  "s_waitcnt lgkmcnt(6)\n"
  "v_mfma_f32_16x16x32_bf16 v[144:147], a[172:175], a[80:83], v[144:147]\n"
  "v_mfma_f32_16x16x32_bf16 v[144:147], a[168:171], a[64:67], v[144:147]\n"
  "v_mfma_f32_16x16x32_bf16 v[160:163], a[176:179], a[64:67], v[160:163]\n"
  "ds_read_b64_tr_b16 a[164:165], v226 offset:7360\n"
  "v_mfma_f32_16x16x32_bf16 v[160:163], a[180:183], a[80:83], v[160:163]\n"
  "ds_read_b64_tr_b16 a[166:167], v226 offset:10944\n"
  "v_mfma_f32_16x16x32_bf16 v[164:167], a[180:183], a[84:87], v[164:167]\n"
  "v_mfma_f32_16x16x32_bf16 v[164:167], a[176:179], a[68:71], v[164:167]\n"
  "v_mfma_f32_16x16x32_bf16 v[168:171], a[176:179], a[72:75], v[168:171]\n"
  "ds_read_b64_tr_b16 a[168:169], v226 offset:14528\n"
  "v_mfma_f32_16x16x32_bf16 v[168:171], a[180:183], a[88:91], v[168:171]\n"
  "ds_read_b64_tr_b16 a[170:171], v226 offset:18112\n"
  "s_waitcnt lgkmcnt(6)\n"
  "v_mfma_f32_16x16x32_bf16 v[172:175], a[180:183], a[92:95], v[172:175]\n"
  "v_mfma_f32_16x16x32_bf16 v[172:175], a[176:179], a[76:79], v[172:175]\n"
  "v_mfma_f32_16x16x32_bf16 v[188:191], a[184:187], a[76:79], v[188:191]\n"
  "ds_read_b64_tr_b16 a[172:173], v226 offset:21696\n"
  "v_mfma_f32_16x16x32_bf16 v[188:191], a[188:191], a[92:95], v[188:191]\n"
  "ds_read_b64_tr_b16 a[174:175], v226 offset:25280\n"
  "v_mfma_f32_16x16x32_bf16 v[184:187], a[188:191], a[88:91], v[184:187]\n"
  "v_mfma_f32_16x16x32_bf16 v[184:187], a[184:187], a[72:75], v[184:187]\n"
  "v_mfma_f32_16x16x32_bf16 v[180:183], a[184:187], a[68:71], v[180:183]\n"
  "s_waitcnt vmcnt(2)\n"
  "s_barrier\n"
  "v_mfma_f32_16x16x32_bf16 v[180:183], a[188:191], a[84:87], v[180:183]\n"
  "ds_read_b64_tr_b16 a[176:177], v224 offset:28672\n"
  "v_mfma_f32_16x16x32_bf16 v[176:179], a[188:191], a[80:83], v[176:179]\n"
  "ds_read_b64_tr_b16 a[178:179], v224 offset:32256\n"
  "s_waitcnt lgkmcnt(6)\n"
  "v_mfma_f32_16x16x32_bf16 v[176:179], a[184:187], a[64:67], v[176:179]\n"
  "v_mfma_f32_16x16x32_bf16 v[192:195], a[160:163], a[64:67], v[192:195]\n"
  "ds_read_b64_tr_b16 a[180:181], v224 offset:35840\n"
  "v_mfma_f32_16x16x32_bf16 v[192:195], a[164:167], a[80:83], v[192:195]\n"
  "ds_read_b64_tr_b16 a[182:183], v224 offset:39424\n"
  "v_mfma_f32_16x16x32_bf16 v[196:199], a[164:167], a[84:87], v[196:199]\n"
  "v_mfma_f32_16x16x32_bf16 v[196:199], a[160:163], a[68:71], v[196:199]\n"
  "v_mfma_f32_16x16x32_bf16 v[200:203], a[160:163], a[72:75], v[200:203]\n"
  "ds_read_b64_tr_b16 a[184:185], v224 offset:43008\n"
  "v_mfma_f32_16x16x32_bf16 v[200:203], a[164:167], a[88:91], v[200:203]\n"
  "ds_read_b64_tr_b16 a[186:187], v224 offset:46592\n"
  "s_waitcnt lgkmcnt(6)\n"
  "v_mfma_f32_16x16x32_bf16 v[204:207], a[164:167], a[92:95], v[204:207]\n"
  "v_mfma_f32_16x16x32_bf16 v[204:207], a[160:163], a[76:79], v[204:207]\n"
  "v_mfma_f32_16x16x32_bf16 v[220:223], a[168:171], a[76:79], v[220:223]\n"
  "ds_read_b64_tr_b16 a[188:189], v224 offset:50176\n"
  "v_mfma_f32_16x16x32_bf16 v[220:223], a[172:175], a[92:95], v[220:223]\n"
  "ds_read_b64_tr_b16 a[190:191], v224 offset:53760\n"
  "v_mfma_f32_16x16x32_bf16 v[216:219], a[172:175], a[88:91], v[216:219]\n"
  "v_mfma_f32_16x16x32_bf16 v[216:219], a[168:171], a[72:75], v[216:219]\n"
  "v_mfma_f32_16x16x32_bf16 v[212:215], a[168:171], a[68:71], v[212:215]\n"
  "ds_read_b64_tr_b16 a[160:161], v225 offset:0\n"
  "v_mfma_f32_16x16x32_bf16 v[212:215], a[172:175], a[84:87], v[212:215]\n"
  "ds_read_b64_tr_b16 a[162:163], v225 offset:3584\n"
  "s_waitcnt lgkmcnt(6)\n"
  "v_mfma_f32_16x16x32_bf16 v[208:211], a[172:175], a[80:83], v[208:211]\n"
  "v_mfma_f32_16x16x32_bf16 v[208:211], a[168:171], a[64:67], v[208:211]\n"
  "v_mfma_f32_16x16x32_bf16 v[0:3], a[176:179], a[96:99], v[0:3]\n"
  "ds_read_b64_tr_b16 a[164:165], v225 offset:7168\n"
  "v_mfma_f32_16x16x32_bf16 v[0:3], a[180:183], a[112:115], v[0:3]\n"
  "ds_read_b64_tr_b16 a[166:167], v225 offset:10752\n"
  "v_mfma_f32_16x16x32_bf16 v[4:7], a[180:183], a[116:119], v[4:7]\n"
  "v_mfma_f32_16x16x32_bf16 v[4:7], a[176:179], a[100:103], v[4:7]\n"
  "v_mfma_f32_16x16x32_bf16 v[8:11], a[176:179], a[104:107], v[8:11]\n"
  "ds_read_b64_tr_b16 a[168:169], v225 offset:14336\n"
  "s_barrier\n"
  "v_mfma_f32_16x16x32_bf16 v[8:11], a[180:183], a[120:123], v[8:11]\n"
  "ds_read_b64_tr_b16 a[170:171], v225 offset:17920\n"
  "s_waitcnt lgkmcnt(6)\n"
  "v_mfma_f32_16x16x32_bf16 v[12:15], a[180:183], a[124:127], v[12:15]\n"
  "s_add_u32 m0, s82, 98304\n"
  "v_mfma_f32_16x16x32_bf16 v[12:15], a[176:179], a[108:111], v[12:15]\n"
  "buffer_load_dwordx4 v228, s[76:79], s81 offen offset:0 lds\n"
  "v_mfma_f32_16x16x32_bf16 v[28:31], a[184:187], a[108:111], v[28:31]\n"
  "ds_read_b64_tr_b16 a[172:173], v225 offset:21504\n"
  "v_mfma_f32_16x16x32_bf16 v[28:31], a[188:191], a[124:127], v[28:31]\n"
  "ds_read_b64_tr_b16 a[174:175], v225 offset:25088\n"
  "v_mfma_f32_16x16x32_bf16 v[24:27], a[188:191], a[120:123], v[24:27]\n"
  "buffer_load_dwordx4 v229, s[76:79], s81 offen offset:1024 lds\n"
  "v_mfma_f32_16x16x32_bf16 v[24:27], a[184:187], a[104:107], v[24:27]\n"
  "v_mfma_f32_16x16x32_bf16 v[20:23], a[184:187], a[100:103], v[20:23]\n"
  "ds_read_b64_tr_b16 a[176:177], v224 offset:0\n"
  "v_mfma_f32_16x16x32_bf16 v[20:23], a[188:191], a[116:119], v[20:23]\n"
  "ds_read_b64_tr_b16 a[178:179], v224 offset:3584\n"
  "s_waitcnt lgkmcnt(6)\n"
  "v_mfma_f32_16x16x32_bf16 v[16:19], a[188:191], a[112:115], v[16:19]\n"
  "buffer_load_dwordx4 v230, s[76:79], s81 offen offset:2048 lds\n"
  "v_mfma_f32_16x16x32_bf16 v[16:19], a[184:187], a[96:99], v[16:19]\n"
  "v_mfma_f32_16x16x32_bf16 v[0:3], a[160:163], a[128:131], v[0:3]\n"
  "ds_read_b64_tr_b16 a[180:181], v224 offset:7168\n"
  "v_mfma_f32_16x16x32_bf16 v[0:3], a[164:167], a[144:147], v[0:3]\n"
  "ds_read_b64_tr_b16 a[182:183], v224 offset:10752\n"
  "v_mfma_f32_16x16x32_bf16 v[4:7], a[164:167], a[148:151], v[4:7]\n"
  "buffer_load_dwordx4 v231, s[76:79], s81 offen offset:3072 lds\n"
  "v_mfma_f32_16x16x32_bf16 v[4:7], a[160:163], a[132:135], v[4:7]\n"
  "s_add_u32 s81, s81, 917504\n"
  "s_add_u32 m0, s82, 114688\n"
  "v_mfma_f32_16x16x32_bf16 v[8:11], a[160:163], a[136:139], v[8:11]\n"
  "ds_read_b64_tr_b16 a[184:185], v224 offset:14336\n"
  "v_mfma_f32_16x16x32_bf16 v[8:11], a[164:167], a[152:155], v[8:11]\n"
  "ds_read_b64_tr_b16 a[186:187], v224 offset:17920\n"
  "s_waitcnt lgkmcnt(6)\n"
  "v_mfma_f32_16x16x32_bf16 v[12:15], a[164:167], a[156:159], v[12:15]\n"
  "buffer_load_dwordx4 v228, s[76:79], s81 offen offset:0 lds\n"
  "v_mfma_f32_16x16x32_bf16 v[12:15], a[160:163], a[140:143], v[12:15]\n"
  "v_mfma_f32_16x16x32_bf16 v[28:31], a[168:171], a[140:143], v[28:31]\n"
  "ds_read_b64_tr_b16 a[188:189], v224 offset:21504\n"
  "v_mfma_f32_16x16x32_bf16 v[28:31], a[172:175], a[156:159], v[28:31]\n"
  "ds_read_b64_tr_b16 a[190:191], v224 offset:25088\n"
  "v_mfma_f32_16x16x32_bf16 v[24:27], a[172:175], a[152:155], v[24:27]\n"
  "buffer_load_dwordx4 v229, s[76:79], s81 offen offset:1024 lds\n"
  "v_mfma_f32_16x16x32_bf16 v[24:27], a[168:171], a[136:139], v[24:27]\n"
  "v_mfma_f32_16x16x32_bf16 v[20:23], a[168:171], a[132:135], v[20:23]\n"
  "ds_read_b64_tr_b16 a[160:161], v224 offset:28704\n"
  "v_mfma_f32_16x16x32_bf16 v[20:23], a[172:175], a[148:151], v[20:23]\n"
  "ds_read_b64_tr_b16 a[162:163], v224 offset:32288\n"
  "s_waitcnt vmcnt(7) lgkmcnt(6)\n"
  "v_mfma_f32_16x16x32_bf16 v[16:19], a[172:175], a[144:147], v[16:19]\n"
  "buffer_load_dwordx4 v230, s[76:79], s81 offen offset:2048 lds\n"
  "v_mfma_f32_16x16x32_bf16 v[16:19], a[168:171], a[128:131], v[16:19]\n"
  "v_mfma_f32_16x16x32_bf16 v[0:3], a[176:179], a[0:3], v[0:3]\n"
  "ds_read_b64_tr_b16 a[164:165], v224 offset:35872\n"
  "v_mfma_f32_16x16x32_bf16 v[0:3], a[180:183], a[16:19], v[0:3]\n"
  "ds_read_b64_tr_b16 a[166:167], v224 offset:39456\n"
  "v_mfma_f32_16x16x32_bf16 v[4:7], a[180:183], a[20:23], v[4:7]\n"
  "buffer_load_dwordx4 v231, s[76:79], s81 offen offset:3072 lds\n"
  "v_mfma_f32_16x16x32_bf16 v[4:7], a[176:179], a[4:7], v[4:7]\n"
  "s_mul_i32 s81, s16, 56\n"
  "s_add_u32 s81, s81, s86\n"
  "s_add_u32 m0, s82, 131072\n"
  "v_mfma_f32_16x16x32_bf16 v[8:11], a[176:179], a[8:11], v[8:11]\n"
  "ds_read_b64_tr_b16 a[168:169], v224 offset:43040\n"
  "v_mfma_f32_16x16x32_bf16 v[8:11], a[180:183], a[24:27], v[8:11]\n"
  "ds_read_b64_tr_b16 a[170:171], v224 offset:46624\n"
  "s_waitcnt lgkmcnt(6)\n"
  "v_mfma_f32_16x16x32_bf16 v[12:15], a[180:183], a[28:31], v[12:15]\n"
  "v_mfma_f32_16x16x32_bf16 v[12:15], a[176:179], a[12:15], v[12:15]\n"
  "v_mfma_f32_16x16x32_bf16 v[28:31], a[184:187], a[12:15], v[28:31]\n"
  "ds_read_b64_tr_b16 a[172:173], v224 offset:50208\n"
  "v_mfma_f32_16x16x32_bf16 v[28:31], a[188:191], a[28:31], v[28:31]\n"
  "ds_read_b64_tr_b16 a[174:175], v224 offset:53792\n"
  "v_mfma_f32_16x16x32_bf16 v[24:27], a[188:191], a[24:27], v[24:27]\n"
  "buffer_load_dwordx4 v228, s[76:79], s81 offen offset:0 lds\n"
  "v_mfma_f32_16x16x32_bf16 v[24:27], a[184:187], a[8:11], v[24:27]\n"
  "v_mfma_f32_16x16x32_bf16 v[20:23], a[184:187], a[4:7], v[20:23]\n"
  "ds_read_b64_tr_b16 a[176:177], v225 offset:32\n"
  "v_mfma_f32_16x16x32_bf16 v[20:23], a[188:191], a[20:23], v[20:23]\n"
  "ds_read_b64_tr_b16 a[178:179], v225 offset:3616\n"
  "s_waitcnt lgkmcnt(6)\n"
  "v_mfma_f32_16x16x32_bf16 v[16:19], a[188:191], a[16:19], v[16:19]\n"
  "buffer_load_dwordx4 v229, s[76:79], s81 offen offset:1024 lds\n"
  "v_mfma_f32_16x16x32_bf16 v[16:19], a[184:187], a[0:3], v[16:19]\n"
  "v_mfma_f32_16x16x32_bf16 v[32:35], a[160:163], a[96:99], v[32:35]\n"
  "ds_read_b64_tr_b16 a[180:181], v225 offset:7200\n"
  "v_mfma_f32_16x16x32_bf16 v[32:35], a[164:167], a[112:115], v[32:35]\n"
  "ds_read_b64_tr_b16 a[182:183], v225 offset:10784\n"
  "v_mfma_f32_16x16x32_bf16 v[36:39], a[164:167], a[116:119], v[36:39]\n"
  "buffer_load_dwordx4 v230, s[76:79], s81 offen offset:2048 lds\n"
  "v_mfma_f32_16x16x32_bf16 v[36:39], a[160:163], a[100:103], v[36:39]\n"
  "v_mfma_f32_16x16x32_bf16 v[40:43], a[160:163], a[104:107], v[40:43]\n"
  "ds_read_b64_tr_b16 a[184:185], v225 offset:14368\n"
  "v_max_f32 v0, 0, v0\n"
  "v_mfma_f32_16x16x32_bf16 v[40:43], a[164:167], a[120:123], v[40:43]\n"
  "ds_read_b64_tr_b16 a[186:187], v225 offset:17952\n"
  "s_waitcnt lgkmcnt(6)\n"
  "v_mfma_f32_16x16x32_bf16 v[44:47], a[164:167], a[124:127], v[44:47]\n"
  "buffer_load_dwordx4 v231, s[76:79], s81 offen offset:3072 lds\n"
  "v_mfma_f32_16x16x32_bf16 v[44:47], a[160:163], a[108:111], v[44:47]\n"
  "s_mul_i32 s81, s17, 56\n"
  "s_add_u32 s81, s81, s86\n"
  "s_add_u32 m0, s82, 147456\n"
  "v_mfma_f32_16x16x32_bf16 v[60:63], a[168:171], a[108:111], v[60:63]\n"
  "ds_read_b64_tr_b16 a[188:189], v225 offset:21536\n"
  "v_max_f32 v1, 0, v1\n"
  "v_mfma_f32_16x16x32_bf16 v[60:63], a[172:175], a[124:127], v[60:63]\n"
  "ds_read_b64_tr_b16 a[190:191], v225 offset:25120\n"
  "v_max_f32 v2, 0, v2\n"
  "v_mfma_f32_16x16x32_bf16 v[56:59], a[172:175], a[120:123], v[56:59]\n"
  "v_max_f32 v3, 0, v3\n"
  "v_mul_f32 v0, v0, v16\n"
  "v_mfma_f32_16x16x32_bf16 v[56:59], a[168:171], a[104:107], v[56:59]\n"
  "v_mul_f32 v1, v1, v17\n"
  "v_mul_f32 v2, v2, v18\n"
  "v_mfma_f32_16x16x32_bf16 v[52:55], a[168:171], a[100:103], v[52:55]\n"
  "ds_read_b64_tr_b16 a[160:161], v224 offset:32\n"
  "v_mul_f32 v3, v3, v19\n"
  "v_mfma_f32_16x16x32_bf16 v[52:55], a[172:175], a[116:119], v[52:55]\n"
  "ds_read_b64_tr_b16 a[162:163], v224 offset:3616\n"
  "s_waitcnt lgkmcnt(6)\n"
  "v_mfma_f32_16x16x32_bf16 v[48:51], a[172:175], a[112:115], v[48:51]\n"
  "buffer_load_dwordx4 v228, s[76:79], s81 offen offset:0 lds\n"
  "v_mfma_f32_16x16x32_bf16 v[48:51], a[168:171], a[96:99], v[48:51]\n"
  "v_cvt_pk_bf16_f32 v16, v0, v1\n"
  "v_cvt_pk_bf16_f32 v17, v2, v3\n"
  "v_mfma_f32_16x16x32_bf16 v[32:35], a[176:179], a[128:131], v[32:35]\n"
  "ds_read_b64_tr_b16 a[164:165], v224 offset:7200\n"
  "v_max_f32 v4, 0, v4\n"
  "v_mfma_f32_16x16x32_bf16 v[32:35], a[180:183], a[144:147], v[32:35]\n"
  "ds_read_b64_tr_b16 a[166:167], v224 offset:10784\n"
  "v_max_f32 v5, 0, v5\n"
  "v_mfma_f32_16x16x32_bf16 v[36:39], a[180:183], a[148:151], v[36:39]\n"
  "buffer_load_dwordx4 v229, s[76:79], s81 offen offset:1024 lds\n"
  "v_mfma_f32_16x16x32_bf16 v[36:39], a[176:179], a[132:135], v[36:39]\n"
  "v_max_f32 v6, 0, v6\n"
  "v_max_f32 v7, 0, v7\n"
  "v_mfma_f32_16x16x32_bf16 v[40:43], a[176:179], a[136:139], v[40:43]\n"
  "ds_read_b64_tr_b16 a[168:169], v224 offset:14368\n"
  "v_mul_f32 v4, v4, v20\n"
  "v_mfma_f32_16x16x32_bf16 v[40:43], a[180:183], a[152:155], v[40:43]\n"
  "ds_read_b64_tr_b16 a[170:171], v224 offset:17952\n"
  "s_waitcnt lgkmcnt(6)\n"
  "v_mfma_f32_16x16x32_bf16 v[44:47], a[180:183], a[156:159], v[44:47]\n"
  "buffer_load_dwordx4 v230, s[76:79], s81 offen offset:2048 lds\n"
  "v_mfma_f32_16x16x32_bf16 v[44:47], a[176:179], a[140:143], v[44:47]\n"
  "v_mul_f32 v5, v5, v21\n"
  "v_mul_f32 v6, v6, v22\n"
  "v_mfma_f32_16x16x32_bf16 v[60:63], a[184:187], a[140:143], v[60:63]\n"
  "ds_read_b64_tr_b16 a[172:173], v224 offset:21536\n"
  "v_mul_f32 v7, v7, v23\n"
  "v_mfma_f32_16x16x32_bf16 v[60:63], a[188:191], a[156:159], v[60:63]\n"
  "ds_read_b64_tr_b16 a[174:175], v224 offset:25120\n"
  "v_cvt_pk_bf16_f32 v18, v4, v5\n"
  "v_mfma_f32_16x16x32_bf16 v[56:59], a[188:191], a[152:155], v[56:59]\n"
  "buffer_load_dwordx4 v231, s[76:79], s81 offen offset:3072 lds\n"
  "v_mfma_f32_16x16x32_bf16 v[56:59], a[184:187], a[136:139], v[56:59]\n"
  "s_mul_i32 s81, s18, 56\n"
  "s_add_u32 s81, s81, s86\n"
  "v_cvt_pk_bf16_f32 v19, v6, v7\n"
  "v_mfma_f32_16x16x32_bf16 v[52:55], a[184:187], a[132:135], v[52:55]\n"
  "ds_read_b64_tr_b16 a[176:177], v224 offset:28736\n"
  "v_max_f32 v8, 0, v8\n"
  "v_mfma_f32_16x16x32_bf16 v[52:55], a[188:191], a[148:151], v[52:55]\n"
  "ds_read_b64_tr_b16 a[178:179], v224 offset:32320\n"
  "s_waitcnt lgkmcnt(6)\n"
  "v_mfma_f32_16x16x32_bf16 v[48:51], a[188:191], a[144:147], v[48:51]\n"
  "buffer_store_dwordx4 v[16:19], off, s[40:43], s56 offset:0 sc1\n"
  "v_mfma_f32_16x16x32_bf16 v[48:51], a[184:187], a[128:131], v[48:51]\n"
  "s_waitcnt vmcnt(17)\n"
  "L_poll6_%=:\n"
  "v_cmp_ne_u64 vcc, s[10:11], v[238:239]\n"
  "s_cbranch_vccz L_ok6_%=\n"
  "s_sub_u32 s99, s99, 1\n"
  "s_cbranch_scc1 L_out6_%=\n"
  "buffer_load_dwordx2 v[238:239], v227, s[52:55], s51 offen sc1\n"
  "s_waitcnt vmcnt(0)\n"
  "s_branch L_poll6_%=\n"
  "L_out6_%=:\n"
  "s_mov_b32 s99, 0\n"
  "L_ok6_%=:\n"
  "v_mfma_f32_16x16x32_bf16 v[32:35], a[160:163], a[0:3], v[32:35]\n"
  "ds_read_b64_tr_b16 a[180:181], v224 offset:35904\n"
  "v_max_f32 v9, 0, v9\n"
  "v_mfma_f32_16x16x32_bf16 v[32:35], a[164:167], a[16:19], v[32:35]\n"
  "ds_read_b64_tr_b16 a[182:183], v224 offset:39488\n"
  "v_max_f32 v10, 0, v10\n"
  "v_mfma_f32_16x16x32_bf16 v[36:39], a[164:167], a[20:23], v[36:39]\n"
  "v_max_f32 v11, 0, v11\n"
  "v_mul_f32 v8, v8, v24\n"
  "v_mfma_f32_16x16x32_bf16 v[36:39], a[160:163], a[4:7], v[36:39]\n"
  "v_mul_f32 v9, v9, v25\n"
  "v_mul_f32 v10, v10, v26\n"
  "v_mfma_f32_16x16x32_bf16 v[40:43], a[160:163], a[8:11], v[40:43]\n"
  "ds_read_b64_tr_b16 a[184:185], v224 offset:43072\n"
  "v_mul_f32 v11, v11, v27\n"
  "v_mfma_f32_16x16x32_bf16 v[40:43], a[164:167], a[24:27], v[40:43]\n"
  "ds_read_b64_tr_b16 a[186:187], v224 offset:46656\n"
  "s_waitcnt lgkmcnt(6)\n"
  "v_mfma_f32_16x16x32_bf16 v[44:47], a[164:167], a[28:31], v[44:47]\n"
  "v_cvt_pk_bf16_f32 v24, v8, v9\n"
  "v_cvt_pk_bf16_f32 v25, v10, v11\n"
  "v_mfma_f32_16x16x32_bf16 v[44:47], a[160:163], a[12:15], v[44:47]\n"
  "v_max_f32 v12, 0, v12\n"
  "v_max_f32 v13, 0, v13\n"
  "v_mfma_f32_16x16x32_bf16 v[60:63], a[168:171], a[12:15], v[60:63]\n"
  "ds_read_b64_tr_b16 a[188:189], v224 offset:50240\n"
  "v_max_f32 v14, 0, v14\n"
  "v_mfma_f32_16x16x32_bf16 v[60:63], a[172:175], a[28:31], v[60:63]\n"
  "ds_read_b64_tr_b16 a[190:191], v224 offset:53824\n"
  "v_max_f32 v15, 0, v15\n"
  "v_mfma_f32_16x16x32_bf16 v[56:59], a[172:175], a[24:27], v[56:59]\n"
  "v_mul_f32 v12, v12, v28\n"
  "v_mul_f32 v13, v13, v29\n"
  "v_mfma_f32_16x16x32_bf16 v[56:59], a[168:171], a[8:11], v[56:59]\n"
  "v_mul_f32 v14, v14, v30\n"
  "v_mul_f32 v15, v15, v31\n"
  "v_mfma_f32_16x16x32_bf16 v[52:55], a[168:171], a[4:7], v[52:55]\n"
  "ds_read_b64_tr_b16 a[160:161], v225 offset:64\n"
  "v_cvt_pk_bf16_f32 v26, v12, v13\n"
  "v_mfma_f32_16x16x32_bf16 v[52:55], a[172:175], a[20:23], v[52:55]\n"
  "ds_read_b64_tr_b16 a[162:163], v225 offset:3648\n"
  "s_waitcnt lgkmcnt(6)\n"
  "v_mfma_f32_16x16x32_bf16 v[48:51], a[172:175], a[16:19], v[48:51]\n"
  "buffer_load_dwordx4 a[192:195], off, s[60:63], s48 offset:0 sc1\n"
  "v_mfma_f32_16x16x32_bf16 v[48:51], a[168:171], a[0:3], v[48:51]\n"
  "v_cvt_pk_bf16_f32 v27, v14, v15\n"
  "v_mfma_f32_16x16x32_bf16 v[64:67], a[176:179], a[96:99], v[64:67]\n"
  "ds_read_b64_tr_b16 a[164:165], v225 offset:7232\n"
  "v_mfma_f32_16x16x32_bf16 v[64:67], a[180:183], a[112:115], v[64:67]\n"
  "ds_read_b64_tr_b16 a[166:167], v225 offset:10816\n"
  "v_mfma_f32_16x16x32_bf16 v[68:71], a[180:183], a[116:119], v[68:71]\n"
  "buffer_store_dwordx4 v[24:27], off, s[40:43], s56 offset:2048 sc1\n"
  "v_mfma_f32_16x16x32_bf16 v[68:71], a[176:179], a[100:103], v[68:71]\n"
  "v_mfma_f32_16x16x32_bf16 v[72:75], a[176:179], a[104:107], v[72:75]\n"
  "ds_read_b64_tr_b16 a[168:169], v225 offset:14400\n"
  "v_max_f32 v32, 0, v32\n"
  "v_mfma_f32_16x16x32_bf16 v[72:75], a[180:183], a[120:123], v[72:75]\n"
  "ds_read_b64_tr_b16 a[170:171], v225 offset:17984\n"
  "s_waitcnt lgkmcnt(6)\n"
  "v_mfma_f32_16x16x32_bf16 v[76:79], a[180:183], a[124:127], v[76:79]\n"
  "buffer_load_dwordx4 a[196:199], off, s[60:63], s48 offset:1024 sc1\n"
  "v_mfma_f32_16x16x32_bf16 v[76:79], a[176:179], a[108:111], v[76:79]\n"
  "v_max_f32 v33, 0, v33\n"
  "v_max_f32 v34, 0, v34\n"
  "v_mfma_f32_16x16x32_bf16 v[92:95], a[184:187], a[108:111], v[92:95]\n"
  "ds_read_b64_tr_b16 a[172:173], v225 offset:21568\n"
  "v_max_f32 v35, 0, v35\n"
  "v_mfma_f32_16x16x32_bf16 v[92:95], a[188:191], a[124:127], v[92:95]\n"
  "ds_read_b64_tr_b16 a[174:175], v225 offset:25152\n"
  "v_mul_f32 v32, v32, v48\n"
  "v_mfma_f32_16x16x32_bf16 v[88:91], a[188:191], a[120:123], v[88:91]\n"
  "buffer_load_dwordx4 a[200:203], off, s[60:63], s48 offset:2048 sc1\n"
  "v_mfma_f32_16x16x32_bf16 v[88:91], a[184:187], a[104:107], v[88:91]\n"
  "v_mul_f32 v33, v33, v49\n"
  "v_mul_f32 v34, v34, v50\n"
  "v_mfma_f32_16x16x32_bf16 v[84:87], a[184:187], a[100:103], v[84:87]\n"
  "ds_read_b64_tr_b16 a[176:177], v224 offset:64\n"
  "v_mul_f32 v35, v35, v51\n"
  "v_mfma_f32_16x16x32_bf16 v[84:87], a[188:191], a[116:119], v[84:87]\n"
  "ds_read_b64_tr_b16 a[178:179], v224 offset:3648\n"
  "s_waitcnt lgkmcnt(6)\n"
  "v_mfma_f32_16x16x32_bf16 v[80:83], a[188:191], a[112:115], v[80:83]\n"
  "buffer_load_dwordx4 a[204:207], off, s[60:63], s48 offset:3072 sc1\n"
  "v_mfma_f32_16x16x32_bf16 v[80:83], a[184:187], a[96:99], v[80:83]\n"
  "v_cvt_pk_bf16_f32 v48, v32, v33\n"
  "v_cvt_pk_bf16_f32 v49, v34, v35\n"
  "v_mfma_f32_16x16x32_bf16 v[64:67], a[160:163], a[128:131], v[64:67]\n"
  "ds_read_b64_tr_b16 a[180:181], v224 offset:7232\n"
  "v_max_f32 v36, 0, v36\n"
  "v_mfma_f32_16x16x32_bf16 v[64:67], a[164:167], a[144:147], v[64:67]\n"
  "ds_read_b64_tr_b16 a[182:183], v224 offset:10816\n"
  "v_max_f32 v37, 0, v37\n"
  "v_mfma_f32_16x16x32_bf16 v[68:71], a[164:167], a[148:151], v[68:71]\n"
  "v_max_f32 v38, 0, v38\n"
  "v_max_f32 v39, 0, v39\n"
  "v_mfma_f32_16x16x32_bf16 v[68:71], a[160:163], a[132:135], v[68:71]\n"
  "v_mul_f32 v36, v36, v52\n"
  "v_mul_f32 v37, v37, v53\n"
  "v_mfma_f32_16x16x32_bf16 v[72:75], a[160:163], a[136:139], v[72:75]\n"
  "ds_read_b64_tr_b16 a[184:185], v224 offset:14400\n"
  "v_mul_f32 v38, v38, v54\n"
  "v_mfma_f32_16x16x32_bf16 v[72:75], a[164:167], a[152:155], v[72:75]\n"
  "ds_read_b64_tr_b16 a[186:187], v224 offset:17984\n"
  "s_waitcnt lgkmcnt(6)\n"
  "v_mfma_f32_16x16x32_bf16 v[76:79], a[164:167], a[156:159], v[76:79]\n"
  "v_mul_f32 v39, v39, v55\n"
  "v_cvt_pk_bf16_f32 v52, v36, v37\n"
  "v_mfma_f32_16x16x32_bf16 v[76:79], a[160:163], a[140:143], v[76:79]\n"
  "v_cvt_pk_bf16_f32 v53, v38, v39\n"
  "v_max_f32 v40, 0, v40\n"
  "v_mfma_f32_16x16x32_bf16 v[92:95], a[168:171], a[140:143], v[92:95]\n"
  "ds_read_b64_tr_b16 a[188:189], v224 offset:21568\n"
  "v_max_f32 v41, 0, v41\n"
  "v_mfma_f32_16x16x32_bf16 v[92:95], a[172:175], a[156:159], v[92:95]\n"
  "ds_read_b64_tr_b16 a[190:191], v224 offset:25152\n"
  "v_max_f32 v42, 0, v42\n"
  "v_mfma_f32_16x16x32_bf16 v[88:91], a[172:175], a[152:155], v[88:91]\n"
  "v_max_f32 v43, 0, v43\n"
  "v_mul_f32 v40, v40, v56\n"
  "v_mfma_f32_16x16x32_bf16 v[88:91], a[168:171], a[136:139], v[88:91]\n"
  "v_mul_f32 v41, v41, v57\n"
  "v_mul_f32 v42, v42, v58\n"
  "v_mfma_f32_16x16x32_bf16 v[84:87], a[168:171], a[132:135], v[84:87]\n"
  "ds_read_b64_tr_b16 a[160:161], v224 offset:28768\n"
  "v_mul_f32 v43, v43, v59\n"
  "v_mfma_f32_16x16x32_bf16 v[84:87], a[172:175], a[148:151], v[84:87]\n"
  "ds_read_b64_tr_b16 a[162:163], v224 offset:32352\n"
  "s_waitcnt lgkmcnt(6)\n"
  "v_mfma_f32_16x16x32_bf16 v[80:83], a[172:175], a[144:147], v[80:83]\n"
  "v_cvt_pk_bf16_f32 v56, v40, v41\n"
  "v_cvt_pk_bf16_f32 v57, v42, v43\n"
  "v_mfma_f32_16x16x32_bf16 v[80:83], a[168:171], a[128:131], v[80:83]\n"
  "v_max_f32 v44, 0, v44\n"
  "v_max_f32 v45, 0, v45\n"
  "v_mfma_f32_16x16x32_bf16 v[64:67], a[176:179], a[0:3], v[64:67]\n"
  "ds_read_b64_tr_b16 a[164:165], v224 offset:35936\n"
  "v_max_f32 v46, 0, v46\n"
  "v_mfma_f32_16x16x32_bf16 v[64:67], a[180:183], a[16:19], v[64:67]\n"
  "ds_read_b64_tr_b16 a[166:167], v224 offset:39520\n"
  "v_max_f32 v47, 0, v47\n"
  "v_mfma_f32_16x16x32_bf16 v[68:71], a[180:183], a[20:23], v[68:71]\n"
  "v_mul_f32 v44, v44, v60\n"
  "v_mul_f32 v45, v45, v61\n"
  "v_mfma_f32_16x16x32_bf16 v[68:71], a[176:179], a[4:7], v[68:71]\n"
  "v_mul_f32 v46, v46, v62\n"
  "v_mul_f32 v47, v47, v63\n"
  "v_mfma_f32_16x16x32_bf16 v[72:75], a[176:179], a[8:11], v[72:75]\n"
  "ds_read_b64_tr_b16 a[168:169], v224 offset:43104\n"
  "v_cvt_pk_bf16_f32 v60, v44, v45\n"
  "v_mfma_f32_16x16x32_bf16 v[72:75], a[180:183], a[24:27], v[72:75]\n"
  "ds_read_b64_tr_b16 a[170:171], v224 offset:46688\n"
  "s_waitcnt lgkmcnt(6)\n"
  "v_mfma_f32_16x16x32_bf16 v[76:79], a[180:183], a[28:31], v[76:79]\n"
  "v_cvt_pk_bf16_f32 v61, v46, v47\n"
  "v_mfma_f32_16x16x32_bf16 v[76:79], a[176:179], a[12:15], v[76:79]\n"
  "v_mfma_f32_16x16x32_bf16 v[92:95], a[184:187], a[12:15], v[92:95]\n"
  "ds_read_b64_tr_b16 a[172:173], v224 offset:50272\n"
  "v_mfma_f32_16x16x32_bf16 v[92:95], a[188:191], a[28:31], v[92:95]\n"
  "ds_read_b64_tr_b16 a[174:175], v224 offset:53856\n"
  "v_mfma_f32_16x16x32_bf16 v[88:91], a[188:191], a[24:27], v[88:91]\n"
  "s_waitcnt vmcnt(3)\n"
  "buffer_store_dwordx4 v[248:251], v251, s[52:55], s64 offen sc1\n"
  "v_mfma_f32_16x16x32_bf16 v[88:91], a[184:187], a[8:11], v[88:91]\n"
  "v_mfma_f32_16x16x32_bf16 v[84:87], a[184:187], a[4:7], v[84:87]\n"
  "ds_read_b64_tr_b16 a[176:177], v225 offset:96\n"
  "v_mfma_f32_16x16x32_bf16 v[84:87], a[188:191], a[20:23], v[84:87]\n"
  "ds_read_b64_tr_b16 a[178:179], v225 offset:3680\n"
  "s_waitcnt lgkmcnt(6)\n"
  "v_mfma_f32_16x16x32_bf16 v[80:83], a[188:191], a[16:19], v[80:83]\n"
  "buffer_load_dwordx4 a[208:211], v254, s[52:55], s49 offen offset:0 sc1\n"
  "v_mfma_f32_16x16x32_bf16 v[80:83], a[184:187], a[0:3], v[80:83]\n"
  "v_mfma_f32_16x16x32_bf16 v[96:99], a[160:163], a[96:99], v[96:99]\n"
  "ds_read_b64_tr_b16 a[180:181], v225 offset:7264\n"
  "v_mfma_f32_16x16x32_bf16 v[96:99], a[164:167], a[112:115], v[96:99]\n"
  "ds_read_b64_tr_b16 a[182:183], v225 offset:10848\n"
  "v_mfma_f32_16x16x32_bf16 v[100:103], a[164:167], a[116:119], v[100:103]\n"
  "buffer_load_dwordx4 a[212:215], v254, s[52:55], s49 offen offset:1024 sc1\n"
  "v_mfma_f32_16x16x32_bf16 v[100:103], a[160:163], a[100:103], v[100:103]\n"
  "v_mfma_f32_16x16x32_bf16 v[104:107], a[160:163], a[104:107], v[104:107]\n"
  "ds_read_b64_tr_b16 a[184:185], v225 offset:14432\n"
  "v_max_f32 v64, 0, v64\n"
  "v_mfma_f32_16x16x32_bf16 v[104:107], a[164:167], a[120:123], v[104:107]\n"
  "ds_read_b64_tr_b16 a[186:187], v225 offset:18016\n"
  "s_waitcnt lgkmcnt(6)\n"
  "v_mfma_f32_16x16x32_bf16 v[108:111], a[164:167], a[124:127], v[108:111]\n"
  "buffer_load_dwordx4 a[216:219], v254, s[52:55], s49 offen offset:2048 sc1\n"
  "v_mfma_f32_16x16x32_bf16 v[108:111], a[160:163], a[108:111], v[108:111]\n"
  "v_max_f32 v65, 0, v65\n"
  "v_max_f32 v66, 0, v66\n"
  "v_mfma_f32_16x16x32_bf16 v[124:127], a[168:171], a[108:111], v[124:127]\n"
  "ds_read_b64_tr_b16 a[188:189], v225 offset:21600\n"
  "v_max_f32 v67, 0, v67\n"
  "v_mfma_f32_16x16x32_bf16 v[124:127], a[172:175], a[124:127], v[124:127]\n"
  "ds_read_b64_tr_b16 a[190:191], v225 offset:25184\n"
  "v_mul_f32 v64, v64, v80\n"
  "v_mfma_f32_16x16x32_bf16 v[120:123], a[172:175], a[120:123], v[120:123]\n"
  "buffer_load_dwordx4 a[220:223], v254, s[52:55], s49 offen offset:3072 sc1\n"
  "v_mfma_f32_16x16x32_bf16 v[120:123], a[168:171], a[104:107], v[120:123]\n"
  "v_mul_f32 v65, v65, v81\n"
  "v_mul_f32 v66, v66, v82\n"
  "v_mfma_f32_16x16x32_bf16 v[116:119], a[168:171], a[100:103], v[116:119]\n"
  "ds_read_b64_tr_b16 a[160:161], v224 offset:96\n"
  "v_mul_f32 v67, v67, v83\n"
  "v_mfma_f32_16x16x32_bf16 v[116:119], a[172:175], a[116:119], v[116:119]\n"
  "ds_read_b64_tr_b16 a[162:163], v224 offset:3680\n"
  "s_waitcnt lgkmcnt(6)\n"
  "v_mfma_f32_16x16x32_bf16 v[112:115], a[172:175], a[112:115], v[112:115]\n"
  "v_cvt_pk_bf16_f32 v50, v64, v65\n"
  "v_cvt_pk_bf16_f32 v51, v66, v67\n"
  "v_mfma_f32_16x16x32_bf16 v[112:115], a[168:171], a[96:99], v[112:115]\n"
  "buffer_store_dwordx4 v[48:51], off, s[40:43], s57 offset:0 sc1\n"
  "v_mfma_f32_16x16x32_bf16 v[96:99], a[176:179], a[128:131], v[96:99]\n"
  "ds_read_b64_tr_b16 a[164:165], v224 offset:7264\n"
  "v_max_f32 v68, 0, v68\n"
  "v_mfma_f32_16x16x32_bf16 v[96:99], a[180:183], a[144:147], v[96:99]\n"
  "ds_read_b64_tr_b16 a[166:167], v224 offset:10848\n"
  "v_max_f32 v69, 0, v69\n"
  "v_mfma_f32_16x16x32_bf16 v[100:103], a[180:183], a[148:151], v[100:103]\n"
  "v_max_f32 v70, 0, v70\n"
  "v_max_f32 v71, 0, v71\n"
  "v_mfma_f32_16x16x32_bf16 v[100:103], a[176:179], a[132:135], v[100:103]\n"
  "v_mul_f32 v68, v68, v84\n"
  "v_mul_f32 v69, v69, v85\n"
  "v_mfma_f32_16x16x32_bf16 v[104:107], a[176:179], a[136:139], v[104:107]\n"
  "ds_read_b64_tr_b16 a[168:169], v224 offset:14432\n"
  "v_mul_f32 v70, v70, v86\n"
  "v_mfma_f32_16x16x32_bf16 v[104:107], a[180:183], a[152:155], v[104:107]\n"
  "ds_read_b64_tr_b16 a[170:171], v224 offset:18016\n"
  "s_waitcnt lgkmcnt(6)\n"
  "v_mfma_f32_16x16x32_bf16 v[108:111], a[180:183], a[156:159], v[108:111]\n"
  "v_mul_f32 v71, v71, v87\n"
  "v_cvt_pk_bf16_f32 v54, v68, v69\n"
  "v_mfma_f32_16x16x32_bf16 v[108:111], a[176:179], a[140:143], v[108:111]\n"
  "v_cvt_pk_bf16_f32 v55, v70, v71\n"
  "v_max_f32 v72, 0, v72\n"
  "v_mfma_f32_16x16x32_bf16 v[124:127], a[184:187], a[140:143], v[124:127]\n"
  "ds_read_b64_tr_b16 a[172:173], v224 offset:21600\n"
  "v_max_f32 v73, 0, v73\n"
  "v_mfma_f32_16x16x32_bf16 v[124:127], a[188:191], a[156:159], v[124:127]\n"
  "ds_read_b64_tr_b16 a[174:175], v224 offset:25184\n"
  "v_max_f32 v74, 0, v74\n"
  "v_mfma_f32_16x16x32_bf16 v[120:123], a[188:191], a[152:155], v[120:123]\n"
  "buffer_store_dwordx4 v[52:55], off, s[40:43], s57 offset:1024 sc1\n"
  "v_mfma_f32_16x16x32_bf16 v[120:123], a[184:187], a[136:139], v[120:123]\n"
  "v_max_f32 v75, 0, v75\n"
  "v_mul_f32 v72, v72, v88\n"
  "v_mfma_f32_16x16x32_bf16 v[116:119], a[184:187], a[132:135], v[116:119]\n"
  "ds_read_b64_tr_b16 a[176:177], v224 offset:28800\n"
  "v_mul_f32 v73, v73, v89\n"
  "v_mfma_f32_16x16x32_bf16 v[116:119], a[188:191], a[148:151], v[116:119]\n"
  "ds_read_b64_tr_b16 a[178:179], v224 offset:32384\n"
  "s_waitcnt lgkmcnt(6)\n"
  "v_mfma_f32_16x16x32_bf16 v[112:115], a[188:191], a[144:147], v[112:115]\n"
  "v_mul_f32 v74, v74, v90\n"
  "v_mul_f32 v75, v75, v91\n"
  "v_mfma_f32_16x16x32_bf16 v[112:115], a[184:187], a[128:131], v[112:115]\n"
  "v_cvt_pk_bf16_f32 v58, v72, v73\n"
  "v_cvt_pk_bf16_f32 v59, v74, v75\n"
  "v_mfma_f32_16x16x32_bf16 v[96:99], a[160:163], a[0:3], v[96:99]\n"
  "ds_read_b64_tr_b16 a[180:181], v224 offset:35968\n"
  "v_max_f32 v76, 0, v76\n"
  "v_mfma_f32_16x16x32_bf16 v[96:99], a[164:167], a[16:19], v[96:99]\n"
  "ds_read_b64_tr_b16 a[182:183], v224 offset:39552\n"
  "v_max_f32 v77, 0, v77\n"
  "v_mfma_f32_16x16x32_bf16 v[100:103], a[164:167], a[20:23], v[100:103]\n"
  "buffer_store_dwordx4 v[56:59], off, s[40:43], s57 offset:2048 sc1\n"
  "v_mfma_f32_16x16x32_bf16 v[100:103], a[160:163], a[4:7], v[100:103]\n"
  "v_max_f32 v78, 0, v78\n"
  "v_max_f32 v79, 0, v79\n"
  "v_mfma_f32_16x16x32_bf16 v[104:107], a[160:163], a[8:11], v[104:107]\n"
  "ds_read_b64_tr_b16 a[184:185], v224 offset:43136\n"
  "v_mul_f32 v76, v76, v92\n"
  "v_mfma_f32_16x16x32_bf16 v[104:107], a[164:167], a[24:27], v[104:107]\n"
  "ds_read_b64_tr_b16 a[186:187], v224 offset:46720\n"
  "s_waitcnt lgkmcnt(6)\n"
  "v_mfma_f32_16x16x32_bf16 v[108:111], a[164:167], a[28:31], v[108:111]\n"
  "v_mul_f32 v77, v77, v93\n"
  "v_mul_f32 v78, v78, v94\n"
  "v_mfma_f32_16x16x32_bf16 v[108:111], a[160:163], a[12:15], v[108:111]\n"
  "v_mul_f32 v79, v79, v95\n"
  "v_cvt_pk_bf16_f32 v62, v76, v77\n"
  "v_mfma_f32_16x16x32_bf16 v[124:127], a[168:171], a[12:15], v[124:127]\n"
  "ds_read_b64_tr_b16 a[188:189], v224 offset:50304\n"
  "v_cvt_pk_bf16_f32 v63, v78, v79\n"
  "v_mfma_f32_16x16x32_bf16 v[124:127], a[172:175], a[28:31], v[124:127]\n"
  "ds_read_b64_tr_b16 a[190:191], v224 offset:53888\n"
  "v_mfma_f32_16x16x32_bf16 v[120:123], a[172:175], a[24:27], v[120:123]\n"
  "buffer_store_dwordx4 v[60:63], off, s[40:43], s57 offset:3072 sc1\n"
  "v_mfma_f32_16x16x32_bf16 v[120:123], a[168:171], a[8:11], v[120:123]\n"
  "v_mfma_f32_16x16x32_bf16 v[116:119], a[168:171], a[4:7], v[116:119]\n"
  "ds_read_b64_tr_b16 a[160:161], v225 offset:128\n"
  "v_mfma_f32_16x16x32_bf16 v[116:119], a[172:175], a[20:23], v[116:119]\n"
  "ds_read_b64_tr_b16 a[162:163], v225 offset:3712\n"
  "s_waitcnt lgkmcnt(6)\n"
  "v_mfma_f32_16x16x32_bf16 v[112:115], a[172:175], a[16:19], v[112:115]\n"
  "v_mfma_f32_16x16x32_bf16 v[112:115], a[168:171], a[0:3], v[112:115]\n"
  "v_mfma_f32_16x16x32_bf16 v[128:131], a[176:179], a[96:99], v[128:131]\n"
  "ds_read_b64_tr_b16 a[164:165], v225 offset:7296\n"
  "v_mfma_f32_16x16x32_bf16 v[128:131], a[180:183], a[112:115], v[128:131]\n"
  "ds_read_b64_tr_b16 a[166:167], v225 offset:10880\n"
  "v_mfma_f32_16x16x32_bf16 v[132:135], a[180:183], a[116:119], v[132:135]\n"
  "buffer_load_dwordx4 a[224:227], v254, s[52:55], s50 offen offset:0 sc1\n"
  "v_mfma_f32_16x16x32_bf16 v[132:135], a[176:179], a[100:103], v[132:135]\n"
  "v_mfma_f32_16x16x32_bf16 v[136:139], a[176:179], a[104:107], v[136:139]\n"
  "ds_read_b64_tr_b16 a[168:169], v225 offset:14464\n"
  "v_max_f32 v96, 0, v96\n"
  "v_mfma_f32_16x16x32_bf16 v[136:139], a[180:183], a[120:123], v[136:139]\n"
  "ds_read_b64_tr_b16 a[170:171], v225 offset:18048\n"
  "s_waitcnt lgkmcnt(6)\n"
  "v_mfma_f32_16x16x32_bf16 v[140:143], a[180:183], a[124:127], v[140:143]\n"
  "buffer_load_dwordx4 a[228:231], v254, s[52:55], s50 offen offset:1024 sc1\n"
  "v_mfma_f32_16x16x32_bf16 v[140:143], a[176:179], a[108:111], v[140:143]\n"
  "v_max_f32 v97, 0, v97\n"
  "v_max_f32 v98, 0, v98\n"
  "v_mfma_f32_16x16x32_bf16 v[156:159], a[184:187], a[108:111], v[156:159]\n"
  "ds_read_b64_tr_b16 a[172:173], v225 offset:21632\n"
  "v_max_f32 v99, 0, v99\n"
  "v_mfma_f32_16x16x32_bf16 v[156:159], a[188:191], a[124:127], v[156:159]\n"
  "ds_read_b64_tr_b16 a[174:175], v225 offset:25216\n"
  "v_mul_f32 v96, v96, v112\n"
  "v_mfma_f32_16x16x32_bf16 v[152:155], a[188:191], a[120:123], v[152:155]\n"
  "buffer_load_dwordx4 a[232:235], v254, s[52:55], s50 offen offset:2048 sc1\n"
  "v_mfma_f32_16x16x32_bf16 v[152:155], a[184:187], a[104:107], v[152:155]\n"
  "v_mul_f32 v97, v97, v113\n"
  "v_mul_f32 v98, v98, v114\n"
  "v_mfma_f32_16x16x32_bf16 v[148:151], a[184:187], a[100:103], v[148:151]\n"
  "ds_read_b64_tr_b16 a[176:177], v224 offset:128\n"
  "v_mul_f32 v99, v99, v115\n"
  "v_mfma_f32_16x16x32_bf16 v[148:151], a[188:191], a[116:119], v[148:151]\n"
  "ds_read_b64_tr_b16 a[178:179], v224 offset:3712\n"
  "s_waitcnt lgkmcnt(6)\n"
  "v_mfma_f32_16x16x32_bf16 v[144:147], a[188:191], a[112:115], v[144:147]\n"
  "buffer_load_dwordx4 a[236:239], v254, s[52:55], s50 offen offset:3072 sc1\n"
  "v_mfma_f32_16x16x32_bf16 v[144:147], a[184:187], a[96:99], v[144:147]\n"
  "v_cvt_pk_bf16_f32 v112, v96, v97\n"
  "v_cvt_pk_bf16_f32 v113, v98, v99\n"
  "v_mfma_f32_16x16x32_bf16 v[128:131], a[160:163], a[128:131], v[128:131]\n"
  "ds_read_b64_tr_b16 a[180:181], v224 offset:7296\n"
  "v_max_f32 v100, 0, v100\n"
  "v_mfma_f32_16x16x32_bf16 v[128:131], a[164:167], a[144:147], v[128:131]\n"
  "ds_read_b64_tr_b16 a[182:183], v224 offset:10880\n"
  "v_max_f32 v101, 0, v101\n"
  "v_mfma_f32_16x16x32_bf16 v[132:135], a[164:167], a[148:151], v[132:135]\n"
  "v_max_f32 v102, 0, v102\n"
  "v_max_f32 v103, 0, v103\n"
  "v_mfma_f32_16x16x32_bf16 v[132:135], a[160:163], a[132:135], v[132:135]\n"
  "v_mul_f32 v100, v100, v116\n"
  "v_mul_f32 v101, v101, v117\n"
  "v_mfma_f32_16x16x32_bf16 v[136:139], a[160:163], a[136:139], v[136:139]\n"
  "ds_read_b64_tr_b16 a[184:185], v224 offset:14464\n"
  "v_mul_f32 v102, v102, v118\n"
  "v_mfma_f32_16x16x32_bf16 v[136:139], a[164:167], a[152:155], v[136:139]\n"
  "ds_read_b64_tr_b16 a[186:187], v224 offset:18048\n"
  "s_waitcnt lgkmcnt(6)\n"
  "v_mfma_f32_16x16x32_bf16 v[140:143], a[164:167], a[156:159], v[140:143]\n"
  "v_mul_f32 v103, v103, v119\n"
  "v_cvt_pk_bf16_f32 v116, v100, v101\n"
  "v_mfma_f32_16x16x32_bf16 v[140:143], a[160:163], a[140:143], v[140:143]\n"
  "v_cvt_pk_bf16_f32 v117, v102, v103\n"
  "v_max_f32 v104, 0, v104\n"
  "v_mfma_f32_16x16x32_bf16 v[156:159], a[168:171], a[140:143], v[156:159]\n"
  "ds_read_b64_tr_b16 a[188:189], v224 offset:21632\n"
  "v_max_f32 v105, 0, v105\n"
  "v_mfma_f32_16x16x32_bf16 v[156:159], a[172:175], a[156:159], v[156:159]\n"
  "ds_read_b64_tr_b16 a[190:191], v224 offset:25216\n"
  "v_max_f32 v106, 0, v106\n"
  "v_mfma_f32_16x16x32_bf16 v[152:155], a[172:175], a[152:155], v[152:155]\n"
  "v_max_f32 v107, 0, v107\n"
  "v_mul_f32 v104, v104, v120\n"
  "v_mfma_f32_16x16x32_bf16 v[152:155], a[168:171], a[136:139], v[152:155]\n"
  "v_mul_f32 v105, v105, v121\n"
  "v_mul_f32 v106, v106, v122\n"
  "v_mfma_f32_16x16x32_bf16 v[148:151], a[168:171], a[132:135], v[148:151]\n"
  "ds_read_b64_tr_b16 a[160:161], v224 offset:28832\n"
  "v_mul_f32 v107, v107, v123\n"
  "v_mfma_f32_16x16x32_bf16 v[148:151], a[172:175], a[148:151], v[148:151]\n"
  "ds_read_b64_tr_b16 a[162:163], v224 offset:32416\n"
  "s_waitcnt lgkmcnt(6)\n"
  "v_mfma_f32_16x16x32_bf16 v[144:147], a[172:175], a[144:147], v[144:147]\n"
  "v_cvt_pk_bf16_f32 v120, v104, v105\n"
  "v_cvt_pk_bf16_f32 v121, v106, v107\n"
  "v_mfma_f32_16x16x32_bf16 v[144:147], a[168:171], a[128:131], v[144:147]\n"
  "v_max_f32 v108, 0, v108\n"
  "v_max_f32 v109, 0, v109\n"
  "v_mfma_f32_16x16x32_bf16 v[128:131], a[176:179], a[0:3], v[128:131]\n"
  "ds_read_b64_tr_b16 a[164:165], v224 offset:36000\n"
  "v_max_f32 v110, 0, v110\n"
  "v_mfma_f32_16x16x32_bf16 v[128:131], a[180:183], a[16:19], v[128:131]\n"
  "ds_read_b64_tr_b16 a[166:167], v224 offset:39584\n"
  "v_max_f32 v111, 0, v111\n"
  "v_mfma_f32_16x16x32_bf16 v[132:135], a[180:183], a[20:23], v[132:135]\n"
  "v_mul_f32 v108, v108, v124\n"
  "v_mul_f32 v109, v109, v125\n"
  "v_mfma_f32_16x16x32_bf16 v[132:135], a[176:179], a[4:7], v[132:135]\n"
  "v_mul_f32 v110, v110, v126\n"
  "v_mul_f32 v111, v111, v127\n"
  "v_mfma_f32_16x16x32_bf16 v[136:139], a[176:179], a[8:11], v[136:139]\n"
  "ds_read_b64_tr_b16 a[168:169], v224 offset:43168\n"
  "v_cvt_pk_bf16_f32 v124, v108, v109\n"
  "v_mfma_f32_16x16x32_bf16 v[136:139], a[180:183], a[24:27], v[136:139]\n"
  "ds_read_b64_tr_b16 a[170:171], v224 offset:46752\n"
  "s_waitcnt lgkmcnt(6)\n"
  "v_mfma_f32_16x16x32_bf16 v[140:143], a[180:183], a[28:31], v[140:143]\n"
  "v_cvt_pk_bf16_f32 v125, v110, v111\n"
  "v_mfma_f32_16x16x32_bf16 v[140:143], a[176:179], a[12:15], v[140:143]\n"
  "v_mfma_f32_16x16x32_bf16 v[156:159], a[184:187], a[12:15], v[156:159]\n"
  "ds_read_b64_tr_b16 a[172:173], v224 offset:50336\n"
  "v_mfma_f32_16x16x32_bf16 v[156:159], a[188:191], a[28:31], v[156:159]\n"
  "ds_read_b64_tr_b16 a[174:175], v224 offset:53920\n"
  "v_mfma_f32_16x16x32_bf16 v[152:155], a[188:191], a[24:27], v[152:155]\n"
  "v_mfma_f32_16x16x32_bf16 v[152:155], a[184:187], a[8:11], v[152:155]\n"
  "v_mfma_f32_16x16x32_bf16 v[148:151], a[184:187], a[4:7], v[148:151]\n"
  "ds_read_b64_tr_b16 a[176:177], v225 offset:160\n"
  "v_mfma_f32_16x16x32_bf16 v[148:151], a[188:191], a[20:23], v[148:151]\n"
  "ds_read_b64_tr_b16 a[178:179], v225 offset:3744\n"
  "s_waitcnt lgkmcnt(6)\n"
  "v_mfma_f32_16x16x32_bf16 v[144:147], a[188:191], a[16:19], v[144:147]\n"
  "v_mfma_f32_16x16x32_bf16 v[144:147], a[184:187], a[0:3], v[144:147]\n"
  "v_mfma_f32_16x16x32_bf16 v[160:163], a[160:163], a[96:99], v[160:163]\n"
  "ds_read_b64_tr_b16 a[180:181], v225 offset:7328\n"
  "v_mfma_f32_16x16x32_bf16 v[160:163], a[164:167], a[112:115], v[160:163]\n"
  "ds_read_b64_tr_b16 a[182:183], v225 offset:10912\n"
  "v_mfma_f32_16x16x32_bf16 v[164:167], a[164:167], a[116:119], v[164:167]\n"
  "buffer_load_dwordx2 v[238:239], v236, s[52:55], s83 offen sc1\n"
  "v_mfma_f32_16x16x32_bf16 v[164:167], a[160:163], a[100:103], v[164:167]\n"
  "v_mfma_f32_16x16x32_bf16 v[168:171], a[160:163], a[104:107], v[168:171]\n"
  "ds_read_b64_tr_b16 a[184:185], v225 offset:14496\n"
  "v_mfma_f32_16x16x32_bf16 v[168:171], a[164:167], a[120:123], v[168:171]\n"
  "ds_read_b64_tr_b16 a[186:187], v225 offset:18080\n"
  "s_waitcnt lgkmcnt(6)\n"
  "v_mfma_f32_16x16x32_bf16 v[172:175], a[164:167], a[124:127], v[172:175]\n"
  "s_waitcnt vmcnt(5)\n"
  "buffer_store_dwordx4 v[248:251], v251, s[52:55], s65 offen sc1\n"
  "v_mfma_f32_16x16x32_bf16 v[172:175], a[160:163], a[108:111], v[172:175]\n"
  "v_max_f32 v128, 0, v128\n"
  "v_max_f32 v129, 0, v129\n"
  "v_mfma_f32_16x16x32_bf16 v[188:191], a[168:171], a[108:111], v[188:191]\n"
  "ds_read_b64_tr_b16 a[188:189], v225 offset:21664\n"
  "v_max_f32 v130, 0, v130\n"
  "v_mfma_f32_16x16x32_bf16 v[188:191], a[172:175], a[124:127], v[188:191]\n"
  "ds_read_b64_tr_b16 a[190:191], v225 offset:25248\n"
  "v_max_f32 v131, 0, v131\n"
  "v_mfma_f32_16x16x32_bf16 v[184:187], a[172:175], a[120:123], v[184:187]\n"
  "v_mul_f32 v128, v128, v144\n"
  "v_mul_f32 v129, v129, v145\n"
  "v_mfma_f32_16x16x32_bf16 v[184:187], a[168:171], a[104:107], v[184:187]\n"
  "v_mul_f32 v130, v130, v146\n"
  "v_mul_f32 v131, v131, v147\n"
  "v_mfma_f32_16x16x32_bf16 v[180:183], a[168:171], a[100:103], v[180:183]\n"
  "ds_read_b64_tr_b16 a[160:161], v224 offset:160\n"
  "v_cvt_pk_bf16_f32 v114, v128, v129\n"
  "v_mfma_f32_16x16x32_bf16 v[180:183], a[172:175], a[116:119], v[180:183]\n"
  "ds_read_b64_tr_b16 a[162:163], v224 offset:3744\n"
  "s_waitcnt lgkmcnt(6)\n"
  "v_mfma_f32_16x16x32_bf16 v[176:179], a[172:175], a[112:115], v[176:179]\n"
  "v_cvt_pk_bf16_f32 v115, v130, v131\n"
  "v_max_f32 v132, 0, v132\n"
  "v_mfma_f32_16x16x32_bf16 v[176:179], a[168:171], a[96:99], v[176:179]\n"
  "buffer_store_dwordx4 v[112:115], off, s[40:43], s58 offset:0 sc1\n"
  "v_mfma_f32_16x16x32_bf16 v[160:163], a[176:179], a[128:131], v[160:163]\n"
  "ds_read_b64_tr_b16 a[164:165], v224 offset:7328\n"
  "v_max_f32 v133, 0, v133\n"
  "v_mfma_f32_16x16x32_bf16 v[160:163], a[180:183], a[144:147], v[160:163]\n"
  "ds_read_b64_tr_b16 a[166:167], v224 offset:10912\n"
  "v_max_f32 v134, 0, v134\n"
  "v_mfma_f32_16x16x32_bf16 v[164:167], a[180:183], a[148:151], v[164:167]\n"
  "v_max_f32 v135, 0, v135\n"
  "v_mul_f32 v132, v132, v148\n"
  "v_mfma_f32_16x16x32_bf16 v[164:167], a[176:179], a[132:135], v[164:167]\n"
  "v_mul_f32 v133, v133, v149\n"
  "v_mul_f32 v134, v134, v150\n"
  "v_mfma_f32_16x16x32_bf16 v[168:171], a[176:179], a[136:139], v[168:171]\n"
  "ds_read_b64_tr_b16 a[168:169], v224 offset:14496\n"
  "v_mul_f32 v135, v135, v151\n"
  "v_mfma_f32_16x16x32_bf16 v[168:171], a[180:183], a[152:155], v[168:171]\n"
  "ds_read_b64_tr_b16 a[170:171], v224 offset:18080\n"
  "s_waitcnt lgkmcnt(6)\n"
  "v_mfma_f32_16x16x32_bf16 v[172:175], a[180:183], a[156:159], v[172:175]\n"
  "v_cvt_pk_bf16_f32 v118, v132, v133\n"
  "v_cvt_pk_bf16_f32 v119, v134, v135\n"
  "v_mfma_f32_16x16x32_bf16 v[172:175], a[176:179], a[140:143], v[172:175]\n"
  "buffer_store_dwordx4 v[116:119], off, s[40:43], s58 offset:1024 sc1\n"
  "v_mfma_f32_16x16x32_bf16 v[188:191], a[184:187], a[140:143], v[188:191]\n"
  "ds_read_b64_tr_b16 a[172:173], v224 offset:21664\n"
  "v_max_f32 v136, 0, v136\n"
  "v_mfma_f32_16x16x32_bf16 v[188:191], a[188:191], a[156:159], v[188:191]\n"
  "ds_read_b64_tr_b16 a[174:175], v224 offset:25248\n"
  "v_max_f32 v137, 0, v137\n"
  "v_mfma_f32_16x16x32_bf16 v[184:187], a[188:191], a[152:155], v[184:187]\n"
  "v_max_f32 v138, 0, v138\n"
  "v_max_f32 v139, 0, v139\n"
  "v_mfma_f32_16x16x32_bf16 v[184:187], a[184:187], a[136:139], v[184:187]\n"
  "v_mul_f32 v136, v136, v152\n"
  "v_mul_f32 v137, v137, v153\n"
  "v_mfma_f32_16x16x32_bf16 v[180:183], a[184:187], a[132:135], v[180:183]\n"
  "ds_read_b64_tr_b16 a[176:177], v224 offset:28864\n"
  "v_mul_f32 v138, v138, v154\n"
  "v_mfma_f32_16x16x32_bf16 v[180:183], a[188:191], a[148:151], v[180:183]\n"
  "ds_read_b64_tr_b16 a[178:179], v224 offset:32448\n"
  "s_waitcnt lgkmcnt(6)\n"
  "v_mfma_f32_16x16x32_bf16 v[176:179], a[188:191], a[144:147], v[176:179]\n"
  "v_mul_f32 v139, v139, v155\n"
  "v_cvt_pk_bf16_f32 v122, v136, v137\n"
  "v_mfma_f32_16x16x32_bf16 v[176:179], a[184:187], a[128:131], v[176:179]\n"
  "v_cvt_pk_bf16_f32 v123, v138, v139\n"
  "v_max_f32 v140, 0, v140\n"
  "v_mfma_f32_16x16x32_bf16 v[160:163], a[160:163], a[0:3], v[160:163]\n"
  "ds_read_b64_tr_b16 a[180:181], v224 offset:36032\n"
  "v_max_f32 v141, 0, v141\n"
  "v_mfma_f32_16x16x32_bf16 v[160:163], a[164:167], a[16:19], v[160:163]\n"
  "ds_read_b64_tr_b16 a[182:183], v224 offset:39616\n"
  "v_max_f32 v142, 0, v142\n"
  "v_mfma_f32_16x16x32_bf16 v[164:167], a[164:167], a[20:23], v[164:167]\n"
  "buffer_store_dwordx4 v[120:123], off, s[40:43], s58 offset:2048 sc1\n"
  "v_mfma_f32_16x16x32_bf16 v[164:167], a[160:163], a[4:7], v[164:167]\n"
  "s_waitcnt vmcnt(4)\n"
  "L_poll7_%=:\n"
  "v_cmp_ne_u64 vcc, s[10:11], v[238:239]\n"
  "s_cbranch_vccz L_ok7_%=\n"
  "s_sub_u32 s99, s99, 1\n"
  "s_cbranch_scc1 L_out7_%=\n"
  "buffer_load_dwordx2 v[238:239], v236, s[52:55], s83 offen sc1\n"
  "s_waitcnt vmcnt(0)\n"
  "s_branch L_poll7_%=\n"
  "L_out7_%=:\n"
  "s_mov_b32 s99, 0\n"
  "L_ok7_%=:\n"
  "v_mfma_f32_16x16x32_bf16 v[168:171], a[160:163], a[8:11], v[168:171]\n"
  "ds_read_b64_tr_b16 a[184:185], v224 offset:43200\n"
  "s_add_u32 s83, s83, 2048\n"
  "v_mfma_f32_16x16x32_bf16 v[168:171], a[164:167], a[24:27], v[168:171]\n"
  "ds_read_b64_tr_b16 a[186:187], v224 offset:46784\n"
  "s_waitcnt lgkmcnt(6)\n"
  "v_mfma_f32_16x16x32_bf16 v[172:175], a[164:167], a[28:31], v[172:175]\n"
  "buffer_load_dwordx4 v[0:3], off, s[72:75], 0 offset:0 sc1\n"
  "v_mfma_f32_16x16x32_bf16 v[172:175], a[160:163], a[12:15], v[172:175]\n"
  "v_max_f32 v143, 0, v143\n"
  "v_mul_f32 v140, v140, v156\n"
  "v_mfma_f32_16x16x32_bf16 v[188:191], a[168:171], a[12:15], v[188:191]\n"
  "ds_read_b64_tr_b16 a[188:189], v224 offset:50368\n"
  "v_mul_f32 v141, v141, v157\n"
  "v_mfma_f32_16x16x32_bf16 v[188:191], a[172:175], a[28:31], v[188:191]\n"
  "ds_read_b64_tr_b16 a[190:191], v224 offset:53952\n"
  "v_mul_f32 v142, v142, v158\n"
  "v_mfma_f32_16x16x32_bf16 v[184:187], a[172:175], a[24:27], v[184:187]\n"
  "buffer_load_dwordx4 v[4:7], off, s[72:75], 0 offset:1024 sc1\n"
  "v_mfma_f32_16x16x32_bf16 v[184:187], a[168:171], a[8:11], v[184:187]\n"
  "v_mul_f32 v143, v143, v159\n"
  "v_cvt_pk_bf16_f32 v126, v140, v141\n"
  "v_mfma_f32_16x16x32_bf16 v[180:183], a[168:171], a[4:7], v[180:183]\n"
  "ds_read_b64_tr_b16 a[160:161], v225 offset:192\n"
  "v_cvt_pk_bf16_f32 v127, v142, v143\n"
  "v_mfma_f32_16x16x32_bf16 v[180:183], a[172:175], a[20:23], v[180:183]\n"
  "ds_read_b64_tr_b16 a[162:163], v225 offset:3776\n"
  "s_waitcnt lgkmcnt(6)\n"
  "v_mfma_f32_16x16x32_bf16 v[176:179], a[172:175], a[16:19], v[176:179]\n"
  "buffer_store_dwordx4 v[124:127], off, s[40:43], s58 offset:3072 sc1\n"
  "v_mfma_f32_16x16x32_bf16 v[176:179], a[168:171], a[0:3], v[176:179]\n"
  "v_mfma_f32_16x16x32_bf16 v[192:195], a[176:179], a[96:99], v[192:195]\n"
  "ds_read_b64_tr_b16 a[164:165], v225 offset:7360\n"
  "v_mfma_f32_16x16x32_bf16 v[192:195], a[180:183], a[112:115], v[192:195]\n"
  "ds_read_b64_tr_b16 a[166:167], v225 offset:10944\n"
  "v_mfma_f32_16x16x32_bf16 v[196:199], a[180:183], a[116:119], v[196:199]\n"
  "buffer_load_dwordx4 v[8:11], off, s[72:75], 0 offset:2048 sc1\n"
  "v_mfma_f32_16x16x32_bf16 v[196:199], a[176:179], a[100:103], v[196:199]\n"
  "v_mfma_f32_16x16x32_bf16 v[200:203], a[176:179], a[104:107], v[200:203]\n"
  "ds_read_b64_tr_b16 a[168:169], v225 offset:14528\n"
  "v_max_f32 v160, 0, v160\n"
  "v_mfma_f32_16x16x32_bf16 v[200:203], a[180:183], a[120:123], v[200:203]\n"
  "ds_read_b64_tr_b16 a[170:171], v225 offset:18112\n"
  "s_waitcnt lgkmcnt(6)\n"
  "v_mfma_f32_16x16x32_bf16 v[204:207], a[180:183], a[124:127], v[204:207]\n"
  "buffer_load_dwordx4 v[12:15], off, s[72:75], 0 offset:3072 sc1\n"
  "v_mfma_f32_16x16x32_bf16 v[204:207], a[176:179], a[108:111], v[204:207]\n"
  "v_max_f32 v161, 0, v161\n"
  "v_max_f32 v162, 0, v162\n"
  "v_mfma_f32_16x16x32_bf16 v[220:223], a[184:187], a[108:111], v[220:223]\n"
  "ds_read_b64_tr_b16 a[172:173], v225 offset:21696\n"
  "v_max_f32 v163, 0, v163\n"
  "v_mfma_f32_16x16x32_bf16 v[220:223], a[188:191], a[124:127], v[220:223]\n"
  "ds_read_b64_tr_b16 a[174:175], v225 offset:25280\n"
  "v_mul_f32 v160, v160, v176\n"
  "v_mfma_f32_16x16x32_bf16 v[216:219], a[188:191], a[120:123], v[216:219]\n"
  "buffer_load_dwordx4 v[16:19], off, s[72:75], s70 offset:0 sc1\n"
  "v_mfma_f32_16x16x32_bf16 v[216:219], a[184:187], a[104:107], v[216:219]\n"
  "v_mul_f32 v161, v161, v177\n"
  "v_mul_f32 v162, v162, v178\n"
  "v_mfma_f32_16x16x32_bf16 v[212:215], a[184:187], a[100:103], v[212:215]\n"
  "ds_read_b64_tr_b16 a[176:177], v224 offset:192\n"
  "v_mul_f32 v163, v163, v179\n"
  "v_mfma_f32_16x16x32_bf16 v[212:215], a[188:191], a[116:119], v[212:215]\n"
  "ds_read_b64_tr_b16 a[178:179], v224 offset:3776\n"
  "s_waitcnt lgkmcnt(6)\n"
  "v_mfma_f32_16x16x32_bf16 v[208:211], a[188:191], a[112:115], v[208:211]\n"
  "buffer_load_dwordx4 v[20:23], off, s[72:75], s70 offset:1024 sc1\n"
  "v_mfma_f32_16x16x32_bf16 v[208:211], a[184:187], a[96:99], v[208:211]\n"
  "v_cvt_pk_bf16_f32 v176, v160, v161\n"
  "v_cvt_pk_bf16_f32 v177, v162, v163\n"
  "v_mfma_f32_16x16x32_bf16 v[192:195], a[160:163], a[128:131], v[192:195]\n"
  "ds_read_b64_tr_b16 a[180:181], v224 offset:7360\n"
  "v_max_f32 v164, 0, v164\n"
  "v_mfma_f32_16x16x32_bf16 v[192:195], a[164:167], a[144:147], v[192:195]\n"
  "ds_read_b64_tr_b16 a[182:183], v224 offset:10944\n"
  "v_max_f32 v165, 0, v165\n"
  "v_mfma_f32_16x16x32_bf16 v[196:199], a[164:167], a[148:151], v[196:199]\n"
  "buffer_load_dwordx4 v[24:27], off, s[72:75], s70 offset:2048 sc1\n"
  "v_mfma_f32_16x16x32_bf16 v[196:199], a[160:163], a[132:135], v[196:199]\n"
  "v_max_f32 v166, 0, v166\n"
  "v_max_f32 v167, 0, v167\n"
  "v_mfma_f32_16x16x32_bf16 v[200:203], a[160:163], a[136:139], v[200:203]\n"
  "ds_read_b64_tr_b16 a[184:185], v224 offset:14528\n"
  "v_mul_f32 v164, v164, v180\n"
  "v_mfma_f32_16x16x32_bf16 v[200:203], a[164:167], a[152:155], v[200:203]\n"
  "ds_read_b64_tr_b16 a[186:187], v224 offset:18112\n"
  "s_waitcnt lgkmcnt(6)\n"
  "v_mfma_f32_16x16x32_bf16 v[204:207], a[164:167], a[156:159], v[204:207]\n"
  "buffer_load_dwordx4 v[28:31], off, s[72:75], s70 offset:3072 sc1\n"
  "v_mfma_f32_16x16x32_bf16 v[204:207], a[160:163], a[140:143], v[204:207]\n"
  "v_mul_f32 v165, v165, v181\n"
  "v_mul_f32 v166, v166, v182\n"
  "v_mfma_f32_16x16x32_bf16 v[220:223], a[168:171], a[140:143], v[220:223]\n"
  "ds_read_b64_tr_b16 a[188:189], v224 offset:21696\n"
  "v_mul_f32 v167, v167, v183\n"
  "v_mfma_f32_16x16x32_bf16 v[220:223], a[172:175], a[156:159], v[220:223]\n"
  "ds_read_b64_tr_b16 a[190:191], v224 offset:25280\n"
  "v_cvt_pk_bf16_f32 v180, v164, v165\n"
  "v_mfma_f32_16x16x32_bf16 v[216:219], a[172:175], a[152:155], v[216:219]\n"
  "buffer_load_dwordx4 v[32:35], off, s[72:75], s71 offset:0 sc1\n"
  "v_mfma_f32_16x16x32_bf16 v[216:219], a[168:171], a[136:139], v[216:219]\n"
  "v_cvt_pk_bf16_f32 v181, v166, v167\n"
  "v_max_f32 v168, 0, v168\n"
  "v_mfma_f32_16x16x32_bf16 v[212:215], a[168:171], a[132:135], v[212:215]\n"
  "buffer_load_dwordx4 v[36:39], off, s[72:75], s71 offset:1024 sc1\n"
  "v_mfma_f32_16x16x32_bf16 v[212:215], a[172:175], a[148:151], v[212:215]\n"
  "v_max_f32 v169, 0, v169\n"
  "v_max_f32 v170, 0, v170\n"
  "v_mfma_f32_16x16x32_bf16 v[208:211], a[172:175], a[144:147], v[208:211]\n"
  "v_max_f32 v171, 0, v171\n"
  "v_mul_f32 v168, v168, v184\n"
  "v_mfma_f32_16x16x32_bf16 v[208:211], a[168:171], a[128:131], v[208:211]\n"
  "s_waitcnt lgkmcnt(4)\n"
  "v_mul_f32 v169, v169, v185\n"
  "v_mul_f32 v170, v170, v186\n"
  "v_mfma_f32_16x16x32_bf16 v[192:195], a[176:179], a[0:3], v[192:195]\n"
  "buffer_load_dwordx4 v[40:43], off, s[72:75], s71 offset:2048 sc1\n"
  "v_mfma_f32_16x16x32_bf16 v[192:195], a[180:183], a[16:19], v[192:195]\n"
  "v_mul_f32 v171, v171, v187\n"
  "v_cvt_pk_bf16_f32 v184, v168, v169\n"
  "v_mfma_f32_16x16x32_bf16 v[196:199], a[180:183], a[20:23], v[196:199]\n"
  "v_cvt_pk_bf16_f32 v185, v170, v171\n"
  "v_max_f32 v172, 0, v172\n"
  "v_mfma_f32_16x16x32_bf16 v[196:199], a[176:179], a[4:7], v[196:199]\n"
  "v_max_f32 v173, 0, v173\n"
  "v_max_f32 v174, 0, v174\n"
  "v_mfma_f32_16x16x32_bf16 v[200:203], a[176:179], a[8:11], v[200:203]\n"
  "buffer_load_dwordx4 v[44:47], off, s[72:75], s71 offset:3072 sc1\n"
  "v_mfma_f32_16x16x32_bf16 v[200:203], a[180:183], a[24:27], v[200:203]\n"
  "v_max_f32 v175, 0, v175\n"
  "v_mul_f32 v172, v172, v188\n"
  "v_mfma_f32_16x16x32_bf16 v[204:207], a[180:183], a[28:31], v[204:207]\n"
  "v_mul_f32 v173, v173, v189\n"
  "v_mul_f32 v174, v174, v190\n"
  "v_mfma_f32_16x16x32_bf16 v[204:207], a[176:179], a[12:15], v[204:207]\n"
  "s_waitcnt lgkmcnt(0)\n"
  "v_mul_f32 v175, v175, v191\n"
  "v_cvt_pk_bf16_f32 v188, v172, v173\n"
  "v_mfma_f32_16x16x32_bf16 v[220:223], a[184:187], a[12:15], v[220:223]\n"
  "buffer_load_dwordx4 v[48:51], off, s[72:75], s80 offset:0 sc1\n"
  "v_mfma_f32_16x16x32_bf16 v[220:223], a[188:191], a[28:31], v[220:223]\n"
  "v_cvt_pk_bf16_f32 v189, v174, v175\n"
  "v_mfma_f32_16x16x32_bf16 v[216:219], a[188:191], a[24:27], v[216:219]\n"
  "v_mfma_f32_16x16x32_bf16 v[216:219], a[184:187], a[8:11], v[216:219]\n"
  "v_mfma_f32_16x16x32_bf16 v[212:215], a[184:187], a[4:7], v[212:215]\n"
  "buffer_load_dwordx4 v[52:55], off, s[72:75], s80 offset:1024 sc1\n"
  "v_mfma_f32_16x16x32_bf16 v[212:215], a[188:191], a[20:23], v[212:215]\n"
  "v_mfma_f32_16x16x32_bf16 v[208:211], a[188:191], a[16:19], v[208:211]\n"
  "v_mfma_f32_16x16x32_bf16 v[208:211], a[184:187], a[0:3], v[208:211]\n"
  "buffer_load_dwordx4 v[56:59], off, s[72:75], s80 offset:2048 sc1\n"
  "buffer_load_dwordx4 v[60:63], off, s[72:75], s80 offset:3072 sc1\n"
  "s_add_u32 s80, s16, 16384\n"
  "buffer_load_dwordx2 v[238:239], v236, s[52:55], s83 offen sc1\n"
  "s_waitcnt vmcnt(15)\n"
  "buffer_store_dwordx4 v[248:251], v251, s[52:55], s66 offen sc1\n"
  "v_max_f32 v192, 0, v192\n"
  "v_max_f32 v193, 0, v193\n"
  "v_max_f32 v194, 0, v194\n"
  "v_max_f32 v195, 0, v195\n"
  "v_mul_f32 v192, v192, v208\n"
  "v_mul_f32 v193, v193, v209\n"
  "v_mul_f32 v194, v194, v210\n"
  "v_mul_f32 v195, v195, v211\n"
  "v_cvt_pk_bf16_f32 v178, v192, v193\n"
  "v_cvt_pk_bf16_f32 v179, v194, v195\n"
  "buffer_store_dwordx4 v[176:179], off, s[40:43], s59 offset:0 sc1\n"
  "v_max_f32 v196, 0, v196\n"
  "v_max_f32 v197, 0, v197\n"
  "v_max_f32 v198, 0, v198\n"
  "v_max_f32 v199, 0, v199\n"
  "v_mul_f32 v196, v196, v212\n"
  "v_mul_f32 v197, v197, v213\n"
  "v_mul_f32 v198, v198, v214\n"
  "v_mul_f32 v199, v199, v215\n"
  "v_cvt_pk_bf16_f32 v182, v196, v197\n"
  "v_cvt_pk_bf16_f32 v183, v198, v199\n"
  "buffer_store_dwordx4 v[180:183], off, s[40:43], s59 offset:1024 sc1\n"
  "v_max_f32 v200, 0, v200\n"
  "v_max_f32 v201, 0, v201\n"
  "v_max_f32 v202, 0, v202\n"
  "v_max_f32 v203, 0, v203\n"
  "v_mul_f32 v200, v200, v216\n"
  "v_mul_f32 v201, v201, v217\n"
  "v_mul_f32 v202, v202, v218\n"
  "v_mul_f32 v203, v203, v219\n"
  "v_cvt_pk_bf16_f32 v186, v200, v201\n"
  "v_cvt_pk_bf16_f32 v187, v202, v203\n"
  "buffer_store_dwordx4 v[184:187], off, s[40:43], s59 offset:2048 sc1\n"
  "v_max_f32 v204, 0, v204\n"
  "v_max_f32 v205, 0, v205\n"
  "v_max_f32 v206, 0, v206\n"
  "v_max_f32 v207, 0, v207\n"
  "v_mul_f32 v204, v204, v220\n"
  "v_mul_f32 v205, v205, v221\n"
  "v_mul_f32 v206, v206, v222\n"
  "v_mul_f32 v207, v207, v223\n"
  "v_cvt_pk_bf16_f32 v190, v204, v205\n"
  "v_cvt_pk_bf16_f32 v191, v206, v207\n"
  "buffer_store_dwordx4 v[188:191], off, s[40:43], s59 offset:3072 sc1\n"
  "; ---- phase 5: down projection + residual\n"
  "s_waitcnt vmcnt(18)\n"
  "v_swap_b32 v2, v4\n"
  "v_swap_b32 v3, v5\n"
  "v_swap_b32 v10, v12\n"
  "v_swap_b32 v11, v13\n"
  "s_waitcnt vmcnt(14)\n"
  "v_swap_b32 v18, v20\n"
  "v_swap_b32 v19, v21\n"
  "v_swap_b32 v26, v28\n"
  "v_swap_b32 v27, v29\n"
  "s_barrier\n"
  "ds_read_b64_tr_b16 a[32:33], v232 offset:0\n"
  "ds_read_b64_tr_b16 a[34:35], v232 offset:4096\n"
  "ds_read_b64_tr_b16 a[36:37], v232 offset:8192\n"
  "ds_read_b64_tr_b16 a[38:39], v232 offset:12288\n"
  "ds_read_b64_tr_b16 a[40:41], v233 offset:0\n"
  "ds_read_b64_tr_b16 a[42:43], v233 offset:4096\n"
  "ds_read_b64_tr_b16 a[44:45], v233 offset:8192\n"
  "ds_read_b64_tr_b16 a[46:47], v233 offset:12288\n"
  "ds_read_b64_tr_b16 a[48:49], v234 offset:0\n"
  "ds_read_b64_tr_b16 a[50:51], v234 offset:4096\n"
  "s_waitcnt lgkmcnt(6)\n"
  "v_mfma_f32_16x16x32_bf16 v[96:99], a[32:35], v[0:3], 0\n"
  "ds_read_b64_tr_b16 a[52:53], v234 offset:8192\n"
  "s_add_u32 m0, s82, 32768\n"
  "v_mfma_f32_16x16x32_bf16 v[96:99], a[36:39], v[16:19], v[96:99]\n"
  "ds_read_b64_tr_b16 a[54:55], v234 offset:12288\n"
  "v_xor_b32 v240, 0x10000, v232\n"
  "v_mfma_f32_16x16x32_bf16 v[100:103], a[36:39], v[20:23], 0\n"
  "buffer_load_dwordx4 v228, s[76:79], s81 offen offset:0 lds\n"
  "v_mfma_f32_16x16x32_bf16 v[100:103], a[32:35], v[4:7], v[100:103]\n"
  "v_xor_b32 v241, 0x10000, v233\n"
  "v_xor_b32 v242, 0x10000, v234\n"
  "v_mfma_f32_16x16x32_bf16 v[104:107], a[32:35], v[8:11], 0\n"
  "ds_read_b64_tr_b16 a[56:57], v235 offset:0\n"
  "v_xor_b32 v243, 0x10000, v235\n"
  "v_mfma_f32_16x16x32_bf16 v[104:107], a[36:39], v[24:27], v[104:107]\n"
  "ds_read_b64_tr_b16 a[58:59], v235 offset:4096\n"
  "s_waitcnt lgkmcnt(6)\n"
  "v_mfma_f32_16x16x32_bf16 v[108:111], a[36:39], v[28:31], 0\n"
  "buffer_load_dwordx4 v229, s[76:79], s81 offen offset:1024 lds\n"
  "v_mfma_f32_16x16x32_bf16 v[108:111], a[32:35], v[12:15], v[108:111]\n"
  "v_mfma_f32_16x16x32_bf16 v[124:127], a[40:43], v[12:15], 0\n"
  "ds_read_b64_tr_b16 a[60:61], v235 offset:8192\n"
  "v_mfma_f32_16x16x32_bf16 v[124:127], a[44:47], v[28:31], v[124:127]\n"
  "ds_read_b64_tr_b16 a[62:63], v235 offset:12288\n"
  "v_mfma_f32_16x16x32_bf16 v[120:123], a[44:47], v[24:27], 0\n"
  "buffer_load_dwordx4 v230, s[76:79], s81 offen offset:2048 lds\n"
  "v_mfma_f32_16x16x32_bf16 v[120:123], a[40:43], v[8:11], v[120:123]\n"
  "v_mfma_f32_16x16x32_bf16 v[116:119], a[40:43], v[4:7], 0\n"
  "ds_read_b64_tr_b16 a[32:33], v232 offset:2048\n"
  "v_mfma_f32_16x16x32_bf16 v[116:119], a[44:47], v[20:23], v[116:119]\n"
  "ds_read_b64_tr_b16 a[34:35], v232 offset:6144\n"
  "s_waitcnt lgkmcnt(6)\n"
  "v_mfma_f32_16x16x32_bf16 v[112:115], a[44:47], v[16:19], 0\n"
  "buffer_load_dwordx4 v231, s[76:79], s81 offen offset:3072 lds\n"
  "v_mfma_f32_16x16x32_bf16 v[112:115], a[40:43], v[0:3], v[112:115]\n"
  "s_mul_i32 s81, s19, 56\n"
  "s_add_u32 s81, s81, s86\n"
  "v_mfma_f32_16x16x32_bf16 v[128:131], a[48:51], v[0:3], 0\n"
  "ds_read_b64_tr_b16 a[36:37], v232 offset:10240\n"
  "v_mfma_f32_16x16x32_bf16 v[128:131], a[52:55], v[16:19], v[128:131]\n"
  "ds_read_b64_tr_b16 a[38:39], v232 offset:14336\n"
  "v_mfma_f32_16x16x32_bf16 v[132:135], a[52:55], v[20:23], 0\n"
  "v_mfma_f32_16x16x32_bf16 v[132:135], a[48:51], v[4:7], v[132:135]\n"
  "v_mfma_f32_16x16x32_bf16 v[136:139], a[48:51], v[8:11], 0\n"
  "ds_read_b64_tr_b16 a[40:41], v233 offset:2048\n"
  "v_mfma_f32_16x16x32_bf16 v[136:139], a[52:55], v[24:27], v[136:139]\n"
  "ds_read_b64_tr_b16 a[42:43], v233 offset:6144\n"
  "s_waitcnt lgkmcnt(6)\n"
  "v_mfma_f32_16x16x32_bf16 v[140:143], a[52:55], v[28:31], 0\n"
  "v_mfma_f32_16x16x32_bf16 v[140:143], a[48:51], v[12:15], v[140:143]\n"
  "v_mfma_f32_16x16x32_bf16 v[156:159], a[56:59], v[12:15], 0\n"
  "ds_read_b64_tr_b16 a[44:45], v233 offset:10240\n"
  "v_mfma_f32_16x16x32_bf16 v[156:159], a[60:63], v[28:31], v[156:159]\n"
  "ds_read_b64_tr_b16 a[46:47], v233 offset:14336\n"
  "v_mfma_f32_16x16x32_bf16 v[152:155], a[60:63], v[24:27], 0\n"
  "s_waitcnt vmcnt(9)\n"
  "L_poll9_%=:\n"
  "v_cmp_ne_u64 vcc, s[10:11], v[238:239]\n"
  "s_cbranch_vccz L_ok9_%=\n"
  "s_sub_u32 s99, s99, 1\n"
  "s_cbranch_scc1 L_out9_%=\n"
  "buffer_load_dwordx2 v[238:239], v236, s[52:55], s83 offen sc1\n"
  "s_waitcnt vmcnt(0)\n"
  "s_branch L_poll9_%=\n"
  "L_out9_%=:\n"
  "s_mov_b32 s99, 0\n"
  "L_ok9_%=:\n"
  "v_mfma_f32_16x16x32_bf16 v[152:155], a[56:59], v[8:11], v[152:155]\n"
  "buffer_load_dwordx4 a[64:67], off, s[72:75], s80 offset:0 sc1\n"
  "v_mfma_f32_16x16x32_bf16 v[148:151], a[56:59], v[4:7], 0\n"
  "ds_read_b64_tr_b16 a[48:49], v234 offset:2048\n"
  "s_add_u32 m0, s82, 49152\n"
  "v_mfma_f32_16x16x32_bf16 v[148:151], a[60:63], v[20:23], v[148:151]\n"
  "ds_read_b64_tr_b16 a[50:51], v234 offset:6144\n"
  "s_waitcnt lgkmcnt(6)\n"
  "v_mfma_f32_16x16x32_bf16 v[144:147], a[60:63], v[16:19], 0\n"
  "buffer_load_dwordx4 a[68:71], off, s[72:75], s80 offset:1024 sc1\n"
  "v_mfma_f32_16x16x32_bf16 v[144:147], a[56:59], v[0:3], v[144:147]\n"
  "v_mfma_f32_16x16x32_bf16 v[160:163], a[32:35], v[0:3], 0\n"
  "ds_read_b64_tr_b16 a[52:53], v234 offset:10240\n"
  "v_mfma_f32_16x16x32_bf16 v[160:163], a[36:39], v[16:19], v[160:163]\n"
  "ds_read_b64_tr_b16 a[54:55], v234 offset:14336\n"
  "v_swap_b32 v34, v36\n"
  "v_mfma_f32_16x16x32_bf16 v[164:167], a[36:39], v[20:23], 0\n"
  "buffer_load_dwordx4 v228, s[76:79], s81 offen offset:0 lds\n"
  "v_mfma_f32_16x16x32_bf16 v[164:167], a[32:35], v[4:7], v[164:167]\n"
  "v_swap_b32 v35, v37\n"
  "v_swap_b32 v42, v44\n"
  "v_mfma_f32_16x16x32_bf16 v[168:171], a[32:35], v[8:11], 0\n"
  "ds_read_b64_tr_b16 a[56:57], v235 offset:2048\n"
  "v_swap_b32 v43, v45\n"
  "v_mfma_f32_16x16x32_bf16 v[168:171], a[36:39], v[24:27], v[168:171]\n"
  "ds_read_b64_tr_b16 a[58:59], v235 offset:6144\n"
  "s_waitcnt lgkmcnt(6)\n"
  "v_mfma_f32_16x16x32_bf16 v[172:175], a[36:39], v[28:31], 0\n"
  "buffer_load_dwordx4 a[72:75], off, s[72:75], s80 offset:2048 sc1\n"
  "v_mfma_f32_16x16x32_bf16 v[172:175], a[32:35], v[12:15], v[172:175]\n"
  "v_swap_b32 v50, v52\n"
  "v_swap_b32 v51, v53\n"
  "v_mfma_f32_16x16x32_bf16 v[188:191], a[40:43], v[12:15], 0\n"
  "ds_read_b64_tr_b16 a[60:61], v235 offset:10240\n"
  "v_swap_b32 v58, v60\n"
  "v_mfma_f32_16x16x32_bf16 v[188:191], a[44:47], v[28:31], v[188:191]\n"
  "ds_read_b64_tr_b16 a[62:63], v235 offset:14336\n"
  "v_swap_b32 v59, v61\n"
  "v_mfma_f32_16x16x32_bf16 v[184:187], a[44:47], v[24:27], 0\n"
  "buffer_load_dwordx4 v229, s[76:79], s81 offen offset:1024 lds\n"
  "v_mfma_f32_16x16x32_bf16 v[184:187], a[40:43], v[8:11], v[184:187]\n"
  "v_mfma_f32_16x16x32_bf16 v[180:183], a[40:43], v[4:7], 0\n"
  "ds_read_b64_tr_b16 a[32:33], v232 offset:16384\n"
  "v_mfma_f32_16x16x32_bf16 v[180:183], a[44:47], v[20:23], v[180:183]\n"
  "ds_read_b64_tr_b16 a[34:35], v232 offset:20480\n"
  "s_waitcnt lgkmcnt(6)\n"
  "v_mfma_f32_16x16x32_bf16 v[176:179], a[44:47], v[16:19], 0\n"
  "buffer_load_dwordx4 a[76:79], off, s[72:75], s80 offset:3072 sc1\n"
  "v_mfma_f32_16x16x32_bf16 v[176:179], a[40:43], v[0:3], v[176:179]\n"
  "v_mfma_f32_16x16x32_bf16 v[192:195], a[48:51], v[0:3], 0\n"
  "ds_read_b64_tr_b16 a[36:37], v232 offset:24576\n"
  "v_mfma_f32_16x16x32_bf16 v[192:195], a[52:55], v[16:19], v[192:195]\n"
  "ds_read_b64_tr_b16 a[38:39], v232 offset:28672\n"
  "v_mfma_f32_16x16x32_bf16 v[196:199], a[52:55], v[20:23], 0\n"
  "buffer_load_dwordx4 v230, s[76:79], s81 offen offset:2048 lds\n"
  "v_mfma_f32_16x16x32_bf16 v[196:199], a[48:51], v[4:7], v[196:199]\n"
  "s_add_u32 s80, s80, 4096\n"
  "v_mfma_f32_16x16x32_bf16 v[200:203], a[48:51], v[8:11], 0\n"
  "ds_read_b64_tr_b16 a[40:41], v233 offset:16384\n"
  "v_mfma_f32_16x16x32_bf16 v[200:203], a[52:55], v[24:27], v[200:203]\n"
  "ds_read_b64_tr_b16 a[42:43], v233 offset:20480\n"
  "s_waitcnt lgkmcnt(6)\n"
  "v_mfma_f32_16x16x32_bf16 v[204:207], a[52:55], v[28:31], 0\n"
  "buffer_load_dwordx4 v231, s[76:79], s81 offen offset:3072 lds\n"
  "v_mfma_f32_16x16x32_bf16 v[204:207], a[48:51], v[12:15], v[204:207]\n"
  "s_mul_i32 s81, s16, 56\n"
  "s_add_u32 s81, s81, s86\n"
  "s_add_u32 s81, s81, 65536\n"
  "v_mfma_f32_16x16x32_bf16 v[220:223], a[56:59], v[12:15], 0\n"
  "ds_read_b64_tr_b16 a[44:45], v233 offset:24576\n"
  "v_mfma_f32_16x16x32_bf16 v[220:223], a[60:63], v[28:31], v[220:223]\n"
  "ds_read_b64_tr_b16 a[46:47], v233 offset:28672\n"
  "v_mfma_f32_16x16x32_bf16 v[216:219], a[60:63], v[24:27], 0\n"
  "buffer_load_dwordx4 a[80:83], off, s[72:75], s80 offset:0 sc1\n"
  "v_mfma_f32_16x16x32_bf16 v[216:219], a[56:59], v[8:11], v[216:219]\n"
  "v_mfma_f32_16x16x32_bf16 v[212:215], a[56:59], v[4:7], 0\n"
  "ds_read_b64_tr_b16 a[48:49], v234 offset:16384\n"
  "v_mfma_f32_16x16x32_bf16 v[212:215], a[60:63], v[20:23], v[212:215]\n"
  "ds_read_b64_tr_b16 a[50:51], v234 offset:20480\n"
  "s_waitcnt lgkmcnt(6)\n"
  "v_mfma_f32_16x16x32_bf16 v[208:211], a[60:63], v[16:19], 0\n"
  "buffer_load_dwordx4 a[84:87], off, s[72:75], s80 offset:1024 sc1\n"
  "v_mfma_f32_16x16x32_bf16 v[208:211], a[56:59], v[0:3], v[208:211]\n"
  "v_mfma_f32_16x16x32_bf16 v[96:99], a[32:35], v[32:35], v[96:99]\n"
  "ds_read_b64_tr_b16 a[52:53], v234 offset:24576\n"
  "v_mfma_f32_16x16x32_bf16 v[96:99], a[36:39], v[48:51], v[96:99]\n"
  "ds_read_b64_tr_b16 a[54:55], v234 offset:28672\n"
  "v_accvgpr_read_b32 v64, a192\n"
  "v_mfma_f32_16x16x32_bf16 v[100:103], a[36:39], v[52:55], v[100:103]\n"
  "buffer_load_dwordx4 a[88:91], off, s[72:75], s80 offset:2048 sc1\n"
  "v_mfma_f32_16x16x32_bf16 v[100:103], a[32:35], v[36:39], v[100:103]\n"
  "v_accvgpr_read_b32 v65, a193\n"
  "v_accvgpr_read_b32 v66, a194\n"
  "v_mfma_f32_16x16x32_bf16 v[104:107], a[32:35], v[40:43], v[104:107]\n"
  "ds_read_b64_tr_b16 a[56:57], v235 offset:16384\n"
  "v_accvgpr_read_b32 v67, a195\n"
  "v_mfma_f32_16x16x32_bf16 v[104:107], a[36:39], v[56:59], v[104:107]\n"
  "ds_read_b64_tr_b16 a[58:59], v235 offset:20480\n"
  "s_waitcnt lgkmcnt(6)\n"
  "v_mfma_f32_16x16x32_bf16 v[108:111], a[36:39], v[60:63], v[108:111]\n"
  "buffer_load_dwordx4 a[92:95], off, s[72:75], s80 offset:3072 sc1\n"
  "v_mfma_f32_16x16x32_bf16 v[108:111], a[32:35], v[44:47], v[108:111]\n"
  "s_add_u32 s80, s17, 16384\n"
  "v_accvgpr_read_b32 v68, a196\n"
  "v_accvgpr_read_b32 v69, a197\n"
  "v_mfma_f32_16x16x32_bf16 v[124:127], a[40:43], v[44:47], v[124:127]\n"
  "ds_read_b64_tr_b16 a[60:61], v235 offset:24576\n"
  "v_accvgpr_read_b32 v70, a198\n"
  "v_mfma_f32_16x16x32_bf16 v[124:127], a[44:47], v[60:63], v[124:127]\n"
  "ds_read_b64_tr_b16 a[62:63], v235 offset:28672\n"
  "v_accvgpr_read_b32 v71, a199\n"
  "v_mfma_f32_16x16x32_bf16 v[120:123], a[44:47], v[56:59], v[120:123]\n"
  "buffer_load_dwordx4 a[96:99], off, s[72:75], s80 offset:0 sc1\n"
  "v_mfma_f32_16x16x32_bf16 v[120:123], a[40:43], v[40:43], v[120:123]\n"
  "s_add_u32 m0, s82, 65536\n"
  "v_accvgpr_read_b32 v72, a200\n"
  "v_accvgpr_read_b32 v73, a201\n"
  "v_mfma_f32_16x16x32_bf16 v[116:119], a[40:43], v[36:39], v[116:119]\n"
  "ds_read_b64_tr_b16 a[32:33], v232 offset:18432\n"
  "v_accvgpr_read_b32 v74, a202\n"
  "v_mfma_f32_16x16x32_bf16 v[116:119], a[44:47], v[52:55], v[116:119]\n"
  "ds_read_b64_tr_b16 a[34:35], v232 offset:22528\n"
  "s_waitcnt lgkmcnt(6)\n"
  "v_mfma_f32_16x16x32_bf16 v[112:115], a[44:47], v[48:51], v[112:115]\n"
  "buffer_load_dwordx4 a[100:103], off, s[72:75], s80 offset:1024 sc1\n"
  "v_mfma_f32_16x16x32_bf16 v[112:115], a[40:43], v[32:35], v[112:115]\n"
  "v_accvgpr_read_b32 v75, a203\n"
  "v_accvgpr_read_b32 v76, a204\n"
  "v_mfma_f32_16x16x32_bf16 v[128:131], a[48:51], v[32:35], v[128:131]\n"
  "ds_read_b64_tr_b16 a[36:37], v232 offset:26624\n"
  "v_accvgpr_read_b32 v77, a205\n"
  "v_mfma_f32_16x16x32_bf16 v[128:131], a[52:55], v[48:51], v[128:131]\n"
  "ds_read_b64_tr_b16 a[38:39], v232 offset:30720\n"
  "v_accvgpr_read_b32 v78, a206\n"
  "v_mfma_f32_16x16x32_bf16 v[132:135], a[52:55], v[52:55], v[132:135]\n"
  "buffer_load_dwordx4 v228, s[76:79], s81 offen offset:0 lds\n"
  "v_mfma_f32_16x16x32_bf16 v[132:135], a[48:51], v[36:39], v[132:135]\n"
  "v_accvgpr_read_b32 v79, a207\n"
  "v_accvgpr_read_b32 v80, a208\n"
  "v_mfma_f32_16x16x32_bf16 v[136:139], a[48:51], v[40:43], v[136:139]\n"
  "ds_read_b64_tr_b16 a[40:41], v233 offset:18432\n"
  "v_accvgpr_read_b32 v84, a209\n"
  "v_mfma_f32_16x16x32_bf16 v[136:139], a[52:55], v[56:59], v[136:139]\n"
  "ds_read_b64_tr_b16 a[42:43], v233 offset:22528\n"
  "s_waitcnt lgkmcnt(6)\n"
  "v_mfma_f32_16x16x32_bf16 v[140:143], a[52:55], v[60:63], v[140:143]\n"
  "buffer_load_dwordx4 a[104:107], off, s[72:75], s80 offset:2048 sc1\n"
  "v_mfma_f32_16x16x32_bf16 v[140:143], a[48:51], v[44:47], v[140:143]\n"
  "v_accvgpr_read_b32 v88, a212\n"
  "v_accvgpr_read_b32 v89, a216\n"
  "v_mfma_f32_16x16x32_bf16 v[156:159], a[56:59], v[44:47], v[156:159]\n"
  "ds_read_b64_tr_b16 a[44:45], v233 offset:26624\n"
  "v_accvgpr_read_b32 v90, a220\n"
  "v_mfma_f32_16x16x32_bf16 v[156:159], a[60:63], v[60:63], v[156:159]\n"
  "ds_read_b64_tr_b16 a[46:47], v233 offset:30720\n"
  "v_add_f32 v80, v80, v88\n"
  "v_mfma_f32_16x16x32_bf16 v[152:155], a[60:63], v[56:59], v[152:155]\n"
  "buffer_load_dwordx4 v229, s[76:79], s81 offen offset:1024 lds\n"
  "v_mfma_f32_16x16x32_bf16 v[152:155], a[56:59], v[40:43], v[152:155]\n"
  "v_add_f32 v89, v89, v90\n"
  "v_add_f32 v80, v80, v89\n"
  "v_mfma_f32_16x16x32_bf16 v[148:151], a[56:59], v[36:39], v[148:151]\n"
  "ds_read_b64_tr_b16 a[48:49], v234 offset:18432\n"
  "v_accvgpr_read_b32 v81, a210\n"
  "v_mfma_f32_16x16x32_bf16 v[148:151], a[60:63], v[52:55], v[148:151]\n"
  "ds_read_b64_tr_b16 a[50:51], v234 offset:22528\n"
  "s_waitcnt lgkmcnt(6)\n"
  "v_mfma_f32_16x16x32_bf16 v[144:147], a[60:63], v[48:51], v[144:147]\n"
  "buffer_load_dwordx4 a[108:111], off, s[72:75], s80 offset:3072 sc1\n"
  "v_mfma_f32_16x16x32_bf16 v[144:147], a[56:59], v[32:35], v[144:147]\n"
  "v_accvgpr_read_b32 v85, a211\n"
  "v_accvgpr_read_b32 v88, a214\n"
  "v_mfma_f32_16x16x32_bf16 v[160:163], a[32:35], v[32:35], v[160:163]\n"
  "ds_read_b64_tr_b16 a[52:53], v234 offset:26624\n"
  "v_accvgpr_read_b32 v89, a218\n"
  "v_mfma_f32_16x16x32_bf16 v[160:163], a[36:39], v[48:51], v[160:163]\n"
  "ds_read_b64_tr_b16 a[54:55], v234 offset:30720\n"
  "v_accvgpr_read_b32 v90, a222\n"
  "v_mfma_f32_16x16x32_bf16 v[164:167], a[36:39], v[52:55], v[164:167]\n"
  "buffer_load_dwordx4 v230, s[76:79], s81 offen offset:2048 lds\n"
  "v_mfma_f32_16x16x32_bf16 v[164:167], a[32:35], v[36:39], v[164:167]\n"
  "s_add_u32 s80, s80, 4096\n"
  "v_add_f32 v81, v81, v88\n"
  "v_add_f32 v89, v89, v90\n"
  "v_mfma_f32_16x16x32_bf16 v[168:171], a[32:35], v[40:43], v[168:171]\n"
  "ds_read_b64_tr_b16 a[56:57], v235 offset:18432\n"
  "v_add_f32 v81, v81, v89\n"
  "v_mfma_f32_16x16x32_bf16 v[168:171], a[36:39], v[56:59], v[168:171]\n"
  "ds_read_b64_tr_b16 a[58:59], v235 offset:22528\n"
  "s_waitcnt lgkmcnt(6)\n"
  "v_mfma_f32_16x16x32_bf16 v[172:175], a[36:39], v[60:63], v[172:175]\n"
  "buffer_load_dwordx4 v231, s[76:79], s81 offen offset:3072 lds\n"
  "v_mfma_f32_16x16x32_bf16 v[172:175], a[32:35], v[44:47], v[172:175]\n"
  "s_mul_i32 s81, s17, 56\n"
  "s_add_u32 s81, s81, s86\n"
  "s_add_u32 s81, s81, 65536\n"
  "v_mfma_f32_16x16x32_bf16 v[188:191], a[40:43], v[44:47], v[188:191]\n"
  "ds_read_b64_tr_b16 a[60:61], v235 offset:26624\n"
  "v_accvgpr_read_b32 v82, a224\n"
  "v_mfma_f32_16x16x32_bf16 v[188:191], a[44:47], v[60:63], v[188:191]\n"
  "ds_read_b64_tr_b16 a[62:63], v235 offset:30720\n"
  "v_accvgpr_read_b32 v86, a225\n"
  "v_mfma_f32_16x16x32_bf16 v[184:187], a[44:47], v[56:59], v[184:187]\n"
  "buffer_load_dwordx4 a[112:115], off, s[72:75], s80 offset:0 sc1\n"
  "v_mfma_f32_16x16x32_bf16 v[184:187], a[40:43], v[40:43], v[184:187]\n"
  "v_accvgpr_read_b32 v88, a228\n"
  "v_accvgpr_read_b32 v89, a232\n"
  "v_mfma_f32_16x16x32_bf16 v[180:183], a[40:43], v[36:39], v[180:183]\n"
  "s_barrier\n"
  "v_accvgpr_read_b32 v90, a236\n"
  "v_mfma_f32_16x16x32_bf16 v[180:183], a[44:47], v[52:55], v[180:183]\n"
  "ds_read_b64_tr_b16 a[32:33], v232 offset:32768\n"
  "v_add_f32 v82, v82, v88\n"
  "v_mfma_f32_16x16x32_bf16 v[176:179], a[44:47], v[48:51], v[176:179]\n"
  "ds_read_b64_tr_b16 a[34:35], v232 offset:36864\n"
  "s_waitcnt lgkmcnt(6)\n"
  "v_mfma_f32_16x16x32_bf16 v[176:179], a[40:43], v[32:35], v[176:179]\n"
  "buffer_load_dwordx4 a[116:119], off, s[72:75], s80 offset:1024 sc1\n"
  "v_mfma_f32_16x16x32_bf16 v[192:195], a[48:51], v[32:35], v[192:195]\n"
  "ds_read_b64_tr_b16 a[36:37], v232 offset:40960\n"
  "v_add_f32 v89, v89, v90\n"
  "v_mfma_f32_16x16x32_bf16 v[192:195], a[52:55], v[48:51], v[192:195]\n"
  "ds_read_b64_tr_b16 a[38:39], v232 offset:45056\n"
  "v_add_f32 v82, v82, v89\n"
  "v_mfma_f32_16x16x32_bf16 v[196:199], a[52:55], v[52:55], v[196:199]\n"
  "buffer_load_dwordx4 a[120:123], off, s[72:75], s80 offset:2048 sc1\n"
  "v_mfma_f32_16x16x32_bf16 v[196:199], a[48:51], v[36:39], v[196:199]\n"
  "v_accvgpr_read_b32 v83, a226\n"
  "v_accvgpr_read_b32 v87, a227\n"
  "v_mfma_f32_16x16x32_bf16 v[200:203], a[48:51], v[40:43], v[200:203]\n"
  "ds_read_b64_tr_b16 a[40:41], v233 offset:32768\n"
  "v_accvgpr_read_b32 v88, a230\n"
  "v_mfma_f32_16x16x32_bf16 v[200:203], a[52:55], v[56:59], v[200:203]\n"
  "ds_read_b64_tr_b16 a[42:43], v233 offset:36864\n"
  "s_waitcnt lgkmcnt(6)\n"
  "v_mfma_f32_16x16x32_bf16 v[204:207], a[52:55], v[60:63], v[204:207]\n"
  "buffer_load_dwordx4 a[124:127], off, s[72:75], s80 offset:3072 sc1\n"
  "v_mfma_f32_16x16x32_bf16 v[204:207], a[48:51], v[44:47], v[204:207]\n"
  "s_add_u32 s80, s18, 16384\n"
  "s_add_u32 s83, s83, 2048\n"
  "v_accvgpr_read_b32 v89, a234\n"
  "v_mfma_f32_16x16x32_bf16 v[220:223], a[56:59], v[44:47], v[220:223]\n"
  "ds_read_b64_tr_b16 a[44:45], v233 offset:40960\n"
  "v_accvgpr_read_b32 v90, a238\n"
  "v_mfma_f32_16x16x32_bf16 v[220:223], a[60:63], v[60:63], v[220:223]\n"
  "ds_read_b64_tr_b16 a[46:47], v233 offset:45056\n"
  "v_add_f32 v83, v83, v88\n"
  "v_mfma_f32_16x16x32_bf16 v[216:219], a[60:63], v[56:59], v[216:219]\n"
  "buffer_load_dwordx2 v[238:239], v236, s[52:55], s83 offen sc1\n"
  "v_mfma_f32_16x16x32_bf16 v[216:219], a[56:59], v[40:43], v[216:219]\n"
  "v_add_f32 v89, v89, v90\n"
  "v_add_f32 v83, v83, v89\n"
  "v_mfma_f32_16x16x32_bf16 v[212:215], a[56:59], v[36:39], v[212:215]\n"
  "ds_read_b64_tr_b16 a[48:49], v234 offset:32768\n"
  "v_mfma_f32_16x16x32_bf16 v[212:215], a[60:63], v[52:55], v[212:215]\n"
  "ds_read_b64_tr_b16 a[50:51], v234 offset:36864\n"
  "s_waitcnt vmcnt(13) lgkmcnt(6)\n"
  "v_mfma_f32_16x16x32_bf16 v[208:211], a[60:63], v[48:51], v[208:211]\n"
  "buffer_load_dwordx4 a[128:131], off, s[72:75], s80 offset:0 sc1\n"
  "v_mfma_f32_16x16x32_bf16 v[208:211], a[56:59], v[32:35], v[208:211]\n"
  "s_add_u32 m0, s82, 81920\n"
  "v_mfma_f32_16x16x32_bf16 v[96:99], a[32:35], a[64:67], v[96:99]\n"
  "ds_read_b64_tr_b16 a[52:53], v234 offset:40960\n"
  "v_mfma_f32_16x16x32_bf16 v[96:99], a[36:39], a[80:83], v[96:99]\n"
  "ds_read_b64_tr_b16 a[54:55], v234 offset:45056\n"
  "v_mfma_f32_16x16x32_bf16 v[100:103], a[36:39], a[84:87], v[100:103]\n"
  "buffer_load_dwordx4 a[132:135], off, s[72:75], s80 offset:1024 sc1\n"
  "v_mfma_f32_16x16x32_bf16 v[100:103], a[32:35], a[68:71], v[100:103]\n"
  "v_mfma_f32_16x16x32_bf16 v[104:107], a[32:35], a[72:75], v[104:107]\n"
  "ds_read_b64_tr_b16 a[56:57], v235 offset:32768\n"
  "v_mfma_f32_16x16x32_bf16 v[104:107], a[36:39], a[88:91], v[104:107]\n"
  "ds_read_b64_tr_b16 a[58:59], v235 offset:36864\n"
  "s_waitcnt lgkmcnt(6)\n"
  "v_mfma_f32_16x16x32_bf16 v[108:111], a[36:39], a[92:95], v[108:111]\n"
  "buffer_load_dwordx4 v228, s[76:79], s81 offen offset:0 lds\n"
  "v_mfma_f32_16x16x32_bf16 v[108:111], a[32:35], a[76:79], v[108:111]\n"
  "v_mfma_f32_16x16x32_bf16 v[124:127], a[40:43], a[76:79], v[124:127]\n"
  "ds_read_b64_tr_b16 a[60:61], v235 offset:40960\n"
  "v_mfma_f32_16x16x32_bf16 v[124:127], a[44:47], a[92:95], v[124:127]\n"
  "ds_read_b64_tr_b16 a[62:63], v235 offset:45056\n"
  "v_mfma_f32_16x16x32_bf16 v[120:123], a[44:47], a[88:91], v[120:123]\n"
  "buffer_load_dwordx4 a[136:139], off, s[72:75], s80 offset:2048 sc1\n"
  "v_mfma_f32_16x16x32_bf16 v[120:123], a[40:43], a[72:75], v[120:123]\n"
  "v_mfma_f32_16x16x32_bf16 v[116:119], a[40:43], a[68:71], v[116:119]\n"
  "ds_read_b64_tr_b16 a[32:33], v232 offset:34816\n"
  "v_mfma_f32_16x16x32_bf16 v[116:119], a[44:47], a[84:87], v[116:119]\n"
  "ds_read_b64_tr_b16 a[34:35], v232 offset:38912\n"
  "s_waitcnt lgkmcnt(6)\n"
  "v_mfma_f32_16x16x32_bf16 v[112:115], a[44:47], a[80:83], v[112:115]\n"
  "buffer_load_dwordx4 v229, s[76:79], s81 offen offset:1024 lds\n"
  "v_mfma_f32_16x16x32_bf16 v[112:115], a[40:43], a[64:67], v[112:115]\n"
  "v_mfma_f32_16x16x32_bf16 v[128:131], a[48:51], a[64:67], v[128:131]\n"
  "ds_read_b64_tr_b16 a[36:37], v232 offset:43008\n"
  "v_mfma_f32_16x16x32_bf16 v[128:131], a[52:55], a[80:83], v[128:131]\n"
  "ds_read_b64_tr_b16 a[38:39], v232 offset:47104\n"
  "v_mfma_f32_16x16x32_bf16 v[132:135], a[52:55], a[84:87], v[132:135]\n"
  "buffer_load_dwordx4 a[140:143], off, s[72:75], s80 offset:3072 sc1\n"
  "v_mfma_f32_16x16x32_bf16 v[132:135], a[48:51], a[68:71], v[132:135]\n"
  "v_mfma_f32_16x16x32_bf16 v[136:139], a[48:51], a[72:75], v[136:139]\n"
  "ds_read_b64_tr_b16 a[40:41], v233 offset:34816\n"
  "v_mfma_f32_16x16x32_bf16 v[136:139], a[52:55], a[88:91], v[136:139]\n"
  "ds_read_b64_tr_b16 a[42:43], v233 offset:38912\n"
  "s_waitcnt lgkmcnt(6)\n"
  "v_mfma_f32_16x16x32_bf16 v[140:143], a[52:55], a[92:95], v[140:143]\n"
  "buffer_load_dwordx4 v230, s[76:79], s81 offen offset:2048 lds\n"
  "v_mfma_f32_16x16x32_bf16 v[140:143], a[48:51], a[76:79], v[140:143]\n"
  "s_add_u32 s80, s80, 4096\n"
  "v_mfma_f32_16x16x32_bf16 v[156:159], a[56:59], a[76:79], v[156:159]\n"
  "ds_read_b64_tr_b16 a[44:45], v233 offset:43008\n"
  "v_mfma_f32_16x16x32_bf16 v[156:159], a[60:63], a[92:95], v[156:159]\n"
  "ds_read_b64_tr_b16 a[46:47], v233 offset:47104\n"
  "v_mfma_f32_16x16x32_bf16 v[152:155], a[60:63], a[88:91], v[152:155]\n"
  "buffer_load_dwordx4 v231, s[76:79], s81 offen offset:3072 lds\n"
  "v_mfma_f32_16x16x32_bf16 v[152:155], a[56:59], a[72:75], v[152:155]\n"
  "s_mul_i32 s81, s18, 56\n"
  "s_add_u32 s81, s81, s86\n"
  "s_add_u32 s81, s81, 65536\n"
  "v_mfma_f32_16x16x32_bf16 v[148:151], a[56:59], a[68:71], v[148:151]\n"
  "ds_read_b64_tr_b16 a[48:49], v234 offset:34816\n"
  "v_mfma_f32_16x16x32_bf16 v[148:151], a[60:63], a[84:87], v[148:151]\n"
  "ds_read_b64_tr_b16 a[50:51], v234 offset:38912\n"
  "s_waitcnt lgkmcnt(6)\n"
  "v_mfma_f32_16x16x32_bf16 v[144:147], a[60:63], a[80:83], v[144:147]\n"
  "buffer_load_dwordx4 a[144:147], off, s[72:75], s80 offset:0 sc1\n"
  "v_mfma_f32_16x16x32_bf16 v[144:147], a[56:59], a[64:67], v[144:147]\n"
  "v_mfma_f32_16x16x32_bf16 v[160:163], a[32:35], a[64:67], v[160:163]\n"
  "ds_read_b64_tr_b16 a[52:53], v234 offset:43008\n"
  "v_mov_b32 v88, v80\n"
  "v_mfma_f32_16x16x32_bf16 v[160:163], a[36:39], a[80:83], v[160:163]\n"
  "ds_read_b64_tr_b16 a[54:55], v234 offset:47104\n"
  "v_mov_b32 v89, v81\n"
  "v_mfma_f32_16x16x32_bf16 v[164:167], a[36:39], a[84:87], v[164:167]\n"
  "buffer_load_dwordx4 a[148:151], off, s[72:75], s80 offset:1024 sc1\n"
  "v_mfma_f32_16x16x32_bf16 v[164:167], a[32:35], a[68:71], v[164:167]\n"
  "v_mov_b32 v90, v82\n"
  "v_mov_b32 v91, v83\n"
  "v_mfma_f32_16x16x32_bf16 v[168:171], a[32:35], a[72:75], v[168:171]\n"
  "ds_read_b64_tr_b16 a[56:57], v235 offset:34816\n"
  "v_permlane16_swap_b32 v88, v80\n"
  "v_mfma_f32_16x16x32_bf16 v[168:171], a[36:39], a[88:91], v[168:171]\n"
  "ds_read_b64_tr_b16 a[58:59], v235 offset:38912\n"
  "s_waitcnt lgkmcnt(6)\n"
  "v_mfma_f32_16x16x32_bf16 v[172:175], a[36:39], a[92:95], v[172:175]\n"
  "buffer_load_dwordx4 a[152:155], off, s[72:75], s80 offset:2048 sc1\n"
  "v_mfma_f32_16x16x32_bf16 v[172:175], a[32:35], a[76:79], v[172:175]\n"
  "v_permlane16_swap_b32 v89, v81\n"
  "v_permlane16_swap_b32 v90, v82\n"
  "v_mfma_f32_16x16x32_bf16 v[188:191], a[40:43], a[76:79], v[188:191]\n"
  "ds_read_b64_tr_b16 a[60:61], v235 offset:43008\n"
  "v_permlane16_swap_b32 v91, v83\n"
  "v_mfma_f32_16x16x32_bf16 v[188:191], a[44:47], a[92:95], v[188:191]\n"
  "ds_read_b64_tr_b16 a[62:63], v235 offset:47104\n"
  "v_add_f32 v80, v88, v80\n"
  "v_mfma_f32_16x16x32_bf16 v[184:187], a[44:47], a[88:91], v[184:187]\n"
  "buffer_load_dwordx4 a[156:159], off, s[72:75], s80 offset:3072 sc1\n"
  "v_mfma_f32_16x16x32_bf16 v[184:187], a[40:43], a[72:75], v[184:187]\n"
  "s_add_u32 s80, s19, 16384\n"
  "v_add_f32 v81, v89, v81\n"
  "v_add_f32 v82, v90, v82\n"
  "v_mfma_f32_16x16x32_bf16 v[180:183], a[40:43], a[68:71], v[180:183]\n"
  "s_barrier\n"
  "v_add_f32 v83, v91, v83\n"
  "v_mfma_f32_16x16x32_bf16 v[180:183], a[44:47], a[84:87], v[180:183]\n"
  "ds_read_b64_tr_b16 a[32:33], v232 offset:49152\n"
  "v_mov_b32 v88, v80\n"
  "v_mfma_f32_16x16x32_bf16 v[176:179], a[44:47], a[80:83], v[176:179]\n"
  "ds_read_b64_tr_b16 a[34:35], v232 offset:53248\n"
  "s_waitcnt lgkmcnt(6)\n"
  "v_mfma_f32_16x16x32_bf16 v[176:179], a[40:43], a[64:67], v[176:179]\n"
  "buffer_store_dwordx4 v[248:251], v251, s[52:55], s67 offen sc1\n"
  "v_mfma_f32_16x16x32_bf16 v[192:195], a[48:51], a[64:67], v[192:195]\n"
  "ds_read_b64_tr_b16 a[36:37], v232 offset:57344\n"
  "v_mov_b32 v89, v81\n"
  "v_mfma_f32_16x16x32_bf16 v[192:195], a[52:55], a[80:83], v[192:195]\n"
  "ds_read_b64_tr_b16 a[38:39], v232 offset:61440\n"
  "v_mov_b32 v90, v82\n"
  "v_mfma_f32_16x16x32_bf16 v[196:199], a[52:55], a[84:87], v[196:199]\n"
  "buffer_load_dwordx4 a[160:163], off, s[72:75], s80 offset:0 sc1\n"
  "v_mfma_f32_16x16x32_bf16 v[196:199], a[48:51], a[68:71], v[196:199]\n"
  "s_mov_b32 s84, s81\n"
  "v_mov_b32 v91, v83\n"
  "v_permlane32_swap_b32 v88, v80\n"
  "v_mfma_f32_16x16x32_bf16 v[200:203], a[48:51], a[72:75], v[200:203]\n"
  "ds_read_b64_tr_b16 a[40:41], v233 offset:49152\n"
  "v_permlane32_swap_b32 v89, v81\n"
  "v_mfma_f32_16x16x32_bf16 v[200:203], a[52:55], a[88:91], v[200:203]\n"
  "ds_read_b64_tr_b16 a[42:43], v233 offset:53248\n"
  "s_waitcnt lgkmcnt(6)\n"
  "v_mfma_f32_16x16x32_bf16 v[204:207], a[52:55], a[92:95], v[204:207]\n"
  "buffer_load_dwordx4 a[164:167], off, s[72:75], s80 offset:1024 sc1\n"
  "v_mfma_f32_16x16x32_bf16 v[204:207], a[48:51], a[76:79], v[204:207]\n"
  "s_add_u32 m0, s82, 98304\n"
  "v_permlane32_swap_b32 v90, v82\n"
  "v_permlane32_swap_b32 v91, v83\n"
  "v_mfma_f32_16x16x32_bf16 v[220:223], a[56:59], a[76:79], v[220:223]\n"
  "ds_read_b64_tr_b16 a[44:45], v233 offset:57344\n"
  "v_add_f32 v80, v88, v80\n"
  "v_mfma_f32_16x16x32_bf16 v[220:223], a[60:63], a[92:95], v[220:223]\n"
  "ds_read_b64_tr_b16 a[46:47], v233 offset:61440\n"
  "v_add_f32 v81, v89, v81\n"
  "v_mfma_f32_16x16x32_bf16 v[216:219], a[60:63], a[88:91], v[216:219]\n"
  "buffer_load_dwordx4 a[168:171], off, s[72:75], s80 offset:2048 sc1\n"
  "v_mfma_f32_16x16x32_bf16 v[216:219], a[56:59], a[72:75], v[216:219]\n"
  "v_add_f32 v82, v90, v82\n"
  "v_add_f32 v83, v91, v83\n"
  "v_mfma_f32_16x16x32_bf16 v[212:215], a[56:59], a[68:71], v[212:215]\n"
  "ds_read_b64_tr_b16 a[48:49], v234 offset:49152\n"
  "v_mov_b32 v88, 0x3727c5ac\n"
  "v_mfma_f32_16x16x32_bf16 v[212:215], a[60:63], a[84:87], v[212:215]\n"
  "ds_read_b64_tr_b16 a[50:51], v234 offset:53248\n"
  "s_waitcnt vmcnt(17) lgkmcnt(6)\n"
  "v_mfma_f32_16x16x32_bf16 v[208:211], a[60:63], a[80:83], v[208:211]\n"
  "buffer_load_dwordx4 v228, s[76:79], s81 offen offset:0 lds\n"
  "v_mfma_f32_16x16x32_bf16 v[208:211], a[56:59], a[64:67], v[208:211]\n"
  "v_fmac_f32 v88, 0x3a800000, v80\n"
  "v_mfma_f32_16x16x32_bf16 v[96:99], a[32:35], a[96:99], v[96:99]\n"
  "ds_read_b64_tr_b16 a[52:53], v234 offset:57344\n"
  "v_mfma_f32_16x16x32_bf16 v[96:99], a[36:39], a[112:115], v[96:99]\n"
  "ds_read_b64_tr_b16 a[54:55], v234 offset:61440\n"
  "v_mfma_f32_16x16x32_bf16 v[100:103], a[36:39], a[116:119], v[100:103]\n"
  "buffer_load_dwordx4 a[172:175], off, s[72:75], s80 offset:3072 sc1\n"
  "v_mfma_f32_16x16x32_bf16 v[100:103], a[32:35], a[100:103], v[100:103]\n"
  "v_rcp_f32 v80, v88\n"
  "v_mfma_f32_16x16x32_bf16 v[104:107], a[32:35], a[104:107], v[104:107]\n"
  "ds_read_b64_tr_b16 a[56:57], v235 offset:49152\n"
  "v_mov_b32 v88, 0x3727c5ac\n"
  "v_mfma_f32_16x16x32_bf16 v[104:107], a[36:39], a[120:123], v[104:107]\n"
  "ds_read_b64_tr_b16 a[58:59], v235 offset:53248\n"
  "s_waitcnt lgkmcnt(6)\n"
  "v_mfma_f32_16x16x32_bf16 v[108:111], a[36:39], a[124:127], v[108:111]\n"
  "buffer_load_dwordx4 v229, s[76:79], s81 offen offset:1024 lds\n"
  "v_mfma_f32_16x16x32_bf16 v[108:111], a[32:35], a[108:111], v[108:111]\n"
  "s_add_u32 s80, s80, 4096\n"
  "s_mul_i32 s81, s19, 56\n"
  "s_add_u32 s81, s81, s86\n"
  "v_mfma_f32_16x16x32_bf16 v[124:127], a[40:43], a[108:111], v[124:127]\n"
  "ds_read_b64_tr_b16 a[60:61], v235 offset:57344\n"
  "s_add_u32 s81, s81, 65536\n"
  "v_mfma_f32_16x16x32_bf16 v[124:127], a[44:47], a[124:127], v[124:127]\n"
  "ds_read_b64_tr_b16 a[62:63], v235 offset:61440\n"
  "v_fmac_f32 v88, 0x3a800000, v81\n"
  "v_mfma_f32_16x16x32_bf16 v[120:123], a[44:47], a[120:123], v[120:123]\n"
  "buffer_load_dwordx4 a[176:179], off, s[72:75], s80 offset:0 sc1\n"
  "v_mfma_f32_16x16x32_bf16 v[120:123], a[40:43], a[104:107], v[120:123]\n"
  "v_rcp_f32 v81, v88\n"
  "v_mfma_f32_16x16x32_bf16 v[116:119], a[40:43], a[100:103], v[116:119]\n"
  "ds_read_b64_tr_b16 a[32:33], v232 offset:51200\n"
  "v_mov_b32 v88, 0x3727c5ac\n"
  "v_mfma_f32_16x16x32_bf16 v[116:119], a[44:47], a[116:119], v[116:119]\n"
  "ds_read_b64_tr_b16 a[34:35], v232 offset:55296\n"
  "s_waitcnt lgkmcnt(6)\n"
  "v_mfma_f32_16x16x32_bf16 v[112:115], a[44:47], a[112:115], v[112:115]\n"
  "buffer_load_dwordx4 a[180:183], off, s[72:75], s80 offset:1024 sc1\n"
  "v_mfma_f32_16x16x32_bf16 v[112:115], a[40:43], a[96:99], v[112:115]\n"
  "v_fmac_f32 v88, 0x3a800000, v82\n"
  "v_mfma_f32_16x16x32_bf16 v[128:131], a[48:51], a[96:99], v[128:131]\n"
  "ds_read_b64_tr_b16 a[36:37], v232 offset:59392\n"
  "v_mfma_f32_16x16x32_bf16 v[128:131], a[52:55], a[112:115], v[128:131]\n"
  "ds_read_b64_tr_b16 a[38:39], v232 offset:63488\n"
  "v_mfma_f32_16x16x32_bf16 v[132:135], a[52:55], a[116:119], v[132:135]\n"
  "buffer_load_dwordx4 a[184:187], off, s[72:75], s80 offset:2048 sc1\n"
  "v_mfma_f32_16x16x32_bf16 v[132:135], a[48:51], a[100:103], v[132:135]\n"
  "v_rcp_f32 v82, v88\n"
  "v_mfma_f32_16x16x32_bf16 v[136:139], a[48:51], a[104:107], v[136:139]\n"
  "ds_read_b64_tr_b16 a[40:41], v233 offset:51200\n"
  "v_mov_b32 v88, 0x3727c5ac\n"
  "v_mfma_f32_16x16x32_bf16 v[136:139], a[52:55], a[120:123], v[136:139]\n"
  "ds_read_b64_tr_b16 a[42:43], v233 offset:55296\n"
  "s_waitcnt lgkmcnt(6)\n"
  "v_mfma_f32_16x16x32_bf16 v[140:143], a[52:55], a[124:127], v[140:143]\n"
  "buffer_load_dwordx4 a[188:191], off, s[72:75], s80 offset:3072 sc1\n"
  "v_mfma_f32_16x16x32_bf16 v[140:143], a[48:51], a[108:111], v[140:143]\n"
  "s_add_u32 s80, s16, 49152\n"
  "v_fmac_f32 v88, 0x3a800000, v83\n"
  "v_mfma_f32_16x16x32_bf16 v[156:159], a[56:59], a[108:111], v[156:159]\n"
  "ds_read_b64_tr_b16 a[44:45], v233 offset:59392\n"
  "v_mfma_f32_16x16x32_bf16 v[156:159], a[60:63], a[124:127], v[156:159]\n"
  "ds_read_b64_tr_b16 a[46:47], v233 offset:63488\n"
  "v_mfma_f32_16x16x32_bf16 v[152:155], a[60:63], a[120:123], v[152:155]\n"
  "s_waitcnt vmcnt(23)\n"
  "L_poll10_%=:\n"
  "v_cmp_ne_u64 vcc, s[10:11], v[238:239]\n"
  "s_cbranch_vccz L_ok10_%=\n"
  "s_sub_u32 s99, s99, 1\n"
  "s_cbranch_scc1 L_out10_%=\n"
  "buffer_load_dwordx2 v[238:239], v236, s[52:55], s83 offen sc1\n"
  "s_waitcnt vmcnt(0)\n"
  "s_branch L_poll10_%=\n"
  "L_out10_%=:\n"
  "s_mov_b32 s99, 0\n"
  "L_ok10_%=:\n"
  "v_mfma_f32_16x16x32_bf16 v[152:155], a[56:59], a[104:107], v[152:155]\n"
  "buffer_load_dwordx4 a[192:195], off, s[72:75], s80 offset:0 sc1\n"
  "v_mfma_f32_16x16x32_bf16 v[148:151], a[56:59], a[100:103], v[148:151]\n"
  "ds_read_b64_tr_b16 a[48:49], v234 offset:51200\n"
  "s_add_u32 m0, s82, 114688\n"
  "v_mfma_f32_16x16x32_bf16 v[148:151], a[60:63], a[116:119], v[148:151]\n"
  "ds_read_b64_tr_b16 a[50:51], v234 offset:55296\n"
  "s_waitcnt lgkmcnt(6)\n"
  "v_mfma_f32_16x16x32_bf16 v[144:147], a[60:63], a[112:115], v[144:147]\n"
  "buffer_load_dwordx4 a[196:199], off, s[72:75], s80 offset:1024 sc1\n"
  "v_mfma_f32_16x16x32_bf16 v[144:147], a[56:59], a[96:99], v[144:147]\n"
  "v_rcp_f32 v83, v88\n"
  "v_mfma_f32_16x16x32_bf16 v[160:163], a[32:35], a[96:99], v[160:163]\n"
  "ds_read_b64_tr_b16 a[52:53], v234 offset:59392\n"
  "v_lshlrev_b32 v32, 16, v64\n"
  "v_mfma_f32_16x16x32_bf16 v[160:163], a[36:39], a[112:115], v[160:163]\n"
  "ds_read_b64_tr_b16 a[54:55], v234 offset:63488\n"
  "v_and_b32 v33, 0xffff0000, v64\n"
  "v_mfma_f32_16x16x32_bf16 v[164:167], a[36:39], a[116:119], v[164:167]\n"
  "buffer_load_dwordx4 v228, s[76:79], s81 offen offset:0 lds\n"
  "v_mfma_f32_16x16x32_bf16 v[164:167], a[32:35], a[100:103], v[164:167]\n"
  "v_add_f32 v32, v32, v84\n"
  "v_add_f32 v33, v33, v84\n"
  "v_mfma_f32_16x16x32_bf16 v[168:171], a[32:35], a[104:107], v[168:171]\n"
  "ds_read_b64_tr_b16 a[56:57], v235 offset:51200\n"
  "v_lshlrev_b32 v34, 16, v65\n"
  "v_mfma_f32_16x16x32_bf16 v[168:171], a[36:39], a[120:123], v[168:171]\n"
  "ds_read_b64_tr_b16 a[58:59], v235 offset:55296\n"
  "s_waitcnt lgkmcnt(6)\n"
  "v_mfma_f32_16x16x32_bf16 v[172:175], a[36:39], a[124:127], v[172:175]\n"
  "buffer_load_dwordx4 a[200:203], off, s[72:75], s80 offset:2048 sc1\n"
  "v_mfma_f32_16x16x32_bf16 v[172:175], a[32:35], a[108:111], v[172:175]\n"
  "v_and_b32 v35, 0xffff0000, v65\n"
  "v_add_f32 v34, v34, v84\n"
  "v_mfma_f32_16x16x32_bf16 v[188:191], a[40:43], a[108:111], v[188:191]\n"
  "ds_read_b64_tr_b16 a[60:61], v235 offset:59392\n"
  "v_add_f32 v35, v35, v84\n"
  "v_mfma_f32_16x16x32_bf16 v[188:191], a[44:47], a[124:127], v[188:191]\n"
  "ds_read_b64_tr_b16 a[62:63], v235 offset:63488\n"
  "v_lshlrev_b32 v36, 16, v66\n"
  "v_mfma_f32_16x16x32_bf16 v[184:187], a[44:47], a[120:123], v[184:187]\n"
  "buffer_load_dwordx4 v229, s[76:79], s81 offen offset:1024 lds\n"
  "v_mfma_f32_16x16x32_bf16 v[184:187], a[40:43], a[104:107], v[184:187]\n"
  "v_and_b32 v37, 0xffff0000, v66\n"
  "v_add_f32 v36, v36, v84\n"
  "v_mfma_f32_16x16x32_bf16 v[180:183], a[40:43], a[100:103], v[180:183]\n"
  "s_barrier\n"
  "v_add_f32 v37, v37, v84\n"
  "v_mfma_f32_16x16x32_bf16 v[180:183], a[44:47], a[116:119], v[180:183]\n"
  "ds_read_b64_tr_b16 a[32:33], v240 offset:0\n"
  "v_lshlrev_b32 v38, 16, v67\n"
  "v_mfma_f32_16x16x32_bf16 v[176:179], a[44:47], a[112:115], v[176:179]\n"
  "ds_read_b64_tr_b16 a[34:35], v240 offset:4096\n"
  "s_waitcnt lgkmcnt(6)\n"
  "v_mfma_f32_16x16x32_bf16 v[176:179], a[40:43], a[96:99], v[176:179]\n"
  "buffer_load_dwordx4 a[204:207], off, s[72:75], s80 offset:3072 sc1\n"
  "v_mfma_f32_16x16x32_bf16 v[192:195], a[48:51], a[96:99], v[192:195]\n"
  "ds_read_b64_tr_b16 a[36:37], v240 offset:8192\n"
  "s_mul_i32 s81, s16, 56\n"
  "v_mfma_f32_16x16x32_bf16 v[192:195], a[52:55], a[112:115], v[192:195]\n"
  "ds_read_b64_tr_b16 a[38:39], v240 offset:12288\n"
  "s_add_u32 s80, s80, 4096\n"
  "v_mfma_f32_16x16x32_bf16 v[196:199], a[52:55], a[116:119], v[196:199]\n"
  "s_add_u32 s81, s81, s86\n"
  "s_add_u32 s81, s81, 131072\n"
  "v_and_b32 v39, 0xffff0000, v67\n"
  "v_mfma_f32_16x16x32_bf16 v[196:199], a[48:51], a[100:103], v[196:199]\n"
  "buffer_load_dwordx4 a[208:211], off, s[72:75], s80 offset:0 sc1\n"
  "v_mfma_f32_16x16x32_bf16 v[200:203], a[48:51], a[104:107], v[200:203]\n"
  "ds_read_b64_tr_b16 a[40:41], v241 offset:0\n"
  "v_add_f32 v38, v38, v84\n"
  "v_mfma_f32_16x16x32_bf16 v[200:203], a[52:55], a[120:123], v[200:203]\n"
  "ds_read_b64_tr_b16 a[42:43], v241 offset:4096\n"
  "s_waitcnt lgkmcnt(6)\n"
  "v_mfma_f32_16x16x32_bf16 v[204:207], a[52:55], a[124:127], v[204:207]\n"
  "buffer_load_dwordx4 a[212:215], off, s[72:75], s80 offset:1024 sc1\n"
  "v_mfma_f32_16x16x32_bf16 v[204:207], a[48:51], a[108:111], v[204:207]\n"
  "v_add_f32 v39, v39, v84\n"
  "v_lshlrev_b32 v40, 16, v68\n"
  "v_mfma_f32_16x16x32_bf16 v[220:223], a[56:59], a[108:111], v[220:223]\n"
  "ds_read_b64_tr_b16 a[44:45], v241 offset:8192\n"
  "v_and_b32 v41, 0xffff0000, v68\n"
  "v_mfma_f32_16x16x32_bf16 v[220:223], a[60:63], a[124:127], v[220:223]\n"
  "ds_read_b64_tr_b16 a[46:47], v241 offset:12288\n"
  "v_add_f32 v40, v40, v85\n"
  "v_mfma_f32_16x16x32_bf16 v[216:219], a[60:63], a[120:123], v[216:219]\n"
  "buffer_load_dwordx4 a[216:219], off, s[72:75], s80 offset:2048 sc1\n"
  "v_mfma_f32_16x16x32_bf16 v[216:219], a[56:59], a[104:107], v[216:219]\n"
  "v_add_f32 v41, v41, v85\n"
  "v_lshlrev_b32 v42, 16, v69\n"
  "v_mfma_f32_16x16x32_bf16 v[212:215], a[56:59], a[100:103], v[212:215]\n"
  "ds_read_b64_tr_b16 a[48:49], v242 offset:0\n"
  "v_and_b32 v43, 0xffff0000, v69\n"
  "v_mfma_f32_16x16x32_bf16 v[212:215], a[60:63], a[116:119], v[212:215]\n"
  "ds_read_b64_tr_b16 a[50:51], v242 offset:4096\n"
  "s_waitcnt vmcnt(20) lgkmcnt(6)\n"
  "v_mfma_f32_16x16x32_bf16 v[208:211], a[60:63], a[112:115], v[208:211]\n"
  "buffer_load_dwordx4 a[220:223], off, s[72:75], s80 offset:3072 sc1\n"
  "v_mfma_f32_16x16x32_bf16 v[208:211], a[56:59], a[96:99], v[208:211]\n"
  "s_add_u32 s80, s17, 49152\n"
  "v_add_f32 v42, v42, v85\n"
  "v_add_f32 v43, v43, v85\n"
  "v_mfma_f32_16x16x32_bf16 v[96:99], a[32:35], a[128:131], v[96:99]\n"
  "ds_read_b64_tr_b16 a[52:53], v242 offset:8192\n"
  "v_lshlrev_b32 v44, 16, v70\n"
  "v_mfma_f32_16x16x32_bf16 v[96:99], a[36:39], a[144:147], v[96:99]\n"
  "ds_read_b64_tr_b16 a[54:55], v242 offset:12288\n"
  "v_and_b32 v45, 0xffff0000, v70\n"
  "v_mfma_f32_16x16x32_bf16 v[100:103], a[36:39], a[148:151], v[100:103]\n"
  "buffer_load_dwordx4 a[224:227], off, s[72:75], s80 offset:0 sc1\n"
  "v_mfma_f32_16x16x32_bf16 v[100:103], a[32:35], a[132:135], v[100:103]\n"
  "s_add_u32 m0, s82, 131072\n"
  "v_add_f32 v44, v44, v85\n"
  "v_add_f32 v45, v45, v85\n"
  "v_mfma_f32_16x16x32_bf16 v[104:107], a[32:35], a[136:139], v[104:107]\n"
  "ds_read_b64_tr_b16 a[56:57], v243 offset:0\n"
  "v_lshlrev_b32 v46, 16, v71\n"
  "v_mfma_f32_16x16x32_bf16 v[104:107], a[36:39], a[152:155], v[104:107]\n"
  "ds_read_b64_tr_b16 a[58:59], v243 offset:4096\n"
  "s_waitcnt lgkmcnt(6)\n"
  "v_mfma_f32_16x16x32_bf16 v[108:111], a[36:39], a[156:159], v[108:111]\n"
  "buffer_load_dwordx4 a[228:231], off, s[72:75], s80 offset:1024 sc1\n"
  "v_mfma_f32_16x16x32_bf16 v[108:111], a[32:35], a[140:143], v[108:111]\n"
  "v_and_b32 v47, 0xffff0000, v71\n"
  "v_add_f32 v46, v46, v85\n"
  "v_mfma_f32_16x16x32_bf16 v[124:127], a[40:43], a[140:143], v[124:127]\n"
  "ds_read_b64_tr_b16 a[60:61], v243 offset:8192\n"
  "v_add_f32 v47, v47, v85\n"
  "v_mfma_f32_16x16x32_bf16 v[124:127], a[44:47], a[156:159], v[124:127]\n"
  "ds_read_b64_tr_b16 a[62:63], v243 offset:12288\n"
  "v_lshlrev_b32 v48, 16, v72\n"
  "v_mfma_f32_16x16x32_bf16 v[120:123], a[44:47], a[152:155], v[120:123]\n"
  "buffer_load_dwordx4 v228, s[76:79], s81 offen offset:0 lds\n"
  "v_mfma_f32_16x16x32_bf16 v[120:123], a[40:43], a[136:139], v[120:123]\n"
  "v_and_b32 v49, 0xffff0000, v72\n"
  "v_add_f32 v48, v48, v86\n"
  "v_mfma_f32_16x16x32_bf16 v[116:119], a[40:43], a[132:135], v[116:119]\n"
  "ds_read_b64_tr_b16 a[32:33], v240 offset:2048\n"
  "v_add_f32 v49, v49, v86\n"
  "v_mfma_f32_16x16x32_bf16 v[116:119], a[44:47], a[148:151], v[116:119]\n"
  "ds_read_b64_tr_b16 a[34:35], v240 offset:6144\n"
  "s_waitcnt lgkmcnt(6)\n"
  "v_mfma_f32_16x16x32_bf16 v[112:115], a[44:47], a[144:147], v[112:115]\n"
  "buffer_load_dwordx4 a[232:235], off, s[72:75], s80 offset:2048 sc1\n"
  "v_mfma_f32_16x16x32_bf16 v[112:115], a[40:43], a[128:131], v[112:115]\n"
  "v_lshlrev_b32 v50, 16, v73\n"
  "v_and_b32 v51, 0xffff0000, v73\n"
  "v_mfma_f32_16x16x32_bf16 v[128:131], a[48:51], a[128:131], v[128:131]\n"
  "ds_read_b64_tr_b16 a[36:37], v240 offset:10240\n"
  "v_add_f32 v50, v50, v86\n"
  "v_mfma_f32_16x16x32_bf16 v[128:131], a[52:55], a[144:147], v[128:131]\n"
  "ds_read_b64_tr_b16 a[38:39], v240 offset:14336\n"
  "v_add_f32 v51, v51, v86\n"
  "v_mfma_f32_16x16x32_bf16 v[132:135], a[52:55], a[148:151], v[132:135]\n"
  "buffer_load_dwordx4 v229, s[76:79], s81 offen offset:1024 lds\n"
  "v_mfma_f32_16x16x32_bf16 v[132:135], a[48:51], a[132:135], v[132:135]\n"
  "v_lshlrev_b32 v52, 16, v74\n"
  "v_and_b32 v53, 0xffff0000, v74\n"
  "v_mfma_f32_16x16x32_bf16 v[136:139], a[48:51], a[136:139], v[136:139]\n"
  "ds_read_b64_tr_b16 a[40:41], v241 offset:2048\n"
  "v_add_f32 v52, v52, v86\n"
  "v_mfma_f32_16x16x32_bf16 v[136:139], a[52:55], a[152:155], v[136:139]\n"
  "ds_read_b64_tr_b16 a[42:43], v241 offset:6144\n"
  "s_waitcnt lgkmcnt(6)\n"
  "v_mfma_f32_16x16x32_bf16 v[140:143], a[52:55], a[156:159], v[140:143]\n"
  "buffer_load_dwordx4 a[236:239], off, s[72:75], s80 offset:3072 sc1\n"
  "v_mfma_f32_16x16x32_bf16 v[140:143], a[48:51], a[140:143], v[140:143]\n"
  "s_mul_i32 s81, s17, 56\n"
  "s_add_u32 s80, s80, 4096\n"
  "s_add_u32 s81, s81, s86\n"
  "v_mfma_f32_16x16x32_bf16 v[156:159], a[56:59], a[140:143], v[156:159]\n"
  "ds_read_b64_tr_b16 a[44:45], v241 offset:10240\n"
  "s_add_u32 s81, s81, 131072\n"
  "v_mfma_f32_16x16x32_bf16 v[156:159], a[60:63], a[156:159], v[156:159]\n"
  "ds_read_b64_tr_b16 a[46:47], v241 offset:14336\n"
  "v_add_f32 v53, v53, v86\n"
  "v_mfma_f32_16x16x32_bf16 v[152:155], a[60:63], a[152:155], v[152:155]\n"
  "buffer_load_dwordx4 a[240:243], off, s[72:75], s80 offset:0 sc1\n"
  "v_mfma_f32_16x16x32_bf16 v[152:155], a[56:59], a[136:139], v[152:155]\n"
  "v_lshlrev_b32 v54, 16, v75\n"
  "v_and_b32 v55, 0xffff0000, v75\n"
  "v_mfma_f32_16x16x32_bf16 v[148:151], a[56:59], a[132:135], v[148:151]\n"
  "ds_read_b64_tr_b16 a[48:49], v242 offset:2048\n"
  "v_add_f32 v54, v54, v86\n"
  "v_mfma_f32_16x16x32_bf16 v[148:151], a[60:63], a[148:151], v[148:151]\n"
  "ds_read_b64_tr_b16 a[50:51], v242 offset:6144\n"
  "s_waitcnt lgkmcnt(6)\n"
  "v_mfma_f32_16x16x32_bf16 v[144:147], a[60:63], a[144:147], v[144:147]\n"
  "buffer_load_dwordx4 a[244:247], off, s[72:75], s80 offset:1024 sc1\n"
  "v_mfma_f32_16x16x32_bf16 v[144:147], a[56:59], a[128:131], v[144:147]\n"
  "v_add_f32 v55, v55, v86\n"
  "v_lshlrev_b32 v56, 16, v76\n"
  "v_mfma_f32_16x16x32_bf16 v[160:163], a[32:35], a[128:131], v[160:163]\n"
  "ds_read_b64_tr_b16 a[52:53], v242 offset:10240\n"
  "v_and_b32 v57, 0xffff0000, v76\n"
  "v_mfma_f32_16x16x32_bf16 v[160:163], a[36:39], a[144:147], v[160:163]\n"
  "ds_read_b64_tr_b16 a[54:55], v242 offset:14336\n"
  "v_add_f32 v56, v56, v87\n"
  "v_mfma_f32_16x16x32_bf16 v[164:167], a[36:39], a[148:151], v[164:167]\n"
  "buffer_load_dwordx4 a[248:251], off, s[72:75], s80 offset:2048 sc1\n"
  "v_mfma_f32_16x16x32_bf16 v[164:167], a[32:35], a[132:135], v[164:167]\n"
  "v_add_f32 v57, v57, v87\n"
  "v_lshlrev_b32 v58, 16, v77\n"
  "v_mfma_f32_16x16x32_bf16 v[168:171], a[32:35], a[136:139], v[168:171]\n"
  "ds_read_b64_tr_b16 a[56:57], v243 offset:2048\n"
  "v_and_b32 v59, 0xffff0000, v77\n"
  "v_mfma_f32_16x16x32_bf16 v[168:171], a[36:39], a[152:155], v[168:171]\n"
  "ds_read_b64_tr_b16 a[58:59], v243 offset:6144\n"
  "s_waitcnt lgkmcnt(6)\n"
  "v_mfma_f32_16x16x32_bf16 v[172:175], a[36:39], a[156:159], v[172:175]\n"
  "buffer_load_dwordx4 a[252:255], off, s[72:75], s80 offset:3072 sc1\n"
  "v_mfma_f32_16x16x32_bf16 v[172:175], a[32:35], a[140:143], v[172:175]\n"
  "s_add_u32 s80, s18, 49152\n"
  "v_add_f32 v58, v58, v87\n"
  "v_add_f32 v59, v59, v87\n"
  "v_mfma_f32_16x16x32_bf16 v[188:191], a[40:43], a[140:143], v[188:191]\n"
  "ds_read_b64_tr_b16 a[60:61], v243 offset:10240\n"
  "v_lshlrev_b32 v60, 16, v78\n"
  "v_mfma_f32_16x16x32_bf16 v[188:191], a[44:47], a[156:159], v[188:191]\n"
  "ds_read_b64_tr_b16 a[62:63], v243 offset:14336\n"
  "v_and_b32 v61, 0xffff0000, v78\n"
  "v_mfma_f32_16x16x32_bf16 v[184:187], a[44:47], a[152:155], v[184:187]\n"
  "v_add_f32 v60, v60, v87\n"
  "v_add_f32 v61, v61, v87\n"
  "v_mfma_f32_16x16x32_bf16 v[184:187], a[40:43], a[136:139], v[184:187]\n"
  "v_lshlrev_b32 v62, 16, v79\n"
  "v_and_b32 v63, 0xffff0000, v79\n"
  "v_mfma_f32_16x16x32_bf16 v[180:183], a[40:43], a[132:135], v[180:183]\n"
  "s_barrier\n"
  "v_add_f32 v62, v62, v87\n"
  "v_mfma_f32_16x16x32_bf16 v[180:183], a[44:47], a[148:151], v[180:183]\n"
  "ds_read_b64_tr_b16 a[32:33], v240 offset:16384\n"
  "v_add_f32 v63, v63, v87\n"
  "v_mfma_f32_16x16x32_bf16 v[176:179], a[44:47], a[144:147], v[176:179]\n"
  "ds_read_b64_tr_b16 a[34:35], v240 offset:20480\n"
  "s_waitcnt lgkmcnt(6)\n"
  "v_mfma_f32_16x16x32_bf16 v[176:179], a[40:43], a[128:131], v[176:179]\n"
  "buffer_load_dwordx4 a[64:67], off, s[72:75], s80 offset:0 sc1\n"
  "v_mfma_f32_16x16x32_bf16 v[192:195], a[48:51], a[128:131], v[192:195]\n"
  "ds_read_b64_tr_b16 a[36:37], v240 offset:24576\n"
  "s_add_u32 m0, s82, 147456\n"
  "v_mfma_f32_16x16x32_bf16 v[192:195], a[52:55], a[144:147], v[192:195]\n"
  "ds_read_b64_tr_b16 a[38:39], v240 offset:28672\n"
  "v_mfma_f32_16x16x32_bf16 v[196:199], a[52:55], a[148:151], v[196:199]\n"
  "buffer_load_dwordx4 a[68:71], off, s[72:75], s80 offset:1024 sc1\n"
  "v_mfma_f32_16x16x32_bf16 v[196:199], a[48:51], a[132:135], v[196:199]\n"
  "v_mfma_f32_16x16x32_bf16 v[200:203], a[48:51], a[136:139], v[200:203]\n"
  "ds_read_b64_tr_b16 a[40:41], v241 offset:16384\n"
  "v_mfma_f32_16x16x32_bf16 v[200:203], a[52:55], a[152:155], v[200:203]\n"
  "ds_read_b64_tr_b16 a[42:43], v241 offset:20480\n"
  "s_waitcnt lgkmcnt(6)\n"
  "v_mfma_f32_16x16x32_bf16 v[204:207], a[52:55], a[156:159], v[204:207]\n"
  "buffer_load_dwordx4 v228, s[76:79], s81 offen offset:0 lds\n"
  "v_mfma_f32_16x16x32_bf16 v[204:207], a[48:51], a[140:143], v[204:207]\n"
  "v_mfma_f32_16x16x32_bf16 v[220:223], a[56:59], a[140:143], v[220:223]\n"
  "ds_read_b64_tr_b16 a[44:45], v241 offset:24576\n"
  "v_mfma_f32_16x16x32_bf16 v[220:223], a[60:63], a[156:159], v[220:223]\n"
  "ds_read_b64_tr_b16 a[46:47], v241 offset:28672\n"
  "v_mfma_f32_16x16x32_bf16 v[216:219], a[60:63], a[152:155], v[216:219]\n"
  "buffer_load_dwordx4 a[72:75], off, s[72:75], s80 offset:2048 sc1\n"
  "v_mfma_f32_16x16x32_bf16 v[216:219], a[56:59], a[136:139], v[216:219]\n"
  "v_mfma_f32_16x16x32_bf16 v[212:215], a[56:59], a[132:135], v[212:215]\n"
  "ds_read_b64_tr_b16 a[48:49], v242 offset:16384\n"
  "v_mfma_f32_16x16x32_bf16 v[212:215], a[60:63], a[148:151], v[212:215]\n"
  "ds_read_b64_tr_b16 a[50:51], v242 offset:20480\n"
  "s_waitcnt vmcnt(24) lgkmcnt(6)\n"
  "v_mfma_f32_16x16x32_bf16 v[208:211], a[60:63], a[144:147], v[208:211]\n"
  "buffer_load_dwordx4 v229, s[76:79], s81 offen offset:1024 lds\n"
  "v_mfma_f32_16x16x32_bf16 v[208:211], a[56:59], a[128:131], v[208:211]\n"
  "v_mfma_f32_16x16x32_bf16 v[96:99], a[32:35], a[160:163], v[96:99]\n"
  "ds_read_b64_tr_b16 a[52:53], v242 offset:24576\n"
  "v_mfma_f32_16x16x32_bf16 v[96:99], a[36:39], a[176:179], v[96:99]\n"
  "ds_read_b64_tr_b16 a[54:55], v242 offset:28672\n"
  "v_mfma_f32_16x16x32_bf16 v[100:103], a[36:39], a[180:183], v[100:103]\n"
  "buffer_load_dwordx4 a[76:79], off, s[72:75], s80 offset:3072 sc1\n"
  "v_mfma_f32_16x16x32_bf16 v[100:103], a[32:35], a[164:167], v[100:103]\n"
  "s_mul_i32 s81, s18, 56\n"
  "s_add_u32 s80, s80, 4096\n"
  "s_add_u32 s81, s81, s86\n"
  "v_mfma_f32_16x16x32_bf16 v[104:107], a[32:35], a[168:171], v[104:107]\n"
  "ds_read_b64_tr_b16 a[56:57], v243 offset:16384\n"
  "s_add_u32 s81, s81, 131072\n"
  "v_mfma_f32_16x16x32_bf16 v[104:107], a[36:39], a[184:187], v[104:107]\n"
  "ds_read_b64_tr_b16 a[58:59], v243 offset:20480\n"
  "s_waitcnt lgkmcnt(6)\n"
  "v_mfma_f32_16x16x32_bf16 v[108:111], a[36:39], a[188:191], v[108:111]\n"
  "buffer_load_dwordx4 a[80:83], off, s[72:75], s80 offset:0 sc1\n"
  "v_mfma_f32_16x16x32_bf16 v[108:111], a[32:35], a[172:175], v[108:111]\n"
  "v_mfma_f32_16x16x32_bf16 v[124:127], a[40:43], a[172:175], v[124:127]\n"
  "ds_read_b64_tr_b16 a[60:61], v243 offset:24576\n"
  "v_mfma_f32_16x16x32_bf16 v[124:127], a[44:47], a[188:191], v[124:127]\n"
  "ds_read_b64_tr_b16 a[62:63], v243 offset:28672\n"
  "v_mfma_f32_16x16x32_bf16 v[120:123], a[44:47], a[184:187], v[120:123]\n"
  "buffer_load_dwordx4 a[84:87], off, s[72:75], s80 offset:1024 sc1\n"
  "v_mfma_f32_16x16x32_bf16 v[120:123], a[40:43], a[168:171], v[120:123]\n"
  "v_mfma_f32_16x16x32_bf16 v[116:119], a[40:43], a[164:167], v[116:119]\n"
  "ds_read_b64_tr_b16 a[32:33], v240 offset:18432\n"
  "v_mfma_f32_16x16x32_bf16 v[116:119], a[44:47], a[180:183], v[116:119]\n"
  "ds_read_b64_tr_b16 a[34:35], v240 offset:22528\n"
  "s_waitcnt lgkmcnt(6)\n"
  "v_mfma_f32_16x16x32_bf16 v[112:115], a[44:47], a[176:179], v[112:115]\n"
  "buffer_load_dwordx4 a[88:91], off, s[72:75], s80 offset:2048 sc1\n"
  "v_mfma_f32_16x16x32_bf16 v[112:115], a[40:43], a[160:163], v[112:115]\n"
  "v_mfma_f32_16x16x32_bf16 v[128:131], a[48:51], a[160:163], v[128:131]\n"
  "ds_read_b64_tr_b16 a[36:37], v240 offset:26624\n"
  "v_mfma_f32_16x16x32_bf16 v[128:131], a[52:55], a[176:179], v[128:131]\n"
  "ds_read_b64_tr_b16 a[38:39], v240 offset:30720\n"
  "v_mfma_f32_16x16x32_bf16 v[132:135], a[52:55], a[180:183], v[132:135]\n"
  "buffer_load_dwordx4 a[92:95], off, s[72:75], s80 offset:3072 sc1\n"
  "v_mfma_f32_16x16x32_bf16 v[132:135], a[48:51], a[164:167], v[132:135]\n"
  "s_add_u32 s80, s19, 49152\n"
  "s_add_u32 s83, s83, 2048\n"
  "v_mfma_f32_16x16x32_bf16 v[136:139], a[48:51], a[168:171], v[136:139]\n"
  "ds_read_b64_tr_b16 a[40:41], v241 offset:18432\n"
  "v_mfma_f32_16x16x32_bf16 v[136:139], a[52:55], a[184:187], v[136:139]\n"
  "ds_read_b64_tr_b16 a[42:43], v241 offset:22528\n"
  "s_waitcnt lgkmcnt(6)\n"
  "v_mfma_f32_16x16x32_bf16 v[140:143], a[52:55], a[188:191], v[140:143]\n"
  "buffer_load_dwordx2 v[238:239], v236, s[52:55], s83 offen sc1\n"
  "v_mfma_f32_16x16x32_bf16 v[140:143], a[48:51], a[172:175], v[140:143]\n"
  "v_mfma_f32_16x16x32_bf16 v[156:159], a[56:59], a[172:175], v[156:159]\n"
  "ds_read_b64_tr_b16 a[44:45], v241 offset:26624\n"
  "v_mfma_f32_16x16x32_bf16 v[156:159], a[60:63], a[188:191], v[156:159]\n"
  "ds_read_b64_tr_b16 a[46:47], v241 offset:30720\n"
  "v_mfma_f32_16x16x32_bf16 v[152:155], a[60:63], a[184:187], v[152:155]\n"
  "v_mfma_f32_16x16x32_bf16 v[152:155], a[56:59], a[168:171], v[152:155]\n"
  "v_mfma_f32_16x16x32_bf16 v[148:151], a[56:59], a[164:167], v[148:151]\n"
  "ds_read_b64_tr_b16 a[48:49], v242 offset:18432\n"
  "v_mfma_f32_16x16x32_bf16 v[148:151], a[60:63], a[180:183], v[148:151]\n"
  "ds_read_b64_tr_b16 a[50:51], v242 offset:22528\n"
  "s_waitcnt lgkmcnt(6)\n"
  "v_mfma_f32_16x16x32_bf16 v[144:147], a[60:63], a[176:179], v[144:147]\n"
  "v_mfma_f32_16x16x32_bf16 v[144:147], a[56:59], a[160:163], v[144:147]\n"
  "v_mfma_f32_16x16x32_bf16 v[160:163], a[32:35], a[160:163], v[160:163]\n"
  "ds_read_b64_tr_b16 a[52:53], v242 offset:26624\n"
  "v_mfma_f32_16x16x32_bf16 v[160:163], a[36:39], a[176:179], v[160:163]\n"
  "ds_read_b64_tr_b16 a[54:55], v242 offset:30720\n"
  "v_mfma_f32_16x16x32_bf16 v[164:167], a[36:39], a[180:183], v[164:167]\n"
  "s_add_u32 s36, s30, 54525952\n"
  "s_addc_u32 s37, s31, 0\n"
  "s_pack_ll_b32_b16 s37, s37, 16\n"
  "v_mfma_f32_16x16x32_bf16 v[164:167], a[32:35], a[164:167], v[164:167]\n"
  "s_mov_b32 s38, 0\n"
  "s_mov_b32 s39, 0x800000\n"
  "s_lshl_b32 s8, s2, 2\n"
  "v_mfma_f32_16x16x32_bf16 v[168:171], a[32:35], a[168:171], v[168:171]\n"
  "ds_read_b64_tr_b16 a[56:57], v243 offset:18432\n"
  "s_add_u32 s8, s8, s3\n"
  "v_mfma_f32_16x16x32_bf16 v[168:171], a[36:39], a[184:187], v[168:171]\n"
  "ds_read_b64_tr_b16 a[58:59], v243 offset:22528\n"
  "s_waitcnt lgkmcnt(6)\n"
  "v_mfma_f32_16x16x32_bf16 v[172:175], a[36:39], a[188:191], v[172:175]\n"
  "s_lshl_b32 s32, s8, 14\n"
  "s_add_u32 s33, s32, 4096\n"
  "s_add_u32 s34, s32, 8192\n"
  "v_mfma_f32_16x16x32_bf16 v[172:175], a[32:35], a[172:175], v[172:175]\n"
  "s_xor_b32 s48, s32, 196608\n"
  "s_xor_b32 s49, s33, 131072\n"
  "s_xor_b32 s50, s34, 65536\n"
  "v_mfma_f32_16x16x32_bf16 v[188:191], a[40:43], a[172:175], v[188:191]\n"
  "ds_read_b64_tr_b16 a[60:61], v243 offset:26624\n"
  "s_mul_i32 s9, s8, 64\n"
  "v_mfma_f32_16x16x32_bf16 v[188:191], a[44:47], a[188:191], v[188:191]\n"
  "ds_read_b64_tr_b16 a[62:63], v243 offset:30720\n"
  "s_add_u32 s64, s9, 1589248\n"
  "v_mfma_f32_16x16x32_bf16 v[184:187], a[44:47], a[184:187], v[184:187]\n"
  "s_add_u32 s65, s9, 1654784\n"
  "s_add_u32 s66, s9, 1720320\n"
  "s_xor_b32 s13, s9, 768\n"
  "v_mfma_f32_16x16x32_bf16 v[184:187], a[40:43], a[168:171], v[184:187]\n"
  "s_add_u32 s58, s13, 1589248\n"
  "s_xor_b32 s13, s9, 512\n"
  "s_add_u32 s59, s13, 1654784\n"
  "v_mfma_f32_16x16x32_bf16 v[180:183], a[40:43], a[164:167], v[180:183]\n"
  "s_barrier\n"
  "s_xor_b32 s13, s9, 256\n"
  "v_mfma_f32_16x16x32_bf16 v[180:183], a[44:47], a[180:183], v[180:183]\n"
  "ds_read_b64_tr_b16 a[32:33], v240 offset:32768\n"
  "s_add_u32 s51, s13, 1720320\n"
  "v_mfma_f32_16x16x32_bf16 v[176:179], a[44:47], a[176:179], v[176:179]\n"
  "ds_read_b64_tr_b16 a[34:35], v240 offset:36864\n"
  "s_waitcnt lgkmcnt(6)\n"
  "v_mfma_f32_16x16x32_bf16 v[176:179], a[40:43], a[160:163], v[176:179]\n"
  "buffer_load_dwordx4 a[96:99], off, s[72:75], s80 offset:0 sc1\n"
  "v_mfma_f32_16x16x32_bf16 v[192:195], a[48:51], a[160:163], v[192:195]\n"
  "ds_read_b64_tr_b16 a[36:37], v240 offset:40960\n"
  "s_add_u32 m0, s82, 32768\n"
  "v_mfma_f32_16x16x32_bf16 v[192:195], a[52:55], a[176:179], v[192:195]\n"
  "ds_read_b64_tr_b16 a[38:39], v240 offset:45056\n"
  "v_mov_b32 v247, 0\n"
  "v_mfma_f32_16x16x32_bf16 v[196:199], a[52:55], a[180:183], v[196:199]\n"
  "buffer_load_dwordx4 a[100:103], off, s[72:75], s80 offset:1024 sc1\n"
  "v_mfma_f32_16x16x32_bf16 v[196:199], a[48:51], a[164:167], v[196:199]\n"
  "s_lshl_b32 s13, s87, 17\n"
  "s_lshl_b32 s8, s88, 6\n"
  "s_add_u32 s13, s13, s8\n"
  "v_mfma_f32_16x16x32_bf16 v[200:203], a[48:51], a[168:171], v[200:203]\n"
  "ds_read_b64_tr_b16 a[40:41], v241 offset:32768\n"
  "v_mfma_f32_16x16x32_bf16 v[200:203], a[52:55], a[184:187], v[200:203]\n"
  "ds_read_b64_tr_b16 a[42:43], v241 offset:36864\n"
  "s_waitcnt lgkmcnt(6)\n"
  "v_mfma_f32_16x16x32_bf16 v[204:207], a[52:55], a[188:191], v[204:207]\n"
  "buffer_load_dwordx4 v228, s[76:79], s81 offen offset:0 lds\n"
  "v_mfma_f32_16x16x32_bf16 v[204:207], a[48:51], a[172:175], v[204:207]\n"
  "s_add_u32 s40, s28, s13\n"
  "s_addc_u32 s41, s29, 0\n"
  "s_and_b32 s41, s41, 0xffff\n"
  "v_mfma_f32_16x16x32_bf16 v[220:223], a[56:59], a[172:175], v[220:223]\n"
  "ds_read_b64_tr_b16 a[44:45], v241 offset:40960\n"
  "s_mov_b32 s42, 129088\n"
  "v_mfma_f32_16x16x32_bf16 v[220:223], a[60:63], a[188:191], v[220:223]\n"
  "ds_read_b64_tr_b16 a[46:47], v241 offset:45056\n"
  "s_mov_b32 s43, 0x20000\n"
  "v_mfma_f32_16x16x32_bf16 v[216:219], a[60:63], a[184:187], v[216:219]\n"
  "buffer_load_dwordx4 a[104:107], off, s[72:75], s80 offset:2048 sc1\n"
  "v_mfma_f32_16x16x32_bf16 v[216:219], a[56:59], a[168:171], v[216:219]\n"
  "s_mov_b32 s45, 32768\n"
  "s_mov_b32 s46, 65536\n"
  "s_mov_b32 s47, 98304\n"
  "v_mfma_f32_16x16x32_bf16 v[212:215], a[56:59], a[164:167], v[212:215]\n"
  "ds_read_b64_tr_b16 a[48:49], v242 offset:32768\n"
  "s_mov_b32 s56, 0x3f80\n"
  "v_mfma_f32_16x16x32_bf16 v[212:215], a[60:63], a[180:183], v[212:215]\n"
  "ds_read_b64_tr_b16 a[50:51], v242 offset:36864\n"
  "s_waitcnt vmcnt(25) lgkmcnt(6)\n"
  "v_mfma_f32_16x16x32_bf16 v[208:211], a[60:63], a[176:179], v[208:211]\n"
  "buffer_load_dwordx4 v229, s[76:79], s81 offen offset:1024 lds\n"
  "v_mfma_f32_16x16x32_bf16 v[208:211], a[56:59], a[160:163], v[208:211]\n"
  "s_mov_b32 s57, 0x3f800000\n"
  "s_mul_i32 s15, s3, 5120\n"
  "v_and_b32 v0, 15, v255\n"
  "v_mfma_f32_16x16x32_bf16 v[96:99], a[32:35], a[192:195], v[96:99]\n"
  "ds_read_b64_tr_b16 a[52:53], v242 offset:40960\n"
  "v_lshrrev_b32 v1, 4, v255\n"
  "v_mfma_f32_16x16x32_bf16 v[96:99], a[36:39], a[208:211], v[96:99]\n"
  "ds_read_b64_tr_b16 a[54:55], v242 offset:45056\n"
  "v_mul_u32_u24 v0, 80, v0\n"
  "v_mfma_f32_16x16x32_bf16 v[100:103], a[36:39], a[212:215], v[100:103]\n"
  "buffer_load_dwordx4 a[108:111], off, s[72:75], s80 offset:3072 sc1\n"
  "v_mfma_f32_16x16x32_bf16 v[100:103], a[32:35], a[196:199], v[100:103]\n"
  "s_mul_i32 s81, s19, 56\n"
  "s_add_u32 s80, s80, 4096\n"
  "s_add_u32 s81, s81, s86\n"
  "v_mfma_f32_16x16x32_bf16 v[104:107], a[32:35], a[200:203], v[104:107]\n"
  "ds_read_b64_tr_b16 a[56:57], v243 offset:32768\n"
  "s_add_u32 s81, s81, 131072\n"
  "v_mfma_f32_16x16x32_bf16 v[104:107], a[36:39], a[216:219], v[104:107]\n"
  "ds_read_b64_tr_b16 a[58:59], v243 offset:36864\n"
  "s_waitcnt lgkmcnt(6)\n"
  "v_mfma_f32_16x16x32_bf16 v[108:111], a[36:39], a[220:223], v[108:111]\n"
  "buffer_load_dwordx4 a[112:115], off, s[72:75], s80 offset:0 sc1\n"
  "v_mfma_f32_16x16x32_bf16 v[108:111], a[32:35], a[204:207], v[108:111]\n"
  "v_lshl_add_u32 v0, v1, 3, v0\n"
  "v_add_u32 v226, s15, v0\n"
  "v_mfma_f32_16x16x32_bf16 v[124:127], a[40:43], a[204:207], v[124:127]\n"
  "ds_read_b64_tr_b16 a[60:61], v243 offset:40960\n"
  "v_lshrrev_b32 v0, 2, v255\n"
  "v_mfma_f32_16x16x32_bf16 v[124:127], a[44:47], a[220:223], v[124:127]\n"
  "ds_read_b64_tr_b16 a[62:63], v243 offset:45056\n"
  "v_and_b32 v1, 3, v255\n"
  "v_mfma_f32_16x16x32_bf16 v[120:123], a[44:47], a[216:219], v[120:123]\n"
  "buffer_load_dwordx4 a[116:119], off, s[72:75], s80 offset:1024 sc1\n"
  "v_mfma_f32_16x16x32_bf16 v[120:123], a[40:43], a[200:203], v[120:123]\n"
  "v_mul_u32_u24 v2, 80, v0\n"
  "v_lshl_add_u32 v2, v1, 4, v2\n"
  "v_mfma_f32_16x16x32_bf16 v[116:119], a[40:43], a[196:199], v[116:119]\n"
  "ds_read_b64_tr_b16 a[32:33], v240 offset:34816\n"
  "v_add_u32 v245, s15, v2\n"
  "v_mfma_f32_16x16x32_bf16 v[116:119], a[44:47], a[212:215], v[116:119]\n"
  "ds_read_b64_tr_b16 a[34:35], v240 offset:38912\n"
  "s_waitcnt lgkmcnt(6)\n"
  "v_mfma_f32_16x16x32_bf16 v[112:115], a[44:47], a[208:211], v[112:115]\n"
  "buffer_load_dwordx4 a[120:123], off, s[72:75], s80 offset:2048 sc1\n"
  "v_mfma_f32_16x16x32_bf16 v[112:115], a[40:43], a[192:195], v[112:115]\n"
  "v_lshlrev_b32 v0, 11, v0\n"
  "v_lshl_add_u32 v244, v1, 4, v0\n"
  "v_mfma_f32_16x16x32_bf16 v[128:131], a[48:51], a[192:195], v[128:131]\n"
  "ds_read_b64_tr_b16 a[36:37], v240 offset:43008\n"
  "v_mfma_f32_16x16x32_bf16 v[128:131], a[52:55], a[208:211], v[128:131]\n"
  "ds_read_b64_tr_b16 a[38:39], v240 offset:47104\n"
  "v_mfma_f32_16x16x32_bf16 v[132:135], a[52:55], a[212:215], v[132:135]\n"
  "buffer_load_dwordx4 a[124:127], off, s[72:75], s80 offset:3072 sc1\n"
  "v_mfma_f32_16x16x32_bf16 v[132:135], a[48:51], a[196:199], v[132:135]\n"
  "s_add_u32 s80, s16, 81920\n"
  "v_mfma_f32_16x16x32_bf16 v[136:139], a[48:51], a[200:203], v[136:139]\n"
  "ds_read_b64_tr_b16 a[40:41], v241 offset:34816\n"
  "v_mfma_f32_16x16x32_bf16 v[136:139], a[52:55], a[216:219], v[136:139]\n"
  "ds_read_b64_tr_b16 a[42:43], v241 offset:38912\n"
  "s_waitcnt lgkmcnt(6)\n"
  "v_mfma_f32_16x16x32_bf16 v[140:143], a[52:55], a[220:223], v[140:143]\n"
  "s_waitcnt vmcnt(10)\n"
  "L_poll11_%=:\n"
  "v_cmp_ne_u64 vcc, s[10:11], v[238:239]\n"
  "s_cbranch_vccz L_ok11_%=\n"
  "s_sub_u32 s99, s99, 1\n"
  "s_cbranch_scc1 L_out11_%=\n"
  "buffer_load_dwordx2 v[238:239], v236, s[52:55], s83 offen sc1\n"
  "s_waitcnt vmcnt(0)\n"
  "s_branch L_poll11_%=\n"
  "L_out11_%=:\n"
  "s_mov_b32 s99, 0\n"
  "L_ok11_%=:\n"
  "v_mfma_f32_16x16x32_bf16 v[140:143], a[48:51], a[204:207], v[140:143]\n"
  "v_mfma_f32_16x16x32_bf16 v[156:159], a[56:59], a[204:207], v[156:159]\n"
  "ds_read_b64_tr_b16 a[44:45], v241 offset:43008\n"
  "v_mfma_f32_16x16x32_bf16 v[156:159], a[60:63], a[220:223], v[156:159]\n"
  "ds_read_b64_tr_b16 a[46:47], v241 offset:47104\n"
  "v_mfma_f32_16x16x32_bf16 v[152:155], a[60:63], a[216:219], v[152:155]\n"
  "v_mfma_f32_16x16x32_bf16 v[152:155], a[56:59], a[200:203], v[152:155]\n"
  "v_mfma_f32_16x16x32_bf16 v[148:151], a[56:59], a[196:199], v[148:151]\n"
  "ds_read_b64_tr_b16 a[48:49], v242 offset:34816\n"
  "v_mfma_f32_16x16x32_bf16 v[148:151], a[60:63], a[212:215], v[148:151]\n"
  "ds_read_b64_tr_b16 a[50:51], v242 offset:38912\n"
  "s_waitcnt lgkmcnt(6)\n"
  "v_mfma_f32_16x16x32_bf16 v[144:147], a[60:63], a[208:211], v[144:147]\n"
  "v_mfma_f32_16x16x32_bf16 v[144:147], a[56:59], a[192:195], v[144:147]\n"
  "v_mfma_f32_16x16x32_bf16 v[160:163], a[32:35], a[192:195], v[160:163]\n"
  "ds_read_b64_tr_b16 a[52:53], v242 offset:43008\n"
  "v_mfma_f32_16x16x32_bf16 v[160:163], a[36:39], a[208:211], v[160:163]\n"
  "ds_read_b64_tr_b16 a[54:55], v242 offset:47104\n"
  "v_mfma_f32_16x16x32_bf16 v[164:167], a[36:39], a[212:215], v[164:167]\n"
  "v_mfma_f32_16x16x32_bf16 v[164:167], a[32:35], a[196:199], v[164:167]\n"
  "v_mfma_f32_16x16x32_bf16 v[168:171], a[32:35], a[200:203], v[168:171]\n"
  "ds_read_b64_tr_b16 a[56:57], v243 offset:34816\n"
  "v_mfma_f32_16x16x32_bf16 v[168:171], a[36:39], a[216:219], v[168:171]\n"
  "ds_read_b64_tr_b16 a[58:59], v243 offset:38912\n"
  "s_waitcnt lgkmcnt(6)\n"
  "v_mfma_f32_16x16x32_bf16 v[172:175], a[36:39], a[220:223], v[172:175]\n"
  "v_mfma_f32_16x16x32_bf16 v[172:175], a[32:35], a[204:207], v[172:175]\n"
  "v_mfma_f32_16x16x32_bf16 v[188:191], a[40:43], a[204:207], v[188:191]\n"
  "ds_read_b64_tr_b16 a[60:61], v243 offset:43008\n"
  "v_mfma_f32_16x16x32_bf16 v[188:191], a[44:47], a[220:223], v[188:191]\n"
  "ds_read_b64_tr_b16 a[62:63], v243 offset:47104\n"
  "v_mfma_f32_16x16x32_bf16 v[184:187], a[44:47], a[216:219], v[184:187]\n"
  "v_mfma_f32_16x16x32_bf16 v[184:187], a[40:43], a[200:203], v[184:187]\n"
  "v_mfma_f32_16x16x32_bf16 v[180:183], a[40:43], a[196:199], v[180:183]\n"
  "s_barrier\n"
  "v_mfma_f32_16x16x32_bf16 v[180:183], a[44:47], a[212:215], v[180:183]\n"
  "ds_read_b64_tr_b16 a[32:33], v240 offset:49152\n"
  "v_mfma_f32_16x16x32_bf16 v[176:179], a[44:47], a[208:211], v[176:179]\n"
  "ds_read_b64_tr_b16 a[34:35], v240 offset:53248\n"
  "s_waitcnt lgkmcnt(6)\n"
  "v_mfma_f32_16x16x32_bf16 v[176:179], a[40:43], a[192:195], v[176:179]\n"
  "buffer_load_dwordx4 a[128:131], off, s[72:75], s80 offset:0 sc1\n"
  "v_mfma_f32_16x16x32_bf16 v[192:195], a[48:51], a[192:195], v[192:195]\n"
  "ds_read_b64_tr_b16 a[36:37], v240 offset:57344\n"
  "s_add_u32 m0, s82, 49152\n"
  "v_mfma_f32_16x16x32_bf16 v[192:195], a[52:55], a[208:211], v[192:195]\n"
  "ds_read_b64_tr_b16 a[38:39], v240 offset:61440\n"
  "v_mfma_f32_16x16x32_bf16 v[196:199], a[52:55], a[212:215], v[196:199]\n"
  "buffer_load_dwordx4 a[132:135], off, s[72:75], s80 offset:1024 sc1\n"
  "v_mfma_f32_16x16x32_bf16 v[196:199], a[48:51], a[196:199], v[196:199]\n"
  "v_mfma_f32_16x16x32_bf16 v[200:203], a[48:51], a[200:203], v[200:203]\n"
  "ds_read_b64_tr_b16 a[40:41], v241 offset:49152\n"
  "v_mfma_f32_16x16x32_bf16 v[200:203], a[52:55], a[216:219], v[200:203]\n"
  "ds_read_b64_tr_b16 a[42:43], v241 offset:53248\n"
  "s_waitcnt lgkmcnt(6)\n"
  "v_mfma_f32_16x16x32_bf16 v[204:207], a[52:55], a[220:223], v[204:207]\n"
  "buffer_load_dwordx4 v228, s[76:79], s81 offen offset:0 lds\n"
  "v_mfma_f32_16x16x32_bf16 v[204:207], a[48:51], a[204:207], v[204:207]\n"
  "v_mfma_f32_16x16x32_bf16 v[220:223], a[56:59], a[204:207], v[220:223]\n"
  "ds_read_b64_tr_b16 a[44:45], v241 offset:57344\n"
  "v_mfma_f32_16x16x32_bf16 v[220:223], a[60:63], a[220:223], v[220:223]\n"
  "ds_read_b64_tr_b16 a[46:47], v241 offset:61440\n"
  "v_mfma_f32_16x16x32_bf16 v[216:219], a[60:63], a[216:219], v[216:219]\n"
  "buffer_load_dwordx4 a[136:139], off, s[72:75], s80 offset:2048 sc1\n"
  "v_mfma_f32_16x16x32_bf16 v[216:219], a[56:59], a[200:203], v[216:219]\n"
  "v_mfma_f32_16x16x32_bf16 v[212:215], a[56:59], a[196:199], v[212:215]\n"
  "ds_read_b64_tr_b16 a[48:49], v242 offset:49152\n"
  "v_mfma_f32_16x16x32_bf16 v[212:215], a[60:63], a[212:215], v[212:215]\n"
  "ds_read_b64_tr_b16 a[50:51], v242 offset:53248\n"
  "s_waitcnt lgkmcnt(6)\n"
  "v_mfma_f32_16x16x32_bf16 v[208:211], a[60:63], a[208:211], v[208:211]\n"
  "buffer_load_dwordx4 v229, s[76:79], s81 offen offset:1024 lds\n"
  "v_mfma_f32_16x16x32_bf16 v[208:211], a[56:59], a[192:195], v[208:211]\n"
  "v_mfma_f32_16x16x32_bf16 v[96:99], a[32:35], a[224:227], v[96:99]\n"
  "ds_read_b64_tr_b16 a[52:53], v242 offset:57344\n"
  "v_mfma_f32_16x16x32_bf16 v[96:99], a[36:39], a[240:243], v[96:99]\n"
  "ds_read_b64_tr_b16 a[54:55], v242 offset:61440\n"
  "v_mfma_f32_16x16x32_bf16 v[100:103], a[36:39], a[244:247], v[100:103]\n"
  "buffer_load_dwordx4 a[140:143], off, s[72:75], s80 offset:3072 sc1\n"
  "v_mfma_f32_16x16x32_bf16 v[100:103], a[32:35], a[228:231], v[100:103]\n"
  "s_mul_i32 s81, s16, 56\n"
  "s_add_u32 s80, s80, 4096\n"
  "s_add_u32 s81, s81, s86\n"
  "v_mfma_f32_16x16x32_bf16 v[104:107], a[32:35], a[232:235], v[104:107]\n"
  "ds_read_b64_tr_b16 a[56:57], v243 offset:49152\n"
  "s_add_u32 s81, s81, 196608\n"
  "v_mfma_f32_16x16x32_bf16 v[104:107], a[36:39], a[248:251], v[104:107]\n"
  "ds_read_b64_tr_b16 a[58:59], v243 offset:53248\n"
  "s_waitcnt lgkmcnt(6)\n"
  "v_mfma_f32_16x16x32_bf16 v[108:111], a[36:39], a[252:255], v[108:111]\n"
  "buffer_load_dwordx4 a[144:147], off, s[72:75], s80 offset:0 sc1\n"
  "v_mfma_f32_16x16x32_bf16 v[108:111], a[32:35], a[236:239], v[108:111]\n"
  "v_mfma_f32_16x16x32_bf16 v[124:127], a[40:43], a[236:239], v[124:127]\n"
  "ds_read_b64_tr_b16 a[60:61], v243 offset:57344\n"
  "v_mfma_f32_16x16x32_bf16 v[124:127], a[44:47], a[252:255], v[124:127]\n"
  "ds_read_b64_tr_b16 a[62:63], v243 offset:61440\n"
  "v_mfma_f32_16x16x32_bf16 v[120:123], a[44:47], a[248:251], v[120:123]\n"
  "buffer_load_dwordx4 a[148:151], off, s[72:75], s80 offset:1024 sc1\n"
  "v_mfma_f32_16x16x32_bf16 v[120:123], a[40:43], a[232:235], v[120:123]\n"
  "v_mfma_f32_16x16x32_bf16 v[116:119], a[40:43], a[228:231], v[116:119]\n"
  "ds_read_b64_tr_b16 a[32:33], v240 offset:51200\n"
  "v_mfma_f32_16x16x32_bf16 v[116:119], a[44:47], a[244:247], v[116:119]\n"
  "ds_read_b64_tr_b16 a[34:35], v240 offset:55296\n"
  "s_waitcnt lgkmcnt(6)\n"
  "v_mfma_f32_16x16x32_bf16 v[112:115], a[44:47], a[240:243], v[112:115]\n"
  "buffer_load_dwordx4 a[152:155], off, s[72:75], s80 offset:2048 sc1\n"
  "v_mfma_f32_16x16x32_bf16 v[112:115], a[40:43], a[224:227], v[112:115]\n"
  "v_mfma_f32_16x16x32_bf16 v[128:131], a[48:51], a[224:227], v[128:131]\n"
  "ds_read_b64_tr_b16 a[36:37], v240 offset:59392\n"
  "v_mfma_f32_16x16x32_bf16 v[128:131], a[52:55], a[240:243], v[128:131]\n"
  "ds_read_b64_tr_b16 a[38:39], v240 offset:63488\n"
  "v_mfma_f32_16x16x32_bf16 v[132:135], a[52:55], a[244:247], v[132:135]\n"
  "buffer_load_dwordx4 a[156:159], off, s[72:75], s80 offset:3072 sc1\n"
  "v_mfma_f32_16x16x32_bf16 v[132:135], a[48:51], a[228:231], v[132:135]\n"
  "s_add_u32 s80, s17, 81920\n"
  "v_mfma_f32_16x16x32_bf16 v[136:139], a[48:51], a[232:235], v[136:139]\n"
  "ds_read_b64_tr_b16 a[40:41], v241 offset:51200\n"
  "v_mfma_f32_16x16x32_bf16 v[136:139], a[52:55], a[248:251], v[136:139]\n"
  "ds_read_b64_tr_b16 a[42:43], v241 offset:55296\n"
  "s_waitcnt lgkmcnt(6)\n"
  "v_mfma_f32_16x16x32_bf16 v[140:143], a[52:55], a[252:255], v[140:143]\n"
  "buffer_load_dwordx4 a[160:163], off, s[72:75], s80 offset:0 sc1\n"
  "v_mfma_f32_16x16x32_bf16 v[140:143], a[48:51], a[236:239], v[140:143]\n"
  "v_mfma_f32_16x16x32_bf16 v[156:159], a[56:59], a[236:239], v[156:159]\n"
  "ds_read_b64_tr_b16 a[44:45], v241 offset:59392\n"
  "v_mfma_f32_16x16x32_bf16 v[156:159], a[60:63], a[252:255], v[156:159]\n"
  "ds_read_b64_tr_b16 a[46:47], v241 offset:63488\n"
  "v_mfma_f32_16x16x32_bf16 v[152:155], a[60:63], a[248:251], v[152:155]\n"
  "buffer_load_dwordx4 a[164:167], off, s[72:75], s80 offset:1024 sc1\n"
  "v_mfma_f32_16x16x32_bf16 v[152:155], a[56:59], a[232:235], v[152:155]\n"
  "v_mfma_f32_16x16x32_bf16 v[148:151], a[56:59], a[228:231], v[148:151]\n"
  "ds_read_b64_tr_b16 a[48:49], v242 offset:51200\n"
  "v_mfma_f32_16x16x32_bf16 v[148:151], a[60:63], a[244:247], v[148:151]\n"
  "ds_read_b64_tr_b16 a[50:51], v242 offset:55296\n"
  "s_waitcnt lgkmcnt(6)\n"
  "v_mfma_f32_16x16x32_bf16 v[144:147], a[60:63], a[240:243], v[144:147]\n"
  "buffer_load_dwordx4 a[168:171], off, s[72:75], s80 offset:2048 sc1\n"
  "v_mfma_f32_16x16x32_bf16 v[144:147], a[56:59], a[224:227], v[144:147]\n"
  "v_mfma_f32_16x16x32_bf16 v[160:163], a[32:35], a[224:227], v[160:163]\n"
  "ds_read_b64_tr_b16 a[52:53], v242 offset:59392\n"
  "v_mfma_f32_16x16x32_bf16 v[160:163], a[36:39], a[240:243], v[160:163]\n"
  "ds_read_b64_tr_b16 a[54:55], v242 offset:63488\n"
  "v_mfma_f32_16x16x32_bf16 v[164:167], a[36:39], a[244:247], v[164:167]\n"
  "buffer_load_dwordx4 a[172:175], off, s[72:75], s80 offset:3072 sc1\n"
  "v_mfma_f32_16x16x32_bf16 v[164:167], a[32:35], a[228:231], v[164:167]\n"
  "s_add_u32 s80, s80, 4096\n"
  "v_mfma_f32_16x16x32_bf16 v[168:171], a[32:35], a[232:235], v[168:171]\n"
  "ds_read_b64_tr_b16 a[56:57], v243 offset:51200\n"
  "v_mfma_f32_16x16x32_bf16 v[168:171], a[36:39], a[248:251], v[168:171]\n"
  "ds_read_b64_tr_b16 a[58:59], v243 offset:55296\n"
  "s_waitcnt lgkmcnt(6)\n"
  "v_mfma_f32_16x16x32_bf16 v[172:175], a[36:39], a[252:255], v[172:175]\n"
  "buffer_load_dwordx4 a[176:179], off, s[72:75], s80 offset:0 sc1\n"
  "v_mfma_f32_16x16x32_bf16 v[172:175], a[32:35], a[236:239], v[172:175]\n"
  "v_mfma_f32_16x16x32_bf16 v[188:191], a[40:43], a[236:239], v[188:191]\n"
  "ds_read_b64_tr_b16 a[60:61], v243 offset:59392\n"
  "v_mfma_f32_16x16x32_bf16 v[188:191], a[44:47], a[252:255], v[188:191]\n"
  "ds_read_b64_tr_b16 a[62:63], v243 offset:63488\n"
  "v_mfma_f32_16x16x32_bf16 v[184:187], a[44:47], a[248:251], v[184:187]\n"
  "buffer_load_dwordx4 a[180:183], off, s[72:75], s80 offset:1024 sc1\n"
  "v_mfma_f32_16x16x32_bf16 v[184:187], a[40:43], a[232:235], v[184:187]\n"
  "v_mfma_f32_16x16x32_bf16 v[180:183], a[40:43], a[228:231], v[180:183]\n"
  "s_waitcnt vmcnt(11)\n"
  "s_barrier\n"
  "v_mfma_f32_16x16x32_bf16 v[180:183], a[44:47], a[244:247], v[180:183]\n"
  "ds_read_b64_tr_b16 a[32:33], v232 offset:0\n"
  "v_mfma_f32_16x16x32_bf16 v[176:179], a[44:47], a[240:243], v[176:179]\n"
  "ds_read_b64_tr_b16 a[34:35], v232 offset:4096\n"
  "s_waitcnt lgkmcnt(6)\n"
  "v_mfma_f32_16x16x32_bf16 v[176:179], a[40:43], a[224:227], v[176:179]\n"
  "buffer_load_dwordx4 a[184:187], off, s[72:75], s80 offset:2048 sc1\n"
  "v_mfma_f32_16x16x32_bf16 v[192:195], a[48:51], a[224:227], v[192:195]\n"
  "ds_read_b64_tr_b16 a[36:37], v232 offset:8192\n"
  "v_mfma_f32_16x16x32_bf16 v[192:195], a[52:55], a[240:243], v[192:195]\n"
  "ds_read_b64_tr_b16 a[38:39], v232 offset:12288\n"
  "v_mfma_f32_16x16x32_bf16 v[196:199], a[52:55], a[244:247], v[196:199]\n"
  "buffer_load_dwordx4 a[188:191], off, s[72:75], s80 offset:3072 sc1\n"
  "v_mfma_f32_16x16x32_bf16 v[196:199], a[48:51], a[228:231], v[196:199]\n"
  "s_add_u32 s80, s18, 81920\n"
  "v_mfma_f32_16x16x32_bf16 v[200:203], a[48:51], a[232:235], v[200:203]\n"
  "ds_read_b64_tr_b16 a[40:41], v233 offset:0\n"
  "v_mfma_f32_16x16x32_bf16 v[200:203], a[52:55], a[248:251], v[200:203]\n"
  "ds_read_b64_tr_b16 a[42:43], v233 offset:4096\n"
  "s_waitcnt lgkmcnt(6)\n"
  "v_mfma_f32_16x16x32_bf16 v[204:207], a[52:55], a[252:255], v[204:207]\n"
  "buffer_load_dwordx4 a[192:195], off, s[72:75], s80 offset:0 sc1\n"
  "v_mfma_f32_16x16x32_bf16 v[204:207], a[48:51], a[236:239], v[204:207]\n"
  "v_mfma_f32_16x16x32_bf16 v[220:223], a[56:59], a[236:239], v[220:223]\n"
  "ds_read_b64_tr_b16 a[44:45], v233 offset:8192\n"
  "v_mfma_f32_16x16x32_bf16 v[220:223], a[60:63], a[252:255], v[220:223]\n"
  "ds_read_b64_tr_b16 a[46:47], v233 offset:12288\n"
  "v_mfma_f32_16x16x32_bf16 v[216:219], a[60:63], a[248:251], v[216:219]\n"
  "buffer_load_dwordx4 a[196:199], off, s[72:75], s80 offset:1024 sc1\n"
  "v_mfma_f32_16x16x32_bf16 v[216:219], a[56:59], a[232:235], v[216:219]\n"
  "v_mfma_f32_16x16x32_bf16 v[212:215], a[56:59], a[228:231], v[212:215]\n"
  "ds_read_b64_tr_b16 a[48:49], v232 offset:16384\n"
  "v_mfma_f32_16x16x32_bf16 v[212:215], a[60:63], a[244:247], v[212:215]\n"
  "ds_read_b64_tr_b16 a[50:51], v232 offset:20480\n"
  "s_waitcnt lgkmcnt(6)\n"
  "v_mfma_f32_16x16x32_bf16 v[208:211], a[60:63], a[240:243], v[208:211]\n"
  "buffer_load_dwordx4 a[200:203], off, s[72:75], s80 offset:2048 sc1\n"
  "v_mfma_f32_16x16x32_bf16 v[208:211], a[56:59], a[224:227], v[208:211]\n"
  "v_mfma_f32_16x16x32_bf16 v[96:99], a[32:35], a[64:67], v[96:99]\n"
  "ds_read_b64_tr_b16 a[52:53], v232 offset:24576\n"
  "v_mfma_f32_16x16x32_bf16 v[96:99], a[36:39], a[80:83], v[96:99]\n"
  "ds_read_b64_tr_b16 a[54:55], v232 offset:28672\n"
  "v_mfma_f32_16x16x32_bf16 v[100:103], a[36:39], a[84:87], v[100:103]\n"
  "buffer_load_dwordx4 a[204:207], off, s[72:75], s80 offset:3072 sc1\n"
  "v_mfma_f32_16x16x32_bf16 v[100:103], a[32:35], a[68:71], v[100:103]\n"
  "s_add_u32 s80, s80, 4096\n"
  "v_mfma_f32_16x16x32_bf16 v[104:107], a[32:35], a[72:75], v[104:107]\n"
  "ds_read_b64_tr_b16 a[56:57], v233 offset:16384\n"
  "v_mfma_f32_16x16x32_bf16 v[104:107], a[36:39], a[88:91], v[104:107]\n"
  "ds_read_b64_tr_b16 a[58:59], v233 offset:20480\n"
  "s_waitcnt lgkmcnt(6)\n"
  "v_mfma_f32_16x16x32_bf16 v[108:111], a[36:39], a[92:95], v[108:111]\n"
  "buffer_load_dwordx4 a[208:211], off, s[72:75], s80 offset:0 sc1\n"
  "v_mfma_f32_16x16x32_bf16 v[108:111], a[32:35], a[76:79], v[108:111]\n"
  "v_mfma_f32_16x16x32_bf16 v[124:127], a[40:43], a[76:79], v[124:127]\n"
  "ds_read_b64_tr_b16 a[60:61], v233 offset:24576\n"
  "v_mfma_f32_16x16x32_bf16 v[124:127], a[44:47], a[92:95], v[124:127]\n"
  "ds_read_b64_tr_b16 a[62:63], v233 offset:28672\n"
  "v_mfma_f32_16x16x32_bf16 v[120:123], a[44:47], a[88:91], v[120:123]\n"
  "buffer_load_dwordx4 a[212:215], off, s[72:75], s80 offset:1024 sc1\n"
  "v_mfma_f32_16x16x32_bf16 v[120:123], a[40:43], a[72:75], v[120:123]\n"
  "v_mfma_f32_16x16x32_bf16 v[116:119], a[40:43], a[68:71], v[116:119]\n"
  "ds_read_b64_tr_b16 a[32:33], v232 offset:32768\n"
  "v_mfma_f32_16x16x32_bf16 v[116:119], a[44:47], a[84:87], v[116:119]\n"
  "ds_read_b64_tr_b16 a[34:35], v232 offset:36864\n"
  "s_waitcnt lgkmcnt(6)\n"
  "v_mfma_f32_16x16x32_bf16 v[112:115], a[44:47], a[80:83], v[112:115]\n"
  "buffer_load_dwordx4 a[216:219], off, s[72:75], s80 offset:2048 sc1\n"
  "v_mfma_f32_16x16x32_bf16 v[112:115], a[40:43], a[64:67], v[112:115]\n"
  "v_mfma_f32_16x16x32_bf16 v[96:99], a[48:51], a[96:99], v[96:99]\n"
  "ds_read_b64_tr_b16 a[36:37], v232 offset:40960\n"
  "v_mfma_f32_16x16x32_bf16 v[96:99], a[52:55], a[112:115], v[96:99]\n"
  "ds_read_b64_tr_b16 a[38:39], v232 offset:45056\n"
  "v_mfma_f32_16x16x32_bf16 v[100:103], a[52:55], a[116:119], v[100:103]\n"
  "buffer_load_dwordx4 a[220:223], off, s[72:75], s80 offset:3072 sc1\n"
  "v_mfma_f32_16x16x32_bf16 v[100:103], a[48:51], a[100:103], v[100:103]\n"
  "s_add_u32 s80, s19, 81920\n"
  "v_mfma_f32_16x16x32_bf16 v[104:107], a[48:51], a[104:107], v[104:107]\n"
  "ds_read_b64_tr_b16 a[40:41], v233 offset:32768\n"
  "v_mfma_f32_16x16x32_bf16 v[104:107], a[52:55], a[120:123], v[104:107]\n"
  "ds_read_b64_tr_b16 a[42:43], v233 offset:36864\n"
  "s_waitcnt lgkmcnt(6)\n"
  "v_mfma_f32_16x16x32_bf16 v[108:111], a[52:55], a[124:127], v[108:111]\n"
  "buffer_load_dwordx4 a[224:227], off, s[72:75], s80 offset:0 sc1\n"
  "v_mfma_f32_16x16x32_bf16 v[108:111], a[48:51], a[108:111], v[108:111]\n"
  "v_mfma_f32_16x16x32_bf16 v[124:127], a[56:59], a[108:111], v[124:127]\n"
  "ds_read_b64_tr_b16 a[44:45], v233 offset:40960\n"
  "v_mfma_f32_16x16x32_bf16 v[124:127], a[60:63], a[124:127], v[124:127]\n"
  "ds_read_b64_tr_b16 a[46:47], v233 offset:45056\n"
  "v_mfma_f32_16x16x32_bf16 v[120:123], a[60:63], a[120:123], v[120:123]\n"
  "buffer_load_dwordx4 a[228:231], off, s[72:75], s80 offset:1024 sc1\n"
  "v_mfma_f32_16x16x32_bf16 v[120:123], a[56:59], a[104:107], v[120:123]\n"
  "v_mfma_f32_16x16x32_bf16 v[116:119], a[56:59], a[100:103], v[116:119]\n"
  "ds_read_b64_tr_b16 a[48:49], v232 offset:49152\n"
  "v_mfma_f32_16x16x32_bf16 v[116:119], a[60:63], a[116:119], v[116:119]\n"
  "ds_read_b64_tr_b16 a[50:51], v232 offset:53248\n"
  "s_waitcnt vmcnt(18) lgkmcnt(6)\n"
  "v_mfma_f32_16x16x32_bf16 v[112:115], a[60:63], a[112:115], v[112:115]\n"
  "buffer_load_dwordx4 a[232:235], off, s[72:75], s80 offset:2048 sc1\n"
  "v_mfma_f32_16x16x32_bf16 v[112:115], a[56:59], a[96:99], v[112:115]\n"
  "v_mfma_f32_16x16x32_bf16 v[96:99], a[32:35], a[128:131], v[96:99]\n"
  "ds_read_b64_tr_b16 a[52:53], v232 offset:57344\n"
  "v_mfma_f32_16x16x32_bf16 v[96:99], a[36:39], a[144:147], v[96:99]\n"
  "ds_read_b64_tr_b16 a[54:55], v232 offset:61440\n"
  "v_mfma_f32_16x16x32_bf16 v[100:103], a[36:39], a[148:151], v[100:103]\n"
  "buffer_load_dwordx4 a[236:239], off, s[72:75], s80 offset:3072 sc1\n"
  "v_mfma_f32_16x16x32_bf16 v[100:103], a[32:35], a[132:135], v[100:103]\n"
  "s_add_u32 s80, s80, 4096\n"
  "v_mfma_f32_16x16x32_bf16 v[104:107], a[32:35], a[136:139], v[104:107]\n"
  "ds_read_b64_tr_b16 a[56:57], v233 offset:49152\n"
  "v_mfma_f32_16x16x32_bf16 v[104:107], a[36:39], a[152:155], v[104:107]\n"
  "ds_read_b64_tr_b16 a[58:59], v233 offset:53248\n"
  "s_waitcnt lgkmcnt(6)\n"
  "v_mfma_f32_16x16x32_bf16 v[108:111], a[36:39], a[156:159], v[108:111]\n"
  "buffer_load_dwordx4 a[240:243], off, s[72:75], s80 offset:0 sc1\n"
  "v_mfma_f32_16x16x32_bf16 v[108:111], a[32:35], a[140:143], v[108:111]\n"
  "v_mfma_f32_16x16x32_bf16 v[124:127], a[40:43], a[140:143], v[124:127]\n"
  "ds_read_b64_tr_b16 a[60:61], v233 offset:57344\n"
  "v_mfma_f32_16x16x32_bf16 v[124:127], a[44:47], a[156:159], v[124:127]\n"
  "ds_read_b64_tr_b16 a[62:63], v233 offset:61440\n"
  "v_mfma_f32_16x16x32_bf16 v[120:123], a[44:47], a[152:155], v[120:123]\n"
  "buffer_load_dwordx4 a[244:247], off, s[72:75], s80 offset:1024 sc1\n"
  "v_mfma_f32_16x16x32_bf16 v[120:123], a[40:43], a[136:139], v[120:123]\n"
  "v_mfma_f32_16x16x32_bf16 v[116:119], a[40:43], a[132:135], v[116:119]\n"
  "ds_read_b64_tr_b16 a[32:33], v240 offset:0\n"
  "v_mfma_f32_16x16x32_bf16 v[116:119], a[44:47], a[148:151], v[116:119]\n"
  "ds_read_b64_tr_b16 a[34:35], v240 offset:4096\n"
  "s_waitcnt vmcnt(14) lgkmcnt(6)\n"
  "v_mfma_f32_16x16x32_bf16 v[112:115], a[44:47], a[144:147], v[112:115]\n"
  "buffer_load_dwordx4 a[248:251], off, s[72:75], s80 offset:2048 sc1\n"
  "v_mfma_f32_16x16x32_bf16 v[112:115], a[40:43], a[128:131], v[112:115]\n"
  "v_mfma_f32_16x16x32_bf16 v[96:99], a[48:51], a[160:163], v[96:99]\n"
  "ds_read_b64_tr_b16 a[36:37], v240 offset:8192\n"
  "v_mfma_f32_16x16x32_bf16 v[96:99], a[52:55], a[176:179], v[96:99]\n"
  "ds_read_b64_tr_b16 a[38:39], v240 offset:12288\n"
  "v_mfma_f32_16x16x32_bf16 v[100:103], a[52:55], a[180:183], v[100:103]\n"
  "buffer_load_dwordx4 a[252:255], off, s[72:75], s80 offset:3072 sc1\n"
  "v_mfma_f32_16x16x32_bf16 v[100:103], a[48:51], a[164:167], v[100:103]\n"
  "s_add_u32 s80, s16, 114688\n"
  "s_add_u32 m0, s82, 98304\n"
  "v_mfma_f32_16x16x32_bf16 v[104:107], a[48:51], a[168:171], v[104:107]\n"
  "ds_read_b64_tr_b16 a[40:41], v241 offset:0\n"
  "v_mfma_f32_16x16x32_bf16 v[104:107], a[52:55], a[184:187], v[104:107]\n"
  "ds_read_b64_tr_b16 a[42:43], v241 offset:4096\n"
  "s_waitcnt lgkmcnt(6)\n"
  "v_mfma_f32_16x16x32_bf16 v[108:111], a[52:55], a[188:191], v[108:111]\n"
  "buffer_load_dwordx4 v230, s[76:79], s84 offen offset:2048 lds\n"
  "v_mfma_f32_16x16x32_bf16 v[108:111], a[48:51], a[172:175], v[108:111]\n"
  "v_mfma_f32_16x16x32_bf16 v[124:127], a[56:59], a[172:175], v[124:127]\n"
  "ds_read_b64_tr_b16 a[44:45], v241 offset:8192\n"
  "v_mfma_f32_16x16x32_bf16 v[124:127], a[60:63], a[188:191], v[124:127]\n"
  "ds_read_b64_tr_b16 a[46:47], v241 offset:12288\n"
  "v_mfma_f32_16x16x32_bf16 v[120:123], a[60:63], a[184:187], v[120:123]\n"
  "buffer_load_dwordx4 v231, s[76:79], s84 offen offset:3072 lds\n"
  "v_mfma_f32_16x16x32_bf16 v[120:123], a[56:59], a[168:171], v[120:123]\n"
  "s_mul_i32 s84, s19, 56\n"
  "s_add_u32 s84, s84, s86\n"
  "s_add_u32 s84, s84, 65536\n"
  "v_mfma_f32_16x16x32_bf16 v[116:119], a[56:59], a[164:167], v[116:119]\n"
  "ds_read_b64_tr_b16 a[48:49], v240 offset:16384\n"
  "s_add_u32 m0, s82, 114688\n"
  "v_mfma_f32_16x16x32_bf16 v[116:119], a[60:63], a[180:183], v[116:119]\n"
  "ds_read_b64_tr_b16 a[50:51], v240 offset:20480\n"
  "s_waitcnt vmcnt(10) lgkmcnt(6)\n"
  "v_mfma_f32_16x16x32_bf16 v[112:115], a[60:63], a[176:179], v[112:115]\n"
  "buffer_load_dwordx4 v230, s[76:79], s84 offen offset:2048 lds\n"
  "v_mfma_f32_16x16x32_bf16 v[112:115], a[56:59], a[160:163], v[112:115]\n"
  "v_mfma_f32_16x16x32_bf16 v[96:99], a[32:35], a[192:195], v[96:99]\n"
  "ds_read_b64_tr_b16 a[52:53], v240 offset:24576\n"
  "v_mfma_f32_16x16x32_bf16 v[96:99], a[36:39], a[208:211], v[96:99]\n"
  "ds_read_b64_tr_b16 a[54:55], v240 offset:28672\n"
  "v_mfma_f32_16x16x32_bf16 v[100:103], a[36:39], a[212:215], v[100:103]\n"
  "buffer_load_dwordx4 v231, s[76:79], s84 offen offset:3072 lds\n"
  "v_mfma_f32_16x16x32_bf16 v[100:103], a[32:35], a[196:199], v[100:103]\n"
  "s_mul_i32 s84, s16, 56\n"
  "s_add_u32 s84, s84, s86\n"
  "s_add_u32 s84, s84, 131072\n"
  "v_mfma_f32_16x16x32_bf16 v[104:107], a[32:35], a[200:203], v[104:107]\n"
  "ds_read_b64_tr_b16 a[56:57], v241 offset:16384\n"
  "s_add_u32 m0, s82, 131072\n"
  "v_mfma_f32_16x16x32_bf16 v[104:107], a[36:39], a[216:219], v[104:107]\n"
  "ds_read_b64_tr_b16 a[58:59], v241 offset:20480\n"
  "s_waitcnt lgkmcnt(6)\n"
  "v_mfma_f32_16x16x32_bf16 v[108:111], a[36:39], a[220:223], v[108:111]\n"
  "buffer_load_dwordx4 v230, s[76:79], s84 offen offset:2048 lds\n"
  "v_mfma_f32_16x16x32_bf16 v[108:111], a[32:35], a[204:207], v[108:111]\n"
  "v_mfma_f32_16x16x32_bf16 v[124:127], a[40:43], a[204:207], v[124:127]\n"
  "ds_read_b64_tr_b16 a[60:61], v241 offset:24576\n"
  "v_mfma_f32_16x16x32_bf16 v[124:127], a[44:47], a[220:223], v[124:127]\n"
  "ds_read_b64_tr_b16 a[62:63], v241 offset:28672\n"
  "v_mfma_f32_16x16x32_bf16 v[120:123], a[44:47], a[216:219], v[120:123]\n"
  "buffer_load_dwordx4 v231, s[76:79], s84 offen offset:3072 lds\n"
  "v_mfma_f32_16x16x32_bf16 v[120:123], a[40:43], a[200:203], v[120:123]\n"
  "s_mul_i32 s84, s17, 56\n"
  "s_add_u32 s84, s84, s86\n"
  "s_add_u32 s84, s84, 131072\n"
  "v_mfma_f32_16x16x32_bf16 v[116:119], a[40:43], a[196:199], v[116:119]\n"
  "ds_read_b64_tr_b16 a[32:33], v234 offset:0\n"
  "s_add_u32 m0, s82, 147456\n"
  "v_mfma_f32_16x16x32_bf16 v[116:119], a[44:47], a[212:215], v[116:119]\n"
  "ds_read_b64_tr_b16 a[34:35], v234 offset:4096\n"
  "s_waitcnt vmcnt(6) lgkmcnt(6)\n"
  "v_mfma_f32_16x16x32_bf16 v[112:115], a[44:47], a[208:211], v[112:115]\n"
  "buffer_load_dwordx4 v230, s[76:79], s84 offen offset:2048 lds\n"
  "v_mfma_f32_16x16x32_bf16 v[112:115], a[40:43], a[192:195], v[112:115]\n"
  "v_mfma_f32_16x16x32_bf16 v[96:99], a[48:51], a[224:227], v[96:99]\n"
  "ds_read_b64_tr_b16 a[36:37], v234 offset:8192\n"
  "v_mfma_f32_16x16x32_bf16 v[96:99], a[52:55], a[240:243], v[96:99]\n"
  "ds_read_b64_tr_b16 a[38:39], v234 offset:12288\n"
  "v_mfma_f32_16x16x32_bf16 v[100:103], a[52:55], a[244:247], v[100:103]\n"
  "buffer_load_dwordx4 v231, s[76:79], s84 offen offset:3072 lds\n"
  "v_mfma_f32_16x16x32_bf16 v[100:103], a[48:51], a[228:231], v[100:103]\n"
  "s_mul_i32 s84, s18, 56\n"
  "s_add_u32 s84, s84, s86\n"
  "s_add_u32 s84, s84, 131072\n"
  "v_mfma_f32_16x16x32_bf16 v[104:107], a[48:51], a[232:235], v[104:107]\n"
  "ds_read_b64_tr_b16 a[40:41], v235 offset:0\n"
  "s_add_u32 m0, s82, 32768\n"
  "v_mfma_f32_16x16x32_bf16 v[104:107], a[52:55], a[248:251], v[104:107]\n"
  "ds_read_b64_tr_b16 a[42:43], v235 offset:4096\n"
  "s_waitcnt lgkmcnt(6)\n"
  "v_mfma_f32_16x16x32_bf16 v[108:111], a[52:55], a[252:255], v[108:111]\n"
  "buffer_load_dwordx4 v230, s[76:79], s84 offen offset:2048 lds\n"
  "v_mfma_f32_16x16x32_bf16 v[108:111], a[48:51], a[236:239], v[108:111]\n"
  "v_mfma_f32_16x16x32_bf16 v[124:127], a[56:59], a[236:239], v[124:127]\n"
  "ds_read_b64_tr_b16 a[44:45], v235 offset:8192\n"
  "v_mfma_f32_16x16x32_bf16 v[124:127], a[60:63], a[252:255], v[124:127]\n"
  "ds_read_b64_tr_b16 a[46:47], v235 offset:12288\n"
  "v_mfma_f32_16x16x32_bf16 v[120:123], a[60:63], a[248:251], v[120:123]\n"
  "buffer_load_dwordx4 v231, s[76:79], s84 offen offset:3072 lds\n"
  "v_mfma_f32_16x16x32_bf16 v[120:123], a[56:59], a[232:235], v[120:123]\n"
  "s_mul_i32 s84, s19, 56\n"
  "s_add_u32 s84, s84, s86\n"
  "s_add_u32 s84, s84, 131072\n"
  "v_mfma_f32_16x16x32_bf16 v[116:119], a[56:59], a[228:231], v[116:119]\n"
  "ds_read_b64_tr_b16 a[48:49], v234 offset:16384\n"
  "s_add_u32 m0, s82, 49152\n"
  "v_mfma_f32_16x16x32_bf16 v[116:119], a[60:63], a[244:247], v[116:119]\n"
  "ds_read_b64_tr_b16 a[50:51], v234 offset:20480\n"
  "s_waitcnt lgkmcnt(6)\n"
  "v_mfma_f32_16x16x32_bf16 v[112:115], a[60:63], a[240:243], v[112:115]\n"
  "buffer_load_dwordx4 v230, s[76:79], s84 offen offset:2048 lds\n"
  "v_mfma_f32_16x16x32_bf16 v[112:115], a[56:59], a[224:227], v[112:115]\n"
  "v_mfma_f32_16x16x32_bf16 v[128:131], a[32:35], a[64:67], v[128:131]\n"
  "ds_read_b64_tr_b16 a[52:53], v234 offset:24576\n"
  "v_mfma_f32_16x16x32_bf16 v[128:131], a[36:39], a[80:83], v[128:131]\n"
  "ds_read_b64_tr_b16 a[54:55], v234 offset:28672\n"
  "v_mfma_f32_16x16x32_bf16 v[132:135], a[36:39], a[84:87], v[132:135]\n"
  "buffer_load_dwordx4 v231, s[76:79], s84 offen offset:3072 lds\n"
  "v_mfma_f32_16x16x32_bf16 v[132:135], a[32:35], a[68:71], v[132:135]\n"
  "s_mul_i32 s84, s16, 56\n"
  "s_add_u32 s84, s84, s86\n"
  "s_add_u32 s84, s84, 196608\n"
  "v_mfma_f32_16x16x32_bf16 v[136:139], a[32:35], a[72:75], v[136:139]\n"
  "ds_read_b64_tr_b16 a[56:57], v235 offset:16384\n"
  "v_mul_f32 v96, v96, v80\n"
  "v_mfma_f32_16x16x32_bf16 v[136:139], a[36:39], a[88:91], v[136:139]\n"
  "ds_read_b64_tr_b16 a[58:59], v235 offset:20480\n"
  "s_waitcnt lgkmcnt(6)\n"
  "v_mfma_f32_16x16x32_bf16 v[140:143], a[36:39], a[92:95], v[140:143]\n"
  "v_mul_f32 v97, v97, v80\n"
  "v_mul_f32 v98, v98, v80\n"
  "v_mfma_f32_16x16x32_bf16 v[140:143], a[32:35], a[76:79], v[140:143]\n"
  "v_mul_f32 v99, v99, v80\n"
  "v_mul_f32 v112, v112, v80\n"
  "v_mfma_f32_16x16x32_bf16 v[156:159], a[40:43], a[76:79], v[156:159]\n"
  "ds_read_b64_tr_b16 a[60:61], v235 offset:24576\n"
  "v_mul_f32 v113, v113, v80\n"
  "v_mfma_f32_16x16x32_bf16 v[156:159], a[44:47], a[92:95], v[156:159]\n"
  "ds_read_b64_tr_b16 a[62:63], v235 offset:28672\n"
  "v_mul_f32 v114, v114, v80\n"
  "v_mfma_f32_16x16x32_bf16 v[152:155], a[44:47], a[88:91], v[152:155]\n"
  "v_mul_f32 v115, v115, v80\n"
  "v_cvt_pk_bf16_f32 v96, v96, v97\n"
  "v_mfma_f32_16x16x32_bf16 v[152:155], a[40:43], a[72:75], v[152:155]\n"
  "v_cvt_pk_bf16_f32 v97, v98, v99\n"
  "v_cvt_pk_bf16_f32 v98, v112, v113\n"
  "v_mfma_f32_16x16x32_bf16 v[148:151], a[40:43], a[68:71], v[148:151]\n"
  "ds_read_b64_tr_b16 a[32:33], v234 offset:32768\n"
  "v_cvt_pk_bf16_f32 v99, v114, v115\n"
  "v_mfma_f32_16x16x32_bf16 v[148:151], a[44:47], a[84:87], v[148:151]\n"
  "ds_read_b64_tr_b16 a[34:35], v234 offset:36864\n"
  "s_waitcnt lgkmcnt(6)\n"
  "v_mfma_f32_16x16x32_bf16 v[144:147], a[44:47], a[80:83], v[144:147]\n"
  "buffer_store_dwordx4 v[96:99], off, s[36:39], s32 offset:0 sc1\n"
  "v_mfma_f32_16x16x32_bf16 v[144:147], a[40:43], a[64:67], v[144:147]\n"
  "v_mul_f32 v100, v100, v81\n"
  "v_mul_f32 v101, v101, v81\n"
  "v_mfma_f32_16x16x32_bf16 v[128:131], a[48:51], a[96:99], v[128:131]\n"
  "ds_read_b64_tr_b16 a[36:37], v234 offset:40960\n"
  "v_mul_f32 v102, v102, v81\n"
  "v_mfma_f32_16x16x32_bf16 v[128:131], a[52:55], a[112:115], v[128:131]\n"
  "ds_read_b64_tr_b16 a[38:39], v234 offset:45056\n"
  "v_mul_f32 v103, v103, v81\n"
  "v_mfma_f32_16x16x32_bf16 v[132:135], a[52:55], a[116:119], v[132:135]\n"
  "v_mul_f32 v116, v116, v81\n"
  "v_mul_f32 v117, v117, v81\n"
  "v_mfma_f32_16x16x32_bf16 v[132:135], a[48:51], a[100:103], v[132:135]\n"
  "v_mul_f32 v118, v118, v81\n"
  "v_mul_f32 v119, v119, v81\n"
  "v_mfma_f32_16x16x32_bf16 v[136:139], a[48:51], a[104:107], v[136:139]\n"
  "ds_read_b64_tr_b16 a[40:41], v235 offset:32768\n"
  "v_cvt_pk_bf16_f32 v100, v100, v101\n"
  "v_mfma_f32_16x16x32_bf16 v[136:139], a[52:55], a[120:123], v[136:139]\n"
  "ds_read_b64_tr_b16 a[42:43], v235 offset:36864\n"
  "s_waitcnt lgkmcnt(6)\n"
  "v_mfma_f32_16x16x32_bf16 v[140:143], a[52:55], a[124:127], v[140:143]\n"
  "v_cvt_pk_bf16_f32 v101, v102, v103\n"
  "v_cvt_pk_bf16_f32 v102, v116, v117\n"
  "v_mfma_f32_16x16x32_bf16 v[140:143], a[48:51], a[108:111], v[140:143]\n"
  "v_cvt_pk_bf16_f32 v103, v118, v119\n"
  "v_mul_f32 v104, v104, v82\n"
  "v_mfma_f32_16x16x32_bf16 v[156:159], a[56:59], a[108:111], v[156:159]\n"
  "ds_read_b64_tr_b16 a[44:45], v235 offset:40960\n"
  "v_mul_f32 v105, v105, v82\n"
  "v_mfma_f32_16x16x32_bf16 v[156:159], a[60:63], a[124:127], v[156:159]\n"
  "ds_read_b64_tr_b16 a[46:47], v235 offset:45056\n"
  "v_mul_f32 v106, v106, v82\n"
  "v_mfma_f32_16x16x32_bf16 v[152:155], a[60:63], a[120:123], v[152:155]\n"
  "buffer_store_dwordx4 v[100:103], off, s[36:39], s32 offset:1024 sc1\n"
  "v_mfma_f32_16x16x32_bf16 v[152:155], a[56:59], a[104:107], v[152:155]\n"
  "v_mul_f32 v107, v107, v82\n"
  "v_mul_f32 v120, v120, v82\n"
  "v_mfma_f32_16x16x32_bf16 v[148:151], a[56:59], a[100:103], v[148:151]\n"
  "ds_read_b64_tr_b16 a[48:49], v234 offset:49152\n"
  "v_mul_f32 v121, v121, v82\n"
  "v_mfma_f32_16x16x32_bf16 v[148:151], a[60:63], a[116:119], v[148:151]\n"
  "ds_read_b64_tr_b16 a[50:51], v234 offset:53248\n"
  "s_waitcnt lgkmcnt(6)\n"
  "v_mfma_f32_16x16x32_bf16 v[144:147], a[60:63], a[112:115], v[144:147]\n"
  "v_mul_f32 v122, v122, v82\n"
  "v_mul_f32 v123, v123, v82\n"
  "v_mfma_f32_16x16x32_bf16 v[144:147], a[56:59], a[96:99], v[144:147]\n"
  "v_cvt_pk_bf16_f32 v104, v104, v105\n"
  "v_cvt_pk_bf16_f32 v105, v106, v107\n"
  "v_mfma_f32_16x16x32_bf16 v[128:131], a[32:35], a[128:131], v[128:131]\n"
  "ds_read_b64_tr_b16 a[52:53], v234 offset:57344\n"
  "v_cvt_pk_bf16_f32 v106, v120, v121\n"
  "v_mfma_f32_16x16x32_bf16 v[128:131], a[36:39], a[144:147], v[128:131]\n"
  "ds_read_b64_tr_b16 a[54:55], v234 offset:61440\n"
  "v_cvt_pk_bf16_f32 v107, v122, v123\n"
  "v_mfma_f32_16x16x32_bf16 v[132:135], a[36:39], a[148:151], v[132:135]\n"
  "buffer_store_dwordx4 v[104:107], off, s[36:39], s32 offset:2048 sc1\n"
  "v_mfma_f32_16x16x32_bf16 v[132:135], a[32:35], a[132:135], v[132:135]\n"
  "v_mul_f32 v108, v108, v83\n"
  "v_mul_f32 v109, v109, v83\n"
  "v_mfma_f32_16x16x32_bf16 v[136:139], a[32:35], a[136:139], v[136:139]\n"
  "ds_read_b64_tr_b16 a[56:57], v235 offset:49152\n"
  "v_mul_f32 v110, v110, v83\n"
  "v_mfma_f32_16x16x32_bf16 v[136:139], a[36:39], a[152:155], v[136:139]\n"
  "ds_read_b64_tr_b16 a[58:59], v235 offset:53248\n"
  "s_waitcnt lgkmcnt(6)\n"
  "v_mfma_f32_16x16x32_bf16 v[140:143], a[36:39], a[156:159], v[140:143]\n"
  "v_mul_f32 v111, v111, v83\n"
  "v_mul_f32 v124, v124, v83\n"
  "v_mfma_f32_16x16x32_bf16 v[140:143], a[32:35], a[140:143], v[140:143]\n"
  "v_mul_f32 v125, v125, v83\n"
  "v_mul_f32 v126, v126, v83\n"
  "v_mfma_f32_16x16x32_bf16 v[156:159], a[40:43], a[140:143], v[156:159]\n"
  "ds_read_b64_tr_b16 a[60:61], v235 offset:57344\n"
  "v_mul_f32 v127, v127, v83\n"
  "v_mfma_f32_16x16x32_bf16 v[156:159], a[44:47], a[156:159], v[156:159]\n"
  "ds_read_b64_tr_b16 a[62:63], v235 offset:61440\n"
  "v_cvt_pk_bf16_f32 v108, v108, v109\n"
  "v_mfma_f32_16x16x32_bf16 v[152:155], a[44:47], a[152:155], v[152:155]\n"
  "v_cvt_pk_bf16_f32 v109, v110, v111\n"
  "v_cvt_pk_bf16_f32 v110, v124, v125\n"
  "v_mfma_f32_16x16x32_bf16 v[152:155], a[40:43], a[136:139], v[152:155]\n"
  "v_cvt_pk_bf16_f32 v111, v126, v127\n"
  "v_mfma_f32_16x16x32_bf16 v[148:151], a[40:43], a[132:135], v[148:151]\n"
  "ds_read_b64_tr_b16 a[32:33], v242 offset:0\n"
  "v_mfma_f32_16x16x32_bf16 v[148:151], a[44:47], a[148:151], v[148:151]\n"
  "ds_read_b64_tr_b16 a[34:35], v242 offset:4096\n"
  "s_waitcnt lgkmcnt(6)\n"
  "v_mfma_f32_16x16x32_bf16 v[144:147], a[44:47], a[144:147], v[144:147]\n"
  "buffer_store_dwordx4 v[108:111], off, s[36:39], s32 offset:3072 sc1\n"
  "v_mfma_f32_16x16x32_bf16 v[144:147], a[40:43], a[128:131], v[144:147]\n"
  "v_mfma_f32_16x16x32_bf16 v[128:131], a[48:51], a[160:163], v[128:131]\n"
  "ds_read_b64_tr_b16 a[36:37], v242 offset:8192\n"
  "v_mfma_f32_16x16x32_bf16 v[128:131], a[52:55], a[176:179], v[128:131]\n"
  "ds_read_b64_tr_b16 a[38:39], v242 offset:12288\n"
  "v_mfma_f32_16x16x32_bf16 v[132:135], a[52:55], a[180:183], v[132:135]\n"
  "v_mfma_f32_16x16x32_bf16 v[132:135], a[48:51], a[164:167], v[132:135]\n"
  "v_mfma_f32_16x16x32_bf16 v[136:139], a[48:51], a[168:171], v[136:139]\n"
  "ds_read_b64_tr_b16 a[40:41], v243 offset:0\n"
  "v_mfma_f32_16x16x32_bf16 v[136:139], a[52:55], a[184:187], v[136:139]\n"
  "ds_read_b64_tr_b16 a[42:43], v243 offset:4096\n"
  "s_waitcnt lgkmcnt(6)\n"
  "v_mfma_f32_16x16x32_bf16 v[140:143], a[52:55], a[188:191], v[140:143]\n"
  "v_mfma_f32_16x16x32_bf16 v[140:143], a[48:51], a[172:175], v[140:143]\n"
  "v_mfma_f32_16x16x32_bf16 v[156:159], a[56:59], a[172:175], v[156:159]\n"
  "ds_read_b64_tr_b16 a[44:45], v243 offset:8192\n"
  "v_mfma_f32_16x16x32_bf16 v[156:159], a[60:63], a[188:191], v[156:159]\n"
  "ds_read_b64_tr_b16 a[46:47], v243 offset:12288\n"
  "v_mfma_f32_16x16x32_bf16 v[152:155], a[60:63], a[184:187], v[152:155]\n"
  "v_mfma_f32_16x16x32_bf16 v[152:155], a[56:59], a[168:171], v[152:155]\n"
  "v_mfma_f32_16x16x32_bf16 v[148:151], a[56:59], a[164:167], v[148:151]\n"
  "ds_read_b64_tr_b16 a[48:49], v242 offset:16384\n"
  "v_mfma_f32_16x16x32_bf16 v[148:151], a[60:63], a[180:183], v[148:151]\n"
  "ds_read_b64_tr_b16 a[50:51], v242 offset:20480\n"
  "s_waitcnt lgkmcnt(6)\n"
  "v_mfma_f32_16x16x32_bf16 v[144:147], a[60:63], a[176:179], v[144:147]\n"
  "v_mfma_f32_16x16x32_bf16 v[144:147], a[56:59], a[160:163], v[144:147]\n"
  "v_mfma_f32_16x16x32_bf16 v[128:131], a[32:35], a[192:195], v[128:131]\n"
  "ds_read_b64_tr_b16 a[52:53], v242 offset:24576\n"
  "v_mfma_f32_16x16x32_bf16 v[128:131], a[36:39], a[208:211], v[128:131]\n"
  "ds_read_b64_tr_b16 a[54:55], v242 offset:28672\n"
  "v_mfma_f32_16x16x32_bf16 v[132:135], a[36:39], a[212:215], v[132:135]\n"
  "v_mfma_f32_16x16x32_bf16 v[132:135], a[32:35], a[196:199], v[132:135]\n"
  "v_mfma_f32_16x16x32_bf16 v[136:139], a[32:35], a[200:203], v[136:139]\n"
  "ds_read_b64_tr_b16 a[56:57], v243 offset:16384\n"
  "v_mfma_f32_16x16x32_bf16 v[136:139], a[36:39], a[216:219], v[136:139]\n"
  "ds_read_b64_tr_b16 a[58:59], v243 offset:20480\n"
  "s_waitcnt lgkmcnt(6)\n"
  "v_mfma_f32_16x16x32_bf16 v[140:143], a[36:39], a[220:223], v[140:143]\n"
  "v_mfma_f32_16x16x32_bf16 v[140:143], a[32:35], a[204:207], v[140:143]\n"
  "v_mfma_f32_16x16x32_bf16 v[156:159], a[40:43], a[204:207], v[156:159]\n"
  "ds_read_b64_tr_b16 a[60:61], v243 offset:24576\n"
  "v_mfma_f32_16x16x32_bf16 v[156:159], a[44:47], a[220:223], v[156:159]\n"
  "ds_read_b64_tr_b16 a[62:63], v243 offset:28672\n"
  "v_mfma_f32_16x16x32_bf16 v[152:155], a[44:47], a[216:219], v[152:155]\n"
  "v_mfma_f32_16x16x32_bf16 v[152:155], a[40:43], a[200:203], v[152:155]\n"
  "v_mfma_f32_16x16x32_bf16 v[148:151], a[40:43], a[196:199], v[148:151]\n"
  "s_waitcnt vmcnt(4)\n"
  "s_barrier\n"
  "v_mfma_f32_16x16x32_bf16 v[148:151], a[44:47], a[212:215], v[148:151]\n"
  "ds_read_b64_tr_b16 a[32:33], v232 offset:2048\n"
  "v_mfma_f32_16x16x32_bf16 v[144:147], a[44:47], a[208:211], v[144:147]\n"
  "ds_read_b64_tr_b16 a[34:35], v232 offset:6144\n"
  "s_waitcnt lgkmcnt(6)\n"
  "v_mfma_f32_16x16x32_bf16 v[144:147], a[40:43], a[192:195], v[144:147]\n"
  "v_mfma_f32_16x16x32_bf16 v[128:131], a[48:51], a[224:227], v[128:131]\n"
  "ds_read_b64_tr_b16 a[36:37], v232 offset:10240\n"
  "v_mfma_f32_16x16x32_bf16 v[128:131], a[52:55], a[240:243], v[128:131]\n"
  "ds_read_b64_tr_b16 a[38:39], v232 offset:14336\n"
  "v_mfma_f32_16x16x32_bf16 v[132:135], a[52:55], a[244:247], v[132:135]\n"
  "v_mfma_f32_16x16x32_bf16 v[132:135], a[48:51], a[228:231], v[132:135]\n"
  "v_mfma_f32_16x16x32_bf16 v[136:139], a[48:51], a[232:235], v[136:139]\n"
  "ds_read_b64_tr_b16 a[40:41], v233 offset:2048\n"
  "v_mfma_f32_16x16x32_bf16 v[136:139], a[52:55], a[248:251], v[136:139]\n"
  "ds_read_b64_tr_b16 a[42:43], v233 offset:6144\n"
  "s_waitcnt lgkmcnt(6)\n"
  "v_mfma_f32_16x16x32_bf16 v[140:143], a[52:55], a[252:255], v[140:143]\n"
  "v_mfma_f32_16x16x32_bf16 v[140:143], a[48:51], a[236:239], v[140:143]\n"
  "v_mfma_f32_16x16x32_bf16 v[156:159], a[56:59], a[236:239], v[156:159]\n"
  "ds_read_b64_tr_b16 a[44:45], v233 offset:10240\n"
  "v_mfma_f32_16x16x32_bf16 v[156:159], a[60:63], a[252:255], v[156:159]\n"
  "ds_read_b64_tr_b16 a[46:47], v233 offset:14336\n"
  "v_mfma_f32_16x16x32_bf16 v[152:155], a[60:63], a[248:251], v[152:155]\n"
  "s_waitcnt vmcnt(0)\n"
  "buffer_store_dwordx4 v[248:251], v251, s[52:55], s64 offen sc1\n"
  "v_mfma_f32_16x16x32_bf16 v[152:155], a[56:59], a[232:235], v[152:155]\n"
  "v_mfma_f32_16x16x32_bf16 v[148:151], a[56:59], a[228:231], v[148:151]\n"
  "ds_read_b64_tr_b16 a[48:49], v232 offset:18432\n"
  "v_mfma_f32_16x16x32_bf16 v[148:151], a[60:63], a[244:247], v[148:151]\n"
  "ds_read_b64_tr_b16 a[50:51], v232 offset:22528\n"
  "s_waitcnt lgkmcnt(6)\n"
  "v_mfma_f32_16x16x32_bf16 v[144:147], a[60:63], a[240:243], v[144:147]\n"
  "v_mfma_f32_16x16x32_bf16 v[144:147], a[56:59], a[224:227], v[144:147]\n"
  "v_mfma_f32_16x16x32_bf16 v[160:163], a[32:35], a[64:67], v[160:163]\n"
  "ds_read_b64_tr_b16 a[52:53], v232 offset:26624\n"
  "v_mfma_f32_16x16x32_bf16 v[160:163], a[36:39], a[80:83], v[160:163]\n"
  "ds_read_b64_tr_b16 a[54:55], v232 offset:30720\n"
  "v_mfma_f32_16x16x32_bf16 v[164:167], a[36:39], a[84:87], v[164:167]\n"
  "v_mfma_f32_16x16x32_bf16 v[164:167], a[32:35], a[68:71], v[164:167]\n"
  "v_mfma_f32_16x16x32_bf16 v[168:171], a[32:35], a[72:75], v[168:171]\n"
  "ds_read_b64_tr_b16 a[56:57], v233 offset:18432\n"
  "v_mul_f32 v128, v128, v80\n"
  "v_mfma_f32_16x16x32_bf16 v[168:171], a[36:39], a[88:91], v[168:171]\n"
  "ds_read_b64_tr_b16 a[58:59], v233 offset:22528\n"
  "s_waitcnt lgkmcnt(6)\n"
  "v_mfma_f32_16x16x32_bf16 v[172:175], a[36:39], a[92:95], v[172:175]\n"
  "v_mul_f32 v129, v129, v80\n"
  "v_mul_f32 v130, v130, v80\n"
  "v_mfma_f32_16x16x32_bf16 v[172:175], a[32:35], a[76:79], v[172:175]\n"
  "v_mul_f32 v131, v131, v80\n"
  "v_mul_f32 v144, v144, v80\n"
  "v_mfma_f32_16x16x32_bf16 v[188:191], a[40:43], a[76:79], v[188:191]\n"
  "ds_read_b64_tr_b16 a[60:61], v233 offset:26624\n"
  "v_mul_f32 v145, v145, v80\n"
  "v_mfma_f32_16x16x32_bf16 v[188:191], a[44:47], a[92:95], v[188:191]\n"
  "ds_read_b64_tr_b16 a[62:63], v233 offset:30720\n"
  "v_mul_f32 v146, v146, v80\n"
  "v_mfma_f32_16x16x32_bf16 v[184:187], a[44:47], a[88:91], v[184:187]\n"
  "v_mul_f32 v147, v147, v80\n"
  "v_cvt_pk_bf16_f32 v128, v128, v129\n"
  "v_mfma_f32_16x16x32_bf16 v[184:187], a[40:43], a[72:75], v[184:187]\n"
  "v_cvt_pk_bf16_f32 v129, v130, v131\n"
  "v_cvt_pk_bf16_f32 v130, v144, v145\n"
  "v_mfma_f32_16x16x32_bf16 v[180:183], a[40:43], a[68:71], v[180:183]\n"
  "ds_read_b64_tr_b16 a[32:33], v232 offset:34816\n"
  "v_cvt_pk_bf16_f32 v131, v146, v147\n"
  "v_mfma_f32_16x16x32_bf16 v[180:183], a[44:47], a[84:87], v[180:183]\n"
  "ds_read_b64_tr_b16 a[34:35], v232 offset:38912\n"
  "s_waitcnt lgkmcnt(6)\n"
  "v_mfma_f32_16x16x32_bf16 v[176:179], a[44:47], a[80:83], v[176:179]\n"
  "buffer_store_dwordx4 v[128:131], off, s[36:39], s33 offset:0 sc1\n"
  "v_mfma_f32_16x16x32_bf16 v[176:179], a[40:43], a[64:67], v[176:179]\n"
  "v_mul_f32 v132, v132, v81\n"
  "v_mul_f32 v133, v133, v81\n"
  "v_mfma_f32_16x16x32_bf16 v[160:163], a[48:51], a[96:99], v[160:163]\n"
  "ds_read_b64_tr_b16 a[36:37], v232 offset:43008\n"
  "v_mul_f32 v134, v134, v81\n"
  "v_mfma_f32_16x16x32_bf16 v[160:163], a[52:55], a[112:115], v[160:163]\n"
  "ds_read_b64_tr_b16 a[38:39], v232 offset:47104\n"
  "v_mul_f32 v135, v135, v81\n"
  "v_mfma_f32_16x16x32_bf16 v[164:167], a[52:55], a[116:119], v[164:167]\n"
  "v_mul_f32 v148, v148, v81\n"
  "v_mul_f32 v149, v149, v81\n"
  "v_mfma_f32_16x16x32_bf16 v[164:167], a[48:51], a[100:103], v[164:167]\n"
  "v_mul_f32 v150, v150, v81\n"
  "v_mul_f32 v151, v151, v81\n"
  "v_mfma_f32_16x16x32_bf16 v[168:171], a[48:51], a[104:107], v[168:171]\n"
  "ds_read_b64_tr_b16 a[40:41], v233 offset:34816\n"
  "v_cvt_pk_bf16_f32 v132, v132, v133\n"
  "v_mfma_f32_16x16x32_bf16 v[168:171], a[52:55], a[120:123], v[168:171]\n"
  "ds_read_b64_tr_b16 a[42:43], v233 offset:38912\n"
  "s_waitcnt lgkmcnt(6)\n"
  "v_mfma_f32_16x16x32_bf16 v[172:175], a[52:55], a[124:127], v[172:175]\n"
  "v_cvt_pk_bf16_f32 v133, v134, v135\n"
  "v_cvt_pk_bf16_f32 v134, v148, v149\n"
  "v_mfma_f32_16x16x32_bf16 v[172:175], a[48:51], a[108:111], v[172:175]\n"
  "v_cvt_pk_bf16_f32 v135, v150, v151\n"
  "v_mul_f32 v136, v136, v82\n"
  "v_mfma_f32_16x16x32_bf16 v[188:191], a[56:59], a[108:111], v[188:191]\n"
  "ds_read_b64_tr_b16 a[44:45], v233 offset:43008\n"
  "v_mul_f32 v137, v137, v82\n"
  "v_mfma_f32_16x16x32_bf16 v[188:191], a[60:63], a[124:127], v[188:191]\n"
  "ds_read_b64_tr_b16 a[46:47], v233 offset:47104\n"
  "v_mul_f32 v138, v138, v82\n"
  "v_mfma_f32_16x16x32_bf16 v[184:187], a[60:63], a[120:123], v[184:187]\n"
  "buffer_store_dwordx4 v[132:135], off, s[36:39], s33 offset:1024 sc1\n"
  "v_mfma_f32_16x16x32_bf16 v[184:187], a[56:59], a[104:107], v[184:187]\n"
  "v_mul_f32 v139, v139, v82\n"
  "v_mul_f32 v152, v152, v82\n"
  "v_mfma_f32_16x16x32_bf16 v[180:183], a[56:59], a[100:103], v[180:183]\n"
  "ds_read_b64_tr_b16 a[48:49], v232 offset:51200\n"
  "v_mul_f32 v153, v153, v82\n"
  "v_mfma_f32_16x16x32_bf16 v[180:183], a[60:63], a[116:119], v[180:183]\n"
  "ds_read_b64_tr_b16 a[50:51], v232 offset:55296\n"
  "s_waitcnt lgkmcnt(6)\n"
  "v_mfma_f32_16x16x32_bf16 v[176:179], a[60:63], a[112:115], v[176:179]\n"
  "v_mul_f32 v154, v154, v82\n"
  "v_mul_f32 v155, v155, v82\n"
  "v_mfma_f32_16x16x32_bf16 v[176:179], a[56:59], a[96:99], v[176:179]\n"
  "v_cvt_pk_bf16_f32 v136, v136, v137\n"
  "v_cvt_pk_bf16_f32 v137, v138, v139\n"
  "v_mfma_f32_16x16x32_bf16 v[160:163], a[32:35], a[128:131], v[160:163]\n"
  "ds_read_b64_tr_b16 a[52:53], v232 offset:59392\n"
  "v_cvt_pk_bf16_f32 v138, v152, v153\n"
  "v_mfma_f32_16x16x32_bf16 v[160:163], a[36:39], a[144:147], v[160:163]\n"
  "ds_read_b64_tr_b16 a[54:55], v232 offset:63488\n"
  "v_cvt_pk_bf16_f32 v139, v154, v155\n"
  "v_mfma_f32_16x16x32_bf16 v[164:167], a[36:39], a[148:151], v[164:167]\n"
  "buffer_store_dwordx4 v[136:139], off, s[36:39], s33 offset:2048 sc1\n"
  "v_mfma_f32_16x16x32_bf16 v[164:167], a[32:35], a[132:135], v[164:167]\n"
  "v_mul_f32 v140, v140, v83\n"
  "v_mul_f32 v141, v141, v83\n"
  "v_mfma_f32_16x16x32_bf16 v[168:171], a[32:35], a[136:139], v[168:171]\n"
  "ds_read_b64_tr_b16 a[56:57], v233 offset:51200\n"
  "v_mul_f32 v142, v142, v83\n"
  "v_mfma_f32_16x16x32_bf16 v[168:171], a[36:39], a[152:155], v[168:171]\n"
  "ds_read_b64_tr_b16 a[58:59], v233 offset:55296\n"
  "s_waitcnt lgkmcnt(6)\n"
  "v_mfma_f32_16x16x32_bf16 v[172:175], a[36:39], a[156:159], v[172:175]\n"
  "v_mul_f32 v143, v143, v83\n"
  "v_mul_f32 v156, v156, v83\n"
  "v_mfma_f32_16x16x32_bf16 v[172:175], a[32:35], a[140:143], v[172:175]\n"
  "v_mul_f32 v157, v157, v83\n"
  "v_mul_f32 v158, v158, v83\n"
  "v_mfma_f32_16x16x32_bf16 v[188:191], a[40:43], a[140:143], v[188:191]\n"
  "ds_read_b64_tr_b16 a[60:61], v233 offset:59392\n"
  "v_mul_f32 v159, v159, v83\n"
  "v_mfma_f32_16x16x32_bf16 v[188:191], a[44:47], a[156:159], v[188:191]\n"
  "ds_read_b64_tr_b16 a[62:63], v233 offset:63488\n"
  "v_cvt_pk_bf16_f32 v140, v140, v141\n"
  "v_mfma_f32_16x16x32_bf16 v[184:187], a[44:47], a[152:155], v[184:187]\n"
  "v_cvt_pk_bf16_f32 v141, v142, v143\n"
  "v_cvt_pk_bf16_f32 v142, v156, v157\n"
  "v_mfma_f32_16x16x32_bf16 v[184:187], a[40:43], a[136:139], v[184:187]\n"
  "v_cvt_pk_bf16_f32 v143, v158, v159\n"
  "v_mfma_f32_16x16x32_bf16 v[180:183], a[40:43], a[132:135], v[180:183]\n"
  "ds_read_b64_tr_b16 a[32:33], v240 offset:2048\n"
  "v_mfma_f32_16x16x32_bf16 v[180:183], a[44:47], a[148:151], v[180:183]\n"
  "ds_read_b64_tr_b16 a[34:35], v240 offset:6144\n"
  "s_waitcnt lgkmcnt(6)\n"
  "v_mfma_f32_16x16x32_bf16 v[176:179], a[44:47], a[144:147], v[176:179]\n"
  "buffer_store_dwordx4 v[140:143], off, s[36:39], s33 offset:3072 sc1\n"
  "v_mfma_f32_16x16x32_bf16 v[176:179], a[40:43], a[128:131], v[176:179]\n"
  "v_mfma_f32_16x16x32_bf16 v[160:163], a[48:51], a[160:163], v[160:163]\n"
  "ds_read_b64_tr_b16 a[36:37], v240 offset:10240\n"
  "v_mfma_f32_16x16x32_bf16 v[160:163], a[52:55], a[176:179], v[160:163]\n"
  "ds_read_b64_tr_b16 a[38:39], v240 offset:14336\n"
  "v_mfma_f32_16x16x32_bf16 v[164:167], a[52:55], a[180:183], v[164:167]\n"
  "v_mfma_f32_16x16x32_bf16 v[164:167], a[48:51], a[164:167], v[164:167]\n"
  "v_mfma_f32_16x16x32_bf16 v[168:171], a[48:51], a[168:171], v[168:171]\n"
  "ds_read_b64_tr_b16 a[40:41], v241 offset:2048\n"
  "v_mfma_f32_16x16x32_bf16 v[168:171], a[52:55], a[184:187], v[168:171]\n"
  "ds_read_b64_tr_b16 a[42:43], v241 offset:6144\n"
  "s_waitcnt lgkmcnt(6)\n"
  "v_mfma_f32_16x16x32_bf16 v[172:175], a[52:55], a[188:191], v[172:175]\n"
  "v_mfma_f32_16x16x32_bf16 v[172:175], a[48:51], a[172:175], v[172:175]\n"
  "v_mfma_f32_16x16x32_bf16 v[188:191], a[56:59], a[172:175], v[188:191]\n"
  "ds_read_b64_tr_b16 a[44:45], v241 offset:10240\n"
  "v_mfma_f32_16x16x32_bf16 v[188:191], a[60:63], a[188:191], v[188:191]\n"
  "ds_read_b64_tr_b16 a[46:47], v241 offset:14336\n"
  "v_mfma_f32_16x16x32_bf16 v[184:187], a[60:63], a[184:187], v[184:187]\n"
  "v_mfma_f32_16x16x32_bf16 v[184:187], a[56:59], a[168:171], v[184:187]\n"
  "v_mfma_f32_16x16x32_bf16 v[180:183], a[56:59], a[164:167], v[180:183]\n"
  "ds_read_b64_tr_b16 a[48:49], v240 offset:18432\n"
  "v_mfma_f32_16x16x32_bf16 v[180:183], a[60:63], a[180:183], v[180:183]\n"
  "ds_read_b64_tr_b16 a[50:51], v240 offset:22528\n"
  "s_waitcnt lgkmcnt(6)\n"
  "v_mfma_f32_16x16x32_bf16 v[176:179], a[60:63], a[176:179], v[176:179]\n"
  "v_mfma_f32_16x16x32_bf16 v[176:179], a[56:59], a[160:163], v[176:179]\n"
  "v_mfma_f32_16x16x32_bf16 v[160:163], a[32:35], a[192:195], v[160:163]\n"
  "ds_read_b64_tr_b16 a[52:53], v240 offset:26624\n"
  "v_mfma_f32_16x16x32_bf16 v[160:163], a[36:39], a[208:211], v[160:163]\n"
  "ds_read_b64_tr_b16 a[54:55], v240 offset:30720\n"
  "v_mfma_f32_16x16x32_bf16 v[164:167], a[36:39], a[212:215], v[164:167]\n"
  "v_mfma_f32_16x16x32_bf16 v[164:167], a[32:35], a[196:199], v[164:167]\n"
  "v_mfma_f32_16x16x32_bf16 v[168:171], a[32:35], a[200:203], v[168:171]\n"
  "ds_read_b64_tr_b16 a[56:57], v241 offset:18432\n"
  "v_mfma_f32_16x16x32_bf16 v[168:171], a[36:39], a[216:219], v[168:171]\n"
  "ds_read_b64_tr_b16 a[58:59], v241 offset:22528\n"
  "s_waitcnt lgkmcnt(6)\n"
  "v_mfma_f32_16x16x32_bf16 v[172:175], a[36:39], a[220:223], v[172:175]\n"
  "v_mfma_f32_16x16x32_bf16 v[172:175], a[32:35], a[204:207], v[172:175]\n"
  "v_mfma_f32_16x16x32_bf16 v[188:191], a[40:43], a[204:207], v[188:191]\n"
  "ds_read_b64_tr_b16 a[60:61], v241 offset:26624\n"
  "v_mfma_f32_16x16x32_bf16 v[188:191], a[44:47], a[220:223], v[188:191]\n"
  "ds_read_b64_tr_b16 a[62:63], v241 offset:30720\n"
  "v_mfma_f32_16x16x32_bf16 v[184:187], a[44:47], a[216:219], v[184:187]\n"
  "v_mfma_f32_16x16x32_bf16 v[184:187], a[40:43], a[200:203], v[184:187]\n"
  "v_mfma_f32_16x16x32_bf16 v[180:183], a[40:43], a[196:199], v[180:183]\n"
  "ds_read_b64_tr_b16 a[32:33], v234 offset:2048\n"
  "v_mfma_f32_16x16x32_bf16 v[180:183], a[44:47], a[212:215], v[180:183]\n"
  "ds_read_b64_tr_b16 a[34:35], v234 offset:6144\n"
  "s_waitcnt lgkmcnt(6)\n"
  "v_mfma_f32_16x16x32_bf16 v[176:179], a[44:47], a[208:211], v[176:179]\n"
  "v_mfma_f32_16x16x32_bf16 v[176:179], a[40:43], a[192:195], v[176:179]\n"
  "v_mfma_f32_16x16x32_bf16 v[160:163], a[48:51], a[224:227], v[160:163]\n"
  "ds_read_b64_tr_b16 a[36:37], v234 offset:10240\n"
  "v_mfma_f32_16x16x32_bf16 v[160:163], a[52:55], a[240:243], v[160:163]\n"
  "ds_read_b64_tr_b16 a[38:39], v234 offset:14336\n"
  "v_mfma_f32_16x16x32_bf16 v[164:167], a[52:55], a[244:247], v[164:167]\n"
  "v_mfma_f32_16x16x32_bf16 v[164:167], a[48:51], a[228:231], v[164:167]\n"
  "v_mfma_f32_16x16x32_bf16 v[168:171], a[48:51], a[232:235], v[168:171]\n"
  "ds_read_b64_tr_b16 a[40:41], v235 offset:2048\n"
  "v_mfma_f32_16x16x32_bf16 v[168:171], a[52:55], a[248:251], v[168:171]\n"
  "ds_read_b64_tr_b16 a[42:43], v235 offset:6144\n"
  "s_waitcnt lgkmcnt(6)\n"
  "v_mfma_f32_16x16x32_bf16 v[172:175], a[52:55], a[252:255], v[172:175]\n"
  "v_mfma_f32_16x16x32_bf16 v[172:175], a[48:51], a[236:239], v[172:175]\n"
  "v_mfma_f32_16x16x32_bf16 v[188:191], a[56:59], a[236:239], v[188:191]\n"
  "ds_read_b64_tr_b16 a[44:45], v235 offset:10240\n"
  "v_mfma_f32_16x16x32_bf16 v[188:191], a[60:63], a[252:255], v[188:191]\n"
  "ds_read_b64_tr_b16 a[46:47], v235 offset:14336\n"
  "v_mfma_f32_16x16x32_bf16 v[184:187], a[60:63], a[248:251], v[184:187]\n"
  "s_waitcnt vmcnt(0)\n"
  "buffer_store_dwordx4 v[248:251], v251, s[52:55], s65 offen sc1\n"
  "v_mfma_f32_16x16x32_bf16 v[184:187], a[56:59], a[232:235], v[184:187]\n"
  "v_mfma_f32_16x16x32_bf16 v[180:183], a[56:59], a[228:231], v[180:183]\n"
  "ds_read_b64_tr_b16 a[48:49], v234 offset:18432\n"
  "v_mfma_f32_16x16x32_bf16 v[180:183], a[60:63], a[244:247], v[180:183]\n"
  "ds_read_b64_tr_b16 a[50:51], v234 offset:22528\n"
  "s_waitcnt lgkmcnt(6)\n"
  "v_mfma_f32_16x16x32_bf16 v[176:179], a[60:63], a[240:243], v[176:179]\n"
  "v_mfma_f32_16x16x32_bf16 v[176:179], a[56:59], a[224:227], v[176:179]\n"
  "v_mfma_f32_16x16x32_bf16 v[192:195], a[32:35], a[64:67], v[192:195]\n"
  "ds_read_b64_tr_b16 a[52:53], v234 offset:26624\n"
  "v_mfma_f32_16x16x32_bf16 v[192:195], a[36:39], a[80:83], v[192:195]\n"
  "ds_read_b64_tr_b16 a[54:55], v234 offset:30720\n"
  "v_mfma_f32_16x16x32_bf16 v[196:199], a[36:39], a[84:87], v[196:199]\n"
  "v_mfma_f32_16x16x32_bf16 v[196:199], a[32:35], a[68:71], v[196:199]\n"
  "v_mfma_f32_16x16x32_bf16 v[200:203], a[32:35], a[72:75], v[200:203]\n"
  "ds_read_b64_tr_b16 a[56:57], v235 offset:18432\n"
  "v_mul_f32 v160, v160, v80\n"
  "v_mul_f32 v161, v161, v80\n"
  "v_mul_f32 v162, v162, v80\n"
  "v_mul_f32 v163, v163, v80\n"
  "v_mul_f32 v176, v176, v80\n"
  "v_mfma_f32_16x16x32_bf16 v[200:203], a[36:39], a[88:91], v[200:203]\n"
  "ds_read_b64_tr_b16 a[58:59], v235 offset:22528\n"
  "s_waitcnt lgkmcnt(6)\n"
  "v_mul_f32 v177, v177, v80\n"
  "v_mul_f32 v178, v178, v80\n"
  "v_mul_f32 v179, v179, v80\n"
  "v_cvt_pk_bf16_f32 v160, v160, v161\n"
  "v_mfma_f32_16x16x32_bf16 v[204:207], a[36:39], a[92:95], v[204:207]\n"
  "v_cvt_pk_bf16_f32 v161, v162, v163\n"
  "v_cvt_pk_bf16_f32 v162, v176, v177\n"
  "v_cvt_pk_bf16_f32 v163, v178, v179\n"
  "v_mul_f32 v164, v164, v81\n"
  "v_mul_f32 v165, v165, v81\n"
  "v_mul_f32 v166, v166, v81\n"
  "v_mfma_f32_16x16x32_bf16 v[204:207], a[32:35], a[76:79], v[204:207]\n"
  "buffer_store_dwordx4 v[160:163], off, s[36:39], s34 offset:0 sc1\n"
  "v_mul_f32 v167, v167, v81\n"
  "v_mul_f32 v180, v180, v81\n"
  "v_mul_f32 v181, v181, v81\n"
  "v_mul_f32 v182, v182, v81\n"
  "v_mfma_f32_16x16x32_bf16 v[220:223], a[40:43], a[76:79], v[220:223]\n"
  "ds_read_b64_tr_b16 a[60:61], v235 offset:26624\n"
  "v_mul_f32 v183, v183, v81\n"
  "v_cvt_pk_bf16_f32 v164, v164, v165\n"
  "v_cvt_pk_bf16_f32 v165, v166, v167\n"
  "v_cvt_pk_bf16_f32 v166, v180, v181\n"
  "v_cvt_pk_bf16_f32 v167, v182, v183\n"
  "v_mfma_f32_16x16x32_bf16 v[220:223], a[44:47], a[92:95], v[220:223]\n"
  "ds_read_b64_tr_b16 a[62:63], v235 offset:30720\n"
  "buffer_store_dwordx4 v[164:167], off, s[36:39], s34 offset:1024 sc1\n"
  "v_mul_f32 v168, v168, v82\n"
  "v_mul_f32 v169, v169, v82\n"
  "v_mfma_f32_16x16x32_bf16 v[216:219], a[44:47], a[88:91], v[216:219]\n"
  "v_mul_f32 v170, v170, v82\n"
  "v_mul_f32 v171, v171, v82\n"
  "v_mul_f32 v184, v184, v82\n"
  "v_mul_f32 v185, v185, v82\n"
  "v_mul_f32 v186, v186, v82\n"
  "v_mul_f32 v187, v187, v82\n"
  "v_mfma_f32_16x16x32_bf16 v[216:219], a[40:43], a[72:75], v[216:219]\n"
  "v_cvt_pk_bf16_f32 v168, v168, v169\n"
  "v_cvt_pk_bf16_f32 v169, v170, v171\n"
  "v_cvt_pk_bf16_f32 v170, v184, v185\n"
  "v_cvt_pk_bf16_f32 v171, v186, v187\n"
  "v_mul_f32 v172, v172, v83\n"
  "v_mul_f32 v173, v173, v83\n"
  "v_mfma_f32_16x16x32_bf16 v[212:215], a[40:43], a[68:71], v[212:215]\n"
  "ds_read_b64_tr_b16 a[32:33], v234 offset:34816\n"
  "buffer_store_dwordx4 v[168:171], off, s[36:39], s34 offset:2048 sc1\n"
  "v_mul_f32 v174, v174, v83\n"
  "v_mul_f32 v175, v175, v83\n"
  "v_mfma_f32_16x16x32_bf16 v[212:215], a[44:47], a[84:87], v[212:215]\n"
  "ds_read_b64_tr_b16 a[34:35], v234 offset:38912\n"
  "s_waitcnt lgkmcnt(6)\n"
  "v_mul_f32 v188, v188, v83\n"
  "v_mul_f32 v189, v189, v83\n"
  "v_mul_f32 v190, v190, v83\n"
  "v_mul_f32 v191, v191, v83\n"
  "v_mfma_f32_16x16x32_bf16 v[208:211], a[44:47], a[80:83], v[208:211]\n"
  "v_cvt_pk_bf16_f32 v172, v172, v173\n"
  "v_cvt_pk_bf16_f32 v173, v174, v175\n"
  "v_cvt_pk_bf16_f32 v174, v188, v189\n"
  "v_cvt_pk_bf16_f32 v175, v190, v191\n"
  "v_mfma_f32_16x16x32_bf16 v[208:211], a[40:43], a[64:67], v[208:211]\n"
  "buffer_store_dwordx4 v[172:175], off, s[36:39], s34 offset:3072 sc1\n"
  "v_mfma_f32_16x16x32_bf16 v[192:195], a[48:51], a[96:99], v[192:195]\n"
  "ds_read_b64_tr_b16 a[36:37], v234 offset:43008\n"
  "v_mfma_f32_16x16x32_bf16 v[192:195], a[52:55], a[112:115], v[192:195]\n"
  "ds_read_b64_tr_b16 a[38:39], v234 offset:47104\n"
  "v_mfma_f32_16x16x32_bf16 v[196:199], a[52:55], a[116:119], v[196:199]\n"
  "v_mfma_f32_16x16x32_bf16 v[196:199], a[48:51], a[100:103], v[196:199]\n"
  "v_mfma_f32_16x16x32_bf16 v[200:203], a[48:51], a[104:107], v[200:203]\n"
  "ds_read_b64_tr_b16 a[40:41], v235 offset:34816\n"
  "v_mfma_f32_16x16x32_bf16 v[200:203], a[52:55], a[120:123], v[200:203]\n"
  "ds_read_b64_tr_b16 a[42:43], v235 offset:38912\n"
  "s_waitcnt lgkmcnt(6)\n"
  "v_mfma_f32_16x16x32_bf16 v[204:207], a[52:55], a[124:127], v[204:207]\n"
  "buffer_load_dwordx2 v[238:239], v247, s[52:55], s58 offen sc1\n"
  "v_mfma_f32_16x16x32_bf16 v[204:207], a[48:51], a[108:111], v[204:207]\n"
  "v_mfma_f32_16x16x32_bf16 v[220:223], a[56:59], a[108:111], v[220:223]\n"
  "ds_read_b64_tr_b16 a[44:45], v235 offset:43008\n"
  "v_mfma_f32_16x16x32_bf16 v[220:223], a[60:63], a[124:127], v[220:223]\n"
  "ds_read_b64_tr_b16 a[46:47], v235 offset:47104\n"
  "v_mfma_f32_16x16x32_bf16 v[216:219], a[60:63], a[120:123], v[216:219]\n"
  "v_mfma_f32_16x16x32_bf16 v[216:219], a[56:59], a[104:107], v[216:219]\n"
  "v_mfma_f32_16x16x32_bf16 v[212:215], a[56:59], a[100:103], v[212:215]\n"
  "ds_read_b64_tr_b16 a[48:49], v234 offset:51200\n"
  "v_mfma_f32_16x16x32_bf16 v[212:215], a[60:63], a[116:119], v[212:215]\n"
  "ds_read_b64_tr_b16 a[50:51], v234 offset:55296\n"
  "s_waitcnt lgkmcnt(6)\n"
  "v_mfma_f32_16x16x32_bf16 v[208:211], a[60:63], a[112:115], v[208:211]\n"
  "v_mfma_f32_16x16x32_bf16 v[208:211], a[56:59], a[96:99], v[208:211]\n"
  "v_mfma_f32_16x16x32_bf16 v[192:195], a[32:35], a[128:131], v[192:195]\n"
  "ds_read_b64_tr_b16 a[52:53], v234 offset:59392\n"
  "v_mfma_f32_16x16x32_bf16 v[192:195], a[36:39], a[144:147], v[192:195]\n"
  "ds_read_b64_tr_b16 a[54:55], v234 offset:63488\n"
  "v_mfma_f32_16x16x32_bf16 v[196:199], a[36:39], a[148:151], v[196:199]\n"
  "v_mfma_f32_16x16x32_bf16 v[196:199], a[32:35], a[132:135], v[196:199]\n"
  "v_mfma_f32_16x16x32_bf16 v[200:203], a[32:35], a[136:139], v[200:203]\n"
  "ds_read_b64_tr_b16 a[56:57], v235 offset:51200\n"
  "v_mfma_f32_16x16x32_bf16 v[200:203], a[36:39], a[152:155], v[200:203]\n"
  "ds_read_b64_tr_b16 a[58:59], v235 offset:55296\n"
  "s_waitcnt lgkmcnt(6)\n"
  "v_mfma_f32_16x16x32_bf16 v[204:207], a[36:39], a[156:159], v[204:207]\n"
  "v_mfma_f32_16x16x32_bf16 v[204:207], a[32:35], a[140:143], v[204:207]\n"
  "v_mfma_f32_16x16x32_bf16 v[220:223], a[40:43], a[140:143], v[220:223]\n"
  "ds_read_b64_tr_b16 a[60:61], v235 offset:59392\n"
  "v_mfma_f32_16x16x32_bf16 v[220:223], a[44:47], a[156:159], v[220:223]\n"
  "ds_read_b64_tr_b16 a[62:63], v235 offset:63488\n"
  "v_mfma_f32_16x16x32_bf16 v[216:219], a[44:47], a[152:155], v[216:219]\n"
  "v_mfma_f32_16x16x32_bf16 v[216:219], a[40:43], a[136:139], v[216:219]\n"
  "v_mfma_f32_16x16x32_bf16 v[212:215], a[40:43], a[132:135], v[212:215]\n"
  "ds_read_b64_tr_b16 a[32:33], v242 offset:2048\n"
  "v_mfma_f32_16x16x32_bf16 v[212:215], a[44:47], a[148:151], v[212:215]\n"
  "ds_read_b64_tr_b16 a[34:35], v242 offset:6144\n"
  "s_waitcnt lgkmcnt(6)\n"
  "v_mfma_f32_16x16x32_bf16 v[208:211], a[44:47], a[144:147], v[208:211]\n"
  "v_mfma_f32_16x16x32_bf16 v[208:211], a[40:43], a[128:131], v[208:211]\n"
  "v_mfma_f32_16x16x32_bf16 v[192:195], a[48:51], a[160:163], v[192:195]\n"
  "ds_read_b64_tr_b16 a[36:37], v242 offset:10240\n"
  "s_waitcnt vmcnt(1)\n"
  "buffer_store_dwordx4 v[248:251], v251, s[52:55], s66 offen sc1\n"
  "v_mfma_f32_16x16x32_bf16 v[192:195], a[52:55], a[176:179], v[192:195]\n"
  "ds_read_b64_tr_b16 a[38:39], v242 offset:14336\n"
  "v_mfma_f32_16x16x32_bf16 v[196:199], a[52:55], a[180:183], v[196:199]\n"
  "v_mfma_f32_16x16x32_bf16 v[196:199], a[48:51], a[164:167], v[196:199]\n"
  "v_mfma_f32_16x16x32_bf16 v[200:203], a[48:51], a[168:171], v[200:203]\n"
  "ds_read_b64_tr_b16 a[40:41], v243 offset:2048\n"
  "v_mfma_f32_16x16x32_bf16 v[200:203], a[52:55], a[184:187], v[200:203]\n"
  "ds_read_b64_tr_b16 a[42:43], v243 offset:6144\n"
  "s_waitcnt lgkmcnt(6)\n"
  "v_mfma_f32_16x16x32_bf16 v[204:207], a[52:55], a[188:191], v[204:207]\n"
  "s_waitcnt vmcnt(1)\n"
  "L_poll8_%=:\n"
  "v_cmp_ne_u64 vcc, s[10:11], v[238:239]\n"
  "s_cbranch_vccz L_ok8_%=\n"
  "s_sub_u32 s99, s99, 1\n"
  "s_cbranch_scc1 L_out8_%=\n"
  "buffer_load_dwordx2 v[238:239], v247, s[52:55], s58 offen sc1\n"
  "s_waitcnt vmcnt(0)\n"
  "s_branch L_poll8_%=\n"
  "L_out8_%=:\n"
  "s_mov_b32 s99, 0\n"
  "L_ok8_%=:\n"
  "v_mfma_f32_16x16x32_bf16 v[204:207], a[48:51], a[172:175], v[204:207]\n"
  "buffer_load_dwordx4 v[0:3], off, s[36:39], s48 offset:0 sc1\n"
  "v_mfma_f32_16x16x32_bf16 v[220:223], a[56:59], a[172:175], v[220:223]\n"
  "ds_read_b64_tr_b16 a[44:45], v243 offset:10240\n"
  "v_mfma_f32_16x16x32_bf16 v[220:223], a[60:63], a[188:191], v[220:223]\n"
  "ds_read_b64_tr_b16 a[46:47], v243 offset:14336\n"
  "v_mfma_f32_16x16x32_bf16 v[216:219], a[60:63], a[184:187], v[216:219]\n"
  "buffer_load_dwordx4 v[4:7], off, s[36:39], s48 offset:1024 sc1\n"
  "v_mfma_f32_16x16x32_bf16 v[216:219], a[56:59], a[168:171], v[216:219]\n"
  "v_mfma_f32_16x16x32_bf16 v[212:215], a[56:59], a[164:167], v[212:215]\n"
  "ds_read_b64_tr_b16 a[48:49], v242 offset:18432\n"
  "v_mfma_f32_16x16x32_bf16 v[212:215], a[60:63], a[180:183], v[212:215]\n"
  "ds_read_b64_tr_b16 a[50:51], v242 offset:22528\n"
  "s_waitcnt lgkmcnt(6)\n"
  "v_mfma_f32_16x16x32_bf16 v[208:211], a[60:63], a[176:179], v[208:211]\n"
  "buffer_load_dwordx4 v[8:11], off, s[36:39], s48 offset:2048 sc1\n"
  "v_mfma_f32_16x16x32_bf16 v[208:211], a[56:59], a[160:163], v[208:211]\n"
  "v_mfma_f32_16x16x32_bf16 v[192:195], a[32:35], a[192:195], v[192:195]\n"
  "ds_read_b64_tr_b16 a[52:53], v242 offset:26624\n"
  "v_mfma_f32_16x16x32_bf16 v[192:195], a[36:39], a[208:211], v[192:195]\n"
  "ds_read_b64_tr_b16 a[54:55], v242 offset:30720\n"
  "v_mfma_f32_16x16x32_bf16 v[196:199], a[36:39], a[212:215], v[196:199]\n"
  "buffer_load_dwordx4 v[12:15], off, s[36:39], s48 offset:3072 sc1\n"
  "v_mfma_f32_16x16x32_bf16 v[196:199], a[32:35], a[196:199], v[196:199]\n"
  "v_mfma_f32_16x16x32_bf16 v[200:203], a[32:35], a[200:203], v[200:203]\n"
  "ds_read_b64_tr_b16 a[56:57], v243 offset:18432\n"
  "v_mfma_f32_16x16x32_bf16 v[200:203], a[36:39], a[216:219], v[200:203]\n"
  "ds_read_b64_tr_b16 a[58:59], v243 offset:22528\n"
  "s_waitcnt lgkmcnt(6)\n"
  "v_mfma_f32_16x16x32_bf16 v[204:207], a[36:39], a[220:223], v[204:207]\n"
  "v_mfma_f32_16x16x32_bf16 v[204:207], a[32:35], a[204:207], v[204:207]\n"
  "v_mfma_f32_16x16x32_bf16 v[220:223], a[40:43], a[204:207], v[220:223]\n"
  "ds_read_b64_tr_b16 a[60:61], v243 offset:26624\n"
  "v_mfma_f32_16x16x32_bf16 v[220:223], a[44:47], a[220:223], v[220:223]\n"
  "ds_read_b64_tr_b16 a[62:63], v243 offset:30720\n"
  "v_mfma_f32_16x16x32_bf16 v[216:219], a[44:47], a[216:219], v[216:219]\n"
  "buffer_load_dwordx2 v[238:239], v247, s[52:55], s59 offen sc1\n"
  "v_mfma_f32_16x16x32_bf16 v[216:219], a[40:43], a[200:203], v[216:219]\n"
  "v_mfma_f32_16x16x32_bf16 v[212:215], a[40:43], a[196:199], v[212:215]\n"
  "v_mfma_f32_16x16x32_bf16 v[212:215], a[44:47], a[212:215], v[212:215]\n"
  "v_mfma_f32_16x16x32_bf16 v[208:211], a[44:47], a[208:211], v[208:211]\n"
  "v_mfma_f32_16x16x32_bf16 v[208:211], a[40:43], a[192:195], v[208:211]\n"
  "s_waitcnt lgkmcnt(4)\n"
  "v_mfma_f32_16x16x32_bf16 v[192:195], a[48:51], a[224:227], v[192:195]\n"
  "buffer_load_dwordx2 v[224:225], v247, s[52:55], s51 offen sc1\n"
  "v_mfma_f32_16x16x32_bf16 v[192:195], a[52:55], a[240:243], v[192:195]\n"
  "v_mfma_f32_16x16x32_bf16 v[196:199], a[52:55], a[244:247], v[196:199]\n"
  "v_mfma_f32_16x16x32_bf16 v[196:199], a[48:51], a[228:231], v[196:199]\n"
  "v_mfma_f32_16x16x32_bf16 v[200:203], a[48:51], a[232:235], v[200:203]\n"
  "v_mfma_f32_16x16x32_bf16 v[200:203], a[52:55], a[248:251], v[200:203]\n"
  "v_mfma_f32_16x16x32_bf16 v[204:207], a[52:55], a[252:255], v[204:207]\n"
  "v_mfma_f32_16x16x32_bf16 v[204:207], a[48:51], a[236:239], v[204:207]\n"
  "s_waitcnt lgkmcnt(0)\n"
  "v_mfma_f32_16x16x32_bf16 v[220:223], a[56:59], a[236:239], v[220:223]\n"
  "v_mfma_f32_16x16x32_bf16 v[220:223], a[60:63], a[252:255], v[220:223]\n"
  "v_mfma_f32_16x16x32_bf16 v[216:219], a[60:63], a[248:251], v[216:219]\n"
  "v_mfma_f32_16x16x32_bf16 v[216:219], a[56:59], a[232:235], v[216:219]\n"
  "v_mfma_f32_16x16x32_bf16 v[212:215], a[56:59], a[228:231], v[212:215]\n"
  "v_mfma_f32_16x16x32_bf16 v[212:215], a[60:63], a[244:247], v[212:215]\n"
  "v_mfma_f32_16x16x32_bf16 v[208:211], a[60:63], a[240:243], v[208:211]\n"
  "v_mfma_f32_16x16x32_bf16 v[208:211], a[56:59], a[224:227], v[208:211]\n"
  "s_waitcnt vmcnt(1)\n"
  "L_poll12_%=:\n"
  "v_cmp_ne_u64 vcc, s[10:11], v[238:239]\n"
  "s_cbranch_vccz L_ok12_%=\n"
  "s_sub_u32 s99, s99, 1\n"
  "s_cbranch_scc1 L_out12_%=\n"
  "buffer_load_dwordx2 v[238:239], v247, s[52:55], s59 offen sc1\n"
  "s_waitcnt vmcnt(0)\n"
  "s_branch L_poll12_%=\n"
  "L_out12_%=:\n"
  "s_mov_b32 s99, 0\n"
  "L_ok12_%=:\n"
  "buffer_load_dwordx4 v[16:19], off, s[36:39], s49 offset:0 sc1\n"
  "buffer_load_dwordx4 v[20:23], off, s[36:39], s49 offset:1024 sc1\n"
  "buffer_load_dwordx4 v[24:27], off, s[36:39], s49 offset:2048 sc1\n"
  "buffer_load_dwordx4 v[28:31], off, s[36:39], s49 offset:3072 sc1\n"
  "v_fmac_f32 v32, v192, v80\n"
  "v_fmac_f32 v33, v193, v80\n"
  "v_fmac_f32 v34, v194, v80\n"
  "v_fmac_f32 v35, v195, v80\n"
  "v_fmac_f32 v36, v208, v80\n"
  "v_fmac_f32 v37, v209, v80\n"
  "v_fmac_f32 v38, v210, v80\n"
  "v_fmac_f32 v39, v211, v80\n"
  "v_fmac_f32 v40, v196, v81\n"
  "v_fmac_f32 v41, v197, v81\n"
  "v_fmac_f32 v42, v198, v81\n"
  "v_fmac_f32 v43, v199, v81\n"
  "v_fmac_f32 v44, v212, v81\n"
  "v_fmac_f32 v45, v213, v81\n"
  "v_fmac_f32 v46, v214, v81\n"
  "v_fmac_f32 v47, v215, v81\n"
  "s_waitcnt vmcnt(4)\n"
  "L_poll13_%=:\n"
  "v_cmp_ne_u64 vcc, s[10:11], v[224:225]\n"
  "s_cbranch_vccz L_ok13_%=\n"
  "s_sub_u32 s99, s99, 1\n"
  "s_cbranch_scc1 L_out13_%=\n"
  "buffer_load_dwordx2 v[224:225], v247, s[52:55], s51 offen sc1\n"
  "s_waitcnt vmcnt(0)\n"
  "s_branch L_poll13_%=\n"
  "L_out13_%=:\n"
  "s_mov_b32 s99, 0\n"
  "L_ok13_%=:\n"
  "buffer_load_dwordx4 v[64:67], off, s[36:39], s50 offset:0 sc1\n"
  "buffer_load_dwordx4 v[68:71], off, s[36:39], s50 offset:1024 sc1\n"
  "buffer_load_dwordx4 v[72:75], off, s[36:39], s50 offset:2048 sc1\n"
  "buffer_load_dwordx4 v[76:79], off, s[36:39], s50 offset:3072 sc1\n"
  "v_fmac_f32 v48, v200, v82\n"
  "v_fmac_f32 v49, v201, v82\n"
  "v_fmac_f32 v50, v202, v82\n"
  "v_fmac_f32 v51, v203, v82\n"
  "v_fmac_f32 v52, v216, v82\n"
  "v_fmac_f32 v53, v217, v82\n"
  "v_fmac_f32 v54, v218, v82\n"
  "v_fmac_f32 v55, v219, v82\n"
  "v_fmac_f32 v56, v204, v83\n"
  "v_fmac_f32 v57, v205, v83\n"
  "v_fmac_f32 v58, v206, v83\n"
  "v_fmac_f32 v59, v207, v83\n"
  "v_fmac_f32 v60, v220, v83\n"
  "v_fmac_f32 v61, v221, v83\n"
  "v_fmac_f32 v62, v222, v83\n"
  "v_fmac_f32 v63, v223, v83\n"
  "v_dot2_f32_bf16 v32, v0, s56, v32\n"
  "v_dot2_f32_bf16 v33, v0, s57, v33\n"
  "v_dot2_f32_bf16 v34, v1, s56, v34\n"
  "v_dot2_f32_bf16 v35, v1, s57, v35\n"
  "v_dot2_f32_bf16 v36, v2, s56, v36\n"
  "v_dot2_f32_bf16 v37, v2, s57, v37\n"
  "v_dot2_f32_bf16 v38, v3, s56, v38\n"
  "v_dot2_f32_bf16 v39, v3, s57, v39\n"
  "v_dot2_f32_bf16 v40, v4, s56, v40\n"
  "v_dot2_f32_bf16 v41, v4, s57, v41\n"
  "v_dot2_f32_bf16 v42, v5, s56, v42\n"
  "v_dot2_f32_bf16 v43, v5, s57, v43\n"
  "v_dot2_f32_bf16 v44, v6, s56, v44\n"
  "v_dot2_f32_bf16 v45, v6, s57, v45\n"
  "v_dot2_f32_bf16 v46, v7, s56, v46\n"
  "v_dot2_f32_bf16 v47, v7, s57, v47\n"
  "v_dot2_f32_bf16 v48, v8, s56, v48\n"
  "v_dot2_f32_bf16 v49, v8, s57, v49\n"
  "v_dot2_f32_bf16 v50, v9, s56, v50\n"
  "v_dot2_f32_bf16 v51, v9, s57, v51\n"
  "v_dot2_f32_bf16 v52, v10, s56, v52\n"
  "v_dot2_f32_bf16 v53, v10, s57, v53\n"
  "v_dot2_f32_bf16 v54, v11, s56, v54\n"
  "v_dot2_f32_bf16 v55, v11, s57, v55\n"
  "v_dot2_f32_bf16 v56, v12, s56, v56\n"
  "v_dot2_f32_bf16 v57, v12, s57, v57\n"
  "v_dot2_f32_bf16 v58, v13, s56, v58\n"
  "v_dot2_f32_bf16 v59, v13, s57, v59\n"
  "v_dot2_f32_bf16 v60, v14, s56, v60\n"
  "v_dot2_f32_bf16 v61, v14, s57, v61\n"
  "v_dot2_f32_bf16 v62, v15, s56, v62\n"
  "v_dot2_f32_bf16 v63, v15, s57, v63\n"
  "s_waitcnt vmcnt(7)\n"
  "v_dot2_f32_bf16 v32, v16, s56, v32\n"
  "v_dot2_f32_bf16 v33, v16, s57, v33\n"
  "v_dot2_f32_bf16 v34, v17, s56, v34\n"
  "v_dot2_f32_bf16 v35, v17, s57, v35\n"
  "v_dot2_f32_bf16 v36, v18, s56, v36\n"
  "v_dot2_f32_bf16 v37, v18, s57, v37\n"
  "v_dot2_f32_bf16 v38, v19, s56, v38\n"
  "v_dot2_f32_bf16 v39, v19, s57, v39\n"
  "s_waitcnt vmcnt(6)\n"
  "v_dot2_f32_bf16 v40, v20, s56, v40\n"
  "v_dot2_f32_bf16 v41, v20, s57, v41\n"
  "v_dot2_f32_bf16 v42, v21, s56, v42\n"
  "v_dot2_f32_bf16 v43, v21, s57, v43\n"
  "v_dot2_f32_bf16 v44, v22, s56, v44\n"
  "v_dot2_f32_bf16 v45, v22, s57, v45\n"
  "v_dot2_f32_bf16 v46, v23, s56, v46\n"
  "v_dot2_f32_bf16 v47, v23, s57, v47\n"
  "s_waitcnt vmcnt(5)\n"
  "v_dot2_f32_bf16 v48, v24, s56, v48\n"
  "v_dot2_f32_bf16 v49, v24, s57, v49\n"
  "v_dot2_f32_bf16 v50, v25, s56, v50\n"
  "v_dot2_f32_bf16 v51, v25, s57, v51\n"
  "v_dot2_f32_bf16 v52, v26, s56, v52\n"
  "v_dot2_f32_bf16 v53, v26, s57, v53\n"
  "v_dot2_f32_bf16 v54, v27, s56, v54\n"
  "v_dot2_f32_bf16 v55, v27, s57, v55\n"
  "s_waitcnt vmcnt(4)\n"
  "v_dot2_f32_bf16 v56, v28, s56, v56\n"
  "v_dot2_f32_bf16 v57, v28, s57, v57\n"
  "v_dot2_f32_bf16 v58, v29, s56, v58\n"
  "v_dot2_f32_bf16 v59, v29, s57, v59\n"
  "v_dot2_f32_bf16 v60, v30, s56, v60\n"
  "v_dot2_f32_bf16 v61, v30, s57, v61\n"
  "v_dot2_f32_bf16 v62, v31, s56, v62\n"
  "v_dot2_f32_bf16 v63, v31, s57, v63\n"
  "s_waitcnt vmcnt(3)\n"
  "v_dot2_f32_bf16 v32, v64, s56, v32\n"
  "v_dot2_f32_bf16 v33, v64, s57, v33\n"
  "v_dot2_f32_bf16 v34, v65, s56, v34\n"
  "v_dot2_f32_bf16 v35, v65, s57, v35\n"
  "v_dot2_f32_bf16 v36, v66, s56, v36\n"
  "v_dot2_f32_bf16 v37, v66, s57, v37\n"
  "v_dot2_f32_bf16 v38, v67, s56, v38\n"
  "v_dot2_f32_bf16 v39, v67, s57, v39\n"
  "s_waitcnt vmcnt(2)\n"
  "v_dot2_f32_bf16 v40, v68, s56, v40\n"
  "v_dot2_f32_bf16 v41, v68, s57, v41\n"
  "v_dot2_f32_bf16 v42, v69, s56, v42\n"
  "v_dot2_f32_bf16 v43, v69, s57, v43\n"
  "v_dot2_f32_bf16 v44, v70, s56, v44\n"
  "v_dot2_f32_bf16 v45, v70, s57, v45\n"
  "v_dot2_f32_bf16 v46, v71, s56, v46\n"
  "v_dot2_f32_bf16 v47, v71, s57, v47\n"
  "v_cvt_pk_bf16_f32 v32, v32, v33\n"
  "v_cvt_pk_bf16_f32 v33, v34, v35\n"
  "v_cvt_pk_bf16_f32 v34, v36, v37\n"
  "v_cvt_pk_bf16_f32 v35, v38, v39\n"
  "ds_write2_b64 v226, v[32:33], v[34:35] offset0:0 offset1:4\n"
  "s_waitcnt vmcnt(1)\n"
  "v_dot2_f32_bf16 v48, v72, s56, v48\n"
  "v_dot2_f32_bf16 v49, v72, s57, v49\n"
  "v_dot2_f32_bf16 v50, v73, s56, v50\n"
  "v_dot2_f32_bf16 v51, v73, s57, v51\n"
  "v_dot2_f32_bf16 v52, v74, s56, v52\n"
  "v_dot2_f32_bf16 v53, v74, s57, v53\n"
  "v_dot2_f32_bf16 v54, v75, s56, v54\n"
  "v_dot2_f32_bf16 v55, v75, s57, v55\n"
  "v_cvt_pk_bf16_f32 v40, v40, v41\n"
  "v_cvt_pk_bf16_f32 v41, v42, v43\n"
  "v_cvt_pk_bf16_f32 v42, v44, v45\n"
  "v_cvt_pk_bf16_f32 v43, v46, v47\n"
  "ds_write2_b64 v226, v[40:41], v[42:43] offset0:160 offset1:164\n"
  "v_add_u32 v226, 2048, v226\n"
  "s_waitcnt vmcnt(0)\n"
  "v_dot2_f32_bf16 v56, v76, s56, v56\n"
  "v_dot2_f32_bf16 v57, v76, s57, v57\n"
  "v_dot2_f32_bf16 v58, v77, s56, v58\n"
  "v_dot2_f32_bf16 v59, v77, s57, v59\n"
  "v_dot2_f32_bf16 v60, v78, s56, v60\n"
  "v_dot2_f32_bf16 v61, v78, s57, v61\n"
  "v_dot2_f32_bf16 v62, v79, s56, v62\n"
  "v_dot2_f32_bf16 v63, v79, s57, v63\n"
  "v_cvt_pk_bf16_f32 v48, v48, v49\n"
  "v_cvt_pk_bf16_f32 v49, v50, v51\n"
  "v_cvt_pk_bf16_f32 v50, v52, v53\n"
  "v_cvt_pk_bf16_f32 v51, v54, v55\n"
  "ds_write2_b64 v226, v[48:49], v[50:51] offset0:64 offset1:68\n"
  "v_cvt_pk_bf16_f32 v56, v56, v57\n"
  "v_cvt_pk_bf16_f32 v57, v58, v59\n"
  "v_cvt_pk_bf16_f32 v58, v60, v61\n"
  "v_cvt_pk_bf16_f32 v59, v62, v63\n"
  "ds_write2_b64 v226, v[56:57], v[58:59] offset0:224 offset1:228\n"
  "s_waitcnt lgkmcnt(0)\n"
  "ds_read_b128 v[0:3], v245 offset:0\n"
  "ds_read_b128 v[4:7], v245 offset:1280\n"
  "ds_read_b128 v[8:11], v245 offset:2560\n"
  "ds_read_b128 v[12:15], v245 offset:3840\n"
  "s_waitcnt lgkmcnt(3)\n"
  "buffer_store_dwordx4 v[0:3], v244, s[40:43], 0 offen sc1\n"
  "s_waitcnt lgkmcnt(2)\n"
  "buffer_store_dwordx4 v[4:7], v244, s[40:43], s45 offen sc1\n"
  "s_waitcnt lgkmcnt(1)\n"
  "buffer_store_dwordx4 v[8:11], v244, s[40:43], s46 offen sc1\n"
  "s_waitcnt lgkmcnt(0)\n"
  "buffer_store_dwordx4 v[12:15], v244, s[40:43], s47 offen sc1\n"
  "s_endpgm\n"
  "L_wbjob_%=:\n"
  "v_mfma_f32_16x16x32_bf16 v[220:223], a[120:123], a[112:115], 0\n"
  "v_mfma_f32_16x16x32_bf16 v[220:223], a[120:123], a[116:119], v[220:223]\n"
  "v_mov_b32 v236, v248\n"
  "v_mov_b32 v238, v249\n"
  "v_lshlrev_b32 v240, 26, v255\n"
  "s_lshl_b32 s88, s12, 4\n"
  "s_add_u32 s88, s88, 1179648\n"
  "s_nop 2\n"
  "v_add_f32_dpp v220, v220, v220 quad_perm:[1,0,3,2] row_mask:0xf bank_mask:0xf\n"
  "s_nop 1\n"
  "v_add_f32_dpp v220, v220, v220 quad_perm:[2,3,0,1] row_mask:0xf bank_mask:0xf\n"
  "s_nop 1\n"
  "v_add_f32_dpp v220, v220, v220 row_half_mirror row_mask:0xf bank_mask:0xf\n"
  "s_nop 1\n"
  "v_add_f32_dpp v220, v220, v220 row_mirror row_mask:0xf bank_mask:0xf\n"
  "v_mul_f32 v220, 0x3a800000, v220\n"
  "v_cvt_pk_bf16_f32 v221, v220, v220\n"
  "v_and_b32 v221, 0xffff0000, v221\n"
  "v_sub_f32 v220, v220, v221\n"
  "v_cvt_pk_bf16_f32 v237, v221, v220\n"
  "v_mov_b32 v239, v237\n"
  "buffer_store_dwordx4 v[236:239], v240, s[52:55], s88 offen sc1\n"
  "s_branch L_wbret_%=\n"
  "L_end_%=:\n"
  "; ---- attention fallback: pass 1 finds the true row maximum, pass 2 redoes the softmax against it\n"
  "L_att_fb_%=:\n"
  "s_waitcnt vmcnt(0)\n"
  "s_lshl_b32 s9, s92, 18\n"
  "s_add_u32 s32, s30, 8388608\n"
  "s_addc_u32 s33, s31, 0\n"
  "s_add_u32 s32, s32, s9\n"
  "s_addc_u32 s33, s33, 0\n"
  "s_and_b32 s33, s33, 0xffff\n"
  "s_mov_b32 s34, 262144\n"
  "s_mov_b32 s35, 0x20000\n"
  "v_mov_b32 v216, 0xff800000\n"
  "v_mov_b32 v217, 0xff800000\n"
  "s_mov_b32 s8, 0\n"
  "L_att_fb1_%=:\n"
  "buffer_load_dwordx4 a[32:35], v254, s[32:35], s8 offen offset:0 sc1\n"
  "buffer_load_dwordx4 a[36:39], v254, s[32:35], s8 offen offset:1024 sc1\n"
  "s_waitcnt vmcnt(0)\n"
  "v_mfma_f32_32x32x16_bf16 v[0:15], a[32:35], a[16:19], 0\n"
  "v_mfma_f32_32x32x16_bf16 v[0:15], a[36:39], a[20:23], v[0:15]\n"
  "v_mfma_f32_32x32x16_bf16 v[16:31], a[32:35], a[24:27], 0\n"
  "v_mfma_f32_32x32x16_bf16 v[16:31], a[36:39], a[28:31], v[16:31]\n"
  "s_nop 9\n"
  "v_max3_f32 v216, v216, v0, v1\n"
  "v_max3_f32 v216, v216, v2, v3\n"
  "v_max3_f32 v216, v216, v4, v5\n"
  "v_max3_f32 v216, v216, v6, v7\n"
  "v_max3_f32 v216, v216, v8, v9\n"
  "v_max3_f32 v216, v216, v10, v11\n"
  "v_max3_f32 v216, v216, v12, v13\n"
  "v_max3_f32 v216, v216, v14, v15\n"
  "v_max3_f32 v217, v217, v16, v17\n"
  "v_max3_f32 v217, v217, v18, v19\n"
  "v_max3_f32 v217, v217, v20, v21\n"
  "v_max3_f32 v217, v217, v22, v23\n"
  "v_max3_f32 v217, v217, v24, v25\n"
  "v_max3_f32 v217, v217, v26, v27\n"
  "v_max3_f32 v217, v217, v28, v29\n"
  "v_max3_f32 v217, v217, v30, v31\n"
  "s_add_u32 s8, s8, 4096\n"
  "s_cmp_lt_u32 s8, 0x40000\n"
  "s_cbranch_scc1 L_att_fb1_%=\n"
  "v_mov_b32 v220, v216\n"
  "v_mov_b32 v221, v217\n"
  "s_nop 0\n"
  "v_permlane32_swap_b32 v220, v216\n"
  "v_permlane32_swap_b32 v221, v217\n"
  "v_max_f32 v216, v220, v216\n"
  "v_max_f32 v217, v221, v217\n"
  "v_sub_f32 v144, 0, v216\n"
  "v_mov_b32 v145, v144\n"
  "v_mov_b32 v146, v144\n"
  "v_mov_b32 v147, v144\n"
  "v_mov_b32 v148, v144\n"
  "v_mov_b32 v149, v144\n"
  "v_mov_b32 v150, v144\n"
  "v_mov_b32 v151, v144\n"
  "v_mov_b32 v152, v144\n"
  "v_mov_b32 v153, v144\n"
  "v_mov_b32 v154, v144\n"
  "v_mov_b32 v155, v144\n"
  "v_mov_b32 v156, v144\n"
  "v_mov_b32 v157, v144\n"
  "v_mov_b32 v158, v144\n"
  "v_mov_b32 v159, v144\n"
  "v_sub_f32 v160, 0, v217\n"
  "v_mov_b32 v161, v160\n"
  "v_mov_b32 v162, v160\n"
  "v_mov_b32 v163, v160\n"
  "v_mov_b32 v164, v160\n"
  "v_mov_b32 v165, v160\n"
  "v_mov_b32 v166, v160\n"
  "v_mov_b32 v167, v160\n"
  "v_mov_b32 v168, v160\n"
  "v_mov_b32 v169, v160\n"
  "v_mov_b32 v170, v160\n"
  "v_mov_b32 v171, v160\n"
  "v_mov_b32 v172, v160\n"
  "v_mov_b32 v173, v160\n"
  "v_mov_b32 v174, v160\n"
  "v_mov_b32 v175, v160\n"
  "v_mov_b32 v96, 0\n"
  "v_mov_b32 v97, 0\n"
  "v_mov_b32 v98, 0\n"
  "v_mov_b32 v99, 0\n"
  "v_mov_b32 v100, 0\n"
  "v_mov_b32 v101, 0\n"
  "v_mov_b32 v102, 0\n"
  "v_mov_b32 v103, 0\n"
  "v_mov_b32 v104, 0\n"
  "v_mov_b32 v105, 0\n"
  "v_mov_b32 v106, 0\n"
  "v_mov_b32 v107, 0\n"
  "v_mov_b32 v108, 0\n"
  "v_mov_b32 v109, 0\n"
  "v_mov_b32 v110, 0\n"
  "v_mov_b32 v111, 0\n"
  "v_mov_b32 v112, 0\n"
  "v_mov_b32 v113, 0\n"
  "v_mov_b32 v114, 0\n"
  "v_mov_b32 v115, 0\n"
  "v_mov_b32 v116, 0\n"
  "v_mov_b32 v117, 0\n"
  "v_mov_b32 v118, 0\n"
  "v_mov_b32 v119, 0\n"
  "v_mov_b32 v120, 0\n"
  "v_mov_b32 v121, 0\n"
  "v_mov_b32 v122, 0\n"
  "v_mov_b32 v123, 0\n"
  "v_mov_b32 v124, 0\n"
  "v_mov_b32 v125, 0\n"
  "v_mov_b32 v126, 0\n"
  "v_mov_b32 v127, 0\n"
  "v_mov_b32 v136, 0\n"
  "v_mov_b32 v137, 0\n"
  "v_mov_b32 v138, 0\n"
  "v_mov_b32 v139, 0\n"
  "v_mov_b32 v140, 0\n"
  "v_mov_b32 v141, 0\n"
  "v_mov_b32 v142, 0\n"
  "v_mov_b32 v143, 0\n"
  "s_mov_b32 s8, 0\n"
  "L_att_fb2_%=:\n"
  "s_nop 15\n"
  "buffer_load_dwordx4 a[32:35], v254, s[32:35], s8 offen offset:0 sc1\n"
  "buffer_load_dwordx4 a[36:39], v254, s[32:35], s8 offen offset:1024 sc1\n"
  "buffer_load_dwordx4 a[40:43], v254, s[32:35], s8 offen offset:2048 sc1\n"
  "buffer_load_dwordx4 a[44:47], v254, s[32:35], s8 offen offset:3072 sc1\n"
  "s_waitcnt vmcnt(0)\n"
  "v_mfma_f32_32x32x16_bf16 v[0:15], a[32:35], a[16:19], v[144:159]\n"
  "v_mfma_f32_32x32x16_bf16 v[0:15], a[36:39], a[20:23], v[0:15]\n"
  "v_mfma_f32_32x32x16_bf16 v[16:31], a[32:35], a[24:27], v[160:175]\n"
  "v_mfma_f32_32x32x16_bf16 v[16:31], a[36:39], a[28:31], v[16:31]\n"
  "s_nop 9\n"
  "v_exp_f32 v0, v0\n"
  "v_exp_f32 v1, v1\n"
  "v_exp_f32 v2, v2\n"
  "v_exp_f32 v3, v3\n"
  "v_exp_f32 v4, v4\n"
  "v_exp_f32 v5, v5\n"
  "v_exp_f32 v6, v6\n"
  "v_exp_f32 v7, v7\n"
  "v_exp_f32 v8, v8\n"
  "v_exp_f32 v9, v9\n"
  "v_exp_f32 v10, v10\n"
  "v_exp_f32 v11, v11\n"
  "v_exp_f32 v12, v12\n"
  "v_exp_f32 v13, v13\n"
  "v_exp_f32 v14, v14\n"
  "v_exp_f32 v15, v15\n"
  "v_smfmac_f32_32x32x32_bf16 v[96:111], a[40:43], v[0:7], v212\n"
  "s_nop 0\n"
  "v_smfmac_f32_32x32x32_bf16 v[96:111], a[44:47], v[8:15], v212\n"
  "v_smfmac_f32_16x16x64_bf16 v[136:139], v[208:211], v[0:7], v212\n"
  "v_smfmac_f32_16x16x64_bf16 v[136:139], v[208:211], v[8:15], v212\n"
  "v_exp_f32 v16, v16\n"
  "v_exp_f32 v17, v17\n"
  "v_exp_f32 v18, v18\n"
  "v_exp_f32 v19, v19\n"
  "v_exp_f32 v20, v20\n"
  "v_exp_f32 v21, v21\n"
  "v_exp_f32 v22, v22\n"
  "v_exp_f32 v23, v23\n"
  "v_exp_f32 v24, v24\n"
  "v_exp_f32 v25, v25\n"
  "v_exp_f32 v26, v26\n"
  "v_exp_f32 v27, v27\n"
  "v_exp_f32 v28, v28\n"
  "v_exp_f32 v29, v29\n"
  "v_exp_f32 v30, v30\n"
  "v_exp_f32 v31, v31\n"
  "v_smfmac_f32_32x32x32_bf16 v[112:127], a[40:43], v[16:23], v212\n"
  "s_nop 0\n"
  "v_smfmac_f32_32x32x32_bf16 v[112:127], a[44:47], v[24:31], v212\n"
  "v_smfmac_f32_16x16x64_bf16 v[140:143], v[208:211], v[16:23], v212\n"
  "v_smfmac_f32_16x16x64_bf16 v[140:143], v[208:211], v[24:31], v212\n"
  "s_add_u32 s8, s8, 4096\n"
  "s_cmp_lt_u32 s8, 0x40000\n"
  "s_cbranch_scc1 L_att_fb2_%=\n"
  "s_nop 15\n"
  "s_nop 7\n"
  "s_branch L_att_norm_%=\n"
  : "+{s[0:1]}"(ka), "+{s2}"(bid), "+{v0}"(tid), "+{s3}"(lb) : : CLOB);
}

extern "C" void kernel_launch(void* const* d_in, const int* in_sizes, int n_in, void* d_out, int out_size, void* d_ws, size_t ws_size, hipStream_t stream) {
  k_block<<<256, 256, 0, stream>>>(d_in[0], d_in[1], d_in[2], d_in[4], d_in[5], d_in[6], d_out, d_ws);
}
```
